```python
import torch

def _softmax(x, axis=-1):
    return torch.softmax(x, dim=axis)

def _gelu(x):
    return torch.nn.functional.gelu(x, approximate='tanh')

def _silu(x):
    return torch.nn.functional.silu(x)

def _sigmoid(x):
    return torch.sigmoid(x)

def _layernorm(x, axis=-1, eps=1e-05):
    m = x.mean(dim=axis, keepdim=True)
    v = x.var(dim=axis, keepdim=True, unbiased=False)
    return (x - m) / torch.sqrt(v + eps)

def _scatter_add(tgt, idx, src, axis=0):
    return tgt.clone().index_add_(axis, idx, src)

def _segment_sum(src, seg, n):
    out = torch.zeros((n,) + src.shape[1:], dtype=src.dtype)
    return out.index_add_(0, seg, src)

def _pad(x, pw):
    flat = tuple((v for pair in reversed(pw) for v in pair))
    return torch.nn.functional.pad(x, flat)
kernel_name = 'interleave_layers__memory_aug_recurrent_mem_tr__sliding_local_dilated__prompt'

def setup_inputs():

    _w = lambda a: (lambda t: t.to(torch.bfloat16) if t.is_floating_point() else t)(torch.as_tensor(a))

    def _A_setup():
        B, S, Dm = (1, 2048, 640)
        H, Dh = (32, 192)
        M, seg = (8, 32)
        seg = min(seg, S)
        S = S // seg * seg
        return {'x': _w((lambda *s: torch.randn(*s, dtype=torch.bfloat16) * 0.05)(B, S, Dm)), 'mem0': _w((lambda *s: torch.randn(*s, dtype=torch.bfloat16) * 0.8)(B, M, Dm)), 'Wq': _w((lambda *s: torch.randn(*s, dtype=torch.bfloat16) * 1.6)(Dm, H, Dh) * Dm ** (-0.5)), 'Wk': _w((lambda *s: torch.randn(*s, dtype=torch.bfloat16) * 1.6)(Dm, H, Dh) * Dm ** (-0.5)), 'Wv': _w((lambda *s: torch.randn(*s, dtype=torch.bfloat16) * 0.8)(Dm, H, Dh) * Dm ** (-0.5)), 'Wo': _w((lambda *s: torch.randn(*s, dtype=torch.bfloat16) * 0.8)(H, Dh, Dm) * (H * Dh) ** (-0.5)), 'seg_len': seg}
    _A_in = _A_setup()

    def _B_setup():
        B, S, D = (1, 2048, 640)
        return {'x': _w((lambda *s: torch.randn(*s, dtype=torch.bfloat16) * 0.05)(B, S, D)), 'wqkv': _w((lambda *s: torch.randn(*s, dtype=torch.bfloat16) * 0.05)(D, 3 * D)), 'wo': _w((lambda *s: torch.randn(*s, dtype=torch.bfloat16) * 0.05)(D, D))}
    _B_in = _B_setup()
    return {'A_x': _A_in['x'], 'A_mem0': _A_in['mem0'], 'A_Wq': _A_in['Wq'], 'A_Wk': _A_in['Wk'], 'A_Wv': _A_in['Wv'], 'A_Wo': _A_in['Wo'], 'A_seg_len': _A_in['seg_len'], 'B_x': _B_in['x'], 'B_wqkv': _B_in['wqkv'], 'B_wo': _B_in['wo']}

def reference(A_x, A_mem0, A_Wq, A_Wk, A_Wv, A_Wo, A_seg_len, B_x, B_wqkv, B_wo):

    def _A_ref(x, mem0, Wq, Wk, Wv, Wo, seg_len):
        B, S, Dm = x.shape
        H, Dh = (Wq.shape[1], Wq.shape[2])
        M = mem0.shape[1]
        scale = Dh ** (-0.5)
        n_seg = S // seg_len
        L = M + seg_len
        idx = torch.arange(L)
        mask = (idx[:, None] < M) | (idx[:, None] >= idx[None, :])
        mem = mem0
        outs = []
        for s in range(n_seg):
            xs = x[:, s * seg_len:(s + 1) * seg_len, :].clone()
            h = torch.concatenate([mem, xs], axis=1)
            q = torch.einsum('btd,dhe->bhte', h, Wq)
            k = torch.einsum('btd,dhe->bhte', h, Wk)
            v = torch.einsum('btd,dhe->bhte', h, Wv)
            scores = torch.einsum('bhqe,bhke->bhqk', q, k) * scale
            scores = torch.where(mask[None, None], scores, -1000000000.0)
            p = _softmax(scores, axis=-1)
            o = torch.einsum('bhqk,bhke->bhqe', p, v)
            o = torch.einsum('bhte,hed->btd', o, Wo)
            mem = o[:, :M, :].clone()
            outs.append(o[:, M:, :])
        return torch.concatenate(outs, axis=1)

    def _B_ref(x, wqkv, wo):
        B, S, D = x.shape
        H = 32
        Dh = D // H
        W, dil = (8, 2)
        qkv = (lambda x, *d: x.permute(*d))((x @ wqkv).reshape(B, S, 3, H, Dh), 2, 0, 3, 1, 4)
        q, k, v = (qkv[0], qkv[1], qkv[2])
        scale = Dh ** (-0.5)
        pos = torch.arange(S)
        off = torch.arange(W) * dil
        idx = pos[:, None] - off[None, :]
        mask = idx >= 0
        idx_c = torch.where(mask, idx, 0).reshape(-1)
        kg = k[:, :, idx_c, :].reshape(B, H, S, W, Dh)
        vg = v[:, :, idx_c, :].reshape(B, H, S, W, Dh)
        logits = torch.einsum('bhsd,bhswd->bhsw', q, kg) * scale
        logits = torch.where(mask[None, None, :, :], logits, -1000000000.0)
        attn = _softmax(logits, axis=-1)
        o = torch.einsum('bhsw,bhswd->bhsd', attn, vg)
        return (lambda x, *d: x.permute(*d))(o, 0, 2, 1, 3).reshape(B, S, D) @ wo
    x = A_x
    x = x + _A_ref(_layernorm(x, axis=-1), A_mem0, A_Wq, A_Wk, A_Wv, A_Wo, A_seg_len)
    x = x + _B_ref(_layernorm(x, axis=-1), B_wqkv, B_wo)
    x = x + _A_ref(_layernorm(x, axis=-1), A_mem0, A_Wq, A_Wk, A_Wv, A_Wo, A_seg_len)
    x = x + _B_ref(_layernorm(x, axis=-1), B_wqkv, B_wo)
    return x
```

```cpp
#include <hip/hip_runtime.h>
#include <hip/hip_bf16.h>
#include <stdint.h>

typedef unsigned short bf16_t;
typedef short bf16x8 __attribute__((ext_vector_type(8)));
typedef short bf16x4 __attribute__((ext_vector_type(4)));
typedef float f32x4 __attribute__((ext_vector_type(4)));
typedef unsigned u32x4 __attribute__((ext_vector_type(4)));
typedef unsigned u32x2 __attribute__((ext_vector_type(2)));
typedef unsigned long long u64;
typedef __bf16 bf16v2 __attribute__((ext_vector_type(2)));
typedef float f32v2 __attribute__((ext_vector_type(2)));

__device__ __forceinline__ float bf2f(bf16_t v) { return __uint_as_float(((unsigned)v) << 16); }
__device__ __forceinline__ unsigned pk2(float a, float b) {
    f32v2 f = {a, b};
    bf16v2 r = __builtin_convertvector(f, bf16v2);
    return __builtin_bit_cast(unsigned, r);
}
__device__ __forceinline__ float rb(float a) { return __uint_as_float((pk2(a, 0.f) & 0xffffu) << 16); }
__device__ __forceinline__ float lo16(unsigned u) { return __uint_as_float(u << 16); }
__device__ __forceinline__ float hi16(unsigned u) { return __uint_as_float(u & 0xffff0000u); }
__device__ __forceinline__ bf16x4 pk4(f32x4 v) {
    u32x2 u = {pk2(v[0], v[1]), pk2(v[2], v[3])};
    return __builtin_bit_cast(bf16x4, u);
}
__device__ __forceinline__ bf16x8 cat8(bf16x4 a, bf16x4 b) { return bf16x8{a[0], a[1], a[2], a[3], b[0], b[1], b[2], b[3]}; }
__device__ __forceinline__ bf16x8 asb8(u32x4 v) { return __builtin_bit_cast(bf16x8, v); }
__device__ __forceinline__ f32x4 asf4(u32x4 v) { return __builtin_bit_cast(f32x4, v); }
__device__ __forceinline__ f32x4 mfma32(bf16x8 a, bf16x8 b, f32x4 c) { return __builtin_amdgcn_mfma_f32_16x16x32_bf16(a, b, c, 0, 0, 0); }
__device__ __forceinline__ f32x4 mfma16(bf16x4 a, bf16x4 b, f32x4 c) { return __builtin_amdgcn_mfma_f32_16x16x16bf16_1k(a, b, c, 0, 0, 0); }

#define MF_(acc, ca, a, cb, b) asm volatile("v_mfma_f32_16x16x32_bf16 %0, %1, %2, %0" : "+v"(acc) : ca(a), cb(b))
#define MF0_(acc, ca, a, cb, b) asm volatile("v_mfma_f32_16x16x32_bf16 %0, %1, %2, 0" : "=&v"(acc) : ca(a), cb(b))
#define MF_AV(acc, a, b) MF_(acc, "a", a, "v", b)
#define MF_VA(acc, a, b) MF_(acc, "v", a, "a", b)
#define MF_VV(acc, a, b) MF_(acc, "v", a, "v", b)
#define MF_AV0(acc, a, b) MF0_(acc, "a", a, "v", b)
#define MF_VA0(acc, a, b) MF0_(acc, "v", a, "a", b)
#define MF_VV0(acc, a, b) MF0_(acc, "v", a, "v", b)
#define MH_(acc, ca, a, cb, b) asm volatile("v_mfma_f32_16x16x16_bf16 %0, %1, %2, %0" : "+v"(acc) : ca(a), cb(b))
#define MH0_(acc, ca, a, cb, b) asm volatile("v_mfma_f32_16x16x16_bf16 %0, %1, %2, 0" : "=&v"(acc) : ca(a), cb(b))

constexpr int S_ = 2048, D_ = 640, H_ = 32, DH_ = 192, M_ = 8, SEG_ = 32, NSEG_ = 64;
constexpr int HD_ = H_ * DH_;
#define RLX_AGENT __ATOMIC_RELAXED, __HIP_MEMORY_SCOPE_AGENT
#define AUX_SC1 16
typedef __amdgpu_buffer_rsrc_t rsrc_t;
__device__ __forceinline__ rsrc_t mk_rsrc(const void* p, unsigned bytes) { return __builtin_amdgcn_make_buffer_rsrc((void*)p, 0, (int)bytes, 0x00020000); }
__device__ __forceinline__ u32x4 bld16(rsrc_t r, unsigned off) { return __builtin_amdgcn_raw_buffer_load_b128(r, off, 0, AUX_SC1); }
__device__ __forceinline__ u32x2 bld8(rsrc_t r, unsigned off) { return __builtin_amdgcn_raw_buffer_load_b64(r, off, 0, AUX_SC1); }
__device__ __forceinline__ unsigned bld4(rsrc_t r, unsigned off) { return __builtin_amdgcn_raw_buffer_load_b32(r, off, 0, AUX_SC1); }
__device__ __forceinline__ void bst16(rsrc_t r, unsigned off, u32x4 v) { __builtin_amdgcn_raw_buffer_store_b128(v, r, off, 0, AUX_SC1); }
__device__ __forceinline__ void bst8(rsrc_t r, unsigned off, u32x2 v) { __builtin_amdgcn_raw_buffer_store_b64(v, r, off, 0, AUX_SC1); }
__device__ __forceinline__ void bst4(rsrc_t r, unsigned off, unsigned v) { __builtin_amdgcn_raw_buffer_store_b32(v, r, off, 0, AUX_SC1); }
#define LDS_BARRIER() asm volatile("s_waitcnt lgkmcnt(0)\n\ts_barrier" ::: "memory")
#define DRAIN() asm volatile("s_waitcnt vmcnt(0)" ::: "memory")
#define MEMFENCE() asm volatile("" ::: "memory")
#define SCHED0() __builtin_amdgcn_sched_barrier(0)
__device__ __forceinline__ float red_max_q(float x) {
    auto a = __builtin_amdgcn_permlane16_swap(__float_as_uint(x), __float_as_uint(x), false, false);
    x = fmaxf(__uint_as_float(a[0]), __uint_as_float(a[1]));
    auto c = __builtin_amdgcn_permlane32_swap(__float_as_uint(x), __float_as_uint(x), false, false);
    return fmaxf(__uint_as_float(c[0]), __uint_as_float(c[1]));
}
__device__ __forceinline__ float max3r(float a, float b, float c) { float r; asm("v_max3_f32 %0, %1, %2, %3" : "=v"(r) : "v"(a), "v"(b), "v"(c)); return r; }
__device__ __forceinline__ float max2r(float a, float b) { float r; asm("v_max_f32 %0, %1, %2" : "=v"(r) : "v"(a), "v"(b)); return r; }
__device__ __forceinline__ void red_max_q2(float& x, float& y) {
    float s, t;
    asm("v_mov_b32 %2, %0\n\tv_mov_b32 %3, %1\n\ts_nop 0\n\tv_permlane16_swap_b32 %0, %2\n\tv_permlane16_swap_b32 %1, %3\n\tv_max_f32 %0, %0, %2\n\tv_max_f32 %1, %1, %3\n\t"
        "v_mov_b32 %2, %0\n\tv_mov_b32 %3, %1\n\ts_nop 0\n\tv_permlane32_swap_b32 %0, %2\n\tv_permlane32_swap_b32 %1, %3\n\tv_max_f32 %0, %0, %2\n\tv_max_f32 %1, %1, %3"
        : "+v"(x), "+v"(y), "=&v"(s), "=&v"(t));
}
__device__ __forceinline__ void red_sum_q2(float& x, float& y) {
    float s, t;
    asm("v_mov_b32 %2, %0\n\tv_mov_b32 %3, %1\n\ts_nop 0\n\tv_permlane16_swap_b32 %0, %2\n\tv_permlane16_swap_b32 %1, %3\n\tv_add_f32 %0, %0, %2\n\tv_add_f32 %1, %1, %3\n\t"
        "v_mov_b32 %2, %0\n\tv_mov_b32 %3, %1\n\ts_nop 0\n\tv_permlane32_swap_b32 %0, %2\n\tv_permlane32_swap_b32 %1, %3\n\tv_add_f32 %0, %0, %2\n\tv_add_f32 %1, %1, %3"
        : "+v"(x), "+v"(y), "=&v"(s), "=&v"(t));
}
__device__ __forceinline__ float red_sum_q(float x) {
    auto a = __builtin_amdgcn_permlane16_swap(__float_as_uint(x), __float_as_uint(x), false, false);
    x = __uint_as_float(a[0]) + __uint_as_float(a[1]);
    auto c = __builtin_amdgcn_permlane32_swap(__float_as_uint(x), __float_as_uint(x), false, false);
    return __uint_as_float(c[0]) + __uint_as_float(c[1]);
}
__device__ __forceinline__ u32x4 pk8(f32x4 a, f32x4 b) { return u32x4{pk2(a[0], a[1]), pk2(a[2], a[3]), pk2(b[0], b[1]), pk2(b[2], b[3])}; }
__device__ __forceinline__ u32x4 tag4(f32x4 v, unsigned tg) {
    return u32x4{(__float_as_uint(v[0]) & ~3u) | tg, (__float_as_uint(v[1]) & ~3u) | tg, (__float_as_uint(v[2]) & ~3u) | tg, (__float_as_uint(v[3]) & ~3u) | tg};
}
__device__ __forceinline__ unsigned tagbad(u32x4 v, unsigned tg) { return ((v[0] ^ tg) | (v[1] ^ tg) | (v[2] ^ tg) | (v[3] ^ tg)) & 3u; }

__device__ __host__ __forceinline__ constexpr unsigned xm(unsigned o, unsigned side) { return ((o >> 13) << 14) | (o & 8191u) | ((((o >> 20) ^ side) & 1u) << 13); }
__device__ __host__ __forceinline__ constexpr unsigned lp(unsigned o) { return ((o >> 13) << 14) | (o & 8191u); }
__device__ __host__ __forceinline__ constexpr int hside(int h) { return h >> 4; }
constexpr unsigned XS_OFF = 16384;
constexpr unsigned XM_OFF = 24576, XM_BUF = 4 * 32768;
constexpr unsigned XP_OFF = XM_OFF + 3 * XM_BUF, XP_BUF = 32768;
constexpr unsigned PT_OFF = XP_OFF + 3 * XP_BUF, PT_BUF = 65536;
constexpr unsigned FI_OFF = PT_OFF + 3 * PT_BUF, FI_N = 80;
constexpr unsigned FX_OFF = FI_OFF + FI_N * 1024;
constexpr unsigned A_END = FX_OFF + FI_N * 256;
constexpr unsigned UM_OFF = 1u << 20, UM_BUF = 40 * 8192;
#define TR 8
constexpr unsigned UT_OFF = 2u << 20;
constexpr unsigned QK_OFF = UT_OFF + TR * (2u << 20);
constexpr unsigned SG_OFF = QK_OFF + TR * (1u << 20);
enum { SG_XL = 0, SG_XR = 40960, SG_X1 = 81920, SG_OB = 122880, SG_ST = 163840, SG_FL = 172032 };
enum { FL_U = 0, FL_X = 2048, FL_O = 4096, ST_2 = 6144 };
constexpr unsigned ARENA = 2 * (SG_OFF + 64 * 262144);
static_assert(A_END <= UM_OFF && UM_OFF + 3 * UM_BUF <= UT_OFF && QK_OFF % (1u << 21) == 0 && SG_OFF % (1u << 21) == 0, "map");
static_assert(ARENA + (1u << 22) + (1u << 19) < (1u << 28), "workspace");
__device__ __forceinline__ unsigned seg_base(int layer, int seg) { return 2 * SG_OFF + (unsigned)seg * 524288 + ((((seg >> 2) ^ layer) & 1) << 13); }
__device__ __forceinline__ unsigned ut_slot(int seg) { return (unsigned)(seg & (TR - 1)) << 22; }
__device__ __forceinline__ unsigned qk_base(int seg, int side) { return 2 * QK_OFF + ((unsigned)(seg & (TR - 1)) << 21) + (((seg ^ side) & 1) << 13); }
__device__ __forceinline__ unsigned fi_step(int it) { return ((it >> 3) << 14) | ((it & 7) << 10); }
__device__ __forceinline__ unsigned fx_step(int it) { return ((it >> 5) << 14) | ((it & 31) << 8); }
static_assert(FI_OFF % 8192 == 0 && FX_OFF % 8192 == 0, "steps");
__device__ __forceinline__ u32x4 sld16(rsrc_t r, unsigned so, unsigned lo) { return __builtin_amdgcn_raw_buffer_load_b128(r, lo, so, AUX_SC1); }
__device__ __forceinline__ u32x2 sld8(rsrc_t r, unsigned so, unsigned lo) { return __builtin_amdgcn_raw_buffer_load_b64(r, lo, so, AUX_SC1); }
__device__ __forceinline__ unsigned sld4(rsrc_t r, unsigned so, unsigned lo) { return __builtin_amdgcn_raw_buffer_load_b32(r, lo, so, AUX_SC1); }
__device__ __forceinline__ void sst4(rsrc_t r, unsigned so, unsigned lo, unsigned v) { __builtin_amdgcn_raw_buffer_store_b32(v, r, lo, so, AUX_SC1); }
__device__ __forceinline__ void sst16(rsrc_t r, unsigned so, unsigned lo, u32x4 v) { __builtin_amdgcn_raw_buffer_store_b128(v, r, lo, so, AUX_SC1); }
__device__ __forceinline__ void sst8(rsrc_t r, unsigned so, unsigned lo, u32x2 v) { __builtin_amdgcn_raw_buffer_store_b64(v, r, lo, so, AUX_SC1); }
__device__ __forceinline__ unsigned umax3(unsigned a, unsigned b, unsigned c) { return max(max(a, b), c); }
__device__ __forceinline__ unsigned mx4(unsigned m, u32x4 v) { return umax3(m, v[0], v[2]); }
__device__ __forceinline__ unsigned nosent(unsigned x) { return min(x, 0xfffffffeu); }
constexpr unsigned OOB = 0x7f000000u;
constexpr unsigned kSpinCap = 1u << 18;

struct TrJobs { const bf16_t* x; const bf16_t* mem0; char* ar; unsigned* tmo; int lag, pad; };
__device__ __forceinline__ void ln_row_frag(const bf16_t* __restrict__ x, char* __restrict__ ar, int row) {
    const int t = threadIdx.x & 63;
    const bf16_t* xr = x + (size_t)row * D_;
    const u32x4 a = *(const u32x4*)(xr + 8 * t);
    const u32x4 b = (t < 16) ? *(const u32x4*)(xr + 512 + 8 * t) : u32x4{0u, 0u, 0u, 0u};
    float v[16];
#pragma unroll
    for (int i = 0; i < 4; ++i) { v[2 * i] = lo16(a[i]); v[2 * i + 1] = hi16(a[i]); v[8 + 2 * i] = lo16(b[i]); v[9 + 2 * i] = hi16(b[i]); }
    float s = 0.f;
#pragma unroll
    for (int i = 0; i < 16; ++i) s += v[i];
    for (int o = 32; o > 0; o >>= 1) s += __shfl_xor(s, o);
    const float mean = s * (1.0f / D_);
    float q = 0.f;
#pragma unroll
    for (int i = 0; i < 16; ++i) { const float d = v[i] - mean; q += (i < 8 || t < 16) ? d * d : 0.f; }
    for (int o = 32; o > 0; o >>= 1) q += __shfl_xor(q, o);
    const float mb = rb(mean), den = rb(sqrtf(rb(rb(q * (1.0f / D_)) + 1e-5f)));
    u32x4 oa, ob;
#pragma unroll
    for (int i = 0; i < 4; ++i) {
        oa[i] = pk2(rb(v[2 * i] - mb) / den, rb(v[2 * i + 1] - mb) / den);
        ob[i] = pk2(rb(v[8 + 2 * i] - mb) / den, rb(v[9 + 2 * i] - mb) / den);
    }
    char* sb = ar + seg_base(0, row >> 5);
    const unsigned o = ((row >> 4) & 1) * 1024 + (row & 15) * 16 + (t >> 2) * 2048 + (t & 1) * 512 + ((t >> 1) & 1) * 8;
    auto put = [&](unsigned p, u32x4 x) { *(u32x2*)(sb + lp(p)) = u32x2{x[0], x[1]}; *(u32x2*)(sb + lp(p + 256)) = u32x2{x[2], x[3]}; };
    put(SG_XL + o, oa); put(SG_XR + o, a);
    if (t < 16) { put(SG_XL + o + 32768, ob); put(SG_XR + o + 32768, b); }
}
__device__ __forceinline__ char* arena_base(char* p) { return (char*)(((size_t)p + 0x3fffff) & ~(size_t)0x3fffff); }
__global__ void __launch_bounds__(256) k_prologue(TrJobs jin) {
    TrJobs jobs = jin;
    jobs.ar = arena_base(jin.ar);
    const int e = blockIdx.x;
    if (e < S_ / 4) { ln_row_frag(jobs.x, jobs.ar, e * 4 + (threadIdx.x >> 6)); return; }
    constexpr unsigned N1 = A_END / 16, N2 = N1 + 3 * UM_BUF / 16, N3 = N2 + 64 * 1024;
    for (unsigned j = (e - S_ / 4) * 256 + threadIdx.x; j < 2 * N3; j += 512 * 256) {
        const unsigned side = j >= N3, i = j - side * N3;
        unsigned f = (i >= XM_OFF / 16 && i < FI_OFF / 16 && (i - (PT_OFF + 2 * PT_BUF) / 16) >= PT_BUF / 16) || (i >= N1 && i < N2) ? ~0u : 0u;
        if (i >= FI_OFF / 16 && i < FX_OFF / 16) {
            const int it = (i - FI_OFF / 16) >> 6, sg = it - (i & 1) * jobs.lag;
            f = sg < 0 || sg >= NSEG_;
        }
        if (i >= N2) f = ((i - N2) & 512) ? 0u : ~0u;
        u32x4 v = {f, f, f, f};
        if (i >= XM_OFF / 16 && i < (XM_OFF + XM_BUF) / 16 && (((i - XM_OFF / 16) >> 6) & 7) < 5) {
            const unsigned p = (i - XM_OFF / 16) & 2047, ks = 5 * (p >> 9) + ((p >> 6) & 7);
            const bf16_t* mp = jobs.mem0 + (p & 7) * D_ + 32 * ks + 4 * ((p >> 4) & 3);
            const u32x2 x = *(const u32x2*)mp, y = *(const u32x2*)(mp + 16);
            v = u32x4{nosent(x[0]), x[1], nosent(y[0]), y[1]};
        }
        *(u32x4*)(jobs.ar + (i < N1 ? xm(i * 16, side) : (i < N2 ? xm(UM_OFF + (i - N1) * 16, side) : seg_base(side, (i - N2) >> 10) + lp(SG_ST + ((i - N2) & 1023) * 16)))) = v;
    }
    if (e == S_ / 4 && threadIdx.x == 0) *jobs.tmo = 0u;
}

typedef __attribute__((address_space(3))) bf16x4 lds_b4;
typedef __attribute__((address_space(3))) volatile int lds_int;
template <int V> struct IC { static constexpr int value = V; };
template <int B, int E, class F> __device__ __forceinline__ void sfor(F&& f) { if constexpr (B < E) { f(IC<B>{}); sfor<B + 1, E>(f); } }
__device__ __forceinline__ void dma_next(const void* base, unsigned& vo, unsigned step, unsigned adv) {
    asm volatile("global_load_lds_dwordx4 %0, %1\n\ts_add_u32 m0, m0, %2\n\tv_add_u32 %0, %3, %0" : "+v"(vo) : "s"(base), "s"(step), "s"(adv) : "memory", "m0", "scc");
}
__device__ __forceinline__ void set_m0(unsigned x) { asm volatile("s_mov_b32 m0, %0\n\ts_nop 0" :: "s"(x) : "memory", "m0"); }
__device__ __forceinline__ bf16x8 tr_read(const char* p, int o0, int o1) { return cat8(__builtin_amdgcn_ds_read_tr16_b64_v4i16((lds_b4*)(p + o0)), __builtin_amdgcn_ds_read_tr16_b64_v4i16((lds_b4*)(p + o1))); }
template <int N, int G, bool PERM, class ADV, class DST>
__device__ __forceinline__ void load_frags(char* smem, const int w, const int l, const void* base, unsigned (&vo)[G], ADV adv, DST dst) {
    constexpr int PB = 16 / G, NB = (N + PB - 1) / PB;
    const unsigned sta = (unsigned)(size_t)(__attribute__((address_space(3))) char*)smem + w * 32768;
    const char* rp = smem + w * 32768 + (PERM ? 4 * (l >> 4) + ((l & 15) >> 2) : 8 * (l >> 4) + ((l & 15) >> 2)) * (32 * G) + (l & 3) * 8;
    auto issue = [&](int b) {
        if (!(b & 1)) set_m0(sta);
#pragma unroll
        for (int m = 0; m < PB * G; ++m) dma_next(base, vo[m % G], 1024u, PB * b + m / G + 1 < N ? adv(PB * b + m / G) : 0u);
    };
    issue(0);
#pragma unroll
    for (int b = 0; b < NB; ++b) {
        if (b + 1 < NB) { issue(b + 1); asm volatile("s_waitcnt vmcnt(%0)" :: "n"(PB * G) : "memory"); }
        else asm volatile("s_waitcnt vmcnt(0)" ::: "memory");
#pragma unroll
        for (int m = 0; m < PB * G; ++m) {
            const int o = ((b & 1) * 16 + m / G * G) * 1024 + (m % G) * 32;
            if (PB * b + m / G < N) dst(PB * b + m / G, m % G) = tr_read(rp, o, o + (PERM ? 16 : 4) * (32 * G));
        }
        asm volatile("s_waitcnt lgkmcnt(0)" ::: "memory");
    }
}
template <int NI, int R, int NTT, int NA = 64>
__device__ __forceinline__ void load_coop(char* smem, const int w, const void* const (&base)[NI], unsigned (&vo)[NI], const unsigned adv,
                                          const int (&rp)[NTT], const int (&rs)[NTT], bf16x8 (&wf)[NTT][20]) {
    constexpr int NS = NI > 4 ? 6 : 8, SL = NI > 4 ? 24576 : 16384, AH = NS - 1;
    const unsigned sta = (unsigned)(size_t)(__attribute__((address_space(3))) char*)smem + w * 1024;
    auto issue = [&](int ks) {
        if (ks % NS == 0) set_m0(sta);
#pragma unroll
        for (int m = 0; m < NI; ++m) dma_next(base[m], vo[m], m + 1 < NI ? 4096u : SL - 4096u * (NI - 1), ks < 19 ? adv : 0u);
    };
#pragma unroll
    for (int ks = 0; ks < AH; ++ks) issue(ks);
    auto run = [&](auto WW) {
        constexpr int ww = decltype(WW)::value;
#pragma unroll
        for (int ks = 0; ks <= 20; ++ks) {
            asm volatile("s_waitcnt vmcnt(%0) lgkmcnt(0)\n\ts_barrier" :: "n"(ks < 20 ? (19 - ks < AH - 1 ? 19 - ks : AH - 1) * NI : 0) : "memory");
#pragma unroll
            for (int tt = 0; tt < NTT; ++tt) {
                if (ks > 0) { bf16x8& d = wf[tt][(ks + 19 - R * ww) % 20]; if (20 * tt + (ks + 19 - R * ww) % 20 < NA) asm volatile("" : "+a"(d)); else asm volatile("" : "+v"(d)); }
            }
            if (ks == 20) break;
            if (ks + AH < 20) issue(ks + AH);
            const int hi = NI > 4 ? ks % NS / 2 * 2 : ks % NS / 4 * 4, lo = ks % NS - hi;
#pragma unroll
            for (int tt = 0; tt < NTT; ++tt) wf[tt][(ks + 20 - R * ww) % 20] = tr_read(smem + hi * SL, rp[tt] + lo * SL, rs[tt] + lo * SL);
        }
    };
    if (R == 0 || w == 0) run(IC<0>{}); else if (w == 1) run(IC<1>{}); else if (w == 2) run(IC<2>{}); else run(IC<3>{});
}

struct MegaArgs {
    const bf16_t* x0; const bf16_t* mem0; const bf16_t* Wq; const bf16_t* Wk; const bf16_t* Wv; const bf16_t* Wo; const bf16_t* wqkv; const bf16_t* wo;
    char* ar; unsigned* tmo; unsigned long long* dbg; bf16_t* out;
    int nit, lag, roles, pad;
};

#define WSLP 2
#define LIVE u64 live_; asm volatile("s_add_u32 %0, %0, 1\n\ts_cmp_gt_u32 %0, %2\n\ts_cselect_b64 %1, 0, -1" : "+s"(spins), "=s"(live_) : "n"(kSpinCap) : "scc")
#define BAD(m) (__ballot(m == ~0u) & live_)
#define SPIN_END if (spins > kSpinCap) __hip_atomic_store(A.tmo, 1u + id, RLX_AGENT)
__device__ __forceinline__ void wave_wait(rsrc_t rx, unsigned off, unsigned so, unsigned& spins) {
#pragma unroll 1
    for (;;) {
        const unsigned v = sld4(rx, so, off);
        LIVE;
        if (!(__ballot(v == 0 && off != OOB) & live_)) break;
        __builtin_amdgcn_s_sleep(WSLP);
        MEMFENCE();
    }
}
__device__ __forceinline__ unsigned rtime() { return (unsigned)__builtin_amdgcn_s_memrealtime(); }
__device__ __forceinline__ void rt_until(unsigned target) {
    asm volatile("1:\n\ts_memrealtime vcc\n\ts_waitcnt lgkmcnt(0)\n\ts_sub_u32 vcc_lo, %0, vcc_lo\n\ts_cmpk_lt_u32 vcc_lo, 600\n\ts_cbranch_scc1 1b" :: "s"(target - 1) : "memory", "scc", "vcc");
}
#define RT_K 26
__device__ __forceinline__ void rt_fine(unsigned target) {
    u64 a, b, c;
    unsigned n;
    asm volatile("1:\n\ts_memrealtime vcc\n\ts_waitcnt lgkmcnt(0)\n\ts_sub_u32 vcc_lo, %4, vcc_lo\n\ts_cmpk_lt_u32 vcc_lo, 600\n\ts_cbranch_scc1 1b\n\t"
                 "s_add_u32 vcc_lo, vcc_lo, 8\n\ts_cbranch_scc0 3f\n\t"
                 "s_memrealtime %0\n\ts_nop 4\n\ts_memrealtime %1\n\ts_nop 4\n\ts_memrealtime %2\n\ts_lshl_b32 %3, %4, 2\n\ts_addk_i32 %3, %5\n\ts_nop 2\n\ts_memrealtime vcc\n\ts_waitcnt lgkmcnt(0)\n\t"
                 "s_sub_u32 %3, %3, vcc_lo\n\ts_mov_b64 vcc, %0\n\ts_sub_u32 %3, %3, vcc_lo\n\ts_mov_b64 vcc, %1\n\ts_sub_u32 %3, %3, vcc_lo\n\ts_mov_b64 vcc, %2\n\ts_sub_u32 %3, %3, vcc_lo\n\t"
                 "s_max_i32 %3, %3, 0\n\ts_min_i32 %3, %3, 64\n\t"
                 "s_branch 2f\n\t"
                 "3:\n\ts_add_u32 %3, %4, 13\n\t"
                 "4:\n\ts_memrealtime vcc\n\ts_waitcnt lgkmcnt(0)\n\ts_sub_u32 vcc_lo, %3, vcc_lo\n\ts_cmpk_lt_u32 vcc_lo, 16\n\ts_cbranch_scc1 4b\n\t"
                 "s_mov_b32 %3, 0\n\t"
                 "2:\n\ts_sub_u32 %3, %3, 5\n\ts_cbranch_scc0 2b"
                 : "=&s"(a), "=&s"(b), "=&s"(c), "=&s"(n) : "s"(target - 14), "n"(56 - RT_K) : "memory", "scc", "vcc");
}
#define LDA(dst, vo, so, imm) asm volatile("buffer_load_dwordx4 %0, %1, %2, %3 offen offset:%4 sc1" : "=v"(dst) : "v"(vo), "s"(rx), "s"(so), "n"(imm) : "memory")
#define LDI(dst, vo, so, imm) asm volatile("buffer_load_dwordx4 %0, %1, %2, %3 offen offset:%4 sc1" : "+v"(dst) : "v"(vo), "s"(rx), "s"(so), "n"(imm) : "memory")
#define LDA8(dst, vo, so, imm) asm volatile("buffer_load_dwordx2 %0, %1, %2, %3 offen offset:%4 sc1" : "=v"(dst) : "v"(vo), "s"(rx), "s"(so), "n"(imm) : "memory")
#define LDA4(dst, vo, so) asm volatile("buffer_load_dword %0, %1, %2, %3 offen sc1" : "=v"(dst) : "v"(vo), "s"(rx), "s"(so) : "memory")
#define WAITV(n, x) asm volatile("s_waitcnt vmcnt(%1)" : "+v"(x) : "n"(n) : "memory")
#define PIN_S(x) asm volatile("" : "+s"(x))
#define SZERO(x) asm volatile("s_mov_b32 %0, 0" : "=s"(x))
#define PIN_V(x) asm volatile("" : "+v"(x))
__device__ __forceinline__ float dpp_ror8(float x) { return __uint_as_float(__builtin_amdgcn_mov_dpp(__float_as_uint(x), 0x128, 0xf, 0xf, true)); }
__device__ __forceinline__ unsigned lds_addr(const void* p) { return (unsigned)(size_t)(__attribute__((address_space(3))) const char*)p; }
#define DMA1(vo, so, dst, imm) asm volatile("s_mov_b32 m0, %3\n\ts_nop 0\n\tbuffer_load_dwordx4 %0, %1, %2 offen offset:%4 sc1 lds" :: "v"(vo), "s"(rx), "s"(so), "s"(dst), "n"(imm) : "memory", "m0")

#define LAGV 7
static_assert(LAGV < TR, "rings");
#define VSLP 5
#define PER 296
#define E_U 120
#define E_P 140
#define E_T 205
#define E_X 40
#define E_B 100
#define E_S 16
#define E_G 54
#define CHAIN_WAIT(pv, off, so, code) if (__builtin_expect(__ballot(pv == 0) != 0, 0)) { if (so != OOB) { wave_wait(rx, off, so, spins); } }
#define GAPN 11
#define GAP() asm volatile("s_nop %0" :: "n"(GAPN))
#define LD5(v, so) do { LDA(v[0], l16b, so, 0); GAP(); LDA(v[1], l16b, so, 1024); GAP(); LDA(v[2], l16b, so, 2048); GAP(); LDA(v[3], l16b, so, 3072); GAP(); LDA(v[4], l16k, so, 0); } while (0)
#define CHAIN_IDS const int t = threadIdx.x, l = t & 63, l16 = l & 15, lq = l >> 4; const int w = __builtin_amdgcn_readfirstlane(t >> 6); \
    const rsrc_t rx = mk_rsrc(arena_base(A.ar), ARENA); const int lag = A.lag, itend = A.nit; const unsigned l16b = l * 16, l16k = l * 16 + 4096; unsigned spins = 0
#define CHAIN_STAGE const char* sg1 = smem + ((w + 1) & 3) * 5120 + l * 16; const char* sg2 = smem + ((w + 2) & 3) * 5120 + l * 16; const char* sg3 = smem + ((w + 3) & 3) * 5120 + l * 16; \
    char* sg0 = smem + w * 5120 + l * 16; auto mrd = [&](int i) { return *(const bf16x8*)((i < 10 ? sg1 : (i < 15 ? sg2 : sg3)) + (i % 5) * 1024); }; \
    const unsigned mo0 = xm(XM_OFF + (pool & 3) * 32768 + w * 8192, pool >> 2), fX = xm(FX_OFF + (pool & 3) * 64, pool >> 2); unsigned fXv = fX; PIN_V(fXv)
#define GATHER(take) do { _Pragma("unroll 1") for (;;) { LD5(v, mo); LIVE; if (!take(live_)) break; } } while (0)
#define CVT2(o0, o1, x) do { o0 = pk2(x[0], x[1]); o1 = pk2(x[2], x[3]); asm volatile("" : "+v"(o0), "+v"(o1)); } while (0)
#define RR_(op, d, x, y, n) op "_dpp " d ", " x ", " y " row_ror:" n " row_mask:0xf bank_mask:0xf\n\t"
#define ROWRED(op, x, y, f0, f1) f0 RR_(op, y, x, x, "1") RR_(op, y, x, y, "2") RR_(op, y, x, y, "3") f1 RR_(op, x, y, y, "4") RR_(op, x, y, x, "8") RR_(op, x, y, x, "12")
#define AND2(o0, o1, a, b, m) do { o0 = a & m; o1 = b & m; asm volatile("" : "+v"(o0), "+v"(o1)); } while (0)

constexpr int SL_S = 20480, SS_W = 3136, SL_KM = SL_S + 4 * SS_W, SL_KT = SL_KM + 6144, SL_W = SL_KT + 49152, SL_END = SL_W + 4 * 18432;
static_assert(SL_END <= 163840, "LDS");
__device__ __forceinline__ void s_role(const MegaArgs& A, char* smem, const int h, const int id) {
    CHAIN_IDS;
    const int pool = h >> 2, hs = hside(h);
    bf16x8 wq[3][20], wk[3][20];
    const char* sW = smem + SL_W + w * 18432 + l * 16 - 2048;
    {
        const void* const bq[3] = {A.Wq + h * DH_, A.Wq + h * DH_, A.Wq + h * DH_};
        const void* const bk[3] = {A.Wk + h * DH_, A.Wk + h * DH_, A.Wk + h * DH_};
        unsigned vo[3], v2[3];
#pragma unroll
        for (int m = 0; m < 3; ++m) { const int idx = (4 * m + w) * 64 + l; v2[m] = vo[m] = (idx / 24) * (2 * HD_) + (idx % 24) * 16; }
        const int row = 4 * lq + (l16 >> 2), pp = (l & 3) * 8;
        const int rp[3] = {row * 384 + 64 * w + pp, row * 384 + 64 * w + 32 + pp, row * 384 + 256 + 32 * w + pp};
        const int rs[3] = {rp[0] + 6144, rp[1] + 6144, rp[2] + 6144};
        load_coop<3, 5, 3>(smem, w, bq, vo, 64 * HD_, rp, rs, wq);
        load_coop<3, 5, 3, 4>(smem, w, bk, v2, 64 * HD_, rp, rs, wk);
#pragma unroll
        for (int i = 2; i < 20; ++i) *(bf16x8*)(sW + i * 1024) = wk[2][i];
    }
#pragma unroll
    for (int i = 0; i < 60; ++i) asm volatile("" : "+a"(wq[i / 20][i % 20]));
#pragma unroll
    for (int i = 0; i < 4; ++i) asm volatile("" : "+a"(wk[0][i]));
    *(f32x4*)(smem + SL_S + w * SS_W + 3120) = f32x4{-INFINITY, -INFINITY, -INFINITY, -INFINITY};
#pragma unroll
    for (int i = 0; i < 12; ++i) *(u32x4*)(smem + SL_KT + w * 12288 + i * 1024 + l * 16) = u32x4{0u, 0u, 0u, 0u};
    asm volatile("s_waitcnt lgkmcnt(0)" ::: "memory");
    CHAIN_STAGE;
    char* sSw = smem + SL_S + w * SS_W + lq * 256 + ((l16 + 4 * lq) & 15) * 16;
    const int kq = l16, sL = w >> 1, rt = w & 1, row8 = (4 * w + lq) & 7;
    const bool kval = kq < 8 || (kq >> 1) == 4 + sL;
    const char* sSr = smem + SL_S + (kval ? (kq >> 2) * 1040 + (kq & 3) * 256 + ((4 * w + lq + 4 * kq) & 15) * 16 : 3120);
    const unsigned ppos = !kval ? OOB : xm(XP_OFF + (h >> 3) * 8192 +
        (kq < 8 ? ((h & 7) >> 1) * 1024 + ((kq & 3) * 16 + (h & 1) * 8 + row8) * 16 + (kq >> 2) * 8 : 4096 + ((h & 3) * 16 + ((h >> 2) & 1) * 8 + row8) * 16 + (kq & 1) * 8), sL);
    const unsigned pt1 = xm(PT_OFF + (h >> 3) * 16384 + (rt ? (h & 7) * 1024 : 8192 + ((h & 7) >> 1) * 1024 + (h & 1) * 8) + l * 16, sL);
    const unsigned pt2 = lq == 2 * sL ? xm(PT_OFF + (h >> 3) * 16384 + 12288 + (((h >> 2) & 1) * 2 + rt) * 1024 + ((h & 3) * 16 + l16) * 16, sL) : OOB;
    const unsigned cm = (1u << max(0, min(4, l16 - 4 * lq + 1))) - 1u;
    const bool mval = (lq >> 1) == sL;
    const unsigned ma = l16 < 8 ? ~0u : 0u, mb2 = ~ma;
    const unsigned fK = xm(FI_OFF + (h & 15) * 64 + (4 * sL + (l & 3)) * 4, hs);
    const unsigned qo = ((sL * 16 + (h & 15)) * 4 + rt) * 16384, ktl = lds_addr(smem + SL_KT) + w * 12288;
    const char* sKT = smem + SL_KT + l * 16;
    char* sKM = smem + SL_KM + l * 16;
    char* sKX = smem + SL_KT + l * 16 + 4096 + (w >> 1) * 1024 + (w & 1) * 8;
    int mb = 0;
    unsigned X = rtime() - 2 * PER;
    auto qk_so = [&](int it) { const int sg = it - sL * lag; return (unsigned)sg < NSEG_ ? qk_base(sg, hs) + qo : OOB; };
    auto dma = [&](int e, unsigned sq, unsigned sk) {
        if (e == 0) asm volatile("s_mov_b32 m0, %0\n\ts_nop 0" :: "s"(ktl) : "m0");
        if (e == 4 || e == 6 || e == 10) asm volatile("s_add_u32 m0, m0, %0\n\ts_nop 0" :: "n"(e == 6 ? 2048 : 4096) : "m0", "scc");
        asm volatile("buffer_load_dwordx4 %0, %1, %2 offen offset:%3 sc1 lds" :: "v"(e % 6 < 4 ? l16b : l16k), "s"(rx), "s"(e < 6 ? sk : sq), "n"((e % 6 & 3) * 1024) : "memory");
    };
    {
        const unsigned pv0 = bld4(rx, fK), z = 0, so = qk_so(0);
        CHAIN_WAIT(pv0, fK, z, 1);
#pragma unroll
        for (int e = 0; e < 12; ++e) dma(e, so, so + 32768);
    }
    for (int it = 0; it < itend; ++it) {
        const int nb = (mb == 2) ? 0 : mb + 1, pb = mb ? mb - 1 : 2;
        unsigned mo = mo0 + mb * (2 * XM_BUF), fs = fx_step(it), fs1 = fi_step(it + 1), ps = mb * (2 * XP_BUF), pr = pb * (2 * XP_BUF);
        unsigned ts = mb * (2 * PT_BUF), tr = nb * (2 * PT_BUF), qn = qk_so(it + 1), kn = qn + 32768;
        PIN_S(mo); PIN_S(fs); PIN_S(fs1); PIN_S(ps); PIN_S(pr); PIN_S(ts); PIN_S(tr); PIN_S(qn); PIN_S(kn);
        X += PER;
        PIN_S(X);
        bf16x8 wl[3];
#pragma unroll
        for (int i = 2; i < 5; ++i) wl[i % 3] = *(const bf16x8*)(sW + i * 1024);
        asm volatile("" : "+v"(wl[0]), "+v"(wl[1]), "+v"(wl[2]));
        rt_fine(X - E_G);
        f32x4 acc[6];
        u32x4 v[5];
        auto mf = [&](int i, int tt, const bf16x8 xb) {
            if (i == 0) { if (tt < 3) MF_AV0(acc[tt], wq[tt % 3][0], xb); else if (tt == 3) MF_AV0(acc[3], wk[0][0], xb); else MF_VV0(acc[tt], wk[tt % 3][0], xb); }
            else if (tt < 3) MF_AV(acc[tt], wq[tt % 3][i], xb);
            else if (tt == 3 && i < 4) MF_AV(acc[3], wk[0][i], xb);
            else if (tt < 5 || i < 2) MF_VV(acc[tt], wk[tt % 3][i], xb);
            else MF_VV(acc[5], wl[i % 3], xb);
        };
        auto take = [&](u64 live_) {
            unsigned m = 0;
            u64 bad;
            sfor<0, 22>([&](auto G) {
                constexpr int g = decltype(G)::value;
                if constexpr (g < 20 && g % 4 == 0) { WAITV(4 - g / 4, v[g / 4]); *(u32x4*)(sg0 + g / 4 * 1024) = v[g / 4]; SCHED0(); }
                mf(g / 6, g % 6, asb8(v[g / 6]));
                SCHED0();
                if constexpr (g < 20 && g % 4 == 0) { m = mx4(m, v[g / 4]); if (g < 16) PIN_V(m); else { bad = __ballot(m == ~0u); PIN_S(bad); } }
                if (g == 18) { bad &= live_; PIN_S(bad); }
                SCHED0();
            });
            return bad;
        };
        GATHER(take);
        unsigned xv, pvK;
        u32x4 ko, kz;
        {
            bf16x8 mm[3];
            u32x4 kp[2][2], kx[2], qp, qa, qb, qz;
            unsigned z0, z1;
            f32x4 s[2], sm;
            sfor<22, 128>([&](auto G) {
                constexpr int g = decltype(G)::value, kt[13] = {5, 5, 5, 3, 4, -1, -1, 3, -1, 4, -1, 3, 4}, ki[13] = {17, 18, 19, 17, 17, 0, 0, 18, 0, 18, 0, 19, 19};
                constexpr int i = g < 102 ? g / 6 : (g < 111 ? 17 + (g - 102) / 3 : (g < 124 ? ki[g < 111 || g > 123 ? 0 : g - 111] : 0));
                constexpr int tt = g < 102 ? g % 6 : (g < 111 ? (g - 102) % 3 : (g < 124 ? kt[g < 111 || g > 123 ? 0 : g - 111] : -1));
                if constexpr (tt >= 0) mf(i, tt, i < 5 ? asb8(v[i < 5 ? i : 0]) : mm[(i < 5 ? 0 : i - 5) % 3]);
                if constexpr (g == 116 || g == 117) MF_VV0(s[g & 1], kp[0][g & 1], qa);
                if constexpr (g == 119 || g == 124) MF_VV(s[~g & 1], kp[1][~g & 1], qb);
                if constexpr (g == 121 || g == 125) MF_VV(s[g == 125], kx[g == 125], qz);
                if constexpr (g == 126) MF_VV0(sm, kz, qz);
                if constexpr (g == 127) MF_VV(sm, ko, qp);
                SCHED0();
                if (g == 22) { LDS_BARRIER(); }
                if (g == 23) mm[0] = mrd(5);
                if (g == 24) wl[2] = *(const bf16x8*)(sW + 5120);
                if constexpr (g > 24 && g < 103 && g % 6 == (g < 102)) mm[(g / 6 - 3) % 3] = mrd(g / 6 + 2);
                if constexpr (g > 24 && g < 102 && g % 6 == 3) wl[(g / 6 + 2) % 3] = *(const bf16x8*)(sW + (g / 6 + 2) * 1024);
                if (g == 104) wl[1] = *(const bf16x8*)(sW + 19 * 1024);
                if (g == 110) PIN_V(wl[1]);
                if constexpr (g >= 60 && g < 84 && g % 6 == 2) kp[(g - 60) / 12][(g / 6) & 1] = *(const u32x4*)(sKT + (g - 60) / 6 * 12288 + w * 1024);
                if constexpr (g == 86 || g == 92) {
                    const u32x2 ka = *(const u32x2*)(sKX + (g - 86) / 6 * 12288), kb = *(const u32x2*)(sKX + (g - 74) / 6 * 12288);
                    kx[(g - 86) / 6] = u32x4{ka[0], ka[1], kb[0], kb[1]};
                }
                if (g == 111) CVT2(qp[0], qp[1], acc[0]);
                if (g == 112) CVT2(qp[2], qp[3], acc[1]);
                if (g == 113) AND2(qa[0], qa[1], qp[0], qp[1], ma);
                if (g == 114) AND2(qa[2], qa[3], qp[2], qp[3], ma);
                if (g == 115) CVT2(z0, z1, acc[2]);
                if (g == 118) AND2(qz[0], qz[1], z0, z1, ma);
                if (g == 119) AND2(qz[2], qz[3], z0, z1, mb2);
                if (g == 116) AND2(qb[0], qb[1], qp[0], qp[1], mb2);
                if (g == 117) AND2(qb[2], qb[3], qp[2], qp[3], mb2);
                if (g == 120) CVT2(kz[0], kz[1], acc[5]);
                if (g == 121) asm volatile("v_cvt_pk_bf16_f32 %0, %2, %3\n\tv_cvt_pk_bf16_f32 %1, %4, %5" : "=&v"(kz[2]), "=&v"(kz[3]) : "v"(acc[5][0]), "v"(acc[5][1]), "v"(acc[5][2]), "v"(acc[5][3]));
                if (g == 123) *(f32x4*)sSw = s[0];
                if (g == 124) CVT2(ko[0], ko[1], acc[3]);
                if (g == 125) CVT2(ko[2], ko[3], acc[4]);
                if (g == 127) *(f32x4*)(sSw + 1040) = s[1];
                SCHED0();
            });
            asm volatile("s_nop 6" : "+v"(sm) :: "memory");
            *(f32x4*)(sSw + 2080) = sm;
        }
        LDS_BARRIER();
        constexpr float SC = 0.07216878364870322f * 1.4426950408889634f;
        {
            const f32x4 s0 = *(const f32x4*)sSr, s1 = *(const f32x4*)(sSr + SS_W), s2 = *(const f32x4*)(sSr + 2 * SS_W), s3 = *(const f32x4*)(sSr + 3 * SS_W);
            LDA4(pvK, fK, fs1);
            SCHED0();
            f32x4 sv = ((s0 + s1) + s2) + s3;
            u32x2 p;
            p[0] = __float_as_uint(fmaxf(fmaxf(fmaxf(sv[0], sv[1]), sv[2]), sv[3]));
            asm volatile(ROWRED("v_max_f32", "%0", "%1", "v_mul_f32 %2, %6, %2\n\tv_mul_f32 %3, %6, %3\n\t", "v_mul_f32 %4, %6, %4\n\tv_mul_f32 %5, %6, %5\n\t")
                         "v_fma_f32 %2, -%0, %6, %2\n\tv_fma_f32 %3, -%0, %6, %3\n\tv_fma_f32 %4, -%0, %6, %4\n\tv_fma_f32 %5, -%0, %6, %5\n\t"
                         "v_exp_f32 %2, %2\n\tv_exp_f32 %3, %3\n\tv_exp_f32 %4, %4\n\tv_exp_f32 %5, %5\n\t"
                         "v_add_f32 %0, %2, %3\n\tv_add_f32 %0, %0, %4\n\tv_add_f32 %0, %0, %5\n\t"
                         ROWRED("v_add_f32", "%0", "%1", "s_nop 1\n\t", "s_nop 1\n\t")
                         "v_rcp_f32 %0, %0\n\ts_nop 0\n\tv_mul_f32 %2, %0, %2\n\tv_mul_f32 %3, %0, %3\n\tv_mul_f32 %4, %0, %4\n\tv_mul_f32 %5, %0, %5\n\t"
                         "v_cvt_pk_bf16_f32 %0, %2, |%3|\n\tv_cvt_pk_bf16_f32 %1, %4, %5"
                         : "+v"(p[0]), "=&v"(p[1]), "+v"(sv[0]), "+v"(sv[1]), "+v"(sv[2]), "+v"(sv[3]) : "s"(SC));
            sst8(rx, ps, ppos, p);
        }
        SCHED0();
        {
            *(u32x4*)(sKM + w * 1024) = ko;
            *(u32x2*)(sKX + (SL_KM - SL_KT)) = u32x2{kz[0], kz[1]};
            SCHED0();
            bf16x8 km[6], ka[3], kb[3], q[6];
            auto krd = [&](int f) {
                q[f] = *(const bf16x8*)(sKT + w * 12288 + 6144 + f * 1024); kb[f % 3] = *(const bf16x8*)(sKT + (w & 2) * 12288 + f * 1024);
                ka[f % 3] = *(const bf16x8*)(sKT + w * 12288 + f * 1024); SCHED0(); };
            krd(0); krd(1); krd(2);
            LDA4(xv, fXv, fs);
            f32x4 sc[3];
#pragma unroll
            for (int f = 0; f < 6; ++f) {
                if (f == 0) { MF_VV0(sc[0], kb[0], q[0]); MF_VV0(sc[1], ka[0], q[0]); } else { MF_VV(sc[0], kb[f % 3], q[f]); MF_VV(sc[1], ka[f % 3], q[f]); }
                if (f == 2) asm volatile("s_barrier" ::: "memory");
                if (f < 3) krd(f + 3); else { km[2 * f - 6] = *(const bf16x8*)(sKM + (2 * f - 6) * 1024); km[2 * f - 5] = *(const bf16x8*)(sKM + (2 * f - 5) * 1024); SCHED0(); }
            }
            asm volatile("s_waitcnt vmcnt(2)\n\ts_barrier" : "+v"(pvK) :: "memory");
            CHAIN_WAIT(pvK, fK, fs1, 1);
#pragma unroll
            for (int f = 0; f < 6; ++f) { if (f % 3 == 0) dma(f / 3, qn, kn); if (f == 0) MF_VV0(sc[2], km[0], q[0]); else MF_VV(sc[2], km[f], q[f]); }
            dma(2, qn, kn);
            SCHED0();
#pragma unroll
            for (int e = 0; e < 3; ++e) {
#pragma unroll
                for (int r = 0; r < 4; ++r) sc[e][r] = (e == 0 ? rt : (e == 1 ? (cm >> r) & 1 : mval)) ? sc[e][r] : -INFINITY;
                PIN_V(sc[e]);
                SCHED0();
            }
            dma(3, qn, kn);
            float mx = max2r(max3r(max3r(sc[0][0], sc[0][1], sc[0][2]), max3r(sc[0][3], sc[1][0], sc[1][1]), max3r(sc[1][2], sc[1][3], sc[2][0])), max3r(sc[2][1], sc[2][2], sc[2][3])), tm;
            PIN_V(mx);
            dma(4, qn, kn);
            asm volatile("v_mov_b32 %1, %0\n\ts_nop 1\n\tv_permlane16_swap_b32 %0, %1\n\tv_max_f32 %0, %0, %1\n\tv_mov_b32 %1, %0\n\ts_nop 1\n\tv_permlane32_swap_b32 %0, %1\n\tv_max_f32 %0, %0, %1" : "+v"(mx), "=&v"(tm));
            dma(5, qn, kn);
            mx *= -SC;
#pragma unroll
            for (int e = 0; e < 3; ++e) {
#pragma unroll
                for (int r = 0; r < 4; ++r) sc[e][r] = __builtin_fmaf(sc[e][r], SC, mx);
                PIN_V(sc[e]);
            }
            dma(6, qn, kn);
#pragma unroll
            for (int e = 0; e < 3; ++e) {
#pragma unroll
                for (int r = 0; r < 4; ++r) sc[e][r] = __builtin_amdgcn_exp2f(sc[e][r]);
                PIN_V(sc[e]);
                if (e) dma(6 + e, qn, kn);
            }
            const f32x4 t4 = (sc[0] + sc[1]) + sc[2];
            float sum = (t4[0] + t4[1]) + (t4[2] + t4[3]);
            PIN_V(sum);
            dma(9, qn, kn);
            float inv = __builtin_amdgcn_rcpf(red_sum_q(sum));
            PIN_V(inv);
            unsigned u[6];
#pragma unroll
            for (int e = 0; e < 6; ++e) { u[e] = pk2(sc[e >> 1][2 * (e & 1)] * inv, sc[e >> 1][2 * (e & 1) + 1] * inv); if (!(e & 1)) u[e] = nosent(u[e]); }
            const auto a4 = __builtin_amdgcn_permlane16_swap(u[4], u[4], false, false), a5 = __builtin_amdgcn_permlane16_swap(u[5], u[5], false, false);
            sst16(rx, ts, pt2, u32x4{u[4], u[5], a4[1], a5[1]});
            u32x4 ones = {~0u, ~0u, ~0u, ~0u};
            PIN_V(ones);
            if (rt) { sst16(rx, ts, pt1, u32x4{u[0], u[1], u[2], u[3]}); sst16(rx, tr, pt1, ones); } else { sst8(rx, ts, pt1, u32x2{u[2], u[3]}); sst8(rx, tr, pt1, u32x2{ones[0], ones[1]}); }
            dma(10, qn, kn); dma(11, qn, kn);
            sst8(rx, pr, ppos, u32x2{ones[0], ones[1]});
            sst16(rx, tr, pt2, ones);
            WAITV(17, xv);
            xv = __builtin_amdgcn_readfirstlane(xv);
            X = xv ? xv : X;
        }
        mb = nb;
    }
    SPIN_END;
}

constexpr int CL_R = 0, CL_C = CL_R + 16384, CL_END = CL_C + 4096;
__device__ __forceinline__ void c_role(const MegaArgs& A, char* smem, const int pool, const int ku, const int id) {
    CHAIN_IDS;
    const int cL = pool >> 2, j = 5 * (pool & 3) + ku;
    unsigned fXv = xm(FX_OFF + (pool & 3) * 64, cL);
    PIN_V(fXv);
    const int hi = (l >> 3) & 1, tk = l & 7;
    const unsigned pos = xm(XM_OFF + w * 32768 + (j / 5) * 8192 + (j % 5) * 1024 + (lq * 16 + cL * 8 + tk) * 16 + hi * 8, 0);
    const unsigned umo = xm(UM_OFF + (16 * j + 2 * w) * 1024, cL), po0 = xm(XP_OFF + w * 8192, cL), pto = xm(PT_OFF + w * 16384, cL), uto = xm(UT_OFF + (64 * j + 8 * w) * 1024, cL);
    const unsigned fU = lp(SG_FL + FL_U + (8 * w + tk) * 64 + (pool & 3) * 4);
    const unsigned xo = lp((2 * j + (w >> 1)) * 1024 + (lq * 16 + 8 * (w & 1) + tk) * 16 + hi * 8), sto = l < 8 ? lp(SG_ST + (4 * j + w) * 64 + l * 8) : OOB;
    u32x2 alt;
    { const u32x2 m = *(const u32x2*)(A.mem0 + tk * D_ + 32 * j + 16 * hi + 4 * lq); alt = u32x2{nosent(m[0]), m[1]}; }
    f32x4* sRw0 = (f32x4*)(smem + CL_R) + w * 256 + l;
    f32x4* sRw1 = (f32x4*)(smem + CL_R) + w * 256 + 128 + (l ^ 8);
    const f32x4* sRr = (const f32x4*)(smem + CL_R) + (hi * 2 + (w >> 1)) * 64 + lq * 16 + ((8 * (w & 1) + tk) ^ (8 * hi));
    f32x4* sC = (f32x4*)(smem + CL_C);
    int mb = 0;
    unsigned X = rtime() - 2 * PER;
    u32x4 ua[16], ub[16], um[4];
#pragma unroll
    for (int e = 0; e < 16; ++e) ua[e] = u32x4{0u, 0u, 0u, 0u};
#pragma unroll
    for (int e = 0; e < 4; ++e) um[e] = u32x4{0u, 0u, 0u, 0u};
    for (int s = 0; s < NSEG_; ++s) sst8(rx, seg_base(cL, s) + lp(SG_X1), xo, u32x2{0u, 0u});
    auto seg_so = [&](int sg) { return (unsigned)sg < NSEG_ ? seg_base(cL, sg) : OOB; };
    unsigned sb0 = OOB, sb1 = seg_so(-cL * lag), pvU = sld4(rx, sb1, fU);
    auto step = [&](const int it, auto LAST, u32x4 (&up)[16], u32x4 (&ut)[16]) {
        constexpr bool last = decltype(LAST)::value;
        const int nb = (mb == 2) ? 0 : mb + 1, pb = mb ? mb - 1 : 2, sg = it - cL * lag;
        unsigned uo = sb1 != OOB ? uto + ut_slot(sg) : OOB, uo2 = uo + 65536;
        PIN_S(uo); PIN_S(uo2);
        auto ut_load = [&](int e) {
            if (!last) LDA(ut[e], (e & 4) ? l16k : l16b, e < 8 ? uo : uo2, (e & 3) * 1024);
        };
        if (!last) CHAIN_WAIT(pvU, fU, sb1, 0);
        ut_load(0);
        SCHED0();
        unsigned ts = pto + pb * (2 * PT_BUF), ts2 = ts + 16384, sbr = (sb0 ^ (cL << 13)) + lp(SG_XR), sbx = sb0 + lp(SG_X1), sb2 = seg_so(sg + 1);
        PIN_S(ts); PIN_S(ts2); PIN_S(sbr); PIN_S(sbx); PIN_S(sb2);
        ut_load(1);
        SCHED0();
        unsigned fs = fx_step(it), cu = umo + mb * (2 * UM_BUF), cu2 = cu + 16384, cp = po0 + mb * (2 * XP_BUF), cr = pb * (2 * XM_BUF), cr2 = cr + 8192;
        PIN_S(fs); PIN_S(cu); PIN_S(cu2); PIN_S(cp); PIN_S(cr); PIN_S(cr2);
        ut_load(2);
        SCHED0();
        unsigned cs = sg < 0 ? OOB : nb * (2 * XM_BUF), cs2 = cs + 8192, tT = X + E_T;
        X += PER;
        PIN_S(cs); PIN_S(cs2); PIN_S(tT); PIN_S(X);
        ut_load(3);
        u32x4 pt[16];
        u32x2 xr;
        auto pt_load = [&](int e) { LDA(pt[e], e < 4 || e >= 12 ? l16k : l16b, e < 8 ? ts2 : ts, (e & 3) * 1024); };
        rt_until(tT);
#pragma unroll
        for (int e = 0; e < 13; ++e) { pt_load(e); if (e < 12) GAP(); }
        u32x2 st;
        {
            f32x4 d[2][2];
            auto check = [&](auto E0, auto N, auto W) {
                constexpr int e0 = decltype(E0)::value, n = decltype(N)::value;
                auto look = [&](auto WW) {
                    asm volatile("s_waitcnt vmcnt(%2)" : "+v"(pt[e0]), "+v"(pt[e0 + 1]) : "n"(decltype(WW)::value) : "memory");
                    if (n == 4) asm volatile("" : "+v"(pt[e0 + n - 2]), "+v"(pt[e0 + n - 1]));
                    unsigned m = 0;
#pragma unroll
                    for (int e = e0; e < e0 + n; ++e) m = mx4(m, pt[e]);
                    return m; };
                if (__builtin_expect(__ballot(look(W) == ~0u) != 0, 0)) {
#pragma unroll 1
                    for (;;) {
#pragma unroll
                        for (int e = e0; e < e0 + n; ++e) pt_load(e);
                        LIVE;
                        if (!BAD(look(IC<0>{}))) break;
                    }
                }
            };
            sfor<0, 40>([&](auto G) {
                constexpr int g = decltype(G)::value, n = g & 1;
                if constexpr (g == 0) check(IC<0>{}, IC<4>{}, IC<9>{});
                if constexpr (g == 8) check(IC<4>{}, IC<2>{}, IC<10>{});
                if constexpr (g == 16) check(IC<6>{}, IC<2>{}, IC<(last ? 9 : 11)>{});
                if constexpr (g == 24) check(IC<8>{}, IC<4>{}, IC<(last ? 5 : 10)>{});
                if constexpr (g == 32) check(IC<12>{}, IC<2>{}, IC<(last ? 3 : 11)>{});
                if constexpr (g == 36) { check(IC<14>{}, IC<2>{}, IC<(last ? 0 : 10)>{}); PIN_V(xr); }
                if constexpr (g < 4) MF_VV0(d[n][g >> 1], um[2 * n], pt[g >> 1]);
                else if constexpr (g < 8) MF_VV(d[n][(g >> 1) & 1], um[2 * n + 1], pt[g >> 1]);
                else if constexpr (g < 24) {
                    constexpr int hh = (g - 8) >> 1;
                    const u32x4 x = up[8 * n + hh], z = pt[4 + (hh >> 1)];
                    const u32x2 xl = {x[0], x[1]}, zl = {z[2 * (hh & 1)], z[2 * (hh & 1) + 1]};
                    MH_(d[n][0], "v", xl, "v", zl);
                } else MF_VV(d[n][1], up[8 * n + ((g - 24) >> 1)], pt[8 + ((g - 24) >> 1)]);
                constexpr int rq = g < 32 ? ((g & 7) == 2 ? 0 : ((g & 7) == 5 ? 1 : ((g & 7) == 7 ? 2 : -99))) + 3 * (g >> 3) : (g == 33 ? 12 : (g == 35 ? 13 : -99));
                if constexpr (rq >= 0 && rq < 3) pt_load(13 + rq); else if constexpr (rq == 3) LDA8(xr, xo, sbr, 0); else if constexpr (rq > 3) ut_load(rq);
                if (g == 24) sRw0[0] = d[0][0];
                if (g == 27) sRw1[0] = d[1][0];
                SCHED0();
            });
            const f32x4 xf = {lo16(xr[0]), hi16(xr[0]), lo16(xr[1]), hi16(xr[1])};
            asm volatile("" : "+v"(d[0][1]) : "v"(xf) : "memory");
            sRw0[64] = d[0][1];
            ut_load(14);
            asm volatile("s_nop 2" : "+v"(d[1][1]) :: "memory");
            sRw1[64] = d[1][1];
            LDS_BARRIER();
            const f32x4 s0 = sRr[0], s1 = sRr[256], s2 = sRr[512], s3 = sRr[768];
            ut_load(15);
            SCHED0();
            f32x4 s = (((xf + s0) + s1) + s2) + s3;
            sst8(rx, sbx, xo, u32x2{pk2(s[0], s[1]), pk2(s[2], s[3])});
            float sm = (s[0] + s[1]) + (s[2] + s[3]);
            sm += dpp_ror8(sm);
            sm = red_sum_q(sm) * (1.0f / 32);
            s -= sm;
            float sq = __builtin_fmaf(s[3], s[3], __builtin_fmaf(s[2], s[2], __builtin_fmaf(s[1], s[1], s[0] * s[0])));
            sq += dpp_ror8(sq);
            sq = red_sum_q(sq);
            st = u32x2{nosent(__float_as_uint(sm)), __float_as_uint(sq)};
            PIN_V(st);
        }
        SCHED0();
        if (last) { DRAIN(); sst8(rx, sb0, sto, st); return; }
        unsigned xv;
        rt_until(X + E_X);
        LDA4(xv, fXv, fs);
        asm volatile("s_waitcnt vmcnt(1)" ::: "memory");
        sst8(rx, sb0, sto, st);
        sst8(rx, cr, pos, u32x2{~0u, ~0u});
        sst8(rx, cr2, pos, u32x2{~0u, ~0u});
        WAITV(3, xv);
        X = __builtin_amdgcn_readfirstlane(xv);
        PIN_S(X);
        if (__builtin_expect(X == 0, 0)) {
#pragma unroll 1
            for (;;) {
                LDA4(xv, fXv, fs);
                LIVE;
                WAITV(0, xv);
                X = __builtin_amdgcn_readfirstlane(xv);
                if (X || !live_) break;
            }
        }
        u32x4 p[5];
        f32x4 a[2], b[2];
        auto take2 = [&](u64 live_) {
            unsigned m = 0;
            u64 bad;
#pragma unroll
            for (int i = 0; i < 4; ++i) { WAITV(9 - i, um[i]); m = mx4(m, um[i]); PIN_V(m); }
#pragma unroll
            for (int i = 0; i < 5; ++i) {
                WAITV(4 - i, p[i]);
#pragma unroll
                for (int e = 0; e < 4; ++e) {
                    f32x4& d = (e & 1) ? b[e >> 1] : a[e >> 1];
                    if (i == 0) MF_VV0(d, ut[8 * (e >> 1) + (e & 1)], p[0]); else if (i < 4) MF_VV(d, ut[8 * (e >> 1) + 2 * (i & 3) + (e & 1)], p[i]); else MF_VV(d, um[e], p[4]);
                    SCHED0();
                    if (e == 0) { m = mx4(m, p[i]); if (i < 4) PIN_V(m); else { bad = __ballot(m == ~0u); PIN_S(bad); } }
                    if (e == 1 && i == 4) { bad &= live_; PIN_S(bad); }
                    SCHED0();
                }
            }
            return bad;
        };
        {
            unsigned tP = X + E_P, tU = X + E_U;
#pragma unroll 1
            for (;;) {
                rt_until(tU);
#pragma unroll
                for (int i = 0; i < 4; ++i) LDI(um[i], l16b, (i & 2) ? cu2 : cu, (i & 1) * 1024);
                LDA4(pvU, fU, sb2);
                LIVE;
                rt_fine(tP);
                LD5(p, cp);
                SCHED0();
                if (!take2(live_)) break;
            }
        }
        f32x4 x;
        asm volatile("v_add_f32_dpp %0, %8, %4 row_ror:8 row_mask:0xf bank_mask:0x3\n\tv_add_f32_dpp %1, %9, %5 row_ror:8 row_mask:0xf bank_mask:0x3\n\t"
                     "v_add_f32_dpp %2, %10, %6 row_ror:8 row_mask:0xf bank_mask:0x3\n\tv_add_f32_dpp %3, %11, %7 row_ror:8 row_mask:0xf bank_mask:0x3"
                     : "=&v"(x[0]), "=&v"(x[1]), "=&v"(x[2]), "=&v"(x[3]) : "v"(a[0][0]), "v"(a[0][1]), "v"(a[0][2]), "v"(a[0][3]), "v"(b[0][0]), "v"(b[0][1]), "v"(b[0][2]), "v"(b[0][3]));
        asm volatile("s_nop 0\n\tv_add_f32_dpp %0, %8, %4 row_ror:8 row_mask:0xf bank_mask:0xc\n\tv_add_f32_dpp %1, %9, %5 row_ror:8 row_mask:0xf bank_mask:0xc\n\t"
                     "v_add_f32_dpp %2, %10, %6 row_ror:8 row_mask:0xf bank_mask:0xc\n\tv_add_f32_dpp %3, %11, %7 row_ror:8 row_mask:0xf bank_mask:0xc"
                     : "+v"(x[0]), "+v"(x[1]), "+v"(x[2]), "+v"(x[3]) : "v"(b[1][0]), "v"(b[1][1]), "v"(b[1][2]), "v"(b[1][3]), "v"(a[1][0]), "v"(a[1][1]), "v"(a[1][2]), "v"(a[1][3]));
        sC[w * 64 + l] = x;
        LDS_BARRIER();
        const f32x4 s = ((sC[l] + sC[64 + l]) + sC[128 + l]) + sC[192 + l];
        const u32x2 r = {nosent(pk2(s[0], s[1])), pk2(s[2], s[3])};
        sst8(rx, cs, pos, r);
        sst8(rx, cs2, pos, r);
        SCHED0();
        if (__builtin_expect(cs == OOB, 0)) { sst8(rx, nb * (2 * XM_BUF), pos, alt); sst8(rx, nb * (2 * XM_BUF) + 8192, pos, alt); }
        PIN_V(pvU);
        mb = nb; sb0 = sb1; sb1 = sb2;
    };
    int it = 0;
    PIN_S(spins); PIN_V(pvU); PIN_V(alt);
#pragma unroll 1
    do { step(it, IC<0>{}, ua, ub); step(it + 1, IC<0>{}, ub, ua); it += 2; } while (it + 1 < itend);
    if (LAGV & 1) { step(it, IC<0>{}, ua, ub); step(it + 1, IC<1>{}, ub, ua); } else step(it, IC<1>{}, ua, ub);
    SPIN_END;
}

constexpr int VL_V = 20480, VL_W = VL_V + 6144, VL_END = VL_W + 81920;
__device__ __forceinline__ void vm_role(const MegaArgs& A, char* smem, const int h, const int id) {
    CHAIN_IDS;
    const int pool = h >> 2;
    bf16x8 wf[3][20], wo[10][4];
    char* sWo = smem + VL_W + w * 20480 + l * 16;
    {
        const void* const base[3] = {A.Wv + h * DH_, A.Wv + h * DH_, A.Wv + h * DH_};
        unsigned vo[3], v2[2];
#pragma unroll
        for (int m = 0; m < 3; ++m) { const int idx = (4 * m + w) * 64 + l; vo[m] = (idx / 24) * (2 * HD_) + (idx % 24) * 16; }
#pragma unroll
        for (int m = 0; m < 2; ++m) v2[m] = (32 * w + 16 * m + (l >> 2)) * (2 * D_) + (l & 3) * 16;
        const int row = 4 * lq + (l16 >> 2), pp = (l & 3) * 8;
        const int rp[3] = {row * 384 + 64 * w + pp, row * 384 + 64 * w + 32 + pp, row * 384 + 256 + 32 * w + pp};
        const int rs[3] = {rp[0] + 6144, rp[1] + 6144, rp[2] + 6144};
        load_coop<3, 5, 3>(smem, w, base, vo, 64 * HD_, rp, rs, wf);
#pragma unroll
        for (int i = 0; i < 60; ++i) asm volatile("" : "+a"(wf[i / 20][i % 20]));
        bf16x8 tmp[20];
        load_frags<30, 2, true>(smem, w, l, A.Wo + (size_t)(h * DH_) * D_ + 160 * w, v2,
            [&](int i) { const int k = i / 5; return i % 5 < 4 ? 64u : (unsigned)((k < 3 ? ((w + k + 1) & 3) : k + 1) - (k < 4 ? ((w + k) & 3) : k)) * (64u * D_) - 256u; },
            [&](int i, int tt) -> bf16x8& { return i < 20 ? wo[2 * (i % 5) + tt][(i / 5) & 3] : tmp[4 * (i % 5) + 2 * (i / 5 - 4) + tt]; });
#pragma unroll
        for (int i = 0; i < 4; ++i) asm volatile("" : "+a"(wo[0][i]));
        LDS_BARRIER();
#pragma unroll
        for (int i = 0; i < 20; ++i) *(bf16x8*)(sWo + i * 1024) = tmp[i];
    }
    CHAIN_STAGE;
    unsigned upos[5];
#pragma unroll
    for (int m = 0; m < 5; ++m) upos[m] = xm(UM_OFF + ((10 * w + 2 * m + (lq & 1)) * 8 + (h >> 2)) * 1024 + ((h & 3) * 16 + l16) * 16, lq >> 1);
    char* sV = smem + VL_V + l * 16;
    const char* sV1 = sV + ((w + 1) & 3) * 1024; const char* sV2 = sV + ((w + 2) & 3) * 1024; const char* sV3 = sV + ((w + 3) & 3) * 1024;
    const unsigned xw = (h == 1 && t < 8) ? xm(FX_OFF + (t & 3) * 64, t >> 2) : OOB;
    int mb = 0;
    unsigned X = rtime() - 2 * PER;
    for (int it = 0; it < itend; ++it) {
        const int nb = (mb == 2) ? 0 : mb + 1, pb = mb ? mb - 1 : 2;
        unsigned mo = mo0 + mb * (2 * XM_BUF), fs = fx_step(it), us = mb * (2 * UM_BUF), ur = pb * (2 * UM_BUF);
        X += PER;
        PIN_S(mo); PIN_S(fs); PIN_S(us); PIN_S(ur); PIN_S(X);
        rt_fine(X - E_G);
        f32x4 acc[3];
        u32x4 v[5];
        auto take = [&](u64 live_) {
            unsigned m = 0;
            u64 bad;
            sfor<0, 11>([&](auto G) {
                constexpr int g = decltype(G)::value, e = g < 9 && !(g & 1) ? g / 2 : -1;
                if constexpr (e >= 0) { WAITV(4 - e, v[e]); *(u32x4*)(sg0 + e * 1024) = v[e]; SCHED0(); }
                if (g < 3) MF_AV0(acc[g % 3], wf[g % 3][0], v[0]); else MF_AV(acc[g % 3], wf[g % 3][g / 3], v[g / 3]);
                SCHED0();
                if constexpr (e >= 0) { m = mx4(m, v[e]); if (e < 4) PIN_V(m); else { bad = __ballot(m == ~0u); PIN_S(bad); } }
                if (g == 9) { bad &= live_; PIN_S(bad); }
                SCHED0();
            });
            return bad;
        };
        GATHER(take);
        bf16x8 wl[4];
        u32x4 vf[6], o;
        f32x4 d[10];
        unsigned xv;
        {
            constexpr int MW = 5;
            bf16x8 mm[MW];
            u32x2 vz;
            u64 tb64;
            unsigned t0, t1;
            asm volatile("s_waitcnt lgkmcnt(0)\n\ts_barrier\n\ts_memrealtime %0" : "=s"(tb64) :: "memory");
            mm[0] = mrd(5);
            SCHED0();
            sfor<11, 60>([&](auto G) {
                constexpr int g = decltype(G)::value, i = g < 45 ? g / 3 : (g < 50 ? g - 30 : 15 + (g - 50) / 2), tt = g < 45 ? g % 3 : (g < 50 ? 2 : g & 1);
                const bf16x8 xb = i < 5 ? asb8(v[i < 5 ? i : 0]) : mm[(i < 5 ? 0 : i - 5) % MW];
                MF_AV(acc[tt], wf[tt][i], xb);
                SCHED0();
                if constexpr (g < 15) mm[g - 10] = mrd(g - 5);
                if constexpr (g >= 18 && g < 46 && g % 3 == 0) mm[(g / 3 - 6) % MW] = mrd(g / 3 + 4);
                if constexpr (g >= 54 && g < 58) wl[g - 54] = *(const bf16x8*)(sWo + (g - 54) * 1024);
                if (g == 17) asm volatile("s_waitcnt lgkmcnt(0)" : "+s"(tb64) :: "memory");
                if (g == 19) asm volatile("s_sub_u32 %0, %2, %3\n\ts_add_u32 %1, %2, %4" : "=&s"(t0), "=&s"(t1) : "s"((unsigned)tb64), "s"(X), "n"(E_S) : "scc");
                if (g == 22) asm volatile("s_cmp_gt_i32 %1, 0\n\ts_cselect_b32 %0, %2, %0" : "+s"(X) : "s"(t0), "s"(t1) : "scc");
                if (g == 23) asm volatile("v_or_b32 %0, 1, %1" : "=v"(xv) : "s"(X));
                if (g == 25) sst4(rx, fs, xw, xv);
                if (g == 52) CVT2(vz[0], vz[1], acc[2]);
                if (g == 53) *(u32x2*)(sV + 4096 + (w >> 1) * 1024 + (w & 1) * 8) = vz;
                SCHED0();
            });
        }
        asm volatile("s_nop 2");
        SCHED0();
        CVT2(vf[0][0], vf[0][1], acc[0]);
        SCHED0();
        asm volatile("s_nop 1");
        SCHED0();
        CVT2(vf[0][2], vf[0][3], acc[1]);
        *(u32x4*)(sV + w * 1024) = vf[0];
        SCHED0();
        asm volatile("s_nop 0");
        sfor<0, 67>([&](auto G) {
            constexpr int g = decltype(G)::value, m = g < 10 ? 0 : (g - 10) / 10, r = g < 10 ? 0 : (g - 10) % 10, kk = g < 10 ? 0 : 1 + r / 2, ct = g < 10 ? g : 2 * m + (r & 1);
            if constexpr (g < 60) {
                if constexpr (kk >= 4) MF_VV(d[ct], vf[kk], wl[2 * (kk & 1) + (r & 1)]);
                else if constexpr (ct == 0) { if (kk == 0) MF_VA0(d[0], vf[0], wo[0][0]); else MF_VA(d[0], vf[kk], wo[0][kk]); }
                else { if (kk == 0) MF_VV0(d[ct], vf[0], wo[ct][0]); else MF_VV(d[ct], vf[kk], wo[ct][kk]); }
            }
            SCHED0();
            if (g == 3) { LDS_BARRIER(); }
            if (g == 4) vf[1] = *(const u32x4*)sV1;
            if (g == 5) vf[2] = *(const u32x4*)sV2;
            if (g == 6) vf[3] = *(const u32x4*)sV3;
            if (g == 7) vf[4] = *(const u32x4*)(sV + 4096);
            if (g == 8) vf[5] = *(const u32x4*)(sV + 5120);
            if constexpr (g >= 17 && g < 51 && (g - 17) % 10 < 4) wl[(g - 17) % 10] = *(const bf16x8*)(sWo + ((g - 17) / 10 * 4 + 4 + (g - 17) % 10) * 1024);
            if (g == 12) LDA4(xv, fXv, fs);
            if constexpr (m > 0) {
                if (g == 60) asm volatile("s_nop 3");
                if (r == 1) CVT2(o[0], o[1], d[2 * m - 2]);
                SCHED0();
                if (g == 61) asm volatile("s_nop 1");
                if (r == 2) CVT2(o[2], o[3], d[2 * m - 1]);
                if (r == 3) { o[0] = nosent(o[0]); o[2] = nosent(o[2]); asm volatile("" : "+v"(o[0]), "+v"(o[2])); }
                if (r == 4) asm volatile("v_permlane16_swap_b32 %0, %1" : "+v"(o[1]), "+v"(o[3]));
                if (r == 5) asm volatile("v_permlane16_swap_b32 %0, %1" : "+v"(o[0]), "+v"(o[2]));
                if (r == 6) sst16(rx, us, upos[m - 1], o);
            }
            SCHED0();
        });
#pragma unroll
        for (int m = 0; m < 5; ++m) { __builtin_amdgcn_s_sleep(VSLP); sst16(rx, ur, upos[m], u32x4{~0u, ~0u, ~0u, ~0u}); SCHED0(); }
        WAITV(10, xv);
        xv = __builtin_amdgcn_readfirstlane(xv);
        X = xv ? xv : X;
        mb = nb;
    }
    SPIN_END;
}
#define DBG_LD 0
#define WORK_IDS const int t = threadIdx.x, l = t & 63, l16 = l & 15, lq = l >> 4; const int w = __builtin_amdgcn_readfirstlane(t >> 6); \
    const rsrc_t rx = mk_rsrc(arena_base(A.ar), ARENA); const unsigned l16b = l * 16, dvo = w * 2048 + l * 16 + DBG_LD, lds0 = lds_addr(smem) + w * 2048; unsigned spins; SZERO(spins)
#define XSET(so, base, ld) asm volatile("s_mov_b32 m0, %2\n\ts_mov_b32 %0, %1" : "=s"(so) : "s"(base), "s"(ld) : "m0")
#define XADV(so) asm volatile("s_add_u32 m0, m0, 0x2000\n\ts_add_u32 %0, %0, 0x4000" : "+s"(so) :: "m0", "scc")
#define XDMA(so, rt) asm volatile("buffer_load_dwordx4 %0, %1, %2 offen offset:%3 sc1 lds" :: "v"(dvo), "s"(rx), "s"(so), "n"((rt) * 1024) : "memory")
#define XALL(so, base, ld) _Pragma("unroll") for (int e_ = 0; e_ < 10; ++e_) { if (e_ % 5 == 0) XSET(so, base, ld); else XADV(so); XDMA(so, e_ / 5); }
#define XALL2(so, base, ld) _Pragma("unroll") for (int e_ = 0; e_ < 5; ++e_) { if (e_ == 0) XSET(so, base, ld); else XADV(so); XDMA(so, 0); XDMA(so, 1); }
#define VMB(n) asm volatile("s_waitcnt vmcnt(%0)\n\ts_barrier" :: "n"(n) : "memory")
#define VMB_LATE(k) asm volatile("s_cmp_lg_u32 %0, %1\n\ts_cbranch_scc1 1f\n\ts_waitcnt vmcnt(%2)\n1:\n\ts_barrier" :: "s"(xs), "n"(OOB), "n"(k) : "memory", "scc")
#define VMW(n) asm volatile("s_waitcnt vmcnt(%0)" :: "n"(n) : "memory")
#define NEXT_DECL const unsigned fXo = l < 4 ? lp(SG_FL + FL_X + ((h & 12) + l) * 128) : OOB, fx1 = (((TR >> 2) & ~L & 1) ^ hside(h)) << 13; const int dl = __builtin_amdgcn_readfirstlane((L ^ 1) * TR); \
    unsigned xb = seg_base(0, 0), xs, fb = seg_base(hside(h), 0), msk, so, pv, pend = OOB; if (L) wave_wait(rx, fXo, fb, spins); XALL(so, xb, lds0); VMB(5); int s; SZERO(s)
#define G_L1 150
#define G_E1 182
#define G_L2 210
#define NEXT_ADDR(a, st) \
    if (g == a) { s = __builtin_amdgcn_readfirstlane(s); PIN_S(s); xb = (s + 1) << 19; PIN_S(xb); } \
    if (g == a + st) { xs = (xb >> 8) & 8192; PIN_S(xs); } \
    if (g == a + 2 * st) { xb = (xb | xs) + 2 * SG_OFF; PIN_S(xb); } \
    if (g == a + 3 * st) { fb = (xb ^ fx1) - (dl << 19); PIN_S(fb); } \
    if (g == a + 5 * st) asm volatile("s_cmp_lt_i32 %1, %2\n\ts_cselect_b32 %0, 0, 15" : "=s"(msk) : "s"(s), "s"(dl - 1) : "scc"); \
    if (g == a + 6 * st) asm volatile("s_cmp_lt_i32 %1, 63\n\ts_cselect_b32 %0, %0, %2" : "+s"(fb) : "s"(s), "n"(OOB) : "scc")
#define NEXT_LOOK(n, ld) \
    if (g == G_L1 || g == G_L2) LDA4(pv, fXo, fb); \
    if (g == G_E1) { WAITV(n, pv); xs = (unsigned)__ballot(pv == 0); PIN_S(xs); } \
    if (g == G_E1 + 2) asm volatile("s_and_b32 %0, %0, %2\n\ts_cselect_b32 %0, %3, %1" : "+s"(xs) : "s"(xb), "s"(msk), "n"(OOB) : "scc")
#define NEXT_GO(n, ld, fl) \
    WAITV(n, pv); \
    if (xs != OOB) { asm volatile("s_barrier" ::: "memory"); prime(); SCHED0(); tail(); } \
    else { \
        tail(); \
        if ((unsigned)__ballot(pv == 0) & msk) { DRAIN(); sst4(rx, pend, fl, 1u); if (s == NSEG_ - 1) { SPIN_END; __builtin_amdgcn_endpgm(); } pend = OOB; wave_wait(rx, fXo, fb, spins); } \
        XALL(so, xb, ld); \
        VMW(10); sst4(rx, pend, fl, 1u); pend = OOB; \
        VMB(6); prime(); \
    }

constexpr int QL_W = 81920;
__device__ __forceinline__ void qk_role(const MegaArgs& A, char* smem, const int L, const int h, const int id) {
    WORK_IDS;
    bf16x8 wf[6][20];
    char* sW = smem + QL_W + w * 20480 + l * 16;
    {
        const void* const bq = A.Wq + h * DH_; const void* const bk = A.Wk + h * DH_;
        const void* const base[6] = {bq, bq, bq, bk, bk, bk};
        unsigned vo[6];
        int rp[6], rs[6];
#pragma unroll
        for (int m = 0; m < 6; ++m) {
            const int idx = (4 * (m % 3) + w) * 64 + l;
            vo[m] = (idx / 24) * (2 * HD_) + (idx % 24) * 16;
            rp[m] = (w >> 1) * 12288 + (4 * lq + (l16 >> 2)) * 384 + 192 * (w & 1) + (l & 3) * 8 + 32 * m; rs[m] = rp[m] + 6144;
        }
        load_coop<6, 0, 6>(smem, w, base, vo, 64 * HD_, rp, rs, wf);
#pragma unroll
        for (int i = 0; i < 20; ++i) *(bf16x8*)(sW + i * 1024) = wf[5][i];
    }
    const unsigned fI = l == 0 ? xm(FI_OFF + (h & 15) * 64 + (4 * L + w) * 4, hside(h)) : OOB;
    const unsigned po0 = 2 * QK_OFF + ((L * 16 + (h & 15)) * 4 + (w & 2)) * 16384 + (w & 1) * 3072 + hside(h) * 8192;
    const int it0 = L * A.lag;
    const unsigned l16s = l16b;
    NEXT_DECL;
    unsigned vx = l16b, ld = lds0, po;
    bf16x8 xw[3], wl[2];
    f32x4 acc[2][6];
    u32x4 o[2];
    auto prime = [&]() { xw[0] = *(const bf16x8*)(smem + vx); xw[1] = *(const bf16x8*)(smem + vx + 2048); };
    auto tail = [&]() {
#pragma unroll
        for (int f = 0; f < 3; ++f) { CVT2(o[f & 1][0], o[f & 1][1], acc[1][2 * f]); CVT2(o[f & 1][2], o[f & 1][3], acc[1][2 * f + 1]); sst16(rx, po, l16s + f * 1024, o[f & 1]); SCHED0(); }
    };
    prime();
    wl[0] = *(const bf16x8*)sW;
#pragma unroll 1
    for (;; ++s) {
        SCHED0();
        sfor<0, 240>([&](auto G) {
            constexpr int g = decltype(G)::value, i = g / 6, tt = g % 6, ks = i % 20;
            f32x4& d = acc[i / 20][tt];
            if constexpr (tt == 5) { if (ks == 0) MF_VV0(d, wl[i & 1], xw[i % 3]); else MF_VV(d, wl[i & 1], xw[i % 3]); }
            else if constexpr (20 * tt + ks < 64) { if (ks == 0) MF_AV0(d, wf[tt][ks], xw[i % 3]); else MF_AV(d, wf[tt][ks], xw[i % 3]); }
            else { if (ks == 0) MF_VV0(d, wf[tt][ks], xw[i % 3]); else MF_VV(d, wf[tt][ks], xw[i % 3]); }
            SCHED0();
            if constexpr (tt == 1 && i < 38) xw[(i + 2) % 3] = *(const bf16x8*)(smem + vx + (i + 2) % 20 * 2048 + (i + 2) / 20 * 1024);
            if constexpr (tt == 3) wl[~i & 1] = *(const bf16x8*)(sW + (i + 1) % 20 * 1024);
            NEXT_ADDR(2, 6);
            if (g == 4) { PIN_S(s); po = (s & (TR - 1)) << 21; PIN_S(po); }
            if (g == 10) { so = (po >> 8) & 8192; PIN_S(so); }
            if (g == 16) { po = (po0 ^ so) + po; PIN_S(po); }
            if (g == 56) { VMW(0); }
            if (g == 58) sst4(rx, pend, fI, 1u);
            if (g == 60) asm volatile("s_barrier" ::: "memory");
            if (g == 62) { PIN_S(s); pend = s + it0; so = pend & 7; PIN_S(pend); PIN_S(so); }
            if (g == 64) { pend = (pend >> 3) << 4; PIN_S(pend); }
            if (g == 66) { pend = (pend | so) << 10; PIN_S(pend); }
            if (g == 68) { ld = 2 * lds0 + 40960 - ld; PIN_S(ld); }
            if constexpr (g >= 122 && g < 140) {
                constexpr int f = (g - 122) / 6;
                if (tt == 2) CVT2(o[0][0], o[0][1], acc[0][2 * f]);
                if (tt == 4) CVT2(o[0][2], o[0][3], acc[0][2 * f + 1]);
                if (tt == 0) sst16(rx, po, l16s + f * 1024, o[0]);
            }
            if (g == 140) { po += 16384; PIN_S(po); }
            NEXT_LOOK(0, ld);
            {
                constexpr int a[10] = {186, 190, 194, 198, 202, 206, 214, 218, 222, 226};
#pragma unroll
                for (int e = 0; e < 10; ++e) {
                    if (g == a[e]) { if (e % 5 == 0) XSET(so, xs, ld); else XADV(so); }
                    if (g == a[e] + (e == 5 ? 6 : 2)) XDMA(so, e / 5);
                }
            }
            if (g == 236) { vx ^= 40960; PIN_V(vx); }
            SCHED0();
        });
        NEXT_GO(5, ld, fI);
    }
}

constexpr int OL_V = 40960, OL_W = OL_V + 12288;
__device__ __forceinline__ void vo_role(const MegaArgs& A, char* smem, const int L, const int h, const int id) {
    WORK_IDS;
    bf16x8 wf[3][20], wo[10][6];
    char* sW = smem + OL_W + w * 24576 + l * 16;
    {
        const void* const base[3] = {A.Wv + h * DH_, A.Wv + h * DH_, A.Wv + h * DH_};
        unsigned vo[3], v2[2];
#pragma unroll
        for (int m = 0; m < 3; ++m) { const int idx = (4 * m + w) * 64 + l; vo[m] = (idx / 24) * (2 * HD_) + (idx % 24) * 16; }
#pragma unroll
        for (int m = 0; m < 2; ++m) v2[m] = (16 * m + (l >> 2)) * (2 * D_) + (l & 3) * 16;
        const int row = 4 * lq + (l16 >> 2), pp = (l & 3) * 8;
        const int rp[3] = {row * 384 + 64 * w + pp, row * 384 + 64 * w + 32 + pp, row * 384 + 256 + 32 * w + pp};
        const int rs[3] = {rp[0] + 6144, rp[1] + 6144, rp[2] + 6144};
        load_coop<3, 0, 3>(smem, w, base, vo, 64 * HD_, rp, rs, wf);
        load_frags<30, 2, true>(smem, w, l, A.Wo + (size_t)(h * DH_) * D_ + 160 * w, v2, [&](int i) { return i % 5 < 4 ? 64u : 64u * D_ - 256u; },
            [&](int i, int tt) -> bf16x8& { return wo[2 * (i % 5) + tt][i / 5]; });
#pragma unroll
        for (int i = 0; i < 4; ++i) asm volatile("" : "+a"(wo[0][i]));
        LDS_BARRIER();
#pragma unroll
        for (int i = 0; i < 24; ++i) *(bf16x8*)(sW + i * 1024) = wo[4 + i / 8 * 2 + (i & 1)][2 + (i >> 1 & 3)];
    }
    const unsigned fU = l == 0 ? lp(SG_FL + FL_U + h * 64 + w * 4) : OOB;
    const unsigned l16s = l16b;
    const unsigned ua = xm(UT_OFF + (320 * w + h) * 1024, L) + l16s, ub = ua ^ (w == 3 ? 8192 : 0);
    char* sV = smem + OL_V + l * 16;
    char* sVw = sV + w * 1024;
    char* sVh = sV + 4096 + (w >> 1) * 1024 + (w & 1) * 8;
    NEXT_DECL;
    unsigned uo;
    constexpr int XW = 4;
    bf16x8 xw[XW], wl[4];
    f32x4 d[4][2];
    u32x4 o[2];
    auto prime = [&]() {
#pragma unroll
        for (int i = 0; i < XW - 1; ++i) xw[i] = *(const bf16x8*)(smem + l16b + i * 2048);
    };
    auto tail = [&]() {
#pragma unroll
        for (int f = 0; f < 2; ++f) { CVT2(o[f][0], o[f][1], d[f][0]); CVT2(o[f][2], o[f][3], d[f][1]); sst16(rx, uo + f * 65536, ub, o[f]); SCHED0(); }
    };
    prime();
#pragma unroll 1
    for (;; ++s) {
        f32x4 acc[2][3];
        u32x4 vf[2][6];
        unsigned cur = xb;
        SCHED0();
        sfor<0, 240>([&](auto G) {
            constexpr int g = decltype(G)::value;
            constexpr int rt = g < 120 ? g / 60 : (g < 144 ? 0 : (g < 168 ? 1 : g & 1));
            constexpr int ct = g < 144 ? g & 3 : (g < 168 ? (g - 144) / 12 * 2 + (g & 1) : (g - 120) / 24 * 2 + (g >> 1 & 1));
            constexpr int kk = g < 144 ? (g + 24) % 24 / 4 : (g < 168 ? g % 12 / 2 : g % 24 / 4), wi = (g - 168) / 24 * 8 + (g % 24 - 8) / 2;
            if constexpr (g < 120) { if (g % 60 < 3) MF_AV0(acc[rt][g % 3], wf[g % 3][0], xw[g / 3 % XW]); else MF_AV(acc[rt][g % 3], wf[g % 3][g % 60 / 3], xw[g / 3 % XW]); }
            else if constexpr (ct >= 4 && kk >= 2) MF_VV(d[ct & 3][rt], vf[rt][kk], wl[wi & 3]);
            else if constexpr (ct == 0 && kk < 4) { if (kk == 0) MF_VA0(d[0][rt], vf[rt][0], wo[0][0]); else MF_VA(d[0][rt], vf[rt][kk], wo[0][kk]); }
            else { if (kk == 0) MF_VV0(d[ct & 3][rt], vf[rt][0], wo[ct][0]); else MF_VV(d[ct & 3][rt], vf[rt][kk], wo[ct][kk]); }
            SCHED0();
            if constexpr (g % 3 == 1 && g / 3 + XW - 1 < 40) { constexpr int i = g / 3 + XW - 1; xw[i % XW] = *(const bf16x8*)(smem + l16b + i % 20 * 2048 + i / 20 * 1024); }
            NEXT_ADDR(2, 3);
            if (g == 24) { PIN_S(s); uo = (s & (TR - 1)) << 22; PIN_S(uo); }
            if (g == 47) { VMW(0); }
            if (g == 48) sst4(rx, pend, fU, 1u);
            if (g == 50) asm volatile("s_barrier" ::: "memory");
            if (g == 51) { pend = cur ^ (L << 13); PIN_S(pend); }
            if constexpr (g >= 60 && g < 140 && g % 60 < 40) {
                constexpr int r = g / 60 - 1, q = r ? g - 120 : (g % 3 == 1 ? -1 : (g - 60) / 3 * 2 + g % 3 / 2);
                if (q == 1) CVT2(o[0][0], o[0][1], acc[r][0]);
                if (q == 2) CVT2(o[0][2], o[0][3], acc[r][1]);
                if (q == 3) CVT2(o[1][0], o[1][1], acc[r][2]);
                if (q == 4) *(u32x4*)(sVw + r * 6144) = o[0];
                if (q == 5) *(u32x2*)(sVh + r * 6144) = u32x2{o[1][0], o[1][1]};
                if (q == 11) asm volatile("s_waitcnt lgkmcnt(%0)\n\ts_barrier" :: "n"(r ? 0 : 3) : "memory");
                if constexpr (q >= 12 && q < 18) vf[r][q - 12] = *(const u32x4*)(sV + r * 6144 + (q - 12) * 1024);
            }
            if constexpr (g >= 168 && g % 24 < 18 && g % 24 != 8 && !(g & 1)) {
                constexpr int j = (g - 168) / 24 * 8 + g % 24 / 2 - (g % 24 > 8);
                wl[j & 3] = *(const bf16x8*)(sW + j * 1024);
            }
            if constexpr (g >= 156) {
                constexpr int p = g < 168 ? 0 : (g - 144) / 24, q = g < 168 ? g - 156 : g % 24, c0 = 2 * p & 3;
                if (q == 1) CVT2(o[0][0], o[0][1], d[c0][0]);
                if (q == 3) CVT2(o[0][2], o[0][3], d[c0][1]);
                if (q == 5) sst16(rx, uo, p ? ub : ua, o[0]);
                if (q == 7) CVT2(o[1][0], o[1][1], d[c0 + 1][0]);
                if (q == 8) { uo += 65536; PIN_S(uo); }
                if (q == 9) CVT2(o[1][2], o[1][3], d[c0 + 1][1]);
                if (q == 11) sst16(rx, uo, p ? ub : ua, o[1]);
            }
            if constexpr (g == 168 || g == 181 || g == 205 || g == 229) { uo += 65536; PIN_S(uo); }
            NEXT_LOOK(4, lds0);
            {
                constexpr int a[10] = {186, 189, 192, 200, 205, 209, 213, 216, 224, 231}, b[10] = {188, 191, 196, 204, 207, 212, 215, 220, 228, 233};
#pragma unroll
                for (int e = 0; e < 10; ++e) {
                    if (g == a[e]) { if (e % 5 == 0) XSET(so, xs, lds0); else XADV(so); }
                    if (g == b[e]) XDMA(so, e / 5);
                }
            }
            SCHED0();
        });
        NEXT_GO(7, lds0, fU);
    }
}

#define G_B 107
#define BSLP 6
constexpr int BL_Q = 40960, BL_P = BL_Q + 49152;
__device__ __forceinline__ void b_role(const MegaArgs& A, char* smem, const int L, const int hb, const int id) {
    WORK_IDS;
    bf16x8 wf[4][20];
    int dst[4];
    const int sw = (l16 & 6) << 3;
    {
        const void* const base[4] = {A.wqkv + 80 * hb, A.wqkv + 80 * hb, A.wqkv + 80 * hb, A.wqkv + 80 * hb};
        unsigned vo[4];
        int rp[4], rs[4];
#pragma unroll
        for (int m = 0; m < 4; ++m) {
            const int idx = min((4 * m + w) * 64 + l, 959), c = idx % 30, T = min(4 * m + w, 14), e = 16 * (T % 5) + 4 * lq;
            vo[m] = (idx / 30) * 3840 + (c / 10) * (2 * D_) + (c % 10) * 16;
            rp[m] = (4 * lq + (l16 >> 2)) * 480 + 32 * T + (l & 3) * 8; rs[m] = rp[m] + 7680;
            dst[m] = BL_Q + (T / 5 * 4 + e / 20) * 4096 + l16 * 64 + ((e % 20 * 2) ^ sw);
        }
        load_coop<4, 0, 4>(smem, w, base, vo, 32 * 3840, rp, rs, wf);
    }
    f32x4 cs[4];
    {
        u32x4 one = {0x3f803f80u, 0x3f803f80u, 0x3f803f80u, 0x3f803f80u};
        asm volatile("s_nop 1" : "+v"(one));
        sfor<0, 80>([&](auto G) { constexpr int g = decltype(G)::value, tt = g & 3, ks = g >> 2;
            if (ks == 0) MF_AV0(cs[tt], wf[tt][0], one); else if (tt < 3 || ks < 4) MF_AV(cs[tt], wf[tt][ks], one); else MF_VV(cs[tt], wf[tt][ks], one); });
        asm volatile("s_nop 7" : "+v"(cs[0]), "+v"(cs[1]), "+v"(cs[2]), "+v"(cs[3]));
    }
#pragma unroll
    for (int i = 0; i < 12; ++i) *(u32x4*)(smem + BL_Q + i * 4096 + t * 16) = u32x4{0u, 0u, 0u, 0u};
    const bool odd = l16 & 1;
    bool wm[4];
#pragma unroll
    for (int e = 0; e < 4; ++e) { const int d = l16 + 16 - 16 * (e >> 1) - 4 * lq - 2 * (e & 1) - (l16 & 1); wm[e] = d >= 0 && d <= 14; }
    const unsigned ps0 = odd ? 0x01000c0cu : 0x0c0c0100u, ps1 = odd ? 0x03020c0cu : 0x0c0c0302u;
    int qa = BL_Q + w * 4096 + l16 * 64 + ((lq * 16) ^ sw), va[2];
    {
        const int row = 4 * lq + (l16 >> 2), vs = (row & 6) << 3;
        va[0] = BL_Q + (8 + w) * 4096 + row * 64 + ((8 * (w & 1) + 8 * (l & 3)) ^ vs);
        va[1] = BL_Q + (8 + w) * 4096 + row * 64 + ((32 * (~w & 1) + 8 * (l & 3)) ^ vs);
    }
    const int g0 = (20 * hb + 5 * w + (w & 1) + (lq & 2)) >> 1, g1 = (20 * hb + 5 * w + 4 * (~w & 1)) >> 1;
    const unsigned oo0 = lp(SG_OB + (g0 >> 2) * 2048 + (lq & 1) * 1024 + (g0 & 3) * 256 + l16 * 16);
    const unsigned oo1 = lq < 2 ? lp(SG_OB + (g1 >> 2) * 2048 + lq * 1024 + (g1 & 3) * 256 + l16 * 16 + (w & 1) * 8) : OOB;
    const unsigned fO = t < 4 ? lp(SG_FL + FL_O + (8 * t + hb) * 128) : OOB;
    const unsigned st0 = lp(SG_ST + (4 * (l >> 5) + w) * 256 + (l & 31) * 8), st2 = l < 32 ? st0 + 4096 : OOB;
    unsigned fXv = xm(FX_OFF + (hb >> 1) * 64, L), xv, X = rtime() - 2 * PER, xs = OOB;
    PIN_V(fXv);
    const float z2 = l < 32 ? 1.f : 0.f;
    f32v2* sP = (f32v2*)(smem + BL_P) + (l & 31);
    unsigned* sK = (unsigned*)(smem + BL_P + 1024);
    unsigned pend = OOB, sb = seg_base(L, 0), so, fs = fx_step(1 + L * A.lag);
    for (int s = 0; s < NSEG_; ++s) { sst16(rx, seg_base(L, s), oo0, u32x4{0u, 0u, 0u, 0u}); sst8(rx, seg_base(L, s), oo1, u32x2{0u, 0u}); }
    u32x2 st[3];
    auto look = [&](int e, unsigned b, unsigned f) { if (e == 0) LDA8(st[0], st0, b, 0); if (e == 1) LDA8(st[1], st0, b, 2048); if (e == 2) LDA8(st[2], st2, b, 0); if (e == 3) LDA4(xv, fXv, f); };
#define SEEN(n) asm volatile("s_waitcnt vmcnt(%4)" : "+v"(st[0]), "+v"(st[1]), "+v"(st[2]), "+v"(xv) : "n"(n) : "memory")
#define SWAP2(op, a, b) asm volatile(op " %0, %1" : "+v"(a), "+v"(b))
    int s;
    SZERO(s);
#pragma unroll 1
    do {
        if (xs == OOB) {
            bool e = (int)(rtime() - X - E_B) >= 0;
#pragma unroll
            for (int i = 0; i < 4; ++i) look(i, e ? sb : OOB, e ? fs : OOB);
            VMB(4);
            sst4(rx, pend, fO, 1u);
            pend = OOB;
            SEEN(1);
#pragma unroll 1
            for (;;) {
                xv = __builtin_amdgcn_readfirstlane(xv);
                X = xv ? xv : X;
                LIVE;
                if (e) { if (!BAD(umax3(st[0][0], st[1][0], st[2][0]))) break; __builtin_amdgcn_s_sleep(BSLP); }
                e = true;
                rt_until(X + E_B);
#pragma unroll
                for (int i = 0; i < 4; ++i) look(i, sb, fs);
                SEEN(0);
            }
            XSET(so, sb + lp(SG_X1), lds0); XDMA(so, 0); XDMA(so, 1); XADV(so); XDMA(so, 0); XDMA(so, 1);
        }
        VMB(2);
        f32x4 acc[4][2];
        unsigned cur;
        {
            bf16x8 xw[3][2];
#pragma unroll
            for (int e = 0; e < 4; ++e) xw[e >> 1][e & 1] = *(const bf16x8*)(smem + l16b + e * 1024);
            SCHED0();
            u32x2 y;
            u64 now, bad;
            float a, b, c, d, mw, mb[2], rd[2];
            unsigned lb, lf;
            f32v2 p0, p1, p2, p3;
            sfor<0, 167>([&](auto G) {
                constexpr int g = decltype(G)::value, ks = g < 136 ? g / 8 : 17 + (g - 136) % 6 / 2, tt = g < 136 ? g % 8 / 2 : (g - 136) / 6, rt = g & 1;
                constexpr int q = g % 8 == 1 || g % 8 == 3 || g % 8 == 7 ? -1 : g / 8 * 5 + (g % 8 < 4 ? g % 8 / 2 : g % 8 - 2);
                if constexpr (g < 160) {
                    if (ks == 0) MF_AV0(acc[tt][rt], wf[tt][0], xw[0][rt]);
                    else if (tt < 3 || ks < 4) MF_AV(acc[tt][rt], wf[tt][ks], xw[ks % 3][rt]); else MF_VV(acc[tt][rt], wf[tt][ks], xw[ks % 3][rt]);
                }
                SCHED0();
                if constexpr (g < 136 && (g % 8 == 1 || g % 8 == 3)) xw[(ks + 2) % 3][g % 8 / 2] = *(const bf16x8*)(smem + l16b + (ks + 2) * 2048 + g % 8 / 2 * 1024);
                if constexpr (g == 135 || g == 136) xw[1][~g & 1] = *(const bf16x8*)(smem + l16b + 19 * 2048 + (~g & 1) * 1024);
                if (q == 0 || q == 5 || q == 11) XADV(so);
                if (q == 1 || q == 6 || q == 13) XDMA(so, 0);
                if (q == 3 || q == 8 || q == 15) XDMA(so, 1);
                if (q == 10) VMB_LATE(4);
                if (q == 2) { a = __uint_as_float(st[0][0]) + __uint_as_float(st[1][0]); a += __uint_as_float(st[2][0]); PIN_V(a); }
                if (q == 4) { b = a; c = __uint_as_float(st[0][1]) + __uint_as_float(st[1][1]); PIN_V(b); PIN_V(c); }
                if (q == 7) { SWAP2("v_permlane32_swap_b32", a, b); c += __uint_as_float(st[2][1]); PIN_V(c); }
                if (q == 9) { mw = (a + b) * 0.2f; PIN_V(mw); }
                if (q == 12) { a = __uint_as_float(st[0][0]) - mw; b = __uint_as_float(st[1][0]) - mw; PIN_V(a); PIN_V(b); }
                if (q == 14) { d = (__uint_as_float(st[2][0]) - mw) * z2; PIN_V(d); }
                if (q == 16) { a = __builtin_fmaf(b, b, a * a); PIN_V(a); }
                if (q == 17) { c = __builtin_fmaf(32.f, __builtin_fmaf(d, d, a), c); PIN_V(c); }
                if (q == 18) { b = c; PIN_V(b); }
                if (q == 19) SWAP2("v_permlane32_swap_b32", c, b);
                if (q == 21) { c += b; sP[w * 32] = f32v2{mw, c}; }
                if (q == 22) { cur = sb; sb = (s + 1) << 19; PIN_S(cur); PIN_S(sb); }
                if (q == 23) { lb = (sb >> 8) & 8192; PIN_S(lb); }
                if (q == 24) { sb = (sb | lb) + 2 * SG_OFF; PIN_S(sb); }
                if (q == 26) { sb ^= L << 13; X += PER; PIN_S(sb); PIN_S(X); }
                if (q == 27) { fs += 256; lb = fs & 8192; PIN_S(fs); PIN_S(lb); }
                if (q == 28) { fs += lb; PIN_S(fs); }
                if (q == 30) asm volatile("s_waitcnt vmcnt(4) lgkmcnt(2)\n\ts_barrier" ::: "memory");
                if (q == 31) sst4(rx, pend, fO, 1u);
                if (q == 32) { p0 = sP[0]; p1 = sP[32]; }
                if (q == 33) { p2 = sP[64]; p3 = sP[96]; }
                if (q == 37) { a = p0[0] + p1[0]; b = p2[0] + p3[0]; PIN_V(a); PIN_V(b); }
                if (q == 38) { mw = (a + b) * 0.25f; PIN_V(mw); }
                if (q == 39) { a = p0[0] - mw; b = p1[0] - mw; PIN_V(a); PIN_V(b); }
                if (q == 40) { c = p2[0] - mw; PIN_V(c); }
                if (q == 41) { d = p3[0] - mw; a *= a; PIN_V(a); PIN_V(d); }
                if (q == 42) { a = __builtin_fmaf(c, c, __builtin_fmaf(b, b, a)); PIN_V(a); }
                if (q == 43) { a = __builtin_fmaf(d, d, a); b = p0[1] + p1[1]; PIN_V(a); PIN_V(b); }
                if (q == 44) { b += p2[1] + p3[1]; PIN_V(b); }
                if (q == 46) { a = __builtin_fmaf(160.f, a, b) * (1.0f / D_); PIN_V(a); }
                if (q == 47) { a = rb(a); PIN_V(a); }
                if (q == 48) { mb[0] = rb(mw); PIN_V(mb[0]); }
                if (q == 49) { a += 1e-5f; mb[1] = mb[0]; PIN_V(a); PIN_V(mb[1]); }
                if (q == 50) VMB(1);
                if (q == 51) { a = rb(a); PIN_V(a); }
                if (q == 52) { a = __builtin_amdgcn_sqrtf(a); PIN_V(a); SWAP2("v_permlane16_swap_b32", mb[0], mb[1]); }
                if (q == 53) { a = rb(a); PIN_V(a); }
                if (q == 54) { rd[0] = __builtin_amdgcn_rcpf(a); PIN_V(rd[0]); }
                if (q == 55) { rd[1] = rd[0]; PIN_V(rd[1]); }
                if (q == 60) SWAP2("v_permlane16_swap_b32", rd[0], rd[1]);
                if constexpr (g >= 88 && g < 104 && q >= 0 && g % 8) {
                    constexpr int e = (g % 8 < 5 ? g % 8 + 2 : 2 * (g % 8) - 10) + (g >= 96);
                    acc[e >> 1][e & 1] = __builtin_elementwise_fma(cs[e >> 1], f32x4{-mb[e & 1], -mb[e & 1], -mb[e & 1], -mb[e & 1]}, acc[e >> 1][e & 1]);
                    PIN_V(acc[e >> 1][e & 1]);
                }
                if (g == G_B) asm volatile("s_memrealtime %0" : "=s"(now));
                if (g == G_B + 5) asm volatile("s_waitcnt lgkmcnt(0)" : "+s"(now));
                if (g == G_B + 7) { lf = (unsigned)now - X - E_B; PIN_S(lf); }
                if (g == G_B + 9) asm volatile("s_cmp_lt_i32 %1, 63\n\ts_cselect_b32 %0, %0, -1" : "+s"(lf) : "s"(s) : "scc");
                if (g == G_B + 10) asm volatile("s_cmp_ge_i32 %1, 0\n\ts_cselect_b32 %0, %2, %4\n\ts_cselect_b32 %1, %3, %4" : "=&s"(lb), "+s"(lf) : "s"(sb), "s"(fs), "n"(OOB) : "scc");
                if constexpr (g >= G_B + 12 && g < G_B + 16) look(g - G_B - 12, lb, lf);
                if constexpr (g >= 143) {
                    constexpr int u = (g - 143) % 3, r = (g - 143) / 3 & 1, e = (g - 143) / 6;
                    if (g == 160) asm volatile("s_nop 3");
                    SCHED0();
                    if (u == 0) { acc[e][r] *= rd[r]; PIN_V(acc[e][r]); }
                    if (u == 1) CVT2(y[0], y[1], acc[e][r]);
                    if (u == 2) *(u32x2*)(smem + dst[e] + r * 1024) = y;
                    if (g == 148) SEEN(0);
                    if (g == 151) { lf = umax3(st[0][0], st[1][0], st[2][0]); PIN_V(lf); }
                    if (g == 154) { bad = __ballot(lf == ~0u); PIN_S(bad); }
                    if (g == 157) asm volatile("s_cmp_eq_u64 %1, 0\n\ts_cselect_b32 %0, %0, %2" : "+s"(lb) : "s"(bad), "n"(OOB) : "scc");
                    if (g == 160) sK[w] = lb;
                }
                SCHED0();
            });
        }
        LDS_BARRIER();
        {
            constexpr float SC = 0.22360679774997896f * 1.4426950408889634f;
            bf16x8 kf[3], qf[2], vx[2][2];
            const char* sq = smem + qa;
            kf[0] = *(const bf16x8*)(smem + (qa ^ 2048) + 17408);
            qf[0] = *(const bf16x8*)sq;
            kf[1] = *(const bf16x8*)(sq + 16384);
            qf[1] = *(const bf16x8*)(sq + 1024);
            kf[2] = *(const bf16x8*)(sq + 17408);
            const u32x4 k4 = *(const u32x4*)sK;
#pragma unroll
            for (int dt = 0; dt < 2; ++dt) { vx[0][dt] = tr_read(smem, (va[dt] ^ 2048) + 1024, va[dt]); vx[1][dt] = tr_read(smem + va[dt], 0, 1024); }
            SCHED0();
            xv = __builtin_amdgcn_readfirstlane(xv);
            X = xv ? xv : X;
            PIN_S(X);
            SCHED0();
            f32x4 sc[2][2];
            MF_VV0(sc[0][0], kf[0], qf[0]); MF_VV0(sc[0][1], kf[1], qf[0]); MF_VV0(sc[1][0], kf[1], qf[1]); MF_VV0(sc[1][1], kf[2], qf[1]);
            qa ^= 2048; va[0] ^= 2048; va[1] ^= 2048;
#pragma unroll
            for (int e = 0; e < 4; ++e) dst[e] ^= 2048;
            asm volatile("" : "+v"(sc[0][0]), "+v"(sc[0][1]), "+v"(sc[1][0]), "+v"(sc[1][1]), "+v"(qa), "+v"(va[0]), "+v"(va[1]), "+v"(dst[0]), "+v"(dst[1]), "+v"(dst[2]), "+v"(dst[3]));
            xs = __builtin_amdgcn_readfirstlane(max(umax3(k4[0], k4[1], k4[2]), k4[3]));
            XSET(so, xs + lp(SG_X1), lds0);
            XDMA(so, 0);
            SCHED0();
            float sv[4][2];
#pragma unroll
            for (int e = 0; e < 4; ++e) {
                const f32x4 x = sc[e >> 1][e & 1];
                const bool live = s > 0 || e > 0;
                sv[e][0] = (wm[2 * (e & 1)] && live) ? (odd ? x[1] : x[0]) : -INFINITY;
                sv[e][1] = (wm[2 * (e & 1) + 1] && live) ? (odd ? x[3] : x[2]) : -INFINITY;
            }
            float m0 = max2r(max3r(sv[0][0], sv[0][1], sv[1][0]), sv[1][1]), m1 = max2r(max3r(sv[2][0], sv[2][1], sv[3][0]), sv[3][1]);
            PIN_V(m0); PIN_V(m1);
            XDMA(so, 1);
            XADV(so);
            SCHED0();
            red_max_q2(m0, m1);
            m0 *= -SC; m1 *= -SC;
            PIN_V(m0); PIN_V(m1);
            XDMA(so, 0);
            SCHED0();
#pragma unroll
            for (int e = 0; e < 8; ++e) sv[e >> 1][e & 1] = __builtin_amdgcn_exp2f(__builtin_fmaf(sv[e >> 1][e & 1], SC, e < 4 ? m0 : m1));
            float s0 = (sv[0][0] + sv[0][1]) + (sv[1][0] + sv[1][1]), s1 = (sv[2][0] + sv[2][1]) + (sv[3][0] + sv[3][1]);
            PIN_V(s0); PIN_V(s1);
            XDMA(so, 1);
            SCHED0();
            red_sum_q2(s0, s1);
            s0 = __builtin_amdgcn_rcpf(s0); s1 = __builtin_amdgcn_rcpf(s1);
            f32x4 o[2][2];
            u32x4 pt[2];
#pragma unroll
            for (int e = 0; e < 4; ++e) {
                const unsigned p = pk2(sv[e][0] * (e < 2 ? s0 : s1), sv[e][1] * (e < 2 ? s0 : s1));
                pt[e >> 1][2 * (e & 1)] = __builtin_amdgcn_perm(p, p, ps0);
                pt[e >> 1][2 * (e & 1) + 1] = __builtin_amdgcn_perm(p, p, ps1);
            }
            asm volatile("s_nop 1" : "+v"(pt[0]), "+v"(pt[1]));
            MF_VV0(o[0][0], vx[0][0], pt[0]); MF_VV0(o[1][0], vx[1][0], pt[1]); MF_VV0(o[0][1], vx[0][1], pt[0]); MF_VV0(o[1][1], vx[1][1], pt[1]);
            asm volatile("s_nop 1" : "+v"(o[0][0]), "+v"(o[1][0]));
            {
                const auto a = __builtin_amdgcn_permlane16_swap(pk2(o[0][0][0], o[0][0][1]), pk2(o[1][0][0], o[1][0][1]), false, false);
                const auto b = __builtin_amdgcn_permlane16_swap(pk2(o[0][0][2], o[0][0][3]), pk2(o[1][0][2], o[1][0][3]), false, false);
                sst16(rx, cur, oo0, u32x4{a[0], b[0], a[1], b[1]});
            }
            SCHED0();
            asm volatile("" : "+v"(o[0][1]), "+v"(o[1][1]));
            {
                const auto a = __builtin_amdgcn_permlane16_swap(pk2(o[0][1][0], o[0][1][1]), pk2(o[1][1][0], o[1][1][1]), false, false);
                const auto b = __builtin_amdgcn_permlane16_swap(pk2(o[0][1][2], o[0][1][3]), pk2(o[1][1][2], o[1][1][3]), false, false);
                sst8(rx, cur, oo1, u32x2{a[0], b[0]});
            }
        }
        pend = cur;
    } while (++s < NSEG_);
    VMB(0);
    sst4(rx, pend, fO, 1u);
    SPIN_END;
}

__device__ __forceinline__ void bo_role(const MegaArgs& A, char* smem, const int L, const int j, const int id) {
    WORK_IDS;
    const int rt = w & 1, hf = w >> 1;
    bf16x8 wf[5][20];
    {
        const void* const base[3] = {A.wo + 160 * j, A.wo + 160 * j, A.wo + 160 * j};
        unsigned vo[3];
        int rp[5], rs[5];
#pragma unroll
        for (int m = 0; m < 3; ++m) { const int idx = min((4 * m + w) * 64 + l, 639); vo[m] = (idx / 20) * (2 * D_) + (idx % 20) * 16; }
#pragma unroll
        for (int m = 0; m < 5; ++m) { rp[m] = (8 * lq + (l16 >> 2)) * 320 + 32 * (m < 4 ? 4 * hf + m : 8 + hf) + (l & 3) * 8; rs[m] = rp[m] + 1280; }
        load_coop<3, 0, 5>(smem, w, base, vo, 64 * D_, rp, rs, wf);
    }
    const unsigned x0 = lp((5 * j + 2 * hf) * 2048 + rt * 1024 + l16b), x1 = lp((5 * j + 2 * hf + 1) * 2048 + rt * 1024 + l16b), x2 = lp((5 * j + 4) * 2048 + rt * 1024 + l16b + hf * 8);
    const unsigned fO = l < 8 ? lp(SG_FL + FL_O + (8 * j + l) * 128) : OOB, fX = t < 8 ? lp(SG_FL + FL_X + (4 * (t & 3) + j) * 128) + (t & 4) * 2048 : OOB;
    const unsigned sr = lp(SG_ST + ST_2 + rt * 1024 + lq * 256 + l16 * 8), sw = l < 16 ? sr + j * 256 + hf * 128 : OOB;
    auto flag = [&](unsigned b) { sst4(rx, b & ~8192u, fX ^ (b & 8192u), 1u); };
    const rsrc_t ro = mk_rsrc(A.out, S_ * D_ * 2);
    const unsigned oo = ((16 * rt + l16) * D_ + 160 * j + 64 * hf + 4 * lq) * 2, oo2 = oo + (128 - 48 * hf) * 2;
    const char* sX = smem + rt * 1024 + l16b;
    unsigned pend = OOB, sb = seg_base(L, 0), so, xs = OOB;
    unsigned* sK = (unsigned*)(smem + 40960);
    auto first = [&](unsigned b) { XSET(so, b + lp(SG_OB), lds0); XDMA(so, 0); XDMA(so, 1); XADV(so); XDMA(so, 0); XDMA(so, 1); XADV(so); XDMA(so, 0); XDMA(so, 1); };
    int s;
    SZERO(s);
#pragma unroll 1
    do {
        if (xs == OOB) {
            unsigned pv;
            LDA4(pv, fO, sb);
            VMB(1);
            flag(pend);
            pend = OOB;
            WAITV(1, pv);
            if (__ballot(pv == 0) & 255) wave_wait(rx, fO, sb, spins);
            first(sb);
            VMB(4);
        } else VMB(5);
        f32x4 acc[5], xf[5];
        u32x4 r0, r1, k4;
        u32x2 r2;
        unsigned cur;
        {
            bf16x8 xw[3];
            unsigned sx, pv;
            xw[0] = *(const bf16x8*)sX; xw[1] = *(const bf16x8*)(sX + 2048);
            SCHED0();
            sfor<0, 100>([&](auto G) {
                constexpr int g = decltype(G)::value, ks = g / 5, tt = g % 5;
                if constexpr (20 * tt + ks < 64) { if (ks == 0) MF_AV0(acc[tt], wf[tt][0], xw[0]); else MF_AV(acc[tt], wf[tt][ks], xw[ks % 3]); }
                else { if (ks == 0) MF_VV0(acc[tt], wf[tt][0], xw[0]); else MF_VV(acc[tt], wf[tt][ks], xw[ks % 3]); }
                SCHED0();
                if (g == 0 || g == 7) XADV(so);
                if (g == 2 || g == 12) XDMA(so, 0);
                if (g == 5 || g == 15) XDMA(so, 1);
                if (g == 3) { cur = sb; sx = sb + lp(SG_X1); PIN_S(cur); PIN_S(sx); }
                if (g == 10) VMB_LATE(4);
                if (g == 18) LDA(r0, x0, sx, 0);
                if (g == 22) LDA(r1, x1, sx, 0);
                if (g == 25) LDA8(r2, x2, sx, 0);
                if (g == 30) VMB(7);
                if (g == 32) flag(pend);
                if (g == 35) { sb = (s + 1) << 19; PIN_S(sb); }
                if (g == 37) { sx = (sb >> 8) & 8192; PIN_S(sx); }
                if (g == 38) { sb = (sb | sx) + 2 * SG_OFF; PIN_S(sb); }
                if (g == 40) { sb ^= L << 13; PIN_S(sb); }
                if (g == 42) asm volatile("s_cmp_lt_i32 %1, 63\n\ts_cselect_b32 %0, %0, %2" : "+s"(sb) : "s"(s), "n"(OOB) : "scc");
                if (g == 50) VMB(4);
                if constexpr (tt == 1 && ks < 18) xw[(ks + 2) % 3] = *(const bf16x8*)(sX + (ks + 2) * 2048);
                if (g == 60) LDA4(pv, fO, sb);
                if (g == 63) asm volatile("s_waitcnt vmcnt(2)" : "+v"(r0), "+v"(r1), "+v"(r2) :: "memory");
                if constexpr (g >= 65 && g < 90 && (tt == 2 || tt == 3)) {
                    constexpr int e = ks - 13, k = tt - 2;
                    const unsigned u = e < 2 ? r0[2 * (e & 1) + k] : (e < 4 ? r1[2 * (e & 1) + k] : r2[k]);
                    xf[e][2 * k] = lo16(u); xf[e][2 * k + 1] = hi16(u);
                    asm volatile("" : "+v"(xf[e][2 * k]), "+v"(xf[e][2 * k + 1]));
                }
                if (g == 90) WAITV(0, pv);
                if (g == 91) { sx = (unsigned)__ballot(pv == 0); PIN_S(sx); }
                if (g == 92) asm volatile("s_and_b32 %0, %0, 255\n\ts_cselect_b32 %0, %2, %1" : "+s"(sx) : "s"(sb), "n"(OOB) : "scc");
                if (g == 93) sK[w] = sx;
                if (g == 97) LDS_BARRIER();
                if (g == 98) k4 = *(const u32x4*)sK;
                SCHED0();
            });
        }
#pragma unroll
        for (int e = 0; e < 5; ++e) { PIN_V(acc[e]); acc[e] += xf[e]; PIN_V(acc[e]); SCHED0(); }
        xs = __builtin_amdgcn_readfirstlane(max(umax3(k4[0], k4[1], k4[2]), k4[3]));
        if (L) {
            if (xs != OOB) first(xs);
            const unsigned so2 = s * (SEG_ * D_ * 2);
#pragma unroll
            for (int e = 0; e < 5; ++e) __builtin_amdgcn_raw_buffer_store_b64(u32x2{pk2(acc[e][0], acc[e][1]), pk2(acc[e][2], acc[e][3])}, ro, e < 4 ? oo + 32 * e : oo2, so2, 0);
        } else {
            f32x4 a = ((acc[0] + acc[1]) + (acc[2] + acc[3])) + acc[4];
            const float mw = red_sum_q((a[0] + a[1]) + (a[2] + a[3])) * (1.0f / 80);
#pragma unroll
            for (int e = 0; e < 5; ++e) { xf[e] = acc[e]; acc[e] -= mw; }
            a = acc[0] * acc[0];
#pragma unroll
            for (int e = 1; e < 5; ++e) a = __builtin_elementwise_fma(acc[e], acc[e], a);
            sst8(rx, cur, sw, u32x2{nosent(__float_as_uint(mw)), __float_as_uint(red_sum_q((a[0] + a[1]) + (a[2] + a[3])))});
            SCHED0();
            if (xs != OOB) first(xs);
            r0 = pk8(xf[0], xf[1]); r1 = pk8(xf[2], xf[3]); r2 = u32x2{pk2(xf[4][0], xf[4][1]), pk2(xf[4][2], xf[4][3])};
            const unsigned so2 = cur + lp(SG_XR);
            sst16(rx, so2, x0, r0); sst16(rx, so2, x1, r1); sst8(rx, so2, x2, r2);
            VMW(3);
            u32x2 p, q;
#pragma unroll 1
            for (;;) {
                LDA8(p, sr, cur, 0); LDA8(q, sr, cur, 128);
                LIVE;
                asm volatile("s_waitcnt vmcnt(0)" : "+v"(p), "+v"(q) :: "memory");
                if (!BAD(max(p[0], q[0]))) break;
            }
            const float m0 = __uint_as_float(p[0]), m1 = __uint_as_float(q[0]), mean = red_sum_q(m0 + m1) * 0.125f, e0 = m0 - mean, e1 = m1 - mean;
            const float var = red_sum_q((__uint_as_float(p[1]) + __uint_as_float(q[1])) + 80.f * (e0 * e0 + e1 * e1)) * (1.0f / D_);
            const float rd = __builtin_amdgcn_rcpf(rb(__builtin_amdgcn_sqrtf(rb(rb(var) + 1e-5f)))), c = (mw - rb(mean)) * rd;
#pragma unroll
            for (int e = 0; e < 5; ++e) acc[e] = acc[e] * rd + c;
            sst16(rx, cur, x0, pk8(acc[0], acc[1])); sst16(rx, cur, x1, pk8(acc[2], acc[3])); sst8(rx, cur, x2, u32x2{pk2(acc[4][0], acc[4][1]), pk2(acc[4][2], acc[4][3])});
            pend = cur;
        }
    } while (++s < NSEG_);
    VMB(0);
    flag(pend);
    SPIN_END;
}

constexpr int MEGA_LDS = 163840;
__global__ void __launch_bounds__(256, 1) mega_kernel(MegaArgs A) {
    extern __shared__ __attribute__((aligned(16))) char smem[];
    int* sRole = (int*)smem;
    if (threadIdx.x < 64) {
        const rsrc_t rf = mk_rsrc(arena_base(A.ar), ARENA);
        unsigned tm[2] = {~0u, ~0u};
#pragma unroll
        for (int k = 0; k < 4; ++k) {
            const unsigned long long t0 = __builtin_amdgcn_s_memtime();
            if (threadIdx.x == 0) bst4(rf, (blockIdx.x >> 7) * 16384 + (blockIdx.x & 127) * 64 + (k & 1) * 8192, 0u);
            DRAIN();
            const unsigned d = (unsigned)(__builtin_amdgcn_s_memtime() - t0);
            tm[k & 1] = d < tm[k & 1] ? d : tm[k & 1];
        }
        const int cls = tm[1] < tm[0] ? 1 : 0;
        if (threadIdx.x == 0) {
            unsigned* cnt = (unsigned*)(arena_base(A.ar) + xm(XS_OFF, 0));
            const int x4 = (int)(__builtin_amdgcn_s_getreg((3 << 11) | 20) & 3u);
            int role = -1;
            for (int p = -1; p < 8 && role < 0; ++p) {
                const int pool = p < 0 ? 4 * cls + x4 : p, k = (int)__hip_atomic_fetch_add(cnt + 32 * pool, 1u, RLX_AGENT);
                if (k < 32) role = 32 * pool + k;
            }
            *sRole = role;
        }
    }
    __syncthreads();
    const int role = __builtin_amdgcn_readfirstlane(*sRole), pool = role >> 5, k = role & 31;
    __syncthreads();
    if (k < 13) {
        if (!(A.roles & 1)) return;
        if (k < 4) s_role(A, smem, 4 * pool + k, role);
        else if (k < 8) vm_role(A, smem, 4 * pool + k - 4, role);
        else c_role(A, smem, pool, k - 8, role);
        return;
    }
    if (k < 21) { if (A.roles & 2) qk_role(A, smem, (k - 13) >> 2, 4 * pool + ((k - 13) & 3), role); }
    else if (k < 29) { if (A.roles & 4) vo_role(A, smem, (k - 21) >> 2, 4 * pool + ((k - 21) & 3), role); }
    else if (k < 31) { if (A.roles & 8) b_role(A, smem, pool >> 2, 2 * (pool & 3) + k - 29, role); }
    else if (A.roles & 16) bo_role(A, smem, pool >> 2, pool & 3, role);
}

#define ROLES 31
extern "C" void kernel_launch(void* const* d_in, const int* in_sizes, int n_in,
                              void* d_out, int out_size, void* d_ws, size_t ws_size,
                              hipStream_t stream) {
    MegaArgs A;
    char* ws = (char*)d_ws;
    A.dbg = (unsigned long long*)ws; ws += 262144;
    A.tmo = (unsigned*)ws; ws += 4096;
    A.ar = ws;
    A.x0 = (const bf16_t*)d_in[0]; A.mem0 = (const bf16_t*)d_in[1]; A.Wq = (const bf16_t*)d_in[2]; A.Wk = (const bf16_t*)d_in[3]; A.Wv = (const bf16_t*)d_in[4];
    A.Wo = (const bf16_t*)d_in[5]; A.wqkv = (const bf16_t*)d_in[8]; A.wo = (const bf16_t*)d_in[9];
    A.out = (bf16_t*)d_out; A.pad = 0;
    A.lag = LAGV; A.nit = NSEG_ + LAGV; A.roles = ROLES;

    (void)hipFuncSetAttribute((const void*)mega_kernel, hipFuncAttributeMaxDynamicSharedMemorySize, MEGA_LDS);

    TrJobs tj;
    tj.x = A.x0; tj.mem0 = A.mem0; tj.ar = A.ar; tj.tmo = A.tmo; tj.lag = LAGV; tj.pad = 0;
    k_prologue<<<S_ / 4 + 512, 256, 0, stream>>>(tj);
    mega_kernel<<<256, 256, MEGA_LDS, stream>>>(A);
}
```

```cpp
#include <hip/hip_runtime.h>
#include <hip/hip_bf16.h>
#include <stdint.h>

typedef unsigned short bf16_t;
typedef short bf16x8 __attribute__((ext_vector_type(8)));
typedef short bf16x4 __attribute__((ext_vector_type(4)));
typedef float f32x4 __attribute__((ext_vector_type(4)));
typedef unsigned u32x4 __attribute__((ext_vector_type(4)));
typedef unsigned u32x2 __attribute__((ext_vector_type(2)));
typedef unsigned long long u64;
typedef __bf16 bf16v2 __attribute__((ext_vector_type(2)));
typedef float f32v2 __attribute__((ext_vector_type(2)));

__device__ __forceinline__ float bf2f(bf16_t v) { return __uint_as_float(((unsigned)v) << 16); }
__device__ __forceinline__ unsigned pk2(float a, float b) {
    f32v2 f = {a, b};
    bf16v2 r = __builtin_convertvector(f, bf16v2);
    return __builtin_bit_cast(unsigned, r);
}
__device__ __forceinline__ float rb(float a) { return __uint_as_float((pk2(a, 0.f) & 0xffffu) << 16); }
__device__ __forceinline__ float lo16(unsigned u) { return __uint_as_float(u << 16); }
__device__ __forceinline__ float hi16(unsigned u) { return __uint_as_float(u & 0xffff0000u); }
__device__ __forceinline__ bf16x4 pk4(f32x4 v) {
    u32x2 u = {pk2(v[0], v[1]), pk2(v[2], v[3])};
    return __builtin_bit_cast(bf16x4, u);
}
__device__ __forceinline__ bf16x8 cat8(bf16x4 a, bf16x4 b) { return bf16x8{a[0], a[1], a[2], a[3], b[0], b[1], b[2], b[3]}; }
__device__ __forceinline__ bf16x8 asb8(u32x4 v) { return __builtin_bit_cast(bf16x8, v); }
__device__ __forceinline__ f32x4 asf4(u32x4 v) { return __builtin_bit_cast(f32x4, v); }
__device__ __forceinline__ f32x4 mfma32(bf16x8 a, bf16x8 b, f32x4 c) { return __builtin_amdgcn_mfma_f32_16x16x32_bf16(a, b, c, 0, 0, 0); }
__device__ __forceinline__ f32x4 mfma16(bf16x4 a, bf16x4 b, f32x4 c) { return __builtin_amdgcn_mfma_f32_16x16x16bf16_1k(a, b, c, 0, 0, 0); }

#define MF_(acc, ca, a, cb, b) asm volatile("v_mfma_f32_16x16x32_bf16 %0, %1, %2, %0" : "+v"(acc) : ca(a), cb(b))
#define MF0_(acc, ca, a, cb, b) asm volatile("v_mfma_f32_16x16x32_bf16 %0, %1, %2, 0" : "=&v"(acc) : ca(a), cb(b))
#define MF_AV(acc, a, b) MF_(acc, "a", a, "v", b)
#define MF_VA(acc, a, b) MF_(acc, "v", a, "a", b)
#define MF_VV(acc, a, b) MF_(acc, "v", a, "v", b)
#define MF_AV0(acc, a, b) MF0_(acc, "a", a, "v", b)
#define MF_VA0(acc, a, b) MF0_(acc, "v", a, "a", b)
#define MF_VV0(acc, a, b) MF0_(acc, "v", a, "v", b)
#define MF_VVC(d, a, b, c) asm volatile("v_mfma_f32_16x16x32_bf16 %0, %1, %2, %3" : "=&v"(d) : "v"(a), "v"(b), "v"(c))
#define MH_(acc, ca, a, cb, b) asm volatile("v_mfma_f32_16x16x16_bf16 %0, %1, %2, %0" : "+v"(acc) : ca(a), cb(b))
#define MH0_(acc, ca, a, cb, b) asm volatile("v_mfma_f32_16x16x16_bf16 %0, %1, %2, 0" : "=&v"(acc) : ca(a), cb(b))

constexpr int S_ = 2048, D_ = 640, H_ = 32, DH_ = 192, M_ = 8, SEG_ = 32, NSEG_ = 64;
constexpr int HD_ = H_ * DH_;
#define RLX_AGENT __ATOMIC_RELAXED, __HIP_MEMORY_SCOPE_AGENT
#define AUX_SC1 16
typedef __amdgpu_buffer_rsrc_t rsrc_t;
__device__ __forceinline__ rsrc_t mk_rsrc(const void* p, unsigned bytes) { return __builtin_amdgcn_make_buffer_rsrc((void*)p, 0, (int)bytes, 0x00020000); }
__device__ __forceinline__ u32x4 bld16(rsrc_t r, unsigned off) { return __builtin_amdgcn_raw_buffer_load_b128(r, off, 0, AUX_SC1); }
__device__ __forceinline__ u32x2 bld8(rsrc_t r, unsigned off) { return __builtin_amdgcn_raw_buffer_load_b64(r, off, 0, AUX_SC1); }
__device__ __forceinline__ unsigned bld4(rsrc_t r, unsigned off) { return __builtin_amdgcn_raw_buffer_load_b32(r, off, 0, AUX_SC1); }
__device__ __forceinline__ void bst16(rsrc_t r, unsigned off, u32x4 v) { __builtin_amdgcn_raw_buffer_store_b128(v, r, off, 0, AUX_SC1); }
__device__ __forceinline__ void bst8(rsrc_t r, unsigned off, u32x2 v) { __builtin_amdgcn_raw_buffer_store_b64(v, r, off, 0, AUX_SC1); }
__device__ __forceinline__ void bst4(rsrc_t r, unsigned off, unsigned v) { __builtin_amdgcn_raw_buffer_store_b32(v, r, off, 0, AUX_SC1); }
#define LDS_BARRIER() asm volatile("s_waitcnt lgkmcnt(0)\n\ts_barrier" ::: "memory")
#define DRAIN() asm volatile("s_waitcnt vmcnt(0)" ::: "memory")
#define MEMFENCE() asm volatile("" ::: "memory")
#define SCHED0() __builtin_amdgcn_sched_barrier(0)
__device__ __forceinline__ float red_max_q(float x) {
    auto a = __builtin_amdgcn_permlane16_swap(__float_as_uint(x), __float_as_uint(x), false, false);
    x = fmaxf(__uint_as_float(a[0]), __uint_as_float(a[1]));
    auto c = __builtin_amdgcn_permlane32_swap(__float_as_uint(x), __float_as_uint(x), false, false);
    return fmaxf(__uint_as_float(c[0]), __uint_as_float(c[1]));
}
__device__ __forceinline__ float max3r(float a, float b, float c) { float r; asm("v_max3_f32 %0, %1, %2, %3" : "=v"(r) : "v"(a), "v"(b), "v"(c)); return r; }
__device__ __forceinline__ float max2r(float a, float b) { float r; asm("v_max_f32 %0, %1, %2" : "=v"(r) : "v"(a), "v"(b)); return r; }
__device__ __forceinline__ void red_max_q2(float& x, float& y) {
    float s, t;
    asm("v_mov_b32 %2, %0\n\tv_mov_b32 %3, %1\n\ts_nop 0\n\tv_permlane16_swap_b32 %0, %2\n\tv_permlane16_swap_b32 %1, %3\n\tv_max_f32 %0, %0, %2\n\tv_max_f32 %1, %1, %3\n\t"
        "v_mov_b32 %2, %0\n\tv_mov_b32 %3, %1\n\ts_nop 0\n\tv_permlane32_swap_b32 %0, %2\n\tv_permlane32_swap_b32 %1, %3\n\tv_max_f32 %0, %0, %2\n\tv_max_f32 %1, %1, %3"
        : "+v"(x), "+v"(y), "=&v"(s), "=&v"(t));
}
__device__ __forceinline__ void red_sum_q2(float& x, float& y) {
    float s, t;
    asm("v_mov_b32 %2, %0\n\tv_mov_b32 %3, %1\n\ts_nop 0\n\tv_permlane16_swap_b32 %0, %2\n\tv_permlane16_swap_b32 %1, %3\n\tv_add_f32 %0, %0, %2\n\tv_add_f32 %1, %1, %3\n\t"
        "v_mov_b32 %2, %0\n\tv_mov_b32 %3, %1\n\ts_nop 0\n\tv_permlane32_swap_b32 %0, %2\n\tv_permlane32_swap_b32 %1, %3\n\tv_add_f32 %0, %0, %2\n\tv_add_f32 %1, %1, %3"
        : "+v"(x), "+v"(y), "=&v"(s), "=&v"(t));
}
__device__ __forceinline__ float red_sum_q(float x) {
    auto a = __builtin_amdgcn_permlane16_swap(__float_as_uint(x), __float_as_uint(x), false, false);
    x = __uint_as_float(a[0]) + __uint_as_float(a[1]);
    auto c = __builtin_amdgcn_permlane32_swap(__float_as_uint(x), __float_as_uint(x), false, false);
    return __uint_as_float(c[0]) + __uint_as_float(c[1]);
}
__device__ __forceinline__ u32x4 pk8(f32x4 a, f32x4 b) { return u32x4{pk2(a[0], a[1]), pk2(a[2], a[3]), pk2(b[0], b[1]), pk2(b[2], b[3])}; }
__device__ __forceinline__ u32x4 tag4(f32x4 v, unsigned tg) {
    return u32x4{(__float_as_uint(v[0]) & ~3u) | tg, (__float_as_uint(v[1]) & ~3u) | tg, (__float_as_uint(v[2]) & ~3u) | tg, (__float_as_uint(v[3]) & ~3u) | tg};
}
__device__ __forceinline__ unsigned tagbad(u32x4 v, unsigned tg) { return ((v[0] ^ tg) | (v[1] ^ tg) | (v[2] ^ tg) | (v[3] ^ tg)) & 3u; }

__device__ __host__ __forceinline__ constexpr unsigned xm(unsigned o, unsigned side) { return ((o >> 13) << 14) | (o & 8191u) | ((((o >> 20) ^ side) & 1u) << 13); }
__device__ __host__ __forceinline__ constexpr unsigned lp(unsigned o) { return ((o >> 13) << 14) | (o & 8191u); }
__device__ __host__ __forceinline__ constexpr int hside(int h) { return h >> 4; }
constexpr unsigned XM_OFF = 24576, XM_BUF = 4 * 32768;
constexpr unsigned XP_OFF = XM_OFF + 3 * XM_BUF, XP_BUF = 32768;
constexpr unsigned PT_OFF = XP_OFF + 3 * XP_BUF, PT_BUF = 65536;
constexpr unsigned FI_OFF = PT_OFF + 3 * PT_BUF, FI_N = 80;
constexpr unsigned FX_OFF = FI_OFF + FI_N * 1024;
constexpr unsigned A_END = FX_OFF + FI_N * 256;
constexpr unsigned UM_OFF = 1u << 20, UM_BUF = 40 * 8192;
#define TR 8
constexpr unsigned UT_OFF = 2u << 20;
constexpr unsigned QK_OFF = UT_OFF + TR * (2u << 20);
constexpr unsigned SG_OFF = QK_OFF + TR * (1u << 20);
enum { SG_XL = 0, SG_XR = 40960, SG_X1 = 81920, SG_OB = 122880, SG_ST = 163840, SG_FL = 172032 };
enum { FL_U = 0, FL_X = 2048, FL_O = 4096, ST_2 = 6144 };
constexpr unsigned ARENA = 2 * (SG_OFF + 64 * 262144);
static_assert(A_END <= UM_OFF && UM_OFF + 3 * UM_BUF <= UT_OFF && QK_OFF % (1u << 21) == 0 && SG_OFF % (1u << 21) == 0, "map");
static_assert(ARENA + (1u << 23) < (1u << 28), "workspace");
__device__ __forceinline__ unsigned seg_base(int layer, int seg) { return 2 * SG_OFF + (unsigned)seg * 524288 + ((((seg >> 2) ^ layer) & 1) << 13); }
__device__ __forceinline__ unsigned ut_slot(int seg) { return (unsigned)(seg & (TR - 1)) << 22; }
__device__ __forceinline__ unsigned qk_base(int seg, int side) { return 2 * QK_OFF + ((unsigned)(seg & (TR - 1)) << 21) + (((seg ^ side) & 1) << 13); }
__device__ __forceinline__ unsigned fi_step(int it) { return ((it >> 3) << 14) | ((it & 7) << 10); }
__device__ __forceinline__ unsigned fx_step(int it) { return ((it >> 5) << 14) | ((it & 31) << 8); }
static_assert(FI_OFF % 8192 == 0 && FX_OFF % 8192 == 0, "steps");
__device__ __forceinline__ u32x4 sld16(rsrc_t r, unsigned so, unsigned lo) { return __builtin_amdgcn_raw_buffer_load_b128(r, lo, so, AUX_SC1); }
__device__ __forceinline__ u32x2 sld8(rsrc_t r, unsigned so, unsigned lo) { return __builtin_amdgcn_raw_buffer_load_b64(r, lo, so, AUX_SC1); }
__device__ __forceinline__ unsigned sld4(rsrc_t r, unsigned so, unsigned lo) { return __builtin_amdgcn_raw_buffer_load_b32(r, lo, so, AUX_SC1); }
__device__ __forceinline__ void sst4(rsrc_t r, unsigned so, unsigned lo, unsigned v) { __builtin_amdgcn_raw_buffer_store_b32(v, r, lo, so, AUX_SC1); }
__device__ __forceinline__ void sst16(rsrc_t r, unsigned so, unsigned lo, u32x4 v) { __builtin_amdgcn_raw_buffer_store_b128(v, r, lo, so, AUX_SC1); }
__device__ __forceinline__ void sst8(rsrc_t r, unsigned so, unsigned lo, u32x2 v) { __builtin_amdgcn_raw_buffer_store_b64(v, r, lo, so, AUX_SC1); }
__device__ __forceinline__ unsigned umax3(unsigned a, unsigned b, unsigned c) { return max(max(a, b), c); }
__device__ __forceinline__ unsigned mx4(unsigned m, u32x4 v) { return umax3(m, v[0], v[2]); }
__device__ __forceinline__ unsigned nosent(unsigned x) { return min(x, 0xfffffffeu); }
constexpr unsigned OOB = 0x7f000000u;
constexpr unsigned kSpinCap = 1u << 18;

__device__ __forceinline__ char* arena_base(char* p) { return (char*)(((size_t)p + 0x3fffff) & ~(size_t)0x3fffff); }

typedef __attribute__((address_space(3))) bf16x4 lds_b4;
typedef __attribute__((address_space(3))) volatile int lds_int;
template <int V> struct IC { static constexpr int value = V; };
template <int B, int E, class F> __device__ __forceinline__ void sfor(F&& f) { if constexpr (B < E) { f(IC<B>{}); sfor<B + 1, E>(f); } }
#define DMA_NEXT(x) asm volatile("global_load_lds_dwordx4 %0, %1" x "\n\ts_add_u32 m0, m0, %2\n\tv_add_u32 %0, %3, %0" : "+v"(vo) : "s"(base), "s"(step), "s"(adv) : "memory", "m0", "scc")
template <bool NT> __device__ __forceinline__ void dma_next(const void* base, unsigned& vo, unsigned step, unsigned adv) { if (NT) DMA_NEXT(" nt"); else DMA_NEXT(""); }
__device__ __forceinline__ void set_m0(unsigned x) { asm volatile("s_mov_b32 m0, %0\n\ts_nop 0" :: "s"(x) : "memory", "m0"); }
__device__ __forceinline__ bf16x8 tr_read(const char* p, int o0, int o1) { return cat8(__builtin_amdgcn_ds_read_tr16_b64_v4i16((lds_b4*)(p + o0)), __builtin_amdgcn_ds_read_tr16_b64_v4i16((lds_b4*)(p + o1))); }
struct Go { rsrc_t rh; unsigned so; u32x2 v; };
template <int N, int N1, int NI, int NJ, int NS, int SL, bool V, class ISSUE, class TAKE>
__device__ __forceinline__ void load_ring(char* smem, const int w, Go& go, ISSUE issue_, TAKE take) {
    constexpr int AH = NS - 1;
    const unsigned sta = (unsigned)(size_t)(__attribute__((address_space(3))) char*)smem + w * 1024;
    auto issue = [&](int st) {
        if (st % NS == 0) set_m0(sta);
        const int n = st < N1 ? NI : NJ;
#pragma unroll
        for (int m = 0; m < n; ++m) issue_(st, m, m + 1 < n ? 4096u : SL - 4096u * (n - 1));
    };
#pragma unroll
    for (int st = 0; st < AH; ++st) issue(st);
    auto run = [&](auto WW) {
        sfor<0, N + 1>([&](auto ST) {
            constexpr int st = decltype(ST)::value, e = st + AH < N ? st + AH : N, a = e < N1 ? e : N1, n1 = a > st + 1 ? a - st - 1 : 0, n = st < N ? n1 * NI + (e - st - 1 - n1) * NJ + (st + AH >= N) : 0;
            asm volatile("s_waitcnt vmcnt(%0) lgkmcnt(0)\n\ts_barrier" :: "n"(n) : "memory");
            if constexpr (st > 0) take(IC<st - 1>{}, WW, nullptr, 0);
            if constexpr (st + AH < N) issue(st + AH);
            if constexpr (st + AH == N - 1) asm volatile("buffer_load_dwordx2 %0, %1, %2, %3 offen sc1" : "=&v"(go.v) : "v"((threadIdx.x & 63) * 8), "s"(go.rh), "s"(go.so) : "memory");
            if constexpr (st == N) asm volatile("" : "+v"(go.v));
            constexpr int hi = st % NS / (65536 / SL) * (65536 / SL);
            if constexpr (st < N) take(ST, WW, smem + hi * SL, (st % NS - hi) * SL);
        });
    };
    if (!V || w == 0) run(IC<0>{}); else if (w == 1) run(IC<1>{}); else if (w == 2) run(IC<2>{}); else run(IC<3>{});
}
template <int R, int NTT> __device__ __forceinline__ void take_k(int ks, int ww, const char* p, int o, const int (&rp)[NTT], int hf, bf16x8 (&wf)[NTT][20]) {
#pragma unroll
    for (int tt = 0; tt < NTT; ++tt) {
        const int i = (ks + 20 - R * ww) % 20;
        if (p) wf[tt][i] = tr_read(p, rp[tt] + o, rp[tt] + hf + o);
        else if (20 * tt + i < 64) asm volatile("" : "+a"(wf[tt][i]));
        else asm volatile("" : "+v"(wf[tt][i]));
    }
}
#define SWP (R != 0)
#define ORD(st) (SWP ? (st & 1 ? 3u : -1u) : 1u)
#define BNT 4
template <int NI, int R, int NTT>
__device__ __forceinline__ void load_coop(char* smem, const int w, Go& go, const void* const (&base)[NI], unsigned (&vo)[NI], const unsigned adv, const int (&rp)[NTT], const int hf, bf16x8 (&wf)[NTT][20]) {
    load_ring<20, 20, NI, NI, (NI > 4 ? 6 : 8), (NI > 4 ? 24576 : 16384), R != 0>(smem, w, go, [&](int st, int m, unsigned step) { dma_next<(NI > BNT)>(base[m], vo[m], step, ORD(st) * adv); },
        [&](auto ST, auto WW, const char* p, int o) { take_k<R, NTT>(decltype(ST)::value ^ SWP, decltype(WW)::value, p, o, rp, hf, wf); });
}
__device__ __forceinline__ unsigned hd_src(int idx) { const int r = idx / 24, u = idx % 24; return r * (2 * HD_) + ((u * 16) ^ ((r & 6) << 4)); }
__device__ __forceinline__ int hd_pos(int l, int tile) { const int r = l >> 2; return r * 384 + ((32 * tile) ^ ((r & 6) << 4)) + (l & 3) * 8; }
__device__ __forceinline__ unsigned row_src(int idx) { return (idx * 16) ^ ((idx / 80 & 7) << 5); }
__device__ __forceinline__ int row_pos(int r, int piece) { return r * 1280 + ((piece ^ (r & 7)) << 5); }
template <int R, class TILE> __device__ __forceinline__ void load_qk(char* smem, int w, int l, Go& go, const bf16_t* Wq, const bf16_t* Wk, TILE tile, bf16x8 (&wf)[6][20]) {
    const void* const base[6] = {Wq, Wq, Wq, Wk, Wk, Wk};
    unsigned vo[6];
    int rp[6];
#pragma unroll
    for (int m = 0; m < 6; ++m) { vo[m] = hd_src((4 * (m % 3) + w) * 64 + l) + SWP * 64 * HD_; rp[m] = tile(m) / 12 * 12288 + hd_pos(l, tile(m) % 12); }
    load_coop<6, R, 6>(smem, w, go, base, vo, 64 * HD_, rp, 6144, wf);
}
template <int R, class DST> __device__ __forceinline__ void load_vo(char* smem, int w, int l, Go& go, const bf16_t* Wv, const bf16_t* Wo, bf16x8 (&wf)[3][20], DST dst) {
    unsigned vo[3], v2[5];
    int r2[10];
    bf16x4 lo[10];
#pragma unroll
    for (int m = 0; m < 3; ++m) vo[m] = hd_src((4 * m + w) * 64 + l) + SWP * 64 * HD_;
#pragma unroll
    for (int m = 0; m < 5; ++m) v2[m] = row_src((4 * m + w) * 64 + l) + SWP * 20480;
#pragma unroll
    for (int m = 0; m < 10; ++m) r2[m] = row_pos(l >> 2, 10 * w + m) + (l & 3) * 8;
    const int rp[3] = {hd_pos(l, 2 * w), hd_pos(l, 2 * w + 1), hd_pos(l, 8 + w)};
    load_ring<32, 20, 3, 5, 8, 20480, R != 0>(smem, w, go, [&](int st, int m, unsigned step) { if (st < 20) dma_next<true>(Wv, vo[m % 3], step, ORD(st) * (64 * HD_)); else dma_next<true>(Wo, v2[m], step, ORD(st) * 20480u); },
        [&](auto ST, auto WW, const char* p, int o) {
            constexpr int st = decltype(ST)::value;
            if constexpr (st < 20) take_k<R, 3>(st ^ SWP, decltype(WW)::value, p, o, rp, 6144, wf);
            else {
#pragma unroll
                for (int t = 0; t < 10; ++t) {
                    if (!p) {
                        if (st & 1) { bf16x8& d = dst(st / 2 - 10, t, decltype(WW)::value); if (t == 0 && st < 28) asm volatile("" : "+a"(d)); else asm volatile("" : "+v"(d)); }
                        continue;
                    }
                    const bf16x4 x = __builtin_amdgcn_ds_read_tr16_b64_v4i16((lds_b4*)(p + r2[t] + o));
                    if (st & 1) dst(st / 2 - 10, t, decltype(WW)::value) = SWP ? cat8(x, lo[t]) : cat8(lo[t], x); else lo[t] = x;
                }
            }
        });
}

struct MegaArgs {
    const bf16_t* x0; const bf16_t* mem0; const bf16_t* Wq; const bf16_t* Wk; const bf16_t* Wv; const bf16_t* Wo; const bf16_t* wqkv; const bf16_t* wo;
    char* ar; unsigned* tmo; char* hs; bf16_t* out;
    int nit, lag, roles, pad;
};

#define WSLP 2
#define LIVE u64 live_; asm volatile("s_add_u32 %0, %0, 1\n\ts_cmp_gt_u32 %0, %2\n\ts_cselect_b64 %1, 0, -1" : "+s"(spins), "=s"(live_) : "n"(kSpinCap) : "scc")
#define BAD(m) (__ballot(m == ~0u) & live_)
#define SPIN_END if (spins > kSpinCap) __hip_atomic_store(A.tmo, 1u + id, RLX_AGENT)
__device__ __forceinline__ void wave_wait(rsrc_t rx, unsigned off, unsigned so, unsigned& spins) {
#pragma unroll 1
    for (;;) {
        const unsigned v = sld4(rx, so, off);
        LIVE;
        if (!(__ballot(v == 0 && off != OOB) & live_)) break;
        __builtin_amdgcn_s_sleep(WSLP);
        MEMFENCE();
    }
}
__device__ __forceinline__ unsigned rtime() { return (unsigned)__builtin_amdgcn_s_memrealtime(); }
__device__ __forceinline__ void rt_until(unsigned target) {
    asm volatile("1:\n\ts_memrealtime vcc\n\ts_waitcnt lgkmcnt(0)\n\ts_sub_u32 vcc_lo, %0, vcc_lo\n\ts_cmpk_lt_u32 vcc_lo, 600\n\ts_cbranch_scc1 1b" :: "s"(target - 1) : "memory", "scc", "vcc");
}
#define RT_K 26
__device__ __forceinline__ void rt_fine(unsigned target) {
    u64 a, b, c;
    unsigned n;
    asm volatile("1:\n\ts_memrealtime vcc\n\ts_waitcnt lgkmcnt(0)\n\ts_sub_u32 vcc_lo, %4, vcc_lo\n\ts_cmpk_lt_u32 vcc_lo, 600\n\ts_cbranch_scc1 1b\n\t"
                 "s_add_u32 vcc_lo, vcc_lo, 8\n\ts_cbranch_scc0 3f\n\t"
                 "s_memrealtime %0\n\ts_nop 4\n\ts_memrealtime %1\n\ts_nop 4\n\ts_memrealtime %2\n\ts_lshl_b32 %3, %4, 2\n\ts_addk_i32 %3, %5\n\ts_nop 2\n\ts_memrealtime vcc\n\ts_waitcnt lgkmcnt(0)\n\t"
                 "s_sub_u32 %3, %3, vcc_lo\n\ts_mov_b64 vcc, %0\n\ts_sub_u32 %3, %3, vcc_lo\n\ts_mov_b64 vcc, %1\n\ts_sub_u32 %3, %3, vcc_lo\n\ts_mov_b64 vcc, %2\n\ts_sub_u32 %3, %3, vcc_lo\n\t"
                 "s_max_i32 %3, %3, 0\n\ts_min_i32 %3, %3, 64\n\t"
                 "s_branch 2f\n\t"
                 "3:\n\ts_add_u32 %3, %4, 13\n\t"
                 "4:\n\ts_memrealtime vcc\n\ts_waitcnt lgkmcnt(0)\n\ts_sub_u32 vcc_lo, %3, vcc_lo\n\ts_cmpk_lt_u32 vcc_lo, 16\n\ts_cbranch_scc1 4b\n\t"
                 "s_mov_b32 %3, 0\n\t"
                 "2:\n\ts_sub_u32 %3, %3, 5\n\ts_cbranch_scc0 2b"
                 : "=&s"(a), "=&s"(b), "=&s"(c), "=&s"(n) : "s"(target - 14), "n"(56 - RT_K) : "memory", "scc", "vcc");
}
#define LDA(dst, vo, so, imm) asm volatile("buffer_load_dwordx4 %0, %1, %2, %3 offen offset:%4 sc1" : "=v"(dst) : "v"(vo), "s"(rx), "s"(so), "n"(imm) : "memory")
#define LDI(dst, vo, so, imm) asm volatile("buffer_load_dwordx4 %0, %1, %2, %3 offen offset:%4 sc1" : "+v"(dst) : "v"(vo), "s"(rx), "s"(so), "n"(imm) : "memory")
#define LDA8(dst, vo, so, imm) asm volatile("buffer_load_dwordx2 %0, %1, %2, %3 offen offset:%4 sc1" : "=v"(dst) : "v"(vo), "s"(rx), "s"(so), "n"(imm) : "memory")
#define LDA4(dst, vo, so) asm volatile("buffer_load_dword %0, %1, %2, %3 offen sc1" : "=v"(dst) : "v"(vo), "s"(rx), "s"(so) : "memory")
#define WAITV(n, x) asm volatile("s_waitcnt vmcnt(%1)" : "+v"(x) : "n"(n) : "memory")
#define PIN_S(x) asm volatile("" : "+s"(x))
#define SZERO(x) asm volatile("s_mov_b32 %0, 0" : "=s"(x))
#define PIN_V(x) asm volatile("" : "+v"(x))
#define USE_V(x) asm volatile("" :: "v"(x))
#define LDS_AT(T, a) (*(__attribute__((address_space(3))) T*)(a))
__device__ __forceinline__ float dpp_ror8(float x) { return __uint_as_float(__builtin_amdgcn_mov_dpp(__float_as_uint(x), 0x128, 0xf, 0xf, true)); }
__device__ __forceinline__ unsigned lds_addr(const void* p) { return (unsigned)(size_t)(__attribute__((address_space(3))) const char*)p; }
#define DMA1(vo, so, dst, imm) asm volatile("s_mov_b32 m0, %3\n\ts_nop 0\n\tbuffer_load_dwordx4 %0, %1, %2 offen offset:%4 sc1 lds" :: "v"(vo), "s"(rx), "s"(so), "s"(dst), "n"(imm) : "memory", "m0")

enum { HS_E = 504, HS_F = 512, N_NOW = 44, N_INIT = 92 };
__device__ __forceinline__ unsigned hs_tags(const Go& go, int l) { return (bld4(go.rh, l < 8 ? l * 1024 + HS_E : OOB) + 2) & ~1u; }
__device__ __forceinline__ rsrc_t arena(const MegaArgs& A, unsigned flip) { return mk_rsrc(arena_base(A.ar) + (flip << 21), ARENA); }
__device__ __forceinline__ unsigned hs_wait(Go& go, int l, int n, unsigned off, bool known, unsigned tag, unsigned& spins) {
#pragma unroll 1
    for (;;) {
        if (!known) tag = (__builtin_amdgcn_readlane(go.v[0], 63) + 2) & ~1u;
        LIVE;
        if (!(__ballot(l < n && ((go.v[0] ^ tag) | (go.v[1] ^ tag)) > 1) & live_)) break;
        __builtin_amdgcn_s_sleep(8);
        go.v = sld8(go.rh, go.so, off);
    }
    return __builtin_amdgcn_readfirstlane(go.v[0]) & 1;
}
#define GO_DECL(pool) Go go; go.rh = mk_rsrc(A.hs, 8192); go.so = (pool) * 1024
#define GO_ON const rsrc_t rx = arena(A, hs_wait(go, l, N_NOW / 2, l * 8, false, 0, spins))
#define INIT_BEGIN(pool) const unsigned tags = hs_tags(go, l); go.v = sld8(go.rh, go.so, HS_F); const unsigned tag = __builtin_amdgcn_readlane(tags, pool), flip = hs_wait(go, l, 1, HS_F, true, tag, spins); \
    const rsrc_t rx = arena(A, flip)
#define INIT_END(ii) asm volatile("s_waitcnt vmcnt(0)\n\ts_barrier" ::: "memory"); bst4(go.rh, t < 8 ? t * 1024 + (ii) * 4 : OOB, tags | flip)
#define INIT_SEEN hs_wait(go, l, N_INIT / 2, l * 8, true, tag, spins)
__device__ __forceinline__ void probe(const MegaArgs& A, const Go& go, int l, int id) {
    const rsrc_t rf = arena(A, 0);
    const unsigned tags = hs_tags(go, l);
    unsigned tm[2] = {~0u, ~0u};
    bst4(rf, l < 2 ? l * 8192 : OOB, 0u);
    DRAIN();
#pragma unroll
    for (int k = 0; k < 4; ++k) {
        const unsigned long long t0 = __builtin_amdgcn_s_memtime();
        bst4(rf, l ? OOB : (k & 1) * 8192, 0u);
        DRAIN();
        const unsigned d = (unsigned)(__builtin_amdgcn_s_memtime() - t0);
        tm[k & 1] = d < tm[k & 1] ? d : tm[k & 1];
    }
    bst8(go.rh, l < 8 ? l * 1024 + HS_F : OOB, u32x2{tags | (tm[1] < tm[0]), tags});
}
#define START_IDS const int t = threadIdx.x, l = t & 63, w = __builtin_amdgcn_readfirstlane(t >> 6); const unsigned l16b = l * 16; GO_DECL(id >> 5); unsigned spins; SZERO(spins)
__device__ __forceinline__ void init_area(const MegaArgs& A, const int ii, const int id) {
    START_IDS;
    INIT_BEGIN(id >> 5);
    const int i = 4 * ii + w;
    for (int u = i; u < 160; u += 96) {
        const bf16_t* mp = A.mem0 + (l & 7) * D_ + 32 * (u % 20) + 4 * (l >> 4);
        const u32x2 x = *(const u32x2*)mp, y = *(const u32x2*)(mp + 16);
        const unsigned f = (unsigned)(u / 2 - (l & 1) * A.lag) >= NSEG_;
        sst16(rx, xm(FI_OFF + u / 2 * 1024, u & 1), l16b, u32x4{f, f, f, f});
        sst16(rx, xm(XM_OFF + (u / 5 & 15) * 8192 + u % 5 * 1024, u / 80), l16b, u32x4{nosent(x[0]), x[1], nosent(y[0]), y[1]});
    }
    auto fill = [&](unsigned o, unsigned n, unsigned f) {
#pragma unroll 2
        for (unsigned u = i * 1024; u < 2 * n; u += 98304) sst16(rx, 2 * o + u, l16b, u32x4{f, f, f, f});
    };
    fill(XM_OFF + XM_BUF, FI_OFF - PT_BUF - XM_OFF - XM_BUF, ~0u);
    fill(FI_OFF - PT_BUF, PT_BUF, 0u);
    fill(FX_OFF, 24576, 0u);
    fill(UM_OFF, 3 * UM_BUF, ~0u);
    INIT_END(ii);
}
static_assert(FI_N == 80 && FX_OFF + 24576 <= UM_OFF && SG_FL == SG_ST + 8192 && SG_ST % 8192 == 0, "init");
#define DLY 1000
#define DLY2 900
__device__ __forceinline__ void help(const MegaArgs& A, char* smem, const int vo, const int h, const unsigned t0) {
    const unsigned i = min(threadIdx.x, 191u), o = (64 + i / 3) * (2 * HD_) + i % 3 * 128;
    const bf16_t* b0 = (vo ? A.Wv : A.Wq) + h * DH_;
    const bf16_t* b1 = vo ? A.Wo + h * (DH_ * D_) : A.Wk + h * DH_;
    set_m0(lds_addr(smem));
    auto touch = [&](const void* b, unsigned x) { asm volatile("global_load_lds_dword %0, %1 nt" :: "v"(x), "s"(b) : "memory"); };
#pragma unroll 1
    for (int e = 0; e < 5; ++e) { touch(b0, o + e * (256 * HD_)); if (!vo) touch(b1, o + e * (256 * HD_)); }
#pragma unroll 1
    for (int e = 0; e < 5; ++e) if (vo) { const unsigned j = e * 192 + i; touch(b1, 40960 + j / 320 * 81920 + j % 320 * 128); }
    while (rtime() - t0 < DLY) __builtin_amdgcn_s_sleep(8);
    asm volatile("s_waitcnt vmcnt(0)\n\ts_barrier" ::: "memory");
}
__device__ __forceinline__ void first_rows(const MegaArgs& A, char* smem, const int sg, const int id, const unsigned t0) {
    START_IDS;
    set_m0(lds_addr(smem) + w * 1024);
    {
        unsigned vo[5];
#pragma unroll
        for (int m = 0; m < 5; ++m) vo[m] = row_src((4 * m + w) * 64 + l);
#pragma unroll
        for (int m = 0; m < 10; ++m) dma_next<true>(A.x0 + sg * (SEG_ * D_), vo[m % 5], 4096u, 20480u);
    }
    asm volatile("s_waitcnt vmcnt(0)\n\ts_barrier" ::: "memory");
    u32x4 xr[2][5], xl[2][5];
#pragma unroll 1
    for (int rt = 0; rt < 2; ++rt) {
        f32x4 v[10], a;
#pragma unroll
        for (int e = 0; e < 5; ++e) { xr[0][e] = xr[1][e]; xl[0][e] = xl[1][e]; }
#pragma unroll
        for (int e = 0; e < 10; ++e) {
            const u32x2 x = *(const u32x2*)(smem + rt * 20480 + row_pos(l & 15, 10 * w + e) + (l >> 4) * 8);
            xr[1][e / 2][2 * (e & 1)] = x[0]; xr[1][e / 2][2 * (e & 1) + 1] = x[1];
            v[e] = f32x4{lo16(x[0]), hi16(x[0]), lo16(x[1]), hi16(x[1])};
            a = e ? a + v[e] : v[0];
        }
        const float mw = red_sum_q((a[0] + a[1]) + (a[2] + a[3])) * (1.0f / 160);
#pragma unroll
        for (int e = 0; e < 10; ++e) { const f32x4 d = v[e] - mw; a = e ? __builtin_elementwise_fma(d, d, a) : d * d; }
        f32v2* sp = (f32v2*)(smem + 49152) + rt * 64 + (l & 15);
        sp[16 * w] = f32v2{mw, red_sum_q((a[0] + a[1]) + (a[2] + a[3]))};
        LDS_BARRIER();
        const f32v2 p = sp[l & 48];
        const float mean = red_sum_q(p[0]) * 0.25f, var = red_sum_q(p[1] + 160.f * (p[0] - mean) * (p[0] - mean)) * (1.0f / D_);
        const float mb = rb(mean), rd = 1.0f / rb(sqrtf(rb(rb(var) + 1e-5f)));
#pragma unroll
        for (int e = 0; e < 10; ++e) {
            const unsigned x = pk2(v[e][0] - mb, v[e][1] - mb), y = pk2(v[e][2] - mb, v[e][3] - mb);
            v[e] = f32x4{lo16(x), hi16(x), lo16(y), hi16(y)} * rd;
            if (e & 1) xl[1][e / 2] = pk8(v[e - 1], v[e]);
        }
    }
    INIT_BEGIN(id >> 5);
    while (sg >= 16 && rtime() - t0 < DLY2) __builtin_amdgcn_s_sleep(8);
#pragma unroll
    for (int e = 0; e < 20; ++e) sst16(rx, seg_base(0, sg) + lp((e & 1 ? SG_XR : SG_XL) + (5 * w + e / 4) * 2048), l16b + (e & 2) * 512, (e & 1 ? xr : xl)[e >> 1 & 1][e / 4]);
    const unsigned f = (w >> 1) - 1;
#pragma unroll
    for (int e = 0; e < 8; ++e) sst16(rx, 2 * (SG_OFF + SG_ST) + sg * 524288 + w * 8192 + e / 4 * 4096, l16b + (e & 3) * 1024, u32x4{f, f, f, f});
    INIT_END(28 + sg);
}
#define LAGV 7
static_assert(LAGV < TR, "rings");
#define VSLP 5
#define PER 296
#define E_U 120
#define E_P 140
#define E_W 95
#define K_W 5
#define E_X 40
#define E_B 100
#define E_S 16
#define E_G 54
#define CHAIN_WAIT(pv, off, so, code) if (__builtin_expect(__ballot(pv == 0) != 0, 0)) { if (so != OOB) { wave_wait(rx, off, so, spins); } }
#define GAPN 11
#define GAP() asm volatile("s_nop %0" :: "n"(GAPN))
#define LD5(v, so) do { LDA(v[0], l16b, so, 0); GAP(); LDA(v[1], l16b, so, 1024); GAP(); LDA(v[2], l16b, so, 2048); GAP(); LDA(v[3], l16b, so, 3072); GAP(); LDA(v[4], l16k, so, 0); } while (0)
#define CHAIN_IDS const int t = threadIdx.x, l = t & 63, l16 = l & 15, lq = l >> 4; const int w = __builtin_amdgcn_readfirstlane(t >> 6); \
    GO_DECL(id >> 5); const int lag = A.lag, itend = A.nit; const unsigned l16b = l * 16, l16k = l * 16 + 4096; unsigned spins = 0
#define CHAIN_STAGE const char* sg1 = smem + ((w + 1) & 3) * 5120 + l * 16; const char* sg2 = smem + ((w + 2) & 3) * 5120 + l * 16; const char* sg3 = smem + ((w + 3) & 3) * 5120 + l * 16; \
    char* sg0 = smem + w * 5120 + l * 16; auto mrd = [&](int i) { return *(const bf16x8*)((i < 10 ? sg1 : (i < 15 ? sg2 : sg3)) + (i % 5) * 1024); }; \
    const unsigned mo0 = xm(XM_OFF + (pool & 3) * 32768 + w * 8192, pool >> 2), fX = xm(FX_OFF + (pool & 3) * 64, pool >> 2); unsigned fXv = fX; PIN_V(fXv)
#define GATHER(take) do { _Pragma("unroll 1") for (;;) { LD5(v, mo); LIVE; if (!take(live_)) break; } } while (0)
#define CVT2(o0, o1, x) do { o0 = pk2(x[0], x[1]); o1 = pk2(x[2], x[3]); asm volatile("" : "+v"(o0), "+v"(o1)); } while (0)
#define RR_(op, d, x, y, n) op "_dpp " d ", " x ", " y " row_ror:" n " row_mask:0xf bank_mask:0xf\n\t"
#define ROWRED(op, x, y, f0, f1) f0 RR_(op, y, x, x, "1") RR_(op, y, x, y, "2") RR_(op, y, x, y, "3") f1 RR_(op, x, y, y, "4") RR_(op, x, y, x, "8") RR_(op, x, y, x, "12")
#define BIAS(x, a, b, c, d) asm volatile("v_mov_b32 %0, 0xff800000\n\tv_cndmask_b32 %1, %0, 0, %5\n\tv_cndmask_b32 %2, %0, 0, %6\n\tv_cndmask_b32 %3, %0, 0, %7\n\tv_cndmask_b32 %0, %0, 0, %4" \
    : "=&v"(x[0]), "=&v"(x[1]), "=&v"(x[2]), "=&v"(x[3]) : "s"(a), "s"(b), "s"(c), "s"(d))
#define AND2(o0, o1, a, b, m) do { o0 = a & m; o1 = b & m; asm volatile("" : "+v"(o0), "+v"(o1)); } while (0)

constexpr int SL_S = 20480, SS_W = 3136, SL_KM = SL_S + 4 * SS_W, SL_KT = SL_KM + 6144, SL_W = SL_KT + 49152, SL_END = SL_W + 4 * 18432;
static_assert(SL_END <= 163840, "LDS");
#define G_K 32
#define G_KS 6
__device__ __forceinline__ void s_role(const MegaArgs& A, char* smem, const int h, const int id) {
    CHAIN_IDS;
    const int pool = h >> 2, hs = hside(h);
    bf16x8 wf[6][20];
    const char* sW = smem + SL_W + w * 18432 + l * 16 - 2048;
    load_qk<5>(smem, w, l, go, A.Wq + h * DH_, A.Wk + h * DH_, [&](int m) { return m / 3 * 12 + (m % 3 < 2 ? 2 * w + m % 3 : 8 + w); }, wf);
#pragma unroll
    for (int i = 2; i < 20; ++i) *(bf16x8*)(sW + i * 1024) = wf[5][i];
    GO_ON;
    *(f32x4*)(smem + SL_S + w * SS_W + 3120) = f32x4{-INFINITY, -INFINITY, -INFINITY, -INFINITY};
#pragma unroll
    for (int i = 0; i < 12; ++i) *(u32x4*)(smem + SL_KT + w * 12288 + i * 1024 + l * 16) = u32x4{0u, 0u, 0u, 0u};
    asm volatile("s_waitcnt lgkmcnt(0)" ::: "memory");
    CHAIN_STAGE;
    char* sSw = smem + SL_S + w * SS_W + lq * 256 + ((l16 + 4 * lq) & 15) * 16;
    const int kq = l16, sL = w >> 1, rt = w & 1, row8 = (4 * w + lq) & 7;
    const bool kval = kq < 8 || (kq >> 1) == 4 + sL;
    const char* sSr = smem + SL_S + (kval ? (kq >> 2) * 1040 + (kq & 3) * 256 + ((4 * w + lq + 4 * kq) & 15) * 16 : 3120);
    const unsigned ppos = !kval ? OOB : xm(XP_OFF + (h >> 3) * 8192 +
        (kq < 8 ? ((h & 7) >> 1) * 1024 + ((kq & 3) * 16 + (h & 1) * 8 + row8) * 16 + (kq >> 2) * 8 : 4096 + ((h & 3) * 16 + ((h >> 2) & 1) * 8 + row8) * 16 + (kq & 1) * 8), sL);
    const unsigned pt1 = xm(PT_OFF + (h >> 3) * 16384 + (rt ? (h & 7) * 1024 : 8192 + ((h & 7) >> 1) * 1024 + (h & 1) * 8) + l * 16, sL);
    const unsigned pt2 = lq == 2 * sL ? xm(PT_OFF + (h >> 3) * 16384 + 12288 + (((h >> 2) & 1) * 2 + rt) * 1024 + ((h & 3) * 16 + l16) * 16, sL) : OOB;
    const u64 cm0 = __ballot(4 * lq <= l16), cm1 = __ballot(4 * lq + 1 <= l16), cm2 = __ballot(4 * lq + 2 <= l16), cm3 = __ballot(4 * lq + 3 <= l16), mval = __ballot((lq >> 1) == sL), oval = __ballot(rt);
    const unsigned ma = l16 < 8 ? ~0u : 0u, mb2 = ~ma;
    const unsigned fK = xm(FI_OFF + (h & 15) * 64 + (4 * sL + (l & 3)) * 4, hs);
    const unsigned qo = ((sL * 16 + (h & 15)) * 4 + rt) * 16384, ktl = lds_addr(smem + SL_KT) + w * 12288;
    const char* sKT = smem + SL_KT + l * 16;
    char* sKM = smem + SL_KM + l * 16;
    char* sKX = smem + SL_KT + l * 16 + 4096 + (w >> 1) * 1024 + (w & 1) * 8;
    unsigned X = rtime() - PER;
    auto qk_so = [&](int it) { const int sg = it - sL * lag; return (unsigned)sg < NSEG_ ? qk_base(sg, hs) + qo : OOB; };
    auto dm0 = [&](int e) {
        if (e == 0) asm volatile("s_mov_b32 m0, %0" :: "s"(ktl) : "m0");
        if (e == 4 || e == 6 || e == 10) asm volatile("s_add_u32 m0, m0, %0" :: "n"(e == 6 ? 2048 : 4096) : "m0", "scc");
    };
    auto dma = [&](int e, unsigned sq, unsigned sk, bool set = true) {
        if (set && (e == 0 || e == 4 || e == 6 || e == 10)) { dm0(e); asm volatile("s_nop 0"); }
        asm volatile("buffer_load_dwordx4 %0, %1, %2 offen offset:%3 sc1 lds" :: "v"(e % 6 < 4 ? l16b : l16k), "s"(rx), "s"(e < 6 ? sk : sq), "n"((e % 6 & 3) * 1024) : "memory");
    };
    {
        const unsigned pv0 = bld4(rx, fK), z = 0, so = qk_so(0);
        CHAIN_WAIT(pv0, fK, z, 1);
#pragma unroll
        for (int e = 0; e < 12; ++e) dma(e, so, so + 32768);
        asm volatile("s_waitcnt vmcnt(0)\n\ts_barrier" ::: "memory");
    }

    unsigned c0 = 0, c1 = 65536, c2 = 131072, mo = mo0, fs = -256, fs1 = 1024, sg = -sL * lag, q0 = 2 * QK_OFF + qo + hs * 8192;
    static_assert(2 * XP_BUF == 65536 && PT_BUF == 65536 && XM_BUF == 131072, "strides");
    PIN_S(c0); PIN_S(c1); PIN_S(c2); PIN_S(fs); PIN_S(fs1); PIN_S(sg); PIN_S(q0);
    for (int it = 0; it < itend; ++it) {
        unsigned ps, pr, ts, tr, qn, kn, tz, fk;
        bf16x8 wl[3];
        f32x4 sc[3];
        u32x4 v[5], kp[2][2], kx[2];
        unsigned pvK;
        {
            bf16x8 ka[3], kb[3], q[3];
            auto krd = [&](int f, int e) {
                if (e == 0) kb[f % 3] = *(const bf16x8*)(sKT + (w & 2) * 12288 + f * 1024);
                if (e == 1) q[f % 3] = *(const bf16x8*)(sKT + w * 12288 + 6144 + f * 1024);
                if (e == 2) ka[f % 3] = *(const bf16x8*)(sKT + w * 12288 + f * 1024);
            };
            BIAS(sc[0], oval, oval, oval, oval); BIAS(sc[1], cm0, cm1, cm2, cm3);
            asm volatile("s_waitcnt vmcnt(%0)" :: "n"(5) : "memory");
#pragma unroll
            for (int e = 0; e < 9; ++e) { krd(e / 3, e % 3); SCHED0(); }
            PIN_V(ka[2]);
            rt_fine(X - E_G);
            sfor<0, 13>([&](auto G) {
                constexpr int g = decltype(G)::value, f = g / 2;
                if constexpr (g % 3 == 0) LDA(v[g / 3], g < 12 ? l16b : l16k, mo, (g / 3 & 3) * 1024);
                if constexpr (g < 12) MF_VV(sc[g & 1], (g & 1 ? ka : kb)[f % 3], q[f % 3]);
                SCHED0();
                if (g < 6) { if (g & 1) { krd(f + 3, 1); krd(f + 3, 2); } else krd(f + 3, 0); }
                SCHED0();
            });
#pragma unroll
            for (int e = 0; e < 2; ++e) {
                const u32x2 ka = *(const u32x2*)(sKX + e * 12288), kb = *(const u32x2*)(sKX + (e + 2) * 12288);
                kx[e] = u32x4{ka[0], ka[1], kb[0], kb[1]};
            }
            sg += 1;
            qn = (sg & (TR - 1)) << 21;
            qn += ((qn >> 8) & 8192) ^ q0;
            qn = sg < NSEG_ ? qn : OOB;
            kn = qn + 32768;
            PIN_S(sg); PIN_S(qn); PIN_S(kn);
            LDA4(pvK, fK, fs1);
            PIN_V(kx[0]); PIN_V(kx[1]);
        }
        f32x4 acc[6];
        auto mf = [&](int i, int tt, const bf16x8 xb) {
            if (i == 0) { if (tt < 4) MF_AV0(acc[tt], wf[tt][0], xb); else MF_VV0(acc[tt], wf[tt][0], xb); }
            else if (20 * tt + i < 64) MF_AV(acc[tt], wf[tt][i], xb);
            else if (tt < 5 || i < 2) MF_VV(acc[tt], wf[tt][i], xb);
            else MF_VV(acc[5], wl[i % 3], xb);
        };
        auto take = [&](u64 live_, auto N) {
            unsigned m = 0;
            u64 bad;
            sfor<0, 22>([&](auto G) {
                constexpr int g = decltype(G)::value;
                if constexpr (g < 20 && g % 4 == 0) { WAITV(4 + decltype(N)::value - g / 4, v[g / 4]); *(u32x4*)(sg0 + g / 4 * 1024) = v[g / 4]; SCHED0(); }
                mf(g / 6, g % 6, asb8(v[g / 6]));
                SCHED0();
                if constexpr (g < 20 && g % 4 == 0) { m = mx4(m, v[g / 4]); if (g < 16) PIN_V(m); else { bad = __ballot(m == ~0u); PIN_S(bad); } }
                if constexpr (g == 1 || g == 2 || g == 6 || g == 13) { constexpr int e = g < 3 ? g - 1 : 2 + g / 8; kp[e >> 1][e & 1] = *(const u32x4*)(sKT + e * 12288 + w * 1024); }
                if constexpr (g == 5 || g == 10 || g == 18) wl[(g / 5 + 1) % 3] = *(const bf16x8*)(sW + (g / 5 + 1) * 1024);
                if (g == 18 && !decltype(N)::value) { bad &= live_; PIN_S(bad); }
                if (g == 20 && decltype(N)::value) WAITV(0, pvK);
                SCHED0();
            });
            return bad;
        };
        if (__builtin_expect(take(~0ull, IC<1>{}) != 0, 0)) {
            auto again = [&](u64 live_) { return take(live_, IC<0>{}); };
            GATHER(again);
            asm volatile("s_nop 7" : "+v"(acc[0]), "+v"(acc[1]), "+v"(acc[2]), "+v"(acc[3]), "+v"(acc[4]), "+v"(acc[5]));
        }
        unsigned xv, kx0;
        u64 nk;
        u32x4 ko, kz;
        {
            bf16x8 mm[3];
            u32x4 qp, qa, qb, qz;
            unsigned z0, z1;
            f32x4 s[2], sm;
            sfor<22, 128>([&](auto G) {
                constexpr int g = decltype(G)::value, kt[13] = {5, 5, 5, 3, 4, -1, -1, 3, -1, 4, -1, 3, 4}, ki[13] = {17, 18, 19, 17, 17, 0, 0, 18, 0, 18, 0, 19, 19};
                constexpr int i = g < 102 ? g / 6 : (g < 111 ? 17 + (g - 102) / 3 : (g < 124 ? ki[g < 111 || g > 123 ? 0 : g - 111] : 0));
                constexpr int tt = g < 102 ? g % 6 : (g < 111 ? (g - 102) % 3 : (g < 124 ? kt[g < 111 || g > 123 ? 0 : g - 111] : -1));
                if constexpr (tt >= 0) mf(i, tt, i < 5 ? asb8(v[i < 5 ? i : 0]) : mm[(i < 5 ? 0 : i - 5) % 3]);
                if constexpr (g == 116 || g == 117) MF_VV0(s[g & 1], kp[0][g & 1], qa);
                if constexpr (g == 119 || g == 124) MF_VV(s[~g & 1], kp[1][~g & 1], qb);
                if constexpr (g == 121 || g == 125) MF_VV(s[g == 125], kx[g == 125], qz);
                if constexpr (g == 126) MF_VV0(sm, kz, qz);
                if constexpr (g == 127) MF_VV(sm, ko, qp);
                SCHED0();
                if (g == 22) { LDS_BARRIER(); }
                if (g == 23) mm[0] = mrd(5);
                if (g == 24) wl[2] = *(const bf16x8*)(sW + 5120);
                if constexpr (g > 24 && g < 103 && g % 6 == (g < 102)) mm[(g / 6 - 3) % 3] = mrd(g / 6 + 2);
                if constexpr (g > 24 && g < 102 && g % 6 == 3) wl[(g / 6 + 2) % 3] = *(const bf16x8*)(sW + (g / 6 + 2) * 1024);
                if (g == 104) wl[1] = *(const bf16x8*)(sW + 19 * 1024);
                if (g == 110) PIN_V(wl[1]);
                if (g == 26) { nk = __ballot(pvK == 0); PIN_S(nk); dm0(0); }
                if (g == 30) asm volatile("s_cmp_eq_u64 %1, 0\n\ts_cselect_b32 %0, %2, %3" : "=s"(kx0) : "s"(nk), "s"(kn), "n"(OOB) : "scc");
                if constexpr (g >= G_K && g <= G_K + 5 * G_KS && (g - G_K) % G_KS == 0) dma((g - G_K) / G_KS, qn, kx0, false);
                if (g == G_K + 4 * G_KS - 2) dm0(4);
                if (g == 36) { ps = c0; pr = c2; PIN_S(ps); PIN_S(pr); }
                if (g == 42) { c2 = c0; c0 = c1; PIN_S(c2); PIN_S(c0); }
                if (g == 48) { c1 = pr; ts = 2 * ps; PIN_S(c1); PIN_S(ts); }
                if (g == 60) { mo = mo0 + 4 * c0; PIN_S(mo); }
                if (g == 66) { fs += 256; tz = fs & 8192; PIN_S(fs); PIN_S(tz); }
                if (g == 72) { fs += tz; tr = 2 * c0; PIN_S(fs); PIN_S(tr); }
                if (g == 84) { fk = fs1; fs1 += 1024; PIN_S(fk); PIN_S(fs1); }
                if (g == 90) { fs1 += fs1 & 8192; PIN_S(fs1); }
                if (g == 96) { X += PER; PIN_S(X); }
                if (g == 111) CVT2(qp[0], qp[1], acc[0]);
                if (g == 112) CVT2(qp[2], qp[3], acc[1]);
                if (g == 113) AND2(qa[0], qa[1], qp[0], qp[1], ma);
                if (g == 114) AND2(qa[2], qa[3], qp[2], qp[3], ma);
                if (g == 115) CVT2(z0, z1, acc[2]);
                if (g == 118) AND2(qz[0], qz[1], z0, z1, ma);
                if (g == 119) AND2(qz[2], qz[3], z0, z1, mb2);
                if (g == 116) AND2(qb[0], qb[1], qp[0], qp[1], mb2);
                if (g == 117) AND2(qb[2], qb[3], qp[2], qp[3], mb2);
                if (g == 120) CVT2(kz[0], kz[1], acc[5]);
                if (g == 121) asm volatile("v_cvt_pk_bf16_f32 %0, %2, %3\n\tv_cvt_pk_bf16_f32 %1, %4, %5" : "=&v"(kz[2]), "=&v"(kz[3]) : "v"(acc[5][0]), "v"(acc[5][1]), "v"(acc[5][2]), "v"(acc[5][3]));
                if (g == 123) *(f32x4*)sSw = s[0];
                if (g == 124) CVT2(ko[0], ko[1], acc[3]);
                if (g == 125) CVT2(ko[2], ko[3], acc[4]);
                if (g == 127) *(f32x4*)(sSw + 1040) = s[1];
                SCHED0();
            });
            asm volatile("s_nop 6" : "+v"(sm) :: "memory");
            *(f32x4*)(sSw + 2080) = sm;
        }
        LDS_BARRIER();
        constexpr float SC = 0.07216878364870322f * 1.4426950408889634f;
        {
            const f32x4 s0 = *(const f32x4*)sSr, s1 = *(const f32x4*)(sSr + SS_W), s2 = *(const f32x4*)(sSr + 2 * SS_W), s3 = *(const f32x4*)(sSr + 3 * SS_W);
            SCHED0();
            f32x4 sv = ((s0 + s1) + s2) + s3;
            u32x2 p;
            p[0] = __float_as_uint(fmaxf(fmaxf(fmaxf(sv[0], sv[1]), sv[2]), sv[3]));
            asm volatile(ROWRED("v_max_f32", "%0", "%1", "v_mul_f32 %2, %6, %2\n\tv_mul_f32 %3, %6, %3\n\t", "v_mul_f32 %4, %6, %4\n\tv_mul_f32 %5, %6, %5\n\t")
                         "v_fma_f32 %2, -%0, %6, %2\n\tv_fma_f32 %3, -%0, %6, %3\n\tv_fma_f32 %4, -%0, %6, %4\n\tv_fma_f32 %5, -%0, %6, %5\n\t"
                         "v_exp_f32 %2, %2\n\tv_exp_f32 %3, %3\n\tv_exp_f32 %4, %4\n\tv_exp_f32 %5, %5\n\t"
                         "v_add_f32 %0, %2, %3\n\tv_add_f32 %0, %0, %4\n\tv_add_f32 %0, %0, %5\n\t"
                         ROWRED("v_add_f32", "%0", "%1", "s_nop 1\n\t", "s_nop 1\n\t")
                         "v_rcp_f32 %0, %0\n\ts_nop 0\n\tv_mul_f32 %2, %0, %2\n\tv_mul_f32 %3, %0, %3\n\tv_mul_f32 %4, %0, %4\n\tv_mul_f32 %5, %0, %5\n\t"
                         "v_cvt_pk_bf16_f32 %0, %2, |%3|\n\tv_cvt_pk_bf16_f32 %1, %4, %5"
                         : "+v"(p[0]), "=&v"(p[1]), "+v"(sv[0]), "+v"(sv[1]), "+v"(sv[2]), "+v"(sv[3]) : "s"(SC));
            sst8(rx, ps, ppos, p);
        }
        SCHED0();
        {
            if (__builtin_expect(nk != 0, 0)) {
                wave_wait(rx, fK, fk, spins);
#pragma unroll
                for (int e = 0; e < 6; ++e) dma(e, qn, kn);
            }
            bf16x8 km[6], q[6];
            *(u32x4*)(sKM + w * 1024) = ko;
            *(u32x2*)(sKX + (SL_KM - SL_KT)) = u32x2{kz[0], kz[1]};
            SCHED0();
#pragma unroll
            for (int f = 0; f < 6; ++f) { q[f] = *(const bf16x8*)(sKT + w * 12288 + 6144 + f * 1024); SCHED0(); }
            LDA4(xv, fXv, fs);
            dm0(6);
            asm volatile("s_waitcnt lgkmcnt(5)\n\ts_barrier" ::: "memory");
#pragma unroll
            for (int f = 0; f < 6; ++f) { km[f] = *(const bf16x8*)(sKM + f * 1024); SCHED0(); }
            dma(6, qn, kn, false);
            BIAS(sc[2], mval, mval, mval, mval);
            PIN_V(q[5]);
            dma(7, qn, kn, false);
            float mx = max3r(max3r(sc[0][0], sc[0][1], sc[0][2]), max3r(sc[0][3], sc[1][0], sc[1][1]), max2r(sc[1][2], sc[1][3])), tm;
            PIN_V(mx);
#pragma unroll
            for (int f = 0; f < 6; ++f) { MF_VV(sc[2], km[f], q[f]); if (f & 1) dma(8 + f / 2, qn, kn, false); if (f == 4) dm0(10); SCHED0(); }
            asm volatile("s_nop 5" : "+v"(sc[2]));
            mx = max3r(max3r(mx, sc[2][0], sc[2][1]), sc[2][2], sc[2][3]);
            asm volatile("v_mov_b32 %1, %0\n\ts_nop 1\n\tv_permlane16_swap_b32 %0, %1\n\tv_max_f32 %0, %0, %1\n\tv_mov_b32 %1, %0\n\ts_nop 1\n\tv_permlane32_swap_b32 %0, %1\n\tv_max_f32 %0, %0, %1" : "+v"(mx), "=&v"(tm));
            dma(11, qn, kn);
            mx *= -SC;
#pragma unroll
            for (int e = 0; e < 3; ++e) {
#pragma unroll
                for (int r = 0; r < 4; ++r) sc[e][r] = __builtin_fmaf(sc[e][r], SC, mx);
                PIN_V(sc[e]);
            }
#pragma unroll
            for (int e = 0; e < 3; ++e) {
#pragma unroll
                for (int r = 0; r < 4; ++r) sc[e][r] = __builtin_amdgcn_exp2f(sc[e][r]);
            }
            const f32x4 t4 = (sc[0] + sc[1]) + sc[2];
            float sum = (t4[0] + t4[1]) + (t4[2] + t4[3]);
            PIN_V(sum);
            float inv = __builtin_amdgcn_rcpf(red_sum_q(sum));
            PIN_V(inv);
            unsigned u[6];
#pragma unroll
            for (int e = 0; e < 6; ++e) { u[e] = pk2(sc[e >> 1][2 * (e & 1)] * inv, sc[e >> 1][2 * (e & 1) + 1] * inv); if (!(e & 1)) u[e] = nosent(u[e]); }
            const auto a4 = __builtin_amdgcn_permlane16_swap(u[4], u[4], false, false), a5 = __builtin_amdgcn_permlane16_swap(u[5], u[5], false, false);
            sst16(rx, ts, pt2, u32x4{u[4], u[5], a4[1], a5[1]});
            u32x2 o0, o1;
            asm volatile("v_mov_b64 %0, -1\n\tv_mov_b64 %1, -1" : "=v"(o0), "=v"(o1));
            const u32x4 ones = {o0[0], o0[1], o1[0], o1[1]};
            if (rt) { sst16(rx, ts, pt1, u32x4{u[0], u[1], u[2], u[3]}); sst16(rx, tr, pt1, ones); } else { sst8(rx, ts, pt1, u32x2{u[2], u[3]}); sst8(rx, tr, pt1, u32x2{ones[0], ones[1]}); }
            sst8(rx, pr, ppos, u32x2{ones[0], ones[1]});
            sst16(rx, tr, pt2, ones);
            asm volatile("s_waitcnt vmcnt(11)\n\ts_barrier" : "+v"(xv) :: "memory");
            xv = __builtin_amdgcn_readfirstlane(xv);
            X = xv ? xv + PER : X;
        }
    }
    SPIN_END;
}

constexpr int CL_R = 0, CL_C = CL_R + 16384, CL_END = CL_C + 4096;
__device__ __forceinline__ void c_role(const MegaArgs& A, char* smem, const int pool, const int ku, const int id) {
    CHAIN_IDS;
    const int cL = pool >> 2, j = 5 * (pool & 3) + ku;
    go.v = sld8(go.rh, go.so, l * 8);
    GO_ON;
    unsigned fXv = xm(FX_OFF + (pool & 3) * 64, cL);
    PIN_V(fXv);
    const int hi = (l >> 3) & 1, tk = l & 7;
    const unsigned pos = xm(XM_OFF + w * 32768 + (j / 5) * 8192 + (j % 5) * 1024 + (lq * 16 + cL * 8 + tk) * 16 + hi * 8, 0);
    const unsigned umo = xm(UM_OFF + (16 * j + 2 * w) * 1024, cL), po0 = xm(XP_OFF + w * 8192, cL), pto = xm(PT_OFF + w * 16384, cL), uto = xm(UT_OFF + (64 * j + 8 * w) * 1024, cL);
    const unsigned fU = lp(SG_FL + FL_U + (8 * w + tk) * 64 + (pool & 3) * 4);
    const unsigned xo = lp((2 * j + (w >> 1)) * 1024 + (lq * 16 + 8 * (w & 1) + tk) * 16 + hi * 8), sto = l < 8 ? lp(SG_ST + (4 * j + w) * 64 + l * 8) : OOB;
    u32x2 alt;
    { const u32x2 m = *(const u32x2*)(A.mem0 + tk * D_ + 32 * j + 16 * hi + 4 * lq); alt = u32x2{nosent(m[0]), m[1]}; }
    f32x4* sRw0 = (f32x4*)(smem + CL_R) + w * 256 + l;
    f32x4* sRw1 = (f32x4*)(smem + CL_R) + w * 256 + 128 + (l ^ 8);
    const f32x4* sRr = (const f32x4*)(smem + CL_R) + (hi * 2 + (w >> 1)) * 64 + lq * 16 + ((8 * (w & 1) + tk) ^ (8 * hi));
    f32x4* sC = (f32x4*)(smem + CL_C);
    int mb = 0;
    u32x4 ua[16], ub[16], um[4];
#pragma unroll
    for (int e = 0; e < 16; ++e) ua[e] = u32x4{0u, 0u, 0u, 0u};
#pragma unroll
    for (int e = 0; e < 4; ++e) um[e] = u32x4{0u, 0u, 0u, 0u};
    for (int s = 0; s < NSEG_; ++s) sst8(rx, seg_base(cL, s) + lp(SG_X1), xo, u32x2{0u, 0u});
    unsigned X = rtime() - 2 * PER;
    auto seg_so = [&](int sg) { return (unsigned)sg < NSEG_ ? seg_base(cL, sg) : OOB; };
    unsigned sb0 = OOB, sb1 = seg_so(-cL * lag), pvU = sld4(rx, sb1, fU), ts = pto + 4 * PT_BUF, ts2 = ts + 16384;
    auto step = [&](const int it, auto LAST, u32x4 (&up)[16], u32x4 (&ut)[16]) {
        constexpr bool last = decltype(LAST)::value;
        const int nb = (mb == 2) ? 0 : mb + 1, pb = mb ? mb - 1 : 2, sg = it - cL * lag;
        if (!last) CHAIN_WAIT(pvU, fU, sb1, 0);
        u32x4 pt[16];
        u32x2 xr;
        auto pt_load = [&](int e) { LDA(pt[e], e < 4 || e >= 12 ? l16k : l16b, e < 8 ? ts2 : ts, (e & 3) * 1024); };
        pt_load(0);
        SCHED0();
        unsigned uo = sb1 != OOB ? uto + ut_slot(sg) : OOB, uo2 = uo + 65536;
        PIN_S(uo); PIN_S(uo2);
        auto ut_load = [&](int e) {
            if (!last) LDA(ut[e], (e & 4) ? l16k : l16b, e < 8 ? uo : uo2, (e & 3) * 1024);
        };
        pt_load(1);
        SCHED0();
        unsigned sbr = (sb0 ^ (cL << 13)) + lp(SG_XR), sbx = sb0 + lp(SG_X1), sb2 = seg_so(sg + 1);
        PIN_S(sbr); PIN_S(sbx); PIN_S(sb2);
        pt_load(2);
        SCHED0();
        unsigned fs = fx_step(it), cu = umo + mb * (2 * UM_BUF), cu2 = cu + 16384, cp = po0 + mb * (2 * XP_BUF), cr = pb * (2 * XM_BUF), cr2 = cr + 8192;
        PIN_S(fs); PIN_S(cu); PIN_S(cu2); PIN_S(cp); PIN_S(cr); PIN_S(cr2);
        pt_load(3);
        SCHED0();
        unsigned cs = sg < 0 ? OOB : nb * (2 * XM_BUF), cs2 = cs + 8192;
        X += PER;
        PIN_S(cs); PIN_S(cs2); PIN_S(X);
#pragma unroll
        for (int e = 4; e < 13; ++e) { pt_load(e); if (e < 12) GAP(); }
        u32x2 st;
        {
            f32x4 d[2][2];
            auto check = [&](auto E0, auto N, auto W) {
                constexpr int e0 = decltype(E0)::value, n = decltype(N)::value;
                auto look = [&](auto WW) {
                    asm volatile("s_waitcnt vmcnt(%2)" : "+v"(pt[e0]), "+v"(pt[e0 + 1]) : "n"(decltype(WW)::value) : "memory");
                    if (n == 4) asm volatile("" : "+v"(pt[e0 + n - 2]), "+v"(pt[e0 + n - 1]));
                    unsigned m = 0;
#pragma unroll
                    for (int e = e0; e < e0 + n; ++e) m = mx4(m, pt[e]);
                    return m; };
                if (__builtin_expect(__ballot(look(W) == ~0u) != 0, 0)) {
#pragma unroll 1
                    for (;;) {
#pragma unroll
                        for (int e = e0; e < e0 + n; ++e) pt_load(e);
                        LIVE;
                        if (!BAD(look(IC<0>{}))) break;
                    }
                }
            };
            sfor<0, 40>([&](auto G) {
                constexpr int g = decltype(G)::value, n = g & 1;
                if constexpr (g == 0) check(IC<0>{}, IC<4>{}, IC<9>{});
                if constexpr (g == 8) check(IC<4>{}, IC<2>{}, IC<10>{});
                if constexpr (g == 16) check(IC<6>{}, IC<2>{}, IC<(last ? 9 : 11)>{});
                if constexpr (g == 24) check(IC<8>{}, IC<4>{}, IC<(last ? 5 : 10)>{});
                if constexpr (g == 32) check(IC<12>{}, IC<2>{}, IC<(last ? 3 : 11)>{});
                if constexpr (g == 36) { check(IC<14>{}, IC<2>{}, IC<(last ? 0 : 10)>{}); PIN_V(xr); }
                if constexpr (g < 4) MF_VV0(d[n][g >> 1], um[2 * n], pt[g >> 1]);
                else if constexpr (g < 8) MF_VV(d[n][(g >> 1) & 1], um[2 * n + 1], pt[g >> 1]);
                else if constexpr (g < 24) {
                    constexpr int hh = (g - 8) >> 1;
                    const u32x4 x = up[8 * n + hh], z = pt[4 + (hh >> 1)];
                    const u32x2 xl = {x[0], x[1]}, zl = {z[2 * (hh & 1)], z[2 * (hh & 1) + 1]};
                    MH_(d[n][0], "v", xl, "v", zl);
                } else MF_VV(d[n][1], up[8 * n + ((g - 24) >> 1)], pt[8 + ((g - 24) >> 1)]);
                constexpr int rq = g < 32 ? ((g & 7) == 2 ? 0 : ((g & 7) == 5 ? 1 : ((g & 7) == 7 ? 2 : -99))) + 3 * (g >> 3) : (g == 33 ? 12 : (g == 35 ? 13 : -99));
                if constexpr (rq >= 0 && rq < 3) pt_load(13 + rq); else if constexpr (rq == 3) LDA8(xr, xo, sbr, 0); else if constexpr (rq > 3) ut_load(rq - 4);
                if (g == 24) sRw0[0] = d[0][0];
                if (g == 27) sRw1[0] = d[1][0];
                SCHED0();
            });
            const f32x4 xf = {lo16(xr[0]), hi16(xr[0]), lo16(xr[1]), hi16(xr[1])};
            asm volatile("" : "+v"(d[0][1]) : "v"(xf) : "memory");
            sRw0[64] = d[0][1];
            ut_load(10);
            asm volatile("s_nop 2" : "+v"(d[1][1]) :: "memory");
            sRw1[64] = d[1][1];
            ut_load(11);
            LDS_BARRIER();
            const f32x4 s0 = sRr[0], s1 = sRr[256], s2 = sRr[512], s3 = sRr[768];
            ut_load(12);
            SCHED0();
            f32x4 s = (((xf + s0) + s1) + s2) + s3;
            sst8(rx, sbx, xo, u32x2{pk2(s[0], s[1]), pk2(s[2], s[3])});
            float sm = (s[0] + s[1]) + (s[2] + s[3]);
            sm += dpp_ror8(sm);
            PIN_V(sm);
            ut_load(13);
            sm = red_sum_q(sm) * (1.0f / 32);
            PIN_V(sm);
            ut_load(14);
            s -= sm;
            float sq = __builtin_fmaf(s[3], s[3], __builtin_fmaf(s[2], s[2], __builtin_fmaf(s[1], s[1], s[0] * s[0])));
            sq += dpp_ror8(sq);
            PIN_V(sq);
            ut_load(15);
            sq = red_sum_q(sq);
            st = u32x2{nosent(__float_as_uint(sm)), __float_as_uint(sq)};
            PIN_V(st);
        }
        SCHED0();
        if (last) { DRAIN(); sst8(rx, sb0, sto, st); return; }
        unsigned xv;
        rt_until(X + E_X);
        LDA4(xv, fXv, fs);
        ts = pto + mb * (2 * PT_BUF); ts2 = ts + 16384;
        PIN_S(ts); PIN_S(ts2);
        asm volatile("s_waitcnt vmcnt(4)" ::: "memory");
        sst8(rx, sb0, sto, st);
        sst8(rx, cr, pos, u32x2{~0u, ~0u});
        sst8(rx, cr2, pos, u32x2{~0u, ~0u});
        WAITV(3, xv);
        X = __builtin_amdgcn_readfirstlane(xv);
        PIN_S(X);
        if (__builtin_expect(X == 0, 0)) {
#pragma unroll 1
            for (;;) {
                LDA4(xv, fXv, fs);
                LIVE;
                WAITV(0, xv);
                X = __builtin_amdgcn_readfirstlane(xv);
                if (X || !live_) break;
            }
        }
        u32x4 p[5];
        f32x4 a[2], b[2];
        auto take2 = [&](u64 live_) {
            unsigned m = 0;
            u64 bad;
#pragma unroll
            for (int i = 0; i < 4; ++i) { WAITV(9 - i, um[i]); m = mx4(m, um[i]); PIN_V(m); }
#pragma unroll
            for (int i = 0; i < 5; ++i) {
                WAITV(4 - i, p[i]);
#pragma unroll
                for (int e = 0; e < 4; ++e) {
                    f32x4& d = (e & 1) ? b[e >> 1] : a[e >> 1];
                    if (i == 0) MF_VV0(d, ut[8 * (e >> 1) + (e & 1)], p[0]); else if (i < 4) MF_VV(d, ut[8 * (e >> 1) + 2 * (i & 3) + (e & 1)], p[i]); else MF_VV(d, um[e], p[4]);
                    SCHED0();
                    if (e == 0) { m = mx4(m, p[i]); if (i < 4) PIN_V(m); else { bad = __ballot(m == ~0u); PIN_S(bad); } }
                    if (e == 1 && i == 4) { bad &= live_; PIN_S(bad); }
                    SCHED0();
                }
            }
            return bad;
        };
        {
            unsigned tP = X + E_P, tU = X + E_U;
#pragma unroll 1
            for (;;) {
                rt_until(tU);
#pragma unroll
                for (int i = 0; i < 4; ++i) LDI(um[i], l16b, (i & 2) ? cu2 : cu, (i & 1) * 1024);
                LDA4(pvU, fU, sb2);
                LIVE;
                rt_fine(tP);
                LD5(p, cp);
                SCHED0();
                if (!take2(live_)) break;
            }
        }
        f32x4 x;
        asm volatile("v_add_f32_dpp %0, %8, %4 row_ror:8 row_mask:0xf bank_mask:0x3\n\tv_add_f32_dpp %1, %9, %5 row_ror:8 row_mask:0xf bank_mask:0x3\n\t"
                     "v_add_f32_dpp %2, %10, %6 row_ror:8 row_mask:0xf bank_mask:0x3\n\tv_add_f32_dpp %3, %11, %7 row_ror:8 row_mask:0xf bank_mask:0x3"
                     : "=&v"(x[0]), "=&v"(x[1]), "=&v"(x[2]), "=&v"(x[3]) : "v"(a[0][0]), "v"(a[0][1]), "v"(a[0][2]), "v"(a[0][3]), "v"(b[0][0]), "v"(b[0][1]), "v"(b[0][2]), "v"(b[0][3]));
        asm volatile("s_nop 0\n\tv_add_f32_dpp %0, %8, %4 row_ror:8 row_mask:0xf bank_mask:0xc\n\tv_add_f32_dpp %1, %9, %5 row_ror:8 row_mask:0xf bank_mask:0xc\n\t"
                     "v_add_f32_dpp %2, %10, %6 row_ror:8 row_mask:0xf bank_mask:0xc\n\tv_add_f32_dpp %3, %11, %7 row_ror:8 row_mask:0xf bank_mask:0xc"
                     : "+v"(x[0]), "+v"(x[1]), "+v"(x[2]), "+v"(x[3]) : "v"(b[1][0]), "v"(b[1][1]), "v"(b[1][2]), "v"(b[1][3]), "v"(a[1][0]), "v"(a[1][1]), "v"(a[1][2]), "v"(a[1][3]));
        sC[w * 64 + l] = x;
        LDS_BARRIER();
        const f32x4 s = ((sC[l] + sC[64 + l]) + sC[128 + l]) + sC[192 + l];
        const u32x2 r = {nosent(pk2(s[0], s[1])), pk2(s[2], s[3])};
        sst8(rx, cs, pos, r);
        sst8(rx, cs2, pos, r);
        SCHED0();
        if (__builtin_expect(cs == OOB, 0)) { sst8(rx, nb * (2 * XM_BUF), pos, alt); sst8(rx, nb * (2 * XM_BUF) + 8192, pos, alt); }
        PIN_V(pvU);
        mb = nb; sb0 = sb1; sb1 = sb2;
    };
    int it = 0;
    PIN_S(spins); PIN_V(pvU); PIN_V(alt);
#pragma unroll 1
    do { step(it, IC<0>{}, ua, ub); step(it + 1, IC<0>{}, ub, ua); it += 2; } while (it + 1 < itend);
    if (LAGV & 1) { step(it, IC<0>{}, ua, ub); step(it + 1, IC<1>{}, ub, ua); } else step(it, IC<1>{}, ua, ub);
    SPIN_END;
}

constexpr int VL_V = 20480, VL_W = VL_V + 6144, VL_END = VL_W + 81920;
__device__ __forceinline__ void vm_role(const MegaArgs& A, char* smem, const int h, const int id) {
    CHAIN_IDS;
    const int pool = h >> 2;
    bf16x8 wf[3][20], wo[10][4];
    char* sWo = smem + VL_W + w * 20480 + l * 16;
    {
        bf16x8 tmp[20];
        load_vo<5>(smem, w, l, go, A.Wv + h * DH_, A.Wo + (size_t)(h * DH_) * D_, wf, [&](int k, int ct, int ww) -> bf16x8& { return k < 4 ? wo[ct][(k - ww) & 3] : tmp[4 * (ct >> 1) + 2 * (k & 1) + (ct & 1)]; });
#pragma unroll
        for (int i = 0; i < 20; ++i) *(bf16x8*)(sWo + i * 1024) = tmp[i];
    }
    GO_ON;
    CHAIN_STAGE;
    unsigned upos[5];
#pragma unroll
    for (int m = 0; m < 5; ++m) upos[m] = xm(UM_OFF + ((10 * w + 2 * m + (lq & 1)) * 8 + (h >> 2)) * 1024 + ((h & 3) * 16 + l16) * 16, lq >> 1);
    char* sV = smem + VL_V + l * 16;
    const char* sV1 = sV + ((w + 1) & 3) * 1024; const char* sV2 = sV + ((w + 2) & 3) * 1024; const char* sV3 = sV + ((w + 3) & 3) * 1024;
    const unsigned xw = (h == 1 && t < 8) ? xm(FX_OFF + (t & 3) * 64, t >> 2) : OOB;
    int mb = 0;
    unsigned X = rtime() - 2 * PER;
    for (int it = 0; it < itend; ++it) {
        const int nb = (mb == 2) ? 0 : mb + 1, pb = mb ? mb - 1 : 2;
        unsigned mo = mo0 + mb * (2 * XM_BUF), fs = fx_step(it), us = mb * (2 * UM_BUF), ur = pb * (2 * UM_BUF);
        X += PER;
        PIN_S(mo); PIN_S(fs); PIN_S(us); PIN_S(ur); PIN_S(X);
        rt_fine(X - E_G);
        f32x4 acc[3];
        u32x4 v[5];
        auto take = [&](u64 live_) {
            unsigned m = 0;
            u64 bad;
            sfor<0, 11>([&](auto G) {
                constexpr int g = decltype(G)::value, e = g < 9 && !(g & 1) ? g / 2 : -1;
                if constexpr (e >= 0) { WAITV(4 - e, v[e]); *(u32x4*)(sg0 + e * 1024) = v[e]; SCHED0(); }
                if (g < 3) MF_AV0(acc[g % 3], wf[g % 3][0], v[0]); else MF_AV(acc[g % 3], wf[g % 3][g / 3], v[g / 3]);
                SCHED0();
                if constexpr (e >= 0) { m = mx4(m, v[e]); if (e < 4) PIN_V(m); else { bad = __ballot(m == ~0u); PIN_S(bad); } }
                if (g == 9) { bad &= live_; PIN_S(bad); }
                SCHED0();
            });
            return bad;
        };
        GATHER(take);
        bf16x8 wl[4];
        u32x4 vf[6], o;
        f32x4 d[10];
        unsigned xv;
        {
            constexpr int MW = 5;
            bf16x8 mm[MW];
            u32x2 vz;
            u64 tb64;
            unsigned t0, t1;
            asm volatile("s_waitcnt lgkmcnt(0)\n\ts_barrier\n\ts_memrealtime %0" : "=s"(tb64) :: "memory");
            mm[0] = mrd(5);
            SCHED0();
            sfor<11, 60>([&](auto G) {
                constexpr int g = decltype(G)::value, i = g < 45 ? g / 3 : (g < 50 ? g - 30 : 15 + (g - 50) / 2), tt = g < 45 ? g % 3 : (g < 50 ? 2 : g & 1);
                const bf16x8 xb = i < 5 ? asb8(v[i < 5 ? i : 0]) : mm[(i < 5 ? 0 : i - 5) % MW];
                MF_AV(acc[tt], wf[tt][i], xb);
                SCHED0();
                if constexpr (g < 15) mm[g - 10] = mrd(g - 5);
                if constexpr (g >= 18 && g < 46 && g % 3 == 0) mm[(g / 3 - 6) % MW] = mrd(g / 3 + 4);
                if constexpr (g >= 54 && g < 58) wl[g - 54] = *(const bf16x8*)(sWo + (g - 54) * 1024);
                if (g == 17) asm volatile("s_waitcnt lgkmcnt(0)" : "+s"(tb64) :: "memory");
                if (g == 19) asm volatile("s_sub_u32 %0, %2, %3\n\ts_add_u32 %1, %2, %4" : "=&s"(t0), "=&s"(t1) : "s"((unsigned)tb64), "s"(X), "n"(E_S) : "scc");
                if (g == 22) asm volatile("s_cmp_gt_i32 %1, 0\n\ts_cselect_b32 %0, %2, %0" : "+s"(X) : "s"(t0), "s"(t1) : "scc");
                if (g == 23) asm volatile("v_or_b32 %0, 1, %1" : "=v"(xv) : "s"(X));
                if (g == 25) sst4(rx, fs, xw, xv);
                if (g == 52) CVT2(vz[0], vz[1], acc[2]);
                if (g == 53) *(u32x2*)(sV + 4096 + (w >> 1) * 1024 + (w & 1) * 8) = vz;
                SCHED0();
            });
        }
        asm volatile("s_nop 2");
        SCHED0();
        CVT2(vf[0][0], vf[0][1], acc[0]);
        SCHED0();
        asm volatile("s_nop 1");
        SCHED0();
        CVT2(vf[0][2], vf[0][3], acc[1]);
        *(u32x4*)(sV + w * 1024) = vf[0];
        SCHED0();
        asm volatile("s_nop 0");
        sfor<0, 67>([&](auto G) {
            constexpr int g = decltype(G)::value, m = g < 10 ? 0 : (g - 10) / 10, r = g < 10 ? 0 : (g - 10) % 10, kk = g < 10 ? 0 : 1 + r / 2, ct = g < 10 ? g : 2 * m + (r & 1);
            if constexpr (g < 60) {
                if constexpr (kk >= 4) MF_VV(d[ct], vf[kk], wl[2 * (kk & 1) + (r & 1)]);
                else if constexpr (ct == 0) { if (kk == 0) MF_VA0(d[0], vf[0], wo[0][0]); else MF_VA(d[0], vf[kk], wo[0][kk]); }
                else { if (kk == 0) MF_VV0(d[ct], vf[0], wo[ct][0]); else MF_VV(d[ct], vf[kk], wo[ct][kk]); }
            }
            SCHED0();
            if (g == 3) { LDS_BARRIER(); }
            if (g == 4) vf[1] = *(const u32x4*)sV1;
            if (g == 5) vf[2] = *(const u32x4*)sV2;
            if (g == 6) vf[3] = *(const u32x4*)sV3;
            if (g == 7) vf[4] = *(const u32x4*)(sV + 4096);
            if (g == 8) vf[5] = *(const u32x4*)(sV + 5120);
            if constexpr (g >= 17 && g < 51 && (g - 17) % 10 < 4) wl[(g - 17) % 10] = *(const bf16x8*)(sWo + ((g - 17) / 10 * 4 + 4 + (g - 17) % 10) * 1024);
            if (g == 12) LDA4(xv, fXv, fs);
            if constexpr (m > 0) {
                if (g == 60) asm volatile("s_nop 3");
                if (r == 1) CVT2(o[0], o[1], d[2 * m - 2]);
                SCHED0();
                if (g == 61) asm volatile("s_nop 1");
                if (r == 2) CVT2(o[2], o[3], d[2 * m - 1]);
                if (r == 3) { o[0] = nosent(o[0]); o[2] = nosent(o[2]); asm volatile("" : "+v"(o[0]), "+v"(o[2])); }
                if (r == 4) asm volatile("v_permlane16_swap_b32 %0, %1" : "+v"(o[1]), "+v"(o[3]));
                if (r == 5) asm volatile("v_permlane16_swap_b32 %0, %1" : "+v"(o[0]), "+v"(o[2]));
                if (r == 6) sst16(rx, us, upos[m - 1], o);
            }
            SCHED0();
        });
#pragma unroll
        for (int m = 0; m < 5; ++m) { __builtin_amdgcn_s_sleep(VSLP); sst16(rx, ur, upos[m], u32x4{~0u, ~0u, ~0u, ~0u}); SCHED0(); }
        WAITV(10, xv);
        xv = __builtin_amdgcn_readfirstlane(xv);
        X = xv ? xv : X;
        mb = nb;
    }
    SPIN_END;
}
#define DBG_LD 0
#define WORK_IDS const int t = threadIdx.x, l = t & 63, l16 = l & 15, lq = l >> 4; const int w = __builtin_amdgcn_readfirstlane(t >> 6); \
    GO_DECL(id >> 5); const unsigned l16b = l * 16, dvo = w * 2048 + l * 16 + DBG_LD, lds0 = lds_addr(smem) + w * 2048; unsigned spins; SZERO(spins)
#define XSET(so, base, ld) asm volatile("s_mov_b32 m0, %2\n\ts_mov_b32 %0, %1" : "=s"(so) : "s"(base), "s"(ld) : "m0")
#define XADV(so) asm volatile("s_add_u32 m0, m0, 0x2000\n\ts_add_u32 %0, %0, 0x4000" : "+s"(so) :: "m0", "scc")
#define XDMA(so, rt) asm volatile("buffer_load_dwordx4 %0, %1, %2 offen offset:%3 sc1 lds" :: "v"(dvo), "s"(rx), "s"(so), "n"((rt) * 1024) : "memory")
#define XALL(so, base, ld) _Pragma("unroll") for (int e_ = 0; e_ < 10; ++e_) { if (e_ % 5 == 0) XSET(so, base, ld); else XADV(so); XDMA(so, e_ / 5); }
#define XALL2(so, base, ld) _Pragma("unroll") for (int e_ = 0; e_ < 5; ++e_) { if (e_ == 0) XSET(so, base, ld); else XADV(so); XDMA(so, 0); XDMA(so, 1); }
#define VMB(n) asm volatile("s_waitcnt vmcnt(%0)\n\ts_barrier" :: "n"(n) : "memory")
#define VMB_LATE(k) asm volatile("s_cmp_lg_u32 %0, %1\n\ts_cbranch_scc1 1f\n\ts_waitcnt vmcnt(%2)\n1:\n\ts_barrier" :: "s"(xs), "n"(OOB), "n"(k) : "memory", "scc")
#define VMW(n) asm volatile("s_waitcnt vmcnt(%0)" :: "n"(n) : "memory")
#define NEXT_DECL const unsigned fXo = l < 4 ? lp(SG_FL + FL_X + ((h & 12) + l) * 128) : OOB, fx1 = (((TR >> 2) & ~L & 1) ^ hside(h)) << 13; const int dl = __builtin_amdgcn_readfirstlane((L ^ 1) * TR); \
    unsigned xb = seg_base(0, 0), xs, fb = seg_base(hside(h), 0), msk, so, pv, pend, fXv = xm(FX_OFF + ((h >> 2) & 3) * 64, hside(h)), xv, fs; PIN_V(fXv); if (L) wave_wait(rx, fXo, fb, spins); XALL(so, xb, lds0); VMB(5); int s; SZERO(s)
#define G_L1 150
#define G_E1 182
#define G_L2 210
#define NEXT_ADDR(a, st) \
    if (g == a) { s = __builtin_amdgcn_readfirstlane(s); PIN_S(s); xb = (s + 1) << 19; PIN_S(xb); } \
    if (g == a + st) { xs = (xb >> 8) & 8192; PIN_S(xs); } \
    if (g == a + 2 * st) { xb = (xb | xs) + 2 * SG_OFF; PIN_S(xb); } \
    if (g == a + 3 * st) { fb = (xb ^ fx1) - (dl << 19); PIN_S(fb); } \
    if (g == a + 5 * st) asm volatile("s_cmp_lt_i32 %1, %2\n\ts_cselect_b32 %0, 0, 15" : "=s"(msk) : "s"(s), "s"(dl - 1) : "scc"); \
    if (g == a + 6 * st) asm volatile("s_cmp_lt_i32 %1, 63\n\ts_cselect_b32 %0, %0, %2" : "+s"(fb) : "s"(s), "n"(OOB) : "scc"); \
    if (g == a + 7 * st) { PIN_S(s); fs = (s - (dl - K_W)) << 8; PIN_S(fs); } \
    if (g == a + 8 * st) { fs += fs & ~8191u; PIN_S(fs); } \
    if (g == a + 9 * st) asm volatile("s_cmp_lt_i32 %0, 0\n\ts_cselect_b32 %0, %1, %0" : "+s"(fs) : "n"(OOB) : "scc")
#define NEXT_LOOK(n, ld) \
    if (g == G_L1 || g == G_L2) LDA4(pv, fXo, fb); \
    if (g == G_E1) { WAITV(n, pv); xs = (unsigned)__ballot(pv == 0); PIN_S(xs); } \
    if (g == G_E1 + 2) asm volatile("s_and_b32 %0, %0, %2\n\ts_cselect_b32 %0, %3, %1" : "+s"(xs) : "s"(xb), "s"(msk), "n"(OOB) : "scc")
#define NEXT_GO(n, ld, fl) \
    WAITV(n, pv); \
    if (xs != OOB) { asm volatile("s_barrier" ::: "memory"); prime(); SCHED0(); tail(); LDA4(xv, fXv, fs); WAITV(0, xv); sst4(rx, pend, fl, 1u); } \
    else { \
        tail(); \
        if ((unsigned)__ballot(pv == 0) & msk) { DRAIN(); sst4(rx, pend, fl, 1u); if (s == NSEG_ - 1) { SPIN_END; __builtin_amdgcn_endpgm(); } pend = OOB; wave_wait(rx, fXo, fb, spins); } \
        LDA4(xv, fXv, fs); \
        XALL(so, xb, ld); \
        WAITV(10, xv); sst4(rx, pend, fl, 1u); \
        VMB(6); prime(); \
    } \
    so = __builtin_amdgcn_readfirstlane(xv); \
    if (__builtin_expect(so == 0, 0) && fs != OOB) { \
        _Pragma("unroll 1") for (int k_ = 0; k_ < 8 && so == 0; ++k_) { __builtin_amdgcn_s_sleep(4); so = __builtin_amdgcn_readfirstlane(sld4(rx, fs, fXv)); MEMFENCE(); } \
    } \
    so += E_W; PIN_S(so); rt_until(so)

constexpr int QL_W = 81920;
__device__ __forceinline__ void qk_role(const MegaArgs& A, char* smem, const int L, const int h, const int id) {
    WORK_IDS;
    bf16x8 wf[6][20];
    char* sW = smem + QL_W + w * 20480 + l * 16;
    load_qk<0>(smem, w, l, go, A.Wq + h * DH_, A.Wk + h * DH_, [&](int m) { return 6 * w + m; }, wf);
#pragma unroll
    for (int i = 0; i < 20; ++i) *(bf16x8*)(sW + i * 1024) = wf[5][i];
    GO_ON;
    const unsigned fI = l == 0 ? xm(FI_OFF + (h & 15) * 64 + (4 * L + w) * 4, hside(h)) : OOB;
    const unsigned po0 = 2 * QK_OFF + ((L * 16 + (h & 15)) * 4 + (w & 2)) * 16384 + (w & 1) * 3072 + hside(h) * 8192;
    const int it0 = L * A.lag;
    const unsigned l16s = l16b;
    NEXT_DECL;
    unsigned vx = l16b, ld = lds0, po;
    bf16x8 xw[3], wl[2];
    f32x4 acc[2][6];
    u32x4 o[2];
    auto prime = [&]() { xw[0] = *(const bf16x8*)(smem + vx); xw[1] = *(const bf16x8*)(smem + vx + 2048); };
    auto tail = [&]() {
#pragma unroll
        for (int f = 0; f < 3; ++f) { CVT2(o[f & 1][0], o[f & 1][1], acc[1][2 * f]); CVT2(o[f & 1][2], o[f & 1][3], acc[1][2 * f + 1]); sst16(rx, po, l16s + f * 1024, o[f & 1]); SCHED0(); }
    };
    prime();
    wl[0] = *(const bf16x8*)sW;
#pragma unroll 1
    for (;; ++s) {
        SCHED0();
        sfor<0, 240>([&](auto G) {
            constexpr int g = decltype(G)::value, i = g / 6, tt = g % 6, ks = i % 20;
            f32x4& d = acc[i / 20][tt];
            if constexpr (tt == 5) { if (ks == 0) MF_VV0(d, wl[i & 1], xw[i % 3]); else MF_VV(d, wl[i & 1], xw[i % 3]); }
            else if constexpr (20 * tt + ks < 64) { if (ks == 0) MF_AV0(d, wf[tt][ks], xw[i % 3]); else MF_AV(d, wf[tt][ks], xw[i % 3]); }
            else { if (ks == 0) MF_VV0(d, wf[tt][ks], xw[i % 3]); else MF_VV(d, wf[tt][ks], xw[i % 3]); }
            SCHED0();
            if constexpr (tt == 1 && i < 38) xw[(i + 2) % 3] = *(const bf16x8*)(smem + vx + (i + 2) % 20 * 2048 + (i + 2) / 20 * 1024);
            if constexpr (tt == 3) wl[~i & 1] = *(const bf16x8*)(sW + (i + 1) % 20 * 1024);
            NEXT_ADDR(2, 6);
            if (g == 4) { PIN_S(s); po = (s & (TR - 1)) << 21; PIN_S(po); }
            if (g == 10) { so = (po >> 8) & 8192; PIN_S(so); }
            if (g == 16) { po = (po0 ^ so) + po; PIN_S(po); }
            if (g == 58) { VMW(0); }
            if (g == 60) asm volatile("s_barrier" ::: "memory");
            if (g == 62) { PIN_S(s); pend = s + it0; so = pend & 7; PIN_S(pend); PIN_S(so); }
            if (g == 64) { pend = (pend >> 3) << 4; PIN_S(pend); }
            if (g == 66) { pend = (pend | so) << 10; PIN_S(pend); }
            if (g == 68) { ld = 2 * lds0 + 40960 - ld; PIN_S(ld); }
            if constexpr (g >= 122 && g < 140) {
                constexpr int f = (g - 122) / 6;
                if (tt == 2) CVT2(o[0][0], o[0][1], acc[0][2 * f]);
                if (tt == 4) CVT2(o[0][2], o[0][3], acc[0][2 * f + 1]);
                if (tt == 0) sst16(rx, po, l16s + f * 1024, o[0]);
            }
            if (g == 140) { po += 16384; PIN_S(po); }
            NEXT_LOOK(0, ld);
            {
                constexpr int a[10] = {186, 190, 194, 198, 202, 206, 214, 218, 222, 226};
#pragma unroll
                for (int e = 0; e < 10; ++e) {
                    if (g == a[e]) { if (e % 5 == 0) XSET(so, xs, ld); else XADV(so); }
                    if (g == a[e] + (e == 5 ? 6 : 2)) XDMA(so, e / 5);
                }
            }
            if (g == 236) { vx ^= 40960; PIN_V(vx); }
            SCHED0();
        });
        NEXT_GO(5, ld, fI);
    }
}

constexpr int OL_V = 40960, OL_W = OL_V + 12288;
__device__ __forceinline__ void vo_role(const MegaArgs& A, char* smem, const int L, const int h, const int id) {
    WORK_IDS;
    bf16x8 wf[3][20], wo[10][6];
    char* sW = smem + OL_W + w * 24576 + l * 16;
    {
        load_vo<0>(smem, w, l, go, A.Wv + h * DH_, A.Wo + (size_t)(h * DH_) * D_, wf, [&](int k, int ct, int) -> bf16x8& { return wo[ct][k]; });
#pragma unroll
        for (int i = 0; i < 24; ++i) *(bf16x8*)(sW + i * 1024) = wo[4 + i / 8 * 2 + (i & 1)][2 + (i >> 1 & 3)];
    }
    GO_ON;
    const unsigned fU = l == 0 ? lp(SG_FL + FL_U + h * 64 + w * 4) : OOB;
    const unsigned l16s = l16b;
    const unsigned ua = xm(UT_OFF + (320 * w + h) * 1024, L) + l16s, ub = ua ^ (w == 3 ? 8192 : 0);
    char* sV = smem + OL_V + l * 16;
    char* sVw = sV + w * 1024;
    char* sVh = sV + 4096 + (w >> 1) * 1024 + (w & 1) * 8;
    NEXT_DECL;
    unsigned uo;
    constexpr int XW = 4;
    bf16x8 xw[XW], wl[4];
    f32x4 d[4][2];
    u32x4 o[2];
    auto prime = [&]() {
#pragma unroll
        for (int i = 0; i < XW - 1; ++i) xw[i] = *(const bf16x8*)(smem + l16b + i * 2048);
    };
    auto tail = [&]() {
#pragma unroll
        for (int f = 0; f < 2; ++f) { CVT2(o[f][0], o[f][1], d[f][0]); CVT2(o[f][2], o[f][3], d[f][1]); sst16(rx, uo + f * 65536, ub, o[f]); SCHED0(); }
    };
    prime();
#pragma unroll 1
    for (;; ++s) {
        f32x4 acc[2][3];
        u32x4 vf[2][6];
        unsigned cur = xb;
        SCHED0();
        sfor<0, 240>([&](auto G) {
            constexpr int g = decltype(G)::value;
            constexpr int rt = g < 120 ? g / 60 : (g < 144 ? 0 : (g < 168 ? 1 : g & 1));
            constexpr int ct = g < 144 ? g & 3 : (g < 168 ? (g - 144) / 12 * 2 + (g & 1) : (g - 120) / 24 * 2 + (g >> 1 & 1));
            constexpr int kk = g < 144 ? (g + 24) % 24 / 4 : (g < 168 ? g % 12 / 2 : g % 24 / 4), wi = (g - 168) / 24 * 8 + (g % 24 - 8) / 2;
            if constexpr (g < 120) { if (g % 60 < 3) MF_AV0(acc[rt][g % 3], wf[g % 3][0], xw[g / 3 % XW]); else MF_AV(acc[rt][g % 3], wf[g % 3][g % 60 / 3], xw[g / 3 % XW]); }
            else if constexpr (ct >= 4 && kk >= 2) MF_VV(d[ct & 3][rt], vf[rt][kk], wl[wi & 3]);
            else if constexpr (ct == 0 && kk < 4) { if (kk == 0) MF_VA0(d[0][rt], vf[rt][0], wo[0][0]); else MF_VA(d[0][rt], vf[rt][kk], wo[0][kk]); }
            else { if (kk == 0) MF_VV0(d[ct & 3][rt], vf[rt][0], wo[ct][0]); else MF_VV(d[ct & 3][rt], vf[rt][kk], wo[ct][kk]); }
            SCHED0();
            if constexpr (g % 3 == 1 && g / 3 + XW - 1 < 40) { constexpr int i = g / 3 + XW - 1; xw[i % XW] = *(const bf16x8*)(smem + l16b + i % 20 * 2048 + i / 20 * 1024); }
            NEXT_ADDR(2, 3);
            if (g == 24) { PIN_S(s); uo = (s & (TR - 1)) << 22; PIN_S(uo); }
            if (g == 48) { VMW(0); }
            if (g == 50) asm volatile("s_barrier" ::: "memory");
            if (g == 51) { pend = cur ^ (L << 13); PIN_S(pend); }
            if constexpr (g >= 60 && g < 140 && g % 60 < 40) {
                constexpr int r = g / 60 - 1, q = r ? g - 120 : (g % 3 == 1 ? -1 : (g - 60) / 3 * 2 + g % 3 / 2);
                if (q == 1) CVT2(o[0][0], o[0][1], acc[r][0]);
                if (q == 2) CVT2(o[0][2], o[0][3], acc[r][1]);
                if (q == 3) CVT2(o[1][0], o[1][1], acc[r][2]);
                if (q == 4) *(u32x4*)(sVw + r * 6144) = o[0];
                if (q == 5) *(u32x2*)(sVh + r * 6144) = u32x2{o[1][0], o[1][1]};
                if (q == 11) asm volatile("s_waitcnt lgkmcnt(%0)\n\ts_barrier" :: "n"(r ? 0 : 3) : "memory");
                if constexpr (q >= 12 && q < 18) vf[r][q - 12] = *(const u32x4*)(sV + r * 6144 + (q - 12) * 1024);
            }
            if constexpr (g >= 168 && g % 24 < 18 && g % 24 != 8 && !(g & 1)) {
                constexpr int j = (g - 168) / 24 * 8 + g % 24 / 2 - (g % 24 > 8);
                wl[j & 3] = *(const bf16x8*)(sW + j * 1024);
            }
            if constexpr (g >= 156) {
                constexpr int p = g < 168 ? 0 : (g - 144) / 24, q = g < 168 ? g - 156 : g % 24, c0 = 2 * p & 3;
                if (q == 1) CVT2(o[0][0], o[0][1], d[c0][0]);
                if (q == 3) CVT2(o[0][2], o[0][3], d[c0][1]);
                if (q == 5) sst16(rx, uo, p ? ub : ua, o[0]);
                if (q == 7) CVT2(o[1][0], o[1][1], d[c0 + 1][0]);
                if (q == 8) { uo += 65536; PIN_S(uo); }
                if (q == 9) CVT2(o[1][2], o[1][3], d[c0 + 1][1]);
                if (q == 11) sst16(rx, uo, p ? ub : ua, o[1]);
            }
            if constexpr (g == 168 || g == 181 || g == 205 || g == 229) { uo += 65536; PIN_S(uo); }
            NEXT_LOOK(4, lds0);
            {
                constexpr int a[10] = {186, 189, 192, 200, 205, 209, 213, 216, 224, 231}, b[10] = {188, 191, 196, 204, 207, 212, 215, 220, 228, 233};
#pragma unroll
                for (int e = 0; e < 10; ++e) {
                    if (g == a[e]) { if (e % 5 == 0) XSET(so, xs, lds0); else XADV(so); }
                    if (g == b[e]) XDMA(so, e / 5);
                }
            }
            SCHED0();
        });
        NEXT_GO(7, lds0, fU);
    }
}

#define G_B 83
#define BSLP 6
constexpr int BL_Q = 40960, BL_P = BL_Q + 49152;
__device__ __forceinline__ void b_role(const MegaArgs& A, char* smem, const int L, const int hb, const int id) {
    WORK_IDS;
    bf16x8 wf[4][20];
    unsigned dst[4];
    const int sw = (l16 & 6) << 3;
    {
        const void* const base[4] = {A.wqkv + 80 * hb, A.wqkv + 80 * hb, A.wqkv + 80 * hb, A.wqkv + 80 * hb};
        unsigned vo[4];
        int rp[4];
#pragma unroll
        for (int m = 0; m < 4; ++m) {
            const int idx = min((4 * m + w) * 64 + l, 959), c = idx % 30, T = min(4 * m + w, 14), e = 16 * (T % 5) + 4 * lq;
            vo[m] = (idx / 30) * 3840 + (c / 10) * (2 * D_) + (c % 10) * 16;
            rp[m] = (4 * lq + (l16 >> 2)) * 480 + 32 * T + (l & 3) * 8;
            dst[m] = lds_addr(smem) + BL_Q + (T / 5 * 4 + e / 20) * 4096 + l16 * 64 + ((e % 20 * 2) ^ sw);
        }
        load_coop<4, 0, 4>(smem, w, go, base, vo, 32 * 3840, rp, 7680, wf);
    }
    GO_ON;
    f32x4 cs[4];
    u32x4 one = {0x3f803f80u, 0x3f803f80u, 0x3f803f80u, 0x3f803f80u};
    {
        asm volatile("s_nop 1" : "+v"(one));
        sfor<0, 80>([&](auto G) { constexpr int g = decltype(G)::value, tt = g & 3, ks = g >> 2;
            if (ks == 0) MF_AV0(cs[tt], wf[tt][0], one); else if (tt < 3 || ks < 4) MF_AV(cs[tt], wf[tt][ks], one); else MF_VV(cs[tt], wf[tt][ks], one); });
        asm volatile("s_nop 7" : "+v"(cs[0]), "+v"(cs[1]), "+v"(cs[2]), "+v"(cs[3]));
    }
#pragma unroll
    for (int i = 0; i < 12; ++i) *(u32x4*)(smem + BL_Q + i * 4096 + t * 16) = u32x4{0u, 0u, 0u, 0u};
    const bool odd = l16 & 1;
    f32x4 wm[3];
#pragma unroll
    for (int e = 0; e < 8; ++e) { const int d = l16 + 16 - 16 * (e >> 2) - 4 * lq - (e & 3); wm[e >> 2][e & 3] = d >= 0 && d <= 14 && !(d & 1) ? 0.f : -INFINITY; }
    wm[2] = f32x4{-INFINITY, -INFINITY, -INFINITY, -INFINITY};
    const unsigned ps0 = odd ? 0x01000c0cu : 0x0c0c0100u, ps1 = odd ? 0x03020c0cu : 0x0c0c0302u;
    unsigned qa = lds_addr(smem) + BL_Q + w * 4096 + l16 * 64 + ((lq * 16) ^ sw), qb = qa ^ 2048, va[6];
    {
        const int row = 4 * lq + (l16 >> 2), vs = (row & 6) << 3;
        va[0] = va[2] = lds_addr(smem) + BL_Q + (8 + w) * 4096 + row * 64 + ((8 * (w & 1) + 8 * (l & 3)) ^ vs);
        va[1] = va[3] = lds_addr(smem) + BL_Q + (8 + w) * 4096 + row * 64 + ((32 * (~w & 1) + 8 * (l & 3)) ^ vs);
        va[4] = va[0] ^ 2048; va[5] = va[1] ^ 2048;
#pragma unroll
        for (int e = 0; e < 6; ++e) PIN_V(va[e]);
    }
    const int g0 = (20 * hb + 5 * w + (w & 1) + (lq & 2)) >> 1, g1 = (20 * hb + 5 * w + 4 * (~w & 1)) >> 1;
    const unsigned oo0 = lp(SG_OB + (g0 >> 2) * 2048 + (lq & 1) * 1024 + (g0 & 3) * 256 + l16 * 16);
    const unsigned oo1 = lq < 2 ? lp(SG_OB + (g1 >> 2) * 2048 + lq * 1024 + (g1 & 3) * 256 + l16 * 16 + (w & 1) * 8) : OOB;
    const unsigned fO = t < 4 ? lp(SG_FL + FL_O + (8 * t + hb) * 128) : OOB;
    const unsigned st0 = lp(SG_ST + (4 * (l >> 5) + w) * 256 + (l & 31) * 8), st2 = l < 32 ? st0 + 4096 : OOB;
    unsigned fXv = xm(FX_OFF + (hb >> 1) * 64, L), xv, X = rtime() - 2 * PER, xs = OOB;
    PIN_V(fXv);
    const float z2 = l < 32 ? 1.f : 0.f;
    f32v2* sP = (f32v2*)(smem + BL_P) + (l & 31);
    unsigned sK = lds_addr(smem) + BL_P + 1024, sKw = sK + 4 * w;
    PIN_V(sK); PIN_V(sKw);
    unsigned pend = OOB, sb = seg_base(L, 0), so, fs = fx_step(1 + L * A.lag);
    for (int s = 0; s < NSEG_; ++s) { sst16(rx, seg_base(L, s), oo0, u32x4{0u, 0u, 0u, 0u}); sst8(rx, seg_base(L, s), oo1, u32x2{0u, 0u}); }
    u32x2 st[3];
    auto look = [&](int e, unsigned b, unsigned f) { if (e == 0) LDA8(st[0], st0, b, 0); if (e == 1) LDA8(st[1], st0, b, 2048); if (e == 2) LDA8(st[2], st2, b, 0); if (e == 3) LDA4(xv, fXv, f); };
#define SEEN(n) asm volatile("s_waitcnt vmcnt(%4)" : "+v"(st[0]), "+v"(st[1]), "+v"(st[2]), "+v"(xv) : "n"(n) : "memory")
#define SWAP2(op, a, b) asm volatile(op " %0, %1" : "+v"(a), "+v"(b))
    int s;
    SZERO(s);
#pragma unroll 1
    do {
        if (xs == OOB) {
            bool e = (int)(rtime() - X - E_B) >= 0;
#pragma unroll
            for (int i = 0; i < 4; ++i) look(i, e ? sb : OOB, e ? fs : OOB);
            VMB(4);
            sst4(rx, pend, fO, 1u);
            pend = OOB;
            SEEN(1);
#pragma unroll 1
            for (;;) {
                xv = __builtin_amdgcn_readfirstlane(xv);
                X = xv ? xv : X;
                LIVE;
                if (e) { if (!BAD(umax3(st[0][0], st[1][0], st[2][0]))) break; __builtin_amdgcn_s_sleep(BSLP); }
                e = true;
                rt_until(X + E_B);
#pragma unroll
                for (int i = 0; i < 4; ++i) look(i, sb, fs);
                SEEN(0);
            }
            XSET(so, sb + lp(SG_X1), lds0); XDMA(so, 0); XDMA(so, 1); XADV(so); XDMA(so, 0); XDMA(so, 1);
        }
        VMB(2);
        f32x4 acc[4][2];
        unsigned cur;
        float rd[2];
        u32x2 y[2];
        bf16x8 kf[3], qf[2];
        u32x4 k4;
#define MUL2(x, i, r) do { asm volatile("v_mul_f32 %0, %1, %0" : "+v"(x[i]) : "v"(r)); x[i + 1] *= r; USE_V(x[i + 1]); } while (0)
#define FMA2(x, i, c, m) do { asm volatile("v_fma_f32 %0, -%2, %1, %0" : "+v"(x[i]) : "v"(c[i]), "v"(m)); x[i + 1] = __builtin_fmaf(-m, c[i + 1], x[i + 1]); USE_V(x[i + 1]); } while (0)
#define ADD1(d, x, z) asm volatile("v_add_f32 %0, %1, %2" : "=v"(d) : "v"(x), "v"(z))
        auto fin = [&](int e, int u) {
            if (u < 6 && u % 3 < 2) MUL2(acc[e][u / 3], 2 * (u % 3), rd[u / 3]);
            if (u % 3 == 2) CVT2(y[u / 3][0], y[u / 3][1], acc[e][u / 3]);
            if (u == 6) { LDS_AT(u32x2, dst[e]) = y[0]; LDS_AT(u32x2, dst[e] + 1024) = y[1]; dst[e] ^= 2048; USE_V(dst[e]); }
        };
        {
            bf16x8 xw[6][2];
#pragma unroll
            for (int e = 0; e < 4; ++e) xw[e >> 1][e & 1] = *(const bf16x8*)(smem + l16b + e * 1024);
            SCHED0();
            u64 now, bad;
            float a, b, c, d, mw, mb[2];
            unsigned lb, lf;
            f32v2 p0, p1, p2, p3;
            sfor<0, 160>([&](auto G) {
                constexpr int g = decltype(G)::value, ks = g < 112 ? g / 8 : (g < 124 ? 14 + (g - 112) / 6 : (g < 148 ? 16 + (g - 124) % 8 / 2 : 14 + (g - 148) / 2));
                constexpr int tt = g < 112 ? g % 8 / 2 : (g < 124 ? (g - 112) % 6 / 2 : (g < 148 ? (g - 124) / 8 : 3)), rt = g & 1;
                constexpr int q = g >= 112 || g % 8 == 1 || g % 8 == 3 || g % 8 == 7 ? -1 : g / 8 * 5 + (g % 8 < 4 ? g % 8 / 2 : g % 8 - 2);
                if (ks == 0) MF_AV0(acc[tt][rt], wf[tt][0], xw[0][rt]);
                else if (tt < 3 || ks < 4) MF_AV(acc[tt][rt], wf[tt][ks], xw[ks % 6][rt]); else MF_VV(acc[tt][rt], wf[tt][ks], xw[ks % 6][rt]);
                SCHED0();
                constexpr int rq = g < 112 ? (g % 8 == 1 || g % 8 == 3 ? 2 * ks + 4 + g % 8 / 2 : -1) : (g < 124 && g % 3 != 0 ? 32 + (g - 112) / 3 * 2 + (g - 112) % 3 : -1);
                if constexpr (rq >= 0) xw[rq / 2 % 6][rq & 1] = *(const bf16x8*)(smem + l16b + rq * 1024);
                if (q == 0 || q == 5 || q == 11) XADV(so);
                if (q == 1 || q == 6 || q == 13) XDMA(so, 0);
                if (q == 3 || q == 8 || q == 15) XDMA(so, 1);
                if (q == 10) VMB_LATE(4);
                if (q == 2) { ADD1(c, st[0][1], st[1][1]); a = __uint_as_float(st[0][0]) + __uint_as_float(st[1][0]); USE_V(a); }
                if (q == 4) { ADD1(b, a, st[2][0]); a += __uint_as_float(st[2][0]); USE_V(a); }
                if (q == 7) { SWAP2("v_permlane32_swap_b32", a, b); SCHED0(); c += __uint_as_float(st[2][1]); USE_V(c); }
                if (q == 9) { mw = (a + b) * 0.2f; USE_V(mw); }
                if (q == 12) { a = __uint_as_float(st[0][0]) - mw; b = __uint_as_float(st[1][0]) - mw; USE_V(a); USE_V(b); }
                if (q == 14) { d = (__uint_as_float(st[2][0]) - mw) * z2; USE_V(d); }
                if (q == 16) { a = __builtin_fmaf(b, b, a * a); USE_V(a); }
                if (q == 17) { c = __builtin_fmaf(32.f, __builtin_fmaf(d, d, a), c); USE_V(c); }
                if (q == 18) { b = c; PIN_V(b); }
                if (q == 19) SWAP2("v_permlane32_swap_b32", c, b);
                if (q == 21) { c += b; sP[w * 32] = f32v2{mw, c}; }
                if (q == 22) { cur = sb; sb = (s + 1) << 19; PIN_S(cur); PIN_S(sb); }
                if (q == 23) { lb = (sb >> 8) & 8192; PIN_S(lb); }
                if (q == 24) { sb = (sb | lb) + 2 * SG_OFF; PIN_S(sb); }
                if (q == 26) { sb ^= L << 13; X += PER; PIN_S(sb); PIN_S(X); }
                if (q == 27) { fs += 256; lb = fs & 8192; PIN_S(fs); PIN_S(lb); }
                if (q == 28) { fs += lb; PIN_S(fs); }
                if (q == 30) asm volatile("s_waitcnt vmcnt(4) lgkmcnt(2)\n\ts_barrier" ::: "memory");
                if (q == 31) sst4(rx, pend, fO, 1u);
                if (q == 32) { p0 = sP[0]; p1 = sP[32]; }
                if (q == 33) { p2 = sP[64]; p3 = sP[96]; }
                if (q == 36) ADD1(a, p0[0], p1[0]);
                if (q == 37) { b = p2[0] + p3[0]; USE_V(b); }
                if (q == 38) { mw = (a + b) * 0.25f; USE_V(mw); }
                if (q == 39) { a = p0[0] - mw; PIN_V(a); b = p1[0] - mw; PIN_V(b); }
                if (q == 40) { c = p2[0] - mw; PIN_V(c); }
                if (q == 41) { mb[0] = rb(mw); PIN_V(mb[0]); }
                if (q == 42) { d = p3[0] - mw; PIN_V(d); mb[1] = mb[0]; PIN_V(mb[1]); }
                if (q == 43) { a = __builtin_fmaf(b, b, a * a); PIN_V(a); }
                if (q == 44) { SWAP2("v_permlane16_swap_b32", mb[0], mb[1]); ADD1(b, p0[1], p1[1]); }
                if (q == 45) { a = __builtin_fmaf(c, c, a); PIN_V(a); }
                if (q == 50) VMB(1);
                if (q == 55) { a = __builtin_fmaf(d, d, a); PIN_V(a); }
                if (g == 73) ADD1(c, p2[1], p3[1]);
                if (g == 75) ADD1(b, b, c);
                if (g == 89) { a = __builtin_fmaf(160.f, a, b); PIN_V(a); }
                if (g == 91) { a *= 1.0f / D_; PIN_V(a); }
                if (q == 60) asm volatile("v_cvt_pk_bf16_f32 %0, 0, %0" : "+v"(a));
                if (g == 97) { a += 1e-5f; PIN_V(a); }
                if (g == 99) asm volatile("v_cvt_pk_bf16_f32 %0, 0, %0" : "+v"(a));
                if (q == 65) { a = __builtin_amdgcn_sqrtf(a); PIN_V(a); }
                if (g == 105) asm volatile("v_cvt_pk_bf16_f32 %0, 0, %0" : "+v"(a));
                if (q == 66) { rd[0] = __builtin_amdgcn_rcpf(a); PIN_V(rd[0]); }
                if (q == 67) { rd[1] = rd[0]; PIN_V(rd[1]); }
                if (q == 69) SWAP2("v_permlane16_swap_b32", rd[0], rd[1]);
                if constexpr (g >= 72 && g < 104 && q >= 0 && g % 8) {
                    constexpr int e = (g % 8 < 5 ? g % 8 + 2 : 2 * (g % 8) - 10) + (g >= 88), i = g >> 2 & 2;
                    FMA2(acc[e >> 1][e & 1], i, cs[e >> 1], mb[e & 1]);
                }
                if (g == G_B) asm volatile("s_memrealtime %0" : "=s"(now));
                if (g == G_B + 4) asm volatile("s_waitcnt lgkmcnt(0)" : "+s"(now));
                if (g == G_B + 12) { lf = (unsigned)now - X; PIN_S(lf); }
                if (g == G_B + 20) { lf -= E_B; PIN_S(lf); }
                if (g == G_B + 26) asm volatile("s_cmp_lt_i32 %1, 63\n\ts_cselect_b32 %0, %0, -1" : "+s"(lf) : "s"(s) : "scc");
                if (g == G_B + 28) asm volatile("s_cmp_ge_i32 %1, 0\n\ts_cselect_b32 %0, %2, %4\n\ts_cselect_b32 %1, %3, %4" : "=&s"(lb), "+s"(lf) : "s"(sb), "s"(fs), "n"(OOB) : "scc");
                if constexpr (g >= G_B + 29 && g < G_B + 33) look(g - G_B - 29, lb, lf);
                if constexpr (g >= 132 && g < 155 && (g - 132) % 8 < 7) fin((g - 132) / 8, (g - 132) % 8);
                if (g == 155) LDS_BARRIER();
                if (g == 156) { kf[0] = LDS_AT(bf16x8, qb + 17408); qf[0] = LDS_AT(bf16x8, qa); }
                if (g == 157) { kf[1] = LDS_AT(bf16x8, qa + 16384); qf[1] = LDS_AT(bf16x8, qa + 1024); }
                if (g == 158) { kf[2] = LDS_AT(bf16x8, qa + 17408); SEEN(0); }
                if (g == 159) { lf = umax3(st[0][0], st[1][0], st[2][0]); bad = __ballot(lf == ~0u); PIN_S(bad); }
                SCHED0();
            });
            xv = __builtin_amdgcn_readfirstlane(xv);
            asm volatile("s_cmp_eq_u64 %1, 0\n\ts_cselect_b32 %0, %0, %2" : "+s"(lb) : "s"(bad), "n"(OOB) : "scc");
            LDS_AT(unsigned, sKw) = lb;
            X = xv ? xv : X;
            PIN_S(X);
        }
        {
            constexpr float SC = 0.22360679774997896f * 1.4426950408889634f;
            bf16x8 vx[2][2];
            f32x4 sc[4];
            SCHED0();
            fin(3, 0); fin(3, 1);
            SCHED0();
            MF_VVC(sc[0], kf[0], qf[0], wm[2]); SCHED0(); fin(3, 2); SCHED0();
            MF_VVC(sc[1], kf[1], qf[0], wm[1]); SCHED0(); fin(3, 3); SCHED0();
            MF_VVC(sc[2], kf[1], qf[1], wm[0]); SCHED0(); fin(3, 4); SCHED0();
            MF_VVC(sc[3], kf[2], qf[1], wm[1]); SCHED0(); fin(3, 5); fin(3, 6);
            SCHED0();
            float sv[4][2];
            float m0, m1;
#define MAXQ(m, x, y, p, q) asm volatile("v_max_f32 %1, %5, %6\n\tv_max_f32 %2, %7, %8\n\tv_max_f32 %3, %9, %10\n\tv_max_f32 %4, %11, %12\n\tv_max3_f32 %0, %1, %2, %3\n\tv_max_f32 %0, %0, %4" \
    : "=&v"(m), "=&v"(x[0]), "=&v"(x[1]), "=&v"(y[0]), "=&v"(y[1]) : "v"(p[0]), "v"(p[1]), "v"(p[2]), "v"(p[3]), "v"(q[0]), "v"(q[1]), "v"(q[2]), "v"(q[3]))
            MAXQ(m0, sv[0], sv[1], sc[0], sc[1]);
            MAXQ(m1, sv[2], sv[3], sc[2], sc[3]);
            LDS_BARRIER();
            k4 = LDS_AT(u32x4, sK);
            SCHED0();
            auto tr = [](unsigned a) { return __builtin_amdgcn_ds_read_tr16_b64_v4i16((lds_b4*)a); };
#pragma unroll
            for (int dt = 0; dt < 2; ++dt) { vx[0][dt] = cat8(tr(va[4 + dt] + 1024), tr(va[dt])); vx[1][dt] = cat8(tr(va[2 + dt]), tr(va[dt] + 1024)); }
            SCHED0();
            red_max_q2(m0, m1);
            m0 *= -SC; m1 *= -SC;
            USE_V(m0); USE_V(m1);
            SCHED0();
            xs = __builtin_amdgcn_readfirstlane(max(umax3(k4[0], k4[1], k4[2]), k4[3]));
            u32x4 pt[2];
            f32x4 o[2][2], sm[2];
#pragma unroll
            for (int qt = 0; qt < 2; ++qt) {
#pragma unroll
                for (int e = 0; e < 4; ++e) sv[2 * qt + (e >> 1)][e & 1] = __builtin_amdgcn_exp2f(__builtin_fmaf(sv[2 * qt + (e >> 1)][e & 1], SC, qt ? m1 : m0));
                if (!qt) XSET(so, xs + lp(SG_X1), lds0);
#pragma unroll
                for (int e = 0; e < 2; ++e) {
                    const unsigned p = pk2(sv[2 * qt + e][0], sv[2 * qt + e][1]);
                    pt[qt][2 * e] = __builtin_amdgcn_perm(p, p, ps0);
                    pt[qt][2 * e + 1] = __builtin_amdgcn_perm(p, p, ps1);
                }
                USE_V(pt[qt]);
                SCHED0();
                XDMA(so, qt);
                asm volatile("s_nop 0");
                MF_VV0(sm[qt], one, pt[qt]);
                if (!qt) MF_VV0(o[0][0], vx[0][0], pt[0]);
                SCHED0();
            }
            XADV(so);
            MF_VV0(o[1][0], vx[1][0], pt[1]);
            PIN_V(sm[0]);
            const float s0 = __builtin_amdgcn_rcpf(sm[0][0]);
            USE_V(s0);
            XDMA(so, 0);
            MF_VV0(o[0][1], vx[0][1], pt[0]);
            SCHED0();
            PIN_V(o[0][0]);
            MUL2(o[0][0], 0, s0);
            SCHED0();
            MF_VV0(o[1][1], vx[1][1], pt[1]);
            SCHED0();
            PIN_V(sm[1]);
            const float s1 = __builtin_amdgcn_rcpf(sm[1][0]);
            USE_V(s1);
            MUL2(o[0][0], 2, s0);
            XDMA(so, 1);
            PIN_V(o[1][0]);
            MUL2(o[1][0], 0, s1); MUL2(o[1][0], 2, s1);
            {
                const auto a = __builtin_amdgcn_permlane16_swap(pk2(o[0][0][0], o[0][0][1]), pk2(o[1][0][0], o[1][0][1]), false, false);
                const auto b = __builtin_amdgcn_permlane16_swap(pk2(o[0][0][2], o[0][0][3]), pk2(o[1][0][2], o[1][0][3]), false, false);
                sst16(rx, cur, oo0, u32x4{a[0], b[0], a[1], b[1]});
            }
            SCHED0();
            asm volatile("" : "+v"(o[0][1]), "+v"(o[1][1]));
            o[0][1] *= s0; o[1][1] *= s1;
            {
                const auto a = __builtin_amdgcn_permlane16_swap(pk2(o[0][1][0], o[0][1][1]), pk2(o[1][1][0], o[1][1][1]), false, false);
                const auto b = __builtin_amdgcn_permlane16_swap(pk2(o[0][1][2], o[0][1][3]), pk2(o[1][1][2], o[1][1][3]), false, false);
                sst8(rx, cur, oo1, u32x2{a[0], b[0]});
            }
            SCHED0();
            wm[2] = wm[0];
            qa ^= 2048; qb ^= 2048;
            PIN_V(wm[2]); PIN_V(qa); PIN_V(qb);
#pragma unroll
            for (int e = 0; e < 6; ++e) { va[e] ^= 2048; PIN_V(va[e]); }
        }
        pend = cur;
    } while (++s < NSEG_);
    VMB(0);
    sst4(rx, pend, fO, 1u);
    SPIN_END;
}

__device__ __forceinline__ void bo_role(const MegaArgs& A, char* smem, const int L, const int j, const int id) {
    WORK_IDS;
    const int rt = w & 1, hf = w >> 1;
    if (id == 31 && w == 0) probe(A, go, l, id);
    INIT_BEGIN(id >> 5);
    if (L) { INIT_END(24 + j); }
    bf16x8 wf[5][20];
    {
        const void* const base[3] = {A.wo + 160 * j, A.wo + 160 * j, A.wo + 160 * j};
        unsigned vo[3];
        int rp[5];
#pragma unroll
        for (int m = 0; m < 3; ++m) { const int idx = min((4 * m + w) * 64 + l, 639); vo[m] = (idx / 20) * (2 * D_) + (idx % 20) * 16; }
#pragma unroll
        for (int m = 0; m < 5; ++m) { rp[m] = (8 * lq + (l16 >> 2)) * 320 + 32 * (m < 4 ? 4 * hf + m : 8 + hf) + (l & 3) * 8; }
        load_coop<3, 0, 5>(smem, w, go, base, vo, 64 * D_, rp, 1280, wf);
    }
    INIT_SEEN;
    const unsigned x0 = lp((5 * j + 2 * hf) * 2048 + rt * 1024 + l16b), x1 = lp((5 * j + 2 * hf + 1) * 2048 + rt * 1024 + l16b), x2 = lp((5 * j + 4) * 2048 + rt * 1024 + l16b + hf * 8);
    const unsigned fO = l < 8 ? lp(SG_FL + FL_O + (8 * j + l) * 128) : OOB, fX = t < 8 ? lp(SG_FL + FL_X + (4 * (t & 3) + j) * 128) + (t & 4) * 2048 : OOB;
    const unsigned sr = lp(SG_ST + ST_2 + rt * 1024 + lq * 256 + l16 * 8), sw = l < 16 ? sr + j * 256 + hf * 128 : OOB;
    auto flag = [&](unsigned b) { sst4(rx, b & ~8192u, fX ^ (b & 8192u), 1u); };
    const rsrc_t ro = mk_rsrc(A.out, S_ * D_ * 2);
    const unsigned oo = ((16 * rt + l16) * D_ + 160 * j + 64 * hf + 4 * lq) * 2, oo2 = oo + (128 - 48 * hf) * 2;
    const char* sX = smem + rt * 1024 + l16b;
    unsigned pend = OOB, sb = seg_base(L, 0), so, xs = OOB;
    unsigned* sK = (unsigned*)(smem + 40960);
    auto first = [&](unsigned b) { XSET(so, b + lp(SG_OB), lds0); XDMA(so, 0); XDMA(so, 1); XADV(so); XDMA(so, 0); XDMA(so, 1); XADV(so); XDMA(so, 0); XDMA(so, 1); };
    int s;
    SZERO(s);
#pragma unroll 1
    do {
        if (xs == OOB) {
            unsigned pv;
            LDA4(pv, fO, sb);
            VMB(1);
            flag(pend);
            pend = OOB;
            WAITV(1, pv);
            if (__ballot(pv == 0) & 255) wave_wait(rx, fO, sb, spins);
            first(sb);
            VMB(4);
        } else VMB(5);
        f32x4 acc[5], xf[5];
        u32x4 r0, r1, k4;
        u32x2 r2;
        unsigned cur;
        {
            bf16x8 xw[3];
            unsigned sx, pv;
            xw[0] = *(const bf16x8*)sX; xw[1] = *(const bf16x8*)(sX + 2048);
            SCHED0();
            sfor<0, 100>([&](auto G) {
                constexpr int g = decltype(G)::value, ks = g / 5, tt = g % 5;
                if constexpr (20 * tt + ks < 64) { if (ks == 0) MF_AV0(acc[tt], wf[tt][0], xw[0]); else MF_AV(acc[tt], wf[tt][ks], xw[ks % 3]); }
                else { if (ks == 0) MF_VV0(acc[tt], wf[tt][0], xw[0]); else MF_VV(acc[tt], wf[tt][ks], xw[ks % 3]); }
                SCHED0();
                if (g == 0 || g == 7) XADV(so);
                if (g == 2 || g == 12) XDMA(so, 0);
                if (g == 5 || g == 15) XDMA(so, 1);
                if (g == 3) { cur = sb; sx = sb + lp(SG_X1); PIN_S(cur); PIN_S(sx); }
                if (g == 10) VMB_LATE(4);
                if (g == 18) LDA(r0, x0, sx, 0);
                if (g == 22) LDA(r1, x1, sx, 0);
                if (g == 25) LDA8(r2, x2, sx, 0);
                if (g == 30) VMB(7);
                if (g == 32) flag(pend);
                if (g == 35) { sb = (s + 1) << 19; PIN_S(sb); }
                if (g == 37) { sx = (sb >> 8) & 8192; PIN_S(sx); }
                if (g == 38) { sb = (sb | sx) + 2 * SG_OFF; PIN_S(sb); }
                if (g == 40) { sb ^= L << 13; PIN_S(sb); }
                if (g == 42) asm volatile("s_cmp_lt_i32 %1, 63\n\ts_cselect_b32 %0, %0, %2" : "+s"(sb) : "s"(s), "n"(OOB) : "scc");
                if (g == 50) VMB(4);
                if constexpr (tt == 1 && ks < 18) xw[(ks + 2) % 3] = *(const bf16x8*)(sX + (ks + 2) * 2048);
                if (g == 60) LDA4(pv, fO, sb);
                if (g == 63) asm volatile("s_waitcnt vmcnt(2)" : "+v"(r0), "+v"(r1), "+v"(r2) :: "memory");
                if constexpr (g >= 65 && g < 90 && (tt == 2 || tt == 3)) {
                    constexpr int e = ks - 13, k = tt - 2;
                    const unsigned u = e < 2 ? r0[2 * (e & 1) + k] : (e < 4 ? r1[2 * (e & 1) + k] : r2[k]);
                    xf[e][2 * k] = lo16(u); xf[e][2 * k + 1] = hi16(u);
                    asm volatile("" : "+v"(xf[e][2 * k]), "+v"(xf[e][2 * k + 1]));
                }
                if (g == 90) WAITV(0, pv);
                if (g == 91) { sx = (unsigned)__ballot(pv == 0); PIN_S(sx); }
                if (g == 92) asm volatile("s_and_b32 %0, %0, 255\n\ts_cselect_b32 %0, %2, %1" : "+s"(sx) : "s"(sb), "n"(OOB) : "scc");
                if (g == 93) sK[w] = sx;
                if (g == 97) LDS_BARRIER();
                if (g == 98) k4 = *(const u32x4*)sK;
                SCHED0();
            });
        }
#pragma unroll
        for (int e = 0; e < 5; ++e) { PIN_V(acc[e]); acc[e] += xf[e]; PIN_V(acc[e]); SCHED0(); }
        xs = __builtin_amdgcn_readfirstlane(max(umax3(k4[0], k4[1], k4[2]), k4[3]));
        if (L) {
            if (xs != OOB) first(xs);
            const unsigned so2 = s * (SEG_ * D_ * 2);
#pragma unroll
            for (int e = 0; e < 5; ++e) __builtin_amdgcn_raw_buffer_store_b64(u32x2{pk2(acc[e][0], acc[e][1]), pk2(acc[e][2], acc[e][3])}, ro, e < 4 ? oo + 32 * e : oo2, so2, 0);
        } else {
            f32x4 a = ((acc[0] + acc[1]) + (acc[2] + acc[3])) + acc[4];
            const float mw = red_sum_q((a[0] + a[1]) + (a[2] + a[3])) * (1.0f / 80);
#pragma unroll
            for (int e = 0; e < 5; ++e) { xf[e] = acc[e]; acc[e] -= mw; }
            a = acc[0] * acc[0];
#pragma unroll
            for (int e = 1; e < 5; ++e) a = __builtin_elementwise_fma(acc[e], acc[e], a);
            sst8(rx, cur, sw, u32x2{nosent(__float_as_uint(mw)), __float_as_uint(red_sum_q((a[0] + a[1]) + (a[2] + a[3])))});
            SCHED0();
            if (xs != OOB) first(xs);
            r0 = pk8(xf[0], xf[1]); r1 = pk8(xf[2], xf[3]); r2 = u32x2{pk2(xf[4][0], xf[4][1]), pk2(xf[4][2], xf[4][3])};
            const unsigned so2 = cur + lp(SG_XR);
            sst16(rx, so2, x0, r0); sst16(rx, so2, x1, r1); sst8(rx, so2, x2, r2);
            VMW(3);
            u32x2 p, q;
#pragma unroll 1
            for (;;) {
                LDA8(p, sr, cur, 0); LDA8(q, sr, cur, 128);
                LIVE;
                asm volatile("s_waitcnt vmcnt(0)" : "+v"(p), "+v"(q) :: "memory");
                if (!BAD(max(p[0], q[0]))) break;
            }
            const float m0 = __uint_as_float(p[0]), m1 = __uint_as_float(q[0]), mean = red_sum_q(m0 + m1) * 0.125f, e0 = m0 - mean, e1 = m1 - mean;
            const float var = red_sum_q((__uint_as_float(p[1]) + __uint_as_float(q[1])) + 80.f * (e0 * e0 + e1 * e1)) * (1.0f / D_);
            const float rd = __builtin_amdgcn_rcpf(rb(__builtin_amdgcn_sqrtf(rb(rb(var) + 1e-5f)))), c = (mw - rb(mean)) * rd;
#pragma unroll
            for (int e = 0; e < 5; ++e) acc[e] = acc[e] * rd + c;
            sst16(rx, cur, x0, pk8(acc[0], acc[1])); sst16(rx, cur, x1, pk8(acc[2], acc[3])); sst8(rx, cur, x2, u32x2{pk2(acc[4][0], acc[4][1]), pk2(acc[4][2], acc[4][3])});
            pend = cur;
        }
    } while (++s < NSEG_);
    if (id == 159) bst4(go.rh, t < 8 ? t * 1024 + HS_E : OOB, tags);
    VMB(0);
    flag(pend);
    SPIN_END;
}

constexpr int MEGA_LDS = 163840;
__global__ void __launch_bounds__(256, 1) mega_kernel(MegaArgs A) {
    extern __shared__ __attribute__((aligned(16))) char smem[];
    int role = 32 * (blockIdx.x & 7) + (blockIdx.x >> 3);
    PIN_S(role);
    const int pool = role >> 5, k = role & 31;
    {
        const unsigned t0 = rtime();
        if (k >= 17 && k < 29 && ((k - 13) & 4)) help(A, smem, k > 21, 4 * pool + ((k - 13) & 3), t0);
        if (k >= 10 && k < 13) init_area(A, 3 * pool + k - 10, role);
#pragma unroll 1
        for (int sg = k >= 8 && k < 13 ? 8 * (k - 8) + pool : (k == 25 ? 56 + pool : NSEG_); sg < NSEG_; sg += sg < 16 ? 40 : NSEG_) first_rows(A, smem, sg, role, t0);
    }
    if (k < 13) {
        if (!(A.roles & 1)) return;
        if (k < 4) s_role(A, smem, 4 * pool + k, role);
        else if (k < 8) vm_role(A, smem, 4 * pool + k - 4, role);
        else c_role(A, smem, pool, k - 8, role);
        return;
    }
    if (k < 21) { if (A.roles & 2) qk_role(A, smem, (k - 13) >> 2, 4 * pool + ((k - 13) & 3), role); }
    else if (k < 29) { if (A.roles & 4) vo_role(A, smem, (k - 21) >> 2, 4 * pool + ((k - 21) & 3), role); }
    else if (k < 31) { if (A.roles & 8) b_role(A, smem, pool >> 2, 2 * (pool & 3) + k - 29, role); }
    else if (A.roles & 16) bo_role(A, smem, pool >> 2, pool & 3, role);
}

#define ROLES 31
extern "C" void kernel_launch(void* const* d_in, const int* in_sizes, int n_in,
                              void* d_out, int out_size, void* d_ws, size_t ws_size,
                              hipStream_t stream) {
    MegaArgs A;
    char* ws = (char*)d_ws;
    A.hs = ws; ws += 262144;
    A.tmo = (unsigned*)ws; ws += 4096;
    A.ar = ws;
    A.x0 = (const bf16_t*)d_in[0]; A.mem0 = (const bf16_t*)d_in[1]; A.Wq = (const bf16_t*)d_in[2]; A.Wk = (const bf16_t*)d_in[3]; A.Wv = (const bf16_t*)d_in[4];
    A.Wo = (const bf16_t*)d_in[5]; A.wqkv = (const bf16_t*)d_in[8]; A.wo = (const bf16_t*)d_in[9];
    A.out = (bf16_t*)d_out; A.pad = 0;
    A.lag = LAGV; A.nit = NSEG_ + LAGV; A.roles = ROLES;

    (void)hipFuncSetAttribute((const void*)mega_kernel, hipFuncAttributeMaxDynamicSharedMemorySize, MEGA_LDS);

    mega_kernel<<<256, 256, MEGA_LDS, stream>>>(A);
}
```

```cpp
#define PER 288
#define E_U 121
#define E_P 139
#define D_1 17
#define D_2 8
#include <hip/hip_runtime.h>
#include <hip/hip_bf16.h>
#include <stdint.h>

typedef unsigned short bf16_t;
typedef short bf16x8 __attribute__((ext_vector_type(8)));
typedef short bf16x4 __attribute__((ext_vector_type(4)));
typedef float f32x4 __attribute__((ext_vector_type(4)));
typedef unsigned u32x4 __attribute__((ext_vector_type(4)));
typedef unsigned u32x2 __attribute__((ext_vector_type(2)));
typedef unsigned long long u64;
typedef __bf16 bf16v2 __attribute__((ext_vector_type(2)));
typedef float f32v2 __attribute__((ext_vector_type(2)));

__device__ __forceinline__ float bf2f(bf16_t v) { return __uint_as_float(((unsigned)v) << 16); }
__device__ __forceinline__ unsigned pk2(float a, float b) {
    f32v2 f = {a, b};
    bf16v2 r = __builtin_convertvector(f, bf16v2);
    return __builtin_bit_cast(unsigned, r);
}
__device__ __forceinline__ float rb(float a) { return __uint_as_float((pk2(a, 0.f) & 0xffffu) << 16); }
__device__ __forceinline__ float lo16(unsigned u) { return __uint_as_float(u << 16); }
__device__ __forceinline__ float hi16(unsigned u) { return __uint_as_float(u & 0xffff0000u); }
__device__ __forceinline__ bf16x4 pk4(f32x4 v) {
    u32x2 u = {pk2(v[0], v[1]), pk2(v[2], v[3])};
    return __builtin_bit_cast(bf16x4, u);
}
__device__ __forceinline__ bf16x8 cat8(bf16x4 a, bf16x4 b) { return bf16x8{a[0], a[1], a[2], a[3], b[0], b[1], b[2], b[3]}; }
__device__ __forceinline__ bf16x8 asb8(u32x4 v) { return __builtin_bit_cast(bf16x8, v); }
__device__ __forceinline__ f32x4 asf4(u32x4 v) { return __builtin_bit_cast(f32x4, v); }
__device__ __forceinline__ f32x4 mfma32(bf16x8 a, bf16x8 b, f32x4 c) { return __builtin_amdgcn_mfma_f32_16x16x32_bf16(a, b, c, 0, 0, 0); }
__device__ __forceinline__ f32x4 mfma16(bf16x4 a, bf16x4 b, f32x4 c) { return __builtin_amdgcn_mfma_f32_16x16x16bf16_1k(a, b, c, 0, 0, 0); }

#define MF_(acc, ca, a, cb, b) asm volatile("v_mfma_f32_16x16x32_bf16 %0, %1, %2, %0" : "+v"(acc) : ca(a), cb(b))
#define MF0_(acc, ca, a, cb, b) asm volatile("v_mfma_f32_16x16x32_bf16 %0, %1, %2, 0" : "=&v"(acc) : ca(a), cb(b))
#define MF_AV(acc, a, b) MF_(acc, "a", a, "v", b)
#define MF_VA(acc, a, b) MF_(acc, "v", a, "a", b)
#define MF_VV(acc, a, b) MF_(acc, "v", a, "v", b)
#define MF_AV0(acc, a, b) MF0_(acc, "a", a, "v", b)
#define MF_VA0(acc, a, b) MF0_(acc, "v", a, "a", b)
#define MF_VV0(acc, a, b) MF0_(acc, "v", a, "v", b)
#define MF_VVC(d, a, b, c) asm volatile("v_mfma_f32_16x16x32_bf16 %0, %1, %2, %3" : "=&v"(d) : "v"(a), "v"(b), "v"(c))
#define MH_(acc, ca, a, cb, b) asm volatile("v_mfma_f32_16x16x16_bf16 %0, %1, %2, %0" : "+v"(acc) : ca(a), cb(b))
#define MH0_(acc, ca, a, cb, b) asm volatile("v_mfma_f32_16x16x16_bf16 %0, %1, %2, 0" : "=&v"(acc) : ca(a), cb(b))

constexpr int S_ = 2048, D_ = 640, H_ = 32, DH_ = 192, M_ = 8, SEG_ = 32, NSEG_ = 64;
constexpr int HD_ = H_ * DH_;
#define RLX_AGENT __ATOMIC_RELAXED, __HIP_MEMORY_SCOPE_AGENT
#define AUX_SC1 16
typedef __amdgpu_buffer_rsrc_t rsrc_t;
__device__ __forceinline__ rsrc_t mk_rsrc(const void* p, unsigned bytes) { return __builtin_amdgcn_make_buffer_rsrc((void*)p, 0, (int)bytes, 0x00020000); }
__device__ __forceinline__ u32x4 bld16(rsrc_t r, unsigned off) { return __builtin_amdgcn_raw_buffer_load_b128(r, off, 0, AUX_SC1); }
__device__ __forceinline__ u32x2 bld8(rsrc_t r, unsigned off) { return __builtin_amdgcn_raw_buffer_load_b64(r, off, 0, AUX_SC1); }
__device__ __forceinline__ unsigned bld4(rsrc_t r, unsigned off) { return __builtin_amdgcn_raw_buffer_load_b32(r, off, 0, AUX_SC1); }
__device__ __forceinline__ void bst16(rsrc_t r, unsigned off, u32x4 v) { __builtin_amdgcn_raw_buffer_store_b128(v, r, off, 0, AUX_SC1); }
__device__ __forceinline__ void bst8(rsrc_t r, unsigned off, u32x2 v) { __builtin_amdgcn_raw_buffer_store_b64(v, r, off, 0, AUX_SC1); }
__device__ __forceinline__ void bst4(rsrc_t r, unsigned off, unsigned v) { __builtin_amdgcn_raw_buffer_store_b32(v, r, off, 0, AUX_SC1); }
#define LDS_BARRIER() asm volatile("s_waitcnt lgkmcnt(0)\n\ts_barrier" ::: "memory")
#define DRAIN() asm volatile("s_waitcnt vmcnt(0)" ::: "memory")
#define MEMFENCE() asm volatile("" ::: "memory")
#define SCHED0() __builtin_amdgcn_sched_barrier(0)
__device__ __forceinline__ float red_max_q(float x) {
    auto a = __builtin_amdgcn_permlane16_swap(__float_as_uint(x), __float_as_uint(x), false, false);
    x = fmaxf(__uint_as_float(a[0]), __uint_as_float(a[1]));
    auto c = __builtin_amdgcn_permlane32_swap(__float_as_uint(x), __float_as_uint(x), false, false);
    return fmaxf(__uint_as_float(c[0]), __uint_as_float(c[1]));
}
__device__ __forceinline__ float max3r(float a, float b, float c) { float r; asm("v_max3_f32 %0, %1, %2, %3" : "=v"(r) : "v"(a), "v"(b), "v"(c)); return r; }
__device__ __forceinline__ float max2r(float a, float b) { float r; asm("v_max_f32 %0, %1, %2" : "=v"(r) : "v"(a), "v"(b)); return r; }
__device__ __forceinline__ void red_max_q2(float& x, float& y) {
    float s, t;
    asm("v_mov_b32 %2, %0\n\tv_mov_b32 %3, %1\n\ts_nop 0\n\tv_permlane16_swap_b32 %0, %2\n\tv_permlane16_swap_b32 %1, %3\n\tv_max_f32 %0, %0, %2\n\tv_max_f32 %1, %1, %3\n\t"
        "v_mov_b32 %2, %0\n\tv_mov_b32 %3, %1\n\ts_nop 0\n\tv_permlane32_swap_b32 %0, %2\n\tv_permlane32_swap_b32 %1, %3\n\tv_max_f32 %0, %0, %2\n\tv_max_f32 %1, %1, %3"
        : "+v"(x), "+v"(y), "=&v"(s), "=&v"(t));
}
__device__ __forceinline__ void red_sum_q2(float& x, float& y) {
    float s, t;
    asm("v_mov_b32 %2, %0\n\tv_mov_b32 %3, %1\n\ts_nop 0\n\tv_permlane16_swap_b32 %0, %2\n\tv_permlane16_swap_b32 %1, %3\n\tv_add_f32 %0, %0, %2\n\tv_add_f32 %1, %1, %3\n\t"
        "v_mov_b32 %2, %0\n\tv_mov_b32 %3, %1\n\ts_nop 0\n\tv_permlane32_swap_b32 %0, %2\n\tv_permlane32_swap_b32 %1, %3\n\tv_add_f32 %0, %0, %2\n\tv_add_f32 %1, %1, %3"
        : "+v"(x), "+v"(y), "=&v"(s), "=&v"(t));
}
__device__ __forceinline__ float red_sum_q(float x) {
    auto a = __builtin_amdgcn_permlane16_swap(__float_as_uint(x), __float_as_uint(x), false, false);
    x = __uint_as_float(a[0]) + __uint_as_float(a[1]);
    auto c = __builtin_amdgcn_permlane32_swap(__float_as_uint(x), __float_as_uint(x), false, false);
    return __uint_as_float(c[0]) + __uint_as_float(c[1]);
}
__device__ __forceinline__ u32x4 pk8(f32x4 a, f32x4 b) { return u32x4{pk2(a[0], a[1]), pk2(a[2], a[3]), pk2(b[0], b[1]), pk2(b[2], b[3])}; }
__device__ __forceinline__ u32x4 tag4(f32x4 v, unsigned tg) {
    return u32x4{(__float_as_uint(v[0]) & ~3u) | tg, (__float_as_uint(v[1]) & ~3u) | tg, (__float_as_uint(v[2]) & ~3u) | tg, (__float_as_uint(v[3]) & ~3u) | tg};
}
__device__ __forceinline__ unsigned tagbad(u32x4 v, unsigned tg) { return ((v[0] ^ tg) | (v[1] ^ tg) | (v[2] ^ tg) | (v[3] ^ tg)) & 3u; }

__device__ __host__ __forceinline__ constexpr unsigned xm(unsigned o, unsigned side) { return ((o >> 13) << 14) | (o & 8191u) | ((((o >> 20) ^ side) & 1u) << 13); }
__device__ __host__ __forceinline__ constexpr unsigned lp(unsigned o) { return ((o >> 13) << 14) | (o & 8191u); }
__device__ __host__ __forceinline__ constexpr int hside(int h) { return h >> 4; }
constexpr unsigned XM_OFF = 24576, XM_BUF = 4 * 32768;
constexpr unsigned XP_OFF = XM_OFF + 3 * XM_BUF, XP_BUF = 32768;
constexpr unsigned PT_OFF = XP_OFF + 3 * XP_BUF, PT_BUF = 65536;
constexpr unsigned FI_OFF = PT_OFF + 3 * PT_BUF, FI_N = 80;
constexpr unsigned FX_OFF = FI_OFF + FI_N * 1024;
constexpr unsigned A_END = FX_OFF + FI_N * 256;
constexpr unsigned UM_OFF = 1u << 20, UM_BUF = 40 * 8192;
#define TR 8
constexpr unsigned UT_OFF = 2u << 20;
constexpr unsigned QK_OFF = UT_OFF + TR * (2u << 20);
constexpr unsigned SG_OFF = QK_OFF + TR * (1u << 20);
enum { SG_XL = 0, SG_XR = 40960, SG_X1 = 81920, SG_OB = 122880, SG_ST = 163840, SG_FL = 172032 };
enum { FL_U = 0, FL_X = 2048, FL_O = 4096, ST_2 = 6144 };
constexpr unsigned ARENA = 2 * (SG_OFF + 64 * 262144);
static_assert(A_END <= UM_OFF && UM_OFF + 3 * UM_BUF <= UT_OFF && QK_OFF % (1u << 21) == 0 && SG_OFF % (1u << 21) == 0, "map");
static_assert(ARENA + (1u << 23) < (1u << 28), "workspace");
__device__ __forceinline__ unsigned seg_base(int layer, int seg) { return 2 * SG_OFF + (unsigned)seg * 524288 + ((((seg >> 2) ^ layer) & 1) << 13); }
__device__ __forceinline__ unsigned ut_slot(int seg) { return (unsigned)(seg & (TR - 1)) << 22; }
__device__ __forceinline__ unsigned qk_base(int seg, int side) { return 2 * QK_OFF + ((unsigned)(seg & (TR - 1)) << 21) + (((seg ^ side) & 1) << 13); }
__device__ __forceinline__ unsigned fi_step(int it) { return ((it >> 3) << 14) | ((it & 7) << 10); }
__device__ __forceinline__ unsigned fx_step(int it) { return ((it >> 5) << 14) | ((it & 31) << 8); }
static_assert(FI_OFF % 8192 == 0 && FX_OFF % 8192 == 0, "steps");
__device__ __forceinline__ u32x4 sld16(rsrc_t r, unsigned so, unsigned lo) { return __builtin_amdgcn_raw_buffer_load_b128(r, lo, so, AUX_SC1); }
__device__ __forceinline__ u32x2 sld8(rsrc_t r, unsigned so, unsigned lo) { return __builtin_amdgcn_raw_buffer_load_b64(r, lo, so, AUX_SC1); }
__device__ __forceinline__ unsigned sld4(rsrc_t r, unsigned so, unsigned lo) { return __builtin_amdgcn_raw_buffer_load_b32(r, lo, so, AUX_SC1); }
__device__ __forceinline__ void sst4(rsrc_t r, unsigned so, unsigned lo, unsigned v) { __builtin_amdgcn_raw_buffer_store_b32(v, r, lo, so, AUX_SC1); }
__device__ __forceinline__ void sst16(rsrc_t r, unsigned so, unsigned lo, u32x4 v) { __builtin_amdgcn_raw_buffer_store_b128(v, r, lo, so, AUX_SC1); }
__device__ __forceinline__ void sst8(rsrc_t r, unsigned so, unsigned lo, u32x2 v) { __builtin_amdgcn_raw_buffer_store_b64(v, r, lo, so, AUX_SC1); }
__device__ __forceinline__ unsigned umax3(unsigned a, unsigned b, unsigned c) { return max(max(a, b), c); }
__device__ __forceinline__ unsigned mx4(unsigned m, u32x4 v) { return umax3(m, v[0], v[2]); }
__device__ __forceinline__ unsigned nosent(unsigned x) { return min(x, 0xfffffffeu); }
constexpr unsigned OOB = 0x7f000000u;
constexpr unsigned kSpinCap = 1u << 18;

__device__ __forceinline__ char* arena_base(char* p) { return (char*)(((size_t)p + 0x3fffff) & ~(size_t)0x3fffff); }

typedef __attribute__((address_space(3))) bf16x4 lds_b4;
typedef __attribute__((address_space(3))) volatile int lds_int;
template <int V> struct IC { static constexpr int value = V; };
template <int B, int E, class F> __device__ __forceinline__ void sfor(F&& f) { if constexpr (B < E) { f(IC<B>{}); sfor<B + 1, E>(f); } }
#define WNT ""
#define DMA_NEXT(x) asm volatile("global_load_lds_dwordx4 %0, %1" x "\n\ts_add_u32 m0, m0, %2\n\tv_add_u32 %0, %3, %0" : "+v"(vo) : "s"(base), "s"(step), "s"(adv) : "memory", "m0", "scc")
template <bool NT> __device__ __forceinline__ void dma_next(const void* base, unsigned& vo, unsigned step, unsigned adv) { if (NT) DMA_NEXT(WNT); else DMA_NEXT(""); }
__device__ __forceinline__ void set_m0(unsigned x) { asm volatile("s_mov_b32 m0, %0\n\ts_nop 0" :: "s"(x) : "memory", "m0"); }
__device__ __forceinline__ bf16x8 tr_read(const char* p, int o0, int o1) { return cat8(__builtin_amdgcn_ds_read_tr16_b64_v4i16((lds_b4*)(p + o0)), __builtin_amdgcn_ds_read_tr16_b64_v4i16((lds_b4*)(p + o1))); }
struct Go { rsrc_t rh; unsigned so; u32x2 v; };
#define G_EB 2
#define GO_ASK asm volatile("buffer_load_dwordx2 %0, %1, %2, %3 offen sc1" : "=&v"(go.v) : "v"((threadIdx.x & 63) * 8), "s"(go.rh), "s"(go.so) : "memory")
struct Nop { __device__ void operator()() const {} };
template <int N, int N1, int NI, int NJ, int NS, int SL, bool V, int NE = 0, class ISSUE, class TAKE, class EARLY = Nop>
__device__ __forceinline__ void load_ring(char* smem, const int w, Go& go, ISSUE issue_, TAKE take, EARLY early = Nop{}) {
    constexpr int AH = NS - 1, SH = NE ? (NS - N % NS) % NS : 0, GE = N - 1 - AH - (NE ? G_EB : 0), HE = GE + AH + 1;
    static_assert(HE >= N - NS + 2, "free slots");
    const unsigned sta = (unsigned)(size_t)(__attribute__((address_space(3))) char*)smem + w * 1024;
    auto issue = [&](int st) {
        if ((st + SH) % NS == 0) set_m0(sta); else if (st == 0) set_m0(sta + SH * SL);
        const int n = st < N1 ? NI : NJ;
#pragma unroll
        for (int m = 0; m < n; ++m) issue_(st, m, m + 1 < n ? 4096u : SL - 4096u * (n - 1));
    };
#pragma unroll
    for (int st = 0; st < AH; ++st) issue(st);
    auto run = [&](auto WW) {
        sfor<0, N + 1>([&](auto ST) {
            constexpr int st = decltype(ST)::value, e = st + AH < N ? st + AH : N, a = e < N1 ? e : N1, n1 = a > st + 1 ? a - st - 1 : 0, n = st < N ? n1 * NI + (e - st - 1 - n1) * NJ + (st > GE && st <= GE + AH) + (st > HE ? NE : 0) : 0;
            asm volatile("s_waitcnt vmcnt(%0) lgkmcnt(0)\n\ts_barrier" :: "n"(n) : "memory");
            if constexpr (st > 0) take(IC<st - 1>{}, WW, smem, -1);
            if constexpr (st + AH < N) issue(st + AH);
            if constexpr (st == GE) GO_ASK;
            if constexpr (st == HE || st == N) asm volatile("" : "+v"(go.v));
            constexpr int sl = (st + SH) % NS, hi = sl / (65536 / SL) * (65536 / SL);
            if constexpr (st < N) take(ST, WW, smem + hi * SL, (sl - hi) * SL);
            if constexpr (NE && st == HE) early();
        });
    };
    if (!V || w == 0) run(IC<0>{}); else if (w == 1) run(IC<1>{}); else if (w == 2) run(IC<2>{}); else run(IC<3>{});
}
template <int R, int NTT> __device__ __forceinline__ void take_k(int ks, int ww, const char* p, int o, const int (&rp)[NTT], int hf, bf16x8 (&wf)[NTT][20]) {
#pragma unroll
    for (int tt = 0; tt < NTT; ++tt) {
        const int i = (ks + 20 - R * ww) % 20;
        if (o >= 0) wf[tt][i] = tr_read(p, rp[tt] + o, rp[tt] + hf + o);
        else if (20 * tt + i < 64) asm volatile("" : "+a"(wf[tt][i]));
        else asm volatile("" : "+v"(wf[tt][i]));
    }
}
#define SWP (R != 0)
#define ORD(st) (SWP ? (st & 1 ? 3u : -1u) : 1u)
#define BNT 4
template <int NI, int R, int NTT, int NE = 0, class EARLY = Nop>
__device__ __forceinline__ void load_coop(char* smem, const int w, Go& go, const void* const (&base)[NI], unsigned (&vo)[NI], const unsigned adv, const int (&rp)[NTT], const int hf, bf16x8 (&wf)[NTT][20], EARLY early = Nop{}) {
    load_ring<20, 20, NI, NI, (NI > 4 ? 6 : 8), (NI > 4 ? 24576 : 16384), R != 0, NE>(smem, w, go, [&](int st, int m, unsigned step) { dma_next<(NI > BNT)>(base[m], vo[m], step, ORD(st) * adv); },
        [&](auto ST, auto WW, const char* p, int o) { take_k<R, NTT>(decltype(ST)::value ^ SWP, decltype(WW)::value, p, o, rp, hf, wf); }, early);
}
__device__ __forceinline__ unsigned hd_src(int idx) { const int r = idx / 24, u = idx % 24; return r * (2 * HD_) + ((u * 16) ^ ((r & 6) << 4)); }
__device__ __forceinline__ int hd_pos(int l, int tile) { const int r = l >> 2; return r * 384 + ((32 * tile) ^ ((r & 6) << 4)) + (l & 3) * 8; }
__device__ __forceinline__ unsigned row_src(int idx) { return (idx * 16) ^ ((idx / 80 & 3) << 6); }
__device__ __forceinline__ int row_pos(int r, int piece) { return r * 1280 + ((piece ^ (r & 3)) << 6); }
template <int R, int NE = 0, class TILE, class EARLY = Nop> __device__ __forceinline__ void load_qk(char* smem, int w, int l, Go& go, const bf16_t* Wq, const bf16_t* Wk, TILE tile, bf16x8 (&wf)[6][20], EARLY early = Nop{}) {
    const void* const base[6] = {Wq, Wq, Wq, Wk, Wk, Wk};
    unsigned vo[6];
    int rp[6];
#pragma unroll
    for (int m = 0; m < 6; ++m) { vo[m] = hd_src((4 * (m % 3) + w) * 64 + l) + SWP * 64 * HD_; rp[m] = tile(m) / 12 * 12288 + hd_pos(l, tile(m) % 12); }
    load_coop<6, R, 6, NE>(smem, w, go, base, vo, 64 * HD_, rp, 6144, wf, early);
}
template <int R, class DST> __device__ __forceinline__ void load_vo(char* smem, int w, int l, Go& go, const bf16_t* Wv, const bf16_t* Wo, bf16x8 (&wf)[3][20], DST dst) {
    unsigned vo[3], v2[5];
    int r2[5];
    bf16x4 lo[10];
#pragma unroll
    for (int m = 0; m < 3; ++m) vo[m] = hd_src((4 * m + w) * 64 + l) + SWP * 64 * HD_;
#pragma unroll
    for (int m = 0; m < 5; ++m) v2[m] = row_src((4 * m + w) * 64 + l) + SWP * 20480;
#pragma unroll
    for (int m = 0; m < 5; ++m) r2[m] = row_pos(l >> 2, 5 * w + m) + (l & 3) * 8;
    const int rp[3] = {hd_pos(l, 2 * w), hd_pos(l, 2 * w + 1), hd_pos(l, 8 + w)};
    load_ring<32, 20, 3, 5, 8, 20480, R != 0>(smem, w, go, [&](int st, int m, unsigned step) { if (st < 20) dma_next<true>(Wv, vo[m % 3], step, ORD(st) * (64 * HD_)); else dma_next<true>(Wo, v2[m], step, ORD(st) * 20480u); },
        [&](auto ST, auto WW, const char* p, int o) {
            constexpr int st = decltype(ST)::value;
            if constexpr (st < 20) take_k<R, 3>(st ^ SWP, decltype(WW)::value, p, o, rp, 6144, wf);
            else {
#pragma unroll
                for (int t = 0; t < 10; ++t) {
                    if (o < 0) {
                        if (st & 1) { bf16x8& d = dst(st / 2 - 10, t, decltype(WW)::value); if (t == 0 && st < 28) asm volatile("" : "+a"(d)); else asm volatile("" : "+v"(d)); }
                        continue;
                    }
                    const bf16x4 x = __builtin_amdgcn_ds_read_tr16_b64_v4i16((lds_b4*)(p + r2[t / 2] + o + (t & 1) * 32));
                    if (st & 1) dst(st / 2 - 10, t, decltype(WW)::value) = SWP ? cat8(x, lo[t]) : cat8(lo[t], x); else lo[t] = x;
                }
            }
        });
}

struct MegaArgs {
    const bf16_t* x0; const bf16_t* mem0; const bf16_t* Wq; const bf16_t* Wk; const bf16_t* Wv; const bf16_t* Wo; const bf16_t* wqkv; const bf16_t* wo;
    char* ar; unsigned* tmo; char* hs; bf16_t* out;
    int nit, lag, roles, pad;
};

#define WSLP 2
#define LIVE u64 live_; asm volatile("s_add_u32 %0, %0, 1\n\ts_cmp_gt_u32 %0, %2\n\ts_cselect_b64 %1, 0, -1" : "+s"(spins), "=s"(live_) : "n"(kSpinCap) : "scc")
#define BAD(m) (__ballot(m == ~0u) & live_)
#define SPIN_END if (spins > kSpinCap) __hip_atomic_store(A.tmo, 1u + id, RLX_AGENT)
__device__ __forceinline__ void wave_wait(rsrc_t rx, unsigned off, unsigned so, unsigned& spins) {
#pragma unroll 1
    for (;;) {
        const unsigned v = sld4(rx, so, off);
        LIVE;
        if (!(__ballot(v == 0 && off != OOB) & live_)) break;
        __builtin_amdgcn_s_sleep(WSLP);
        MEMFENCE();
    }
}
__device__ __forceinline__ unsigned rtime() { return (unsigned)__builtin_amdgcn_s_memrealtime(); }
__device__ __forceinline__ void rt_until(unsigned target) {
    asm volatile("1:\n\ts_memrealtime vcc\n\ts_waitcnt lgkmcnt(0)\n\ts_sub_u32 vcc_lo, %0, vcc_lo\n\ts_cmpk_lt_u32 vcc_lo, 600\n\ts_cbranch_scc1 1b" :: "s"(target - 1) : "memory", "scc", "vcc");
}
#define RT_K 26
__device__ __forceinline__ void rt_fine(unsigned target) {
    u64 a, b, c;
    unsigned n;
    asm volatile("1:\n\ts_memrealtime vcc\n\ts_waitcnt lgkmcnt(0)\n\ts_sub_u32 vcc_lo, %4, vcc_lo\n\ts_cmpk_lt_u32 vcc_lo, 600\n\ts_cbranch_scc1 1b\n\t"
                 "s_add_u32 vcc_lo, vcc_lo, 8\n\ts_cbranch_scc0 3f\n\t"
                 "s_memrealtime %0\n\ts_nop 4\n\ts_memrealtime %1\n\ts_nop 4\n\ts_memrealtime %2\n\ts_lshl_b32 %3, %4, 2\n\ts_addk_i32 %3, %5\n\ts_nop 2\n\ts_memrealtime vcc\n\ts_waitcnt lgkmcnt(0)\n\t"
                 "s_sub_u32 %3, %3, vcc_lo\n\ts_mov_b64 vcc, %0\n\ts_sub_u32 %3, %3, vcc_lo\n\ts_mov_b64 vcc, %1\n\ts_sub_u32 %3, %3, vcc_lo\n\ts_mov_b64 vcc, %2\n\ts_sub_u32 %3, %3, vcc_lo\n\t"
                 "s_max_i32 %3, %3, 0\n\ts_min_i32 %3, %3, 64\n\t"
                 "s_branch 2f\n\t"
                 "3:\n\ts_add_u32 %3, %4, 13\n\t"
                 "4:\n\ts_memrealtime vcc\n\ts_waitcnt lgkmcnt(0)\n\ts_sub_u32 vcc_lo, %3, vcc_lo\n\ts_cmpk_lt_u32 vcc_lo, 16\n\ts_cbranch_scc1 4b\n\t"
                 "s_mov_b32 %3, 0\n\t"
                 "2:\n\ts_sub_u32 %3, %3, 5\n\ts_cbranch_scc0 2b"
                 : "=&s"(a), "=&s"(b), "=&s"(c), "=&s"(n) : "s"(target - 14), "n"(56 - RT_K) : "memory", "scc", "vcc");
}
#define LDA(dst, vo, so, imm) asm volatile("buffer_load_dwordx4 %0, %1, %2, %3 offen offset:%4 sc1" : "=v"(dst) : "v"(vo), "s"(rx), "s"(so), "n"(imm) : "memory")
#define LDI(dst, vo, so, imm) asm volatile("buffer_load_dwordx4 %0, %1, %2, %3 offen offset:%4 sc1" : "+v"(dst) : "v"(vo), "s"(rx), "s"(so), "n"(imm) : "memory")
#define LDA8(dst, vo, so, imm) asm volatile("buffer_load_dwordx2 %0, %1, %2, %3 offen offset:%4 sc1" : "=v"(dst) : "v"(vo), "s"(rx), "s"(so), "n"(imm) : "memory")
#define LDA4(dst, vo, so) asm volatile("buffer_load_dword %0, %1, %2, %3 offen sc1" : "=v"(dst) : "v"(vo), "s"(rx), "s"(so) : "memory")
#define WAITV(n, x) asm volatile("s_waitcnt vmcnt(%1)" : "+v"(x) : "n"(n) : "memory")
#define PIN_S(x) asm volatile("" : "+s"(x))
#define SZERO(x) asm volatile("s_mov_b32 %0, 0" : "=s"(x))
#define PIN_V(x) asm volatile("" : "+v"(x))
#define USE_V(x) asm volatile("" :: "v"(x))
#define LDS_AT(T, a) (*(__attribute__((address_space(3))) T*)(a))
__device__ __forceinline__ float dpp_ror8(float x) { return __uint_as_float(__builtin_amdgcn_mov_dpp(__float_as_uint(x), 0x128, 0xf, 0xf, true)); }
__device__ __forceinline__ unsigned lds_addr(const void* p) { return (unsigned)(size_t)(__attribute__((address_space(3))) const char*)p; }
#define DMA1(vo, so, dst, imm) asm volatile("s_mov_b32 m0, %3\n\ts_nop 0\n\tbuffer_load_dwordx4 %0, %1, %2 offen offset:%4 sc1 lds" :: "v"(vo), "s"(rx), "s"(so), "s"(dst), "n"(imm) : "memory", "m0")

enum { HS_A = 432, HS_E = 504, HS_F = 512, N_1 = 60, N_INIT = 108 };
__device__ __forceinline__ unsigned hs_tags(const Go& go, int l) { return (bld4(go.rh, l < 8 ? l * 1024 + HS_E : OOB) + 2) & ~1u; }
__device__ __forceinline__ rsrc_t arena(const MegaArgs& A, unsigned flip) { return mk_rsrc(arena_base(A.ar) + (flip << 21), ARENA); }
__device__ __forceinline__ unsigned hs_wait(Go& go, int l, int n, unsigned off, bool known, unsigned tag, unsigned& spins) {
#pragma unroll 1
    for (;;) {
        if (!known) tag = (__builtin_amdgcn_readlane(go.v[0], 63) + 2) & ~1u;
        LIVE;
        if (!(__ballot(l < n && ((go.v[0] ^ tag) | (go.v[1] ^ tag)) > 1) & live_)) break;
        __builtin_amdgcn_s_sleep(8);
        go.v = sld8(go.rh, go.so, off);
    }
    return __builtin_amdgcn_readfirstlane(go.v[0]) & 1;
}
#define GO_DECL(pool) Go go; go.rh = mk_rsrc(A.hs, 8192); go.so = (pool) * 1024
#define GO_ON(n) const rsrc_t rx = arena(A, hs_wait(go, l, n / 2, l * 8, false, 0, spins))
#define INIT_BEGIN(pool) const unsigned tags = hs_tags(go, l); go.v = sld8(go.rh, go.so, HS_F); INIT_FLIP(pool)
#define INIT_FLIP(pool) const unsigned tag = __builtin_amdgcn_readlane(tags, pool), flip = hs_wait(go, l, 1, HS_F, true, tag, spins); \
    const rsrc_t rx = arena(A, flip)
#define INIT_END(ii) asm volatile("s_waitcnt vmcnt(0)\n\ts_barrier" ::: "memory"); bst4(go.rh, t < 8 ? t * 1024 + (ii) * 4 : OOB, tags | flip)
#define INIT_SEEN hs_wait(go, l, N_INIT / 2, l * 8, true, tag, spins)
__device__ __forceinline__ void probe(const MegaArgs& A, const Go& go, int l, int id) {
    const rsrc_t rf = arena(A, 0);
    const unsigned tags = hs_tags(go, l);
    unsigned tm[2] = {~0u, ~0u};
    bst4(rf, l < 2 ? l * 8192 : OOB, 0u);
    DRAIN();
#pragma unroll
    for (int k = 0; k < 4; ++k) {
        const unsigned long long t0 = __builtin_amdgcn_s_memtime();
        bst4(rf, l ? OOB : (k & 1) * 8192, 0u);
        DRAIN();
        const unsigned d = (unsigned)(__builtin_amdgcn_s_memtime() - t0);
        tm[k & 1] = d < tm[k & 1] ? d : tm[k & 1];
    }
    bst8(go.rh, l < 8 ? l * 1024 + HS_F : OOB, u32x2{tags | (tm[1] < tm[0]), tags});
}
#define OUTAUX 0
#define DLYB0 0u
#define DLYB1 2000u
#define DLY1 0
#define DLY2 900
#define START_IDS const int t = threadIdx.x, l = t & 63, w = __builtin_amdgcn_readfirstlane(t >> 6); const unsigned l16b = l * 16; GO_DECL(id >> 5); unsigned spins; SZERO(spins)
__device__ __forceinline__ void init_area(const MegaArgs& A, const int ii, const int id, const unsigned t0) {
    START_IDS;
    INIT_BEGIN(id >> 5);
    const int i = 4 * ii + w;
    const bf16_t* mp = A.mem0 + (l & 7) * D_ + 32 * (i % 20) + 4 * (l >> 4);
    const u32x2 x = *(const u32x2*)mp, y = *(const u32x2*)(mp + 16);
    while (rtime() - t0 < DLY1) __builtin_amdgcn_s_sleep(4);
    sst16(rx, xm(XM_OFF + (i / 5 & 15) * 8192 + i % 5 * 1024, i / 80), l16b, u32x4{nosent(x[0]), x[1], nosent(y[0]), y[1]});
    auto fill = [&](unsigned o, unsigned n, unsigned f) {
#pragma unroll 2
        for (unsigned u = i * 1024; u < 2 * n; u += 163840) sst16(rx, 2 * o + u, l16b, u32x4{f, f, f, f});
    };
    fill(XM_OFF + XM_BUF, FI_OFF - PT_BUF - XM_OFF - XM_BUF, ~0u);
    fill(FI_OFF - PT_BUF, PT_BUF, 0u);
    fill(UM_OFF, 3 * UM_BUF, ~0u);
    for (unsigned u = i; u < 208; u += 160) {
        const unsigned f = u < 160 && (u / 16 * 8 + (u & 7) - (l & 1) * A.lag) >= NSEG_;
        sst16(rx, 2 * FI_OFF + u * 1024, l16b, u32x4{f, f, f, f});
    }
    INIT_END(16 + ii);
}
static_assert(FI_N == 80 && FX_OFF + 24576 <= UM_OFF && SG_FL == SG_ST + 8192 && SG_ST % 8192 == 0, "init");
__device__ __forceinline__ void first_rows(const MegaArgs& A, char* smem, const int sg, const int id, const unsigned t0) {
    START_IDS;
    set_m0(lds_addr(smem) + w * 1024);
    {
        unsigned vo[5];
#pragma unroll
        for (int m = 0; m < 5; ++m) vo[m] = row_src((4 * m + w) * 64 + l);
#pragma unroll
        for (int m = 0; m < 10; ++m) dma_next<true>(A.x0 + sg * (SEG_ * D_), vo[m % 5], 4096u, 20480u);
    }
    asm volatile("s_waitcnt vmcnt(0)\n\ts_barrier" ::: "memory");
    const unsigned tags = hs_tags(go, l);
    u32x4 xr[2][5], xl[2][5];
#pragma unroll 1
    for (int rt = 0; rt < 2; ++rt) {
        f32x4 v[10], a;
#pragma unroll
        for (int e = 0; e < 5; ++e) { xr[0][e] = xr[1][e]; xl[0][e] = xl[1][e]; }
#pragma unroll
        for (int e = 0; e < 10; ++e) {
            const u32x2 x = *(const u32x2*)(smem + rt * 20480 + row_pos(l & 15, 5 * w + e / 2) + (e & 1) * 32 + (l >> 4) * 8);
            xr[1][e / 2][2 * (e & 1)] = x[0]; xr[1][e / 2][2 * (e & 1) + 1] = x[1];
            v[e] = f32x4{lo16(x[0]), hi16(x[0]), lo16(x[1]), hi16(x[1])};
            a = e ? a + v[e] : v[0];
        }
        const float mw = red_sum_q((a[0] + a[1]) + (a[2] + a[3])) * (1.0f / 160);
#pragma unroll
        for (int e = 0; e < 10; ++e) { const f32x4 d = v[e] - mw; a = e ? __builtin_elementwise_fma(d, d, a) : d * d; }
        f32v2* sp = (f32v2*)(smem + 49152) + rt * 64 + (l & 15);
        sp[16 * w] = f32v2{mw, red_sum_q((a[0] + a[1]) + (a[2] + a[3]))};
        LDS_BARRIER();
        go.v = sld8(go.rh, go.so, HS_F);
        const f32v2 p = sp[l & 48];
        const float mean = red_sum_q(p[0]) * 0.25f, var = red_sum_q(p[1] + 160.f * (p[0] - mean) * (p[0] - mean)) * (1.0f / D_);
        const float mb = rb(mean), rd = 1.0f / rb(sqrtf(rb(rb(var) + 1e-5f)));
#pragma unroll
        for (int e = 0; e < 10; ++e) {
            const unsigned x = pk2(v[e][0] - mb, v[e][1] - mb), y = pk2(v[e][2] - mb, v[e][3] - mb);
            v[e] = f32x4{lo16(x), hi16(x), lo16(y), hi16(y)} * rd;
            if (e & 1) xl[1][e / 2] = pk8(v[e - 1], v[e]);
        }
    }
    INIT_FLIP(id >> 5);
    while (rtime() - t0 < (sg < 8 ? 0 : (sg < 16 ? DLY1 : DLY2))) __builtin_amdgcn_s_sleep(4);
#pragma unroll
    for (int e = 0; e < 20; ++e) {
        sst16(rx, seg_base(0, sg) + lp((e < 10 ? SG_XL : SG_XR) + (5 * w + e % 10 / 2) * 2048), l16b + (e & 1) * 1024, (e < 10 ? xl : xr)[e & 1][e % 10 / 2]);
        if (e == 9 && sg == 0) { asm volatile("s_waitcnt vmcnt(0)\n\ts_barrier" ::: "memory"); bst8(go.rh, t < 8 ? t * 1024 + HS_A : OOB, u32x2{tags | flip, tags}); }
    }
    const unsigned f = (w >> 1) - 1;
#pragma unroll
    for (int e = 0; e < 8; ++e) sst16(rx, 2 * (SG_OFF + SG_ST) + sg * 524288 + w * 8192 + e / 4 * 4096, l16b + (e & 3) * 1024, u32x4{f, f, f, f});
    INIT_END(sg + (sg < 16 ? 0 : 44));
}
#define LAGV 7
static_assert(LAGV < TR, "rings");
#define NTOUCH 0
#define VSLP 5
#define E_W 105
#define K_W 5
#define K_0 2
#define E_X 40
#define E_B 95
#define E_S 16
#define E_T 10
#define E_G 54
#define IT_1 61
#define E_L 160
#define E_Z 60
#define FASTR 1
#define TIGHT(x, it, d) asm volatile("s_cmp_ge_i32 %1, %2\n\ts_cselect_b32 %0, %3, 0" : "=s"(x) : "s"(it), "s"(IT_1), "s"(d) : "scc")
#define CHAIN_WAIT(pv, off, so, code) if (__builtin_expect(__ballot(pv == 0) != 0, 0)) { if (so != OOB) { wave_wait(rx, off, so, spins); } }
#define GAPN 11
#define GAP() asm volatile("s_nop %0" :: "n"(GAPN))
#define LD5(v, so) do { LDA(v[0], l16b, so, 0); GAP(); LDA(v[1], l16b, so, 1024); GAP(); LDA(v[2], l16b, so, 2048); GAP(); LDA(v[3], l16b, so, 3072); GAP(); LDA(v[4], l16k, so, 0); } while (0)
#define CHAIN_IDS const int t = threadIdx.x, l = t & 63, l16 = l & 15, lq = l >> 4; const int w = __builtin_amdgcn_readfirstlane(t >> 6); \
    GO_DECL(id >> 5); const int lag = A.lag, itend = A.nit; const unsigned l16b = l * 16, l16k = l * 16 + 4096; unsigned spins = 0
#define CHAIN_STAGE const char* sg1 = smem + ((w + 1) & 3) * 5120 + l * 16; const char* sg2 = smem + ((w + 2) & 3) * 5120 + l * 16; const char* sg3 = smem + ((w + 3) & 3) * 5120 + l * 16; \
    char* sg0 = smem + w * 5120 + l * 16; auto mrd = [&](int i) { return *(const bf16x8*)((i < 10 ? sg1 : (i < 15 ? sg2 : sg3)) + (i % 5) * 1024); }; \
    const unsigned mo0 = xm(XM_OFF + (pool & 3) * 32768 + w * 8192, pool >> 2), fX = xm(FX_OFF + (pool & 3) * 64, pool >> 2); unsigned fXv = fX; PIN_V(fXv)
#define GATHER(take) do { _Pragma("unroll 1") for (;;) { LD5(v, mo); LIVE; if (!take(live_)) break; } } while (0)
#define CVT2(o0, o1, x) do { o0 = pk2(x[0], x[1]); o1 = pk2(x[2], x[3]); asm volatile("" : "+v"(o0), "+v"(o1)); } while (0)
#define RR_(op, d, x, y, n) op "_dpp " d ", " x ", " y " row_ror:" n " row_mask:0xf bank_mask:0xf\n\t"
#define ROWRED(op, x, y, f0, f1) f0 RR_(op, y, x, x, "1") RR_(op, y, x, y, "2") RR_(op, y, x, y, "3") f1 RR_(op, x, y, y, "4") RR_(op, x, y, x, "8") RR_(op, x, y, x, "12")
#define BIAS(x, a, b, c, d) asm volatile("v_mov_b32 %0, 0xff800000\n\tv_cndmask_b32 %1, %0, 0, %5\n\tv_cndmask_b32 %2, %0, 0, %6\n\tv_cndmask_b32 %3, %0, 0, %7\n\tv_cndmask_b32 %0, %0, 0, %4" \
    : "=&v"(x[0]), "=&v"(x[1]), "=&v"(x[2]), "=&v"(x[3]) : "s"(a), "s"(b), "s"(c), "s"(d))
#define AND2(o0, o1, a, b, m) do { o0 = a & m; o1 = b & m; asm volatile("" : "+v"(o0), "+v"(o1)); } while (0)

constexpr int SL_S = 20480, SS_W = 3136, SL_KM = SL_S + 4 * SS_W, SL_KT = SL_KM + 6144, SL_W = SL_KT + 49152, SL_END = SL_W + 4 * 18432;
static_assert(SL_END <= 163840, "LDS");
#define G_K 32
#define G_KS 6
__device__ __forceinline__ void s_role(const MegaArgs& A, char* smem, const int h, const int id) {
    CHAIN_IDS;
    const int pool = h >> 2, hs = hside(h);
    bf16x8 wf[6][20];
    const char* sW = smem + SL_W + w * 18432 + l * 16 - 2048;
    load_qk<5>(smem, w, l, go, A.Wq + h * DH_, A.Wk + h * DH_, [&](int m) { return m / 3 * 12 + (m % 3 < 2 ? 2 * w + m % 3 : 8 + w); }, wf);
#pragma unroll
    for (int i = 2; i < 20; ++i) *(bf16x8*)(sW + i * 1024) = wf[5][i];
    GO_ON(N_1);
    *(f32x4*)(smem + SL_S + w * SS_W + 3120) = f32x4{-INFINITY, -INFINITY, -INFINITY, -INFINITY};
#pragma unroll
    for (int i = 0; i < 12; ++i) *(u32x4*)(smem + SL_KT + w * 12288 + i * 1024 + l * 16) = u32x4{0u, 0u, 0u, 0u};
    asm volatile("s_waitcnt lgkmcnt(0)" ::: "memory");
    CHAIN_STAGE;
    char* sSw = smem + SL_S + w * SS_W + lq * 256 + ((l16 + 4 * lq) & 15) * 16;
    const int kq = l16, sL = w >> 1, rt = w & 1, row8 = (4 * w + lq) & 7;
    const bool kval = kq < 8 || (kq >> 1) == 4 + sL;
    const char* sSr = smem + SL_S + (kval ? (kq >> 2) * 1040 + (kq & 3) * 256 + ((4 * w + lq + 4 * kq) & 15) * 16 : 3120);
    const unsigned ppos = !kval ? OOB : xm(XP_OFF + (h >> 3) * 8192 +
        (kq < 8 ? ((h & 7) >> 1) * 1024 + ((kq & 3) * 16 + (h & 1) * 8 + row8) * 16 + (kq >> 2) * 8 : 4096 + ((h & 3) * 16 + ((h >> 2) & 1) * 8 + row8) * 16 + (kq & 1) * 8), sL);
    const unsigned pt1 = xm(PT_OFF + (h >> 3) * 16384 + (rt ? (h & 7) * 1024 : 8192 + ((h & 7) >> 1) * 1024 + (h & 1) * 8) + l * 16, sL);
    const unsigned pt2 = lq == 2 * sL ? xm(PT_OFF + (h >> 3) * 16384 + 12288 + (((h >> 2) & 1) * 2 + rt) * 1024 + ((h & 3) * 16 + l16) * 16, sL) : OOB;
    const u64 cm0 = __ballot(4 * lq <= l16), cm1 = __ballot(4 * lq + 1 <= l16), cm2 = __ballot(4 * lq + 2 <= l16), cm3 = __ballot(4 * lq + 3 <= l16), mval = __ballot((lq >> 1) == sL), oval = __ballot(rt);
    const unsigned ma = l16 < 8 ? ~0u : 0u, mb2 = ~ma;
    const unsigned fK = xm(FI_OFF + (h & 15) * 64 + (4 * sL + (l & 3)) * 4, hs);
    const unsigned qo = ((sL * 16 + (h & 15)) * 4 + rt) * 16384, ktl = lds_addr(smem + SL_KT) + w * 12288;
    const char* sKT = smem + SL_KT + l * 16;
    char* sKM = smem + SL_KM + l * 16;
    char* sKX = smem + SL_KT + l * 16 + 4096 + (w >> 1) * 1024 + (w & 1) * 8;
    unsigned X = rtime() - PER;
    auto qk_so = [&](int it) { const int sg = it - sL * lag; return (unsigned)sg < NSEG_ ? qk_base(sg, hs) + qo : OOB; };
    auto dm0 = [&](int e) {
        if (e == 0) asm volatile("s_mov_b32 m0, %0" :: "s"(ktl) : "m0");
        if (e == 4 || e == 6 || e == 10) asm volatile("s_add_u32 m0, m0, %0" :: "n"(e == 6 ? 2048 : 4096) : "m0", "scc");
    };
    auto dma = [&](int e, unsigned sq, unsigned sk, bool set = true) {
        if (set && (e == 0 || e == 4 || e == 6 || e == 10)) { dm0(e); asm volatile("s_nop 0"); }
        asm volatile("buffer_load_dwordx4 %0, %1, %2 offen offset:%3 sc1 lds" :: "v"(e % 6 < 4 ? l16b : l16k), "s"(rx), "s"(e < 6 ? sk : sq), "n"((e % 6 & 3) * 1024) : "memory");
    };
    {
        const unsigned pv0 = bld4(rx, fK), z = 0, so = qk_so(0);
        CHAIN_WAIT(pv0, fK, z, 1);
#pragma unroll
        for (int e = 0; e < 12; ++e) dma(e, so, so + 32768);
        asm volatile("s_waitcnt vmcnt(0)\n\ts_barrier" ::: "memory");
    }

    unsigned c0 = 0, c1 = 65536, c2 = 131072, mo = mo0, fs = -256, fs1 = 1024, sg = -sL * lag, q0 = 2 * QK_OFF + qo + hs * 8192;
    static_assert(2 * XP_BUF == 65536 && PT_BUF == 65536 && XM_BUF == 131072, "strides");
    PIN_S(c0); PIN_S(c1); PIN_S(c2); PIN_S(fs); PIN_S(fs1); PIN_S(sg); PIN_S(q0);
    for (int it = 0; it < itend; ++it) {
        unsigned ps, pr, ts, tr, qn, kn, tz, fk, per;
        bf16x8 wl[3];
        f32x4 sc[3];
        u32x4 v[5], kp[2][2], kx[2];
        unsigned pvK;
        {
            bf16x8 ka[3], kb[3], q[3];
            auto krd = [&](int f, int e) {
                if (e == 0) kb[f % 3] = *(const bf16x8*)(sKT + (w & 2) * 12288 + f * 1024);
                if (e == 1) q[f % 3] = *(const bf16x8*)(sKT + w * 12288 + 6144 + f * 1024);
                if (e == 2) ka[f % 3] = *(const bf16x8*)(sKT + w * 12288 + f * 1024);
            };
            BIAS(sc[0], oval, oval, oval, oval); BIAS(sc[1], cm0, cm1, cm2, cm3);
            asm volatile("s_waitcnt vmcnt(%0)" :: "n"(5) : "memory");
#pragma unroll
            for (int e = 0; e < 9; ++e) { krd(e / 3, e % 3); SCHED0(); }
            PIN_V(ka[2]);
            rt_fine(X - E_G);
            sfor<0, 13>([&](auto G) {
                constexpr int g = decltype(G)::value, f = g / 2;
                if constexpr (g % 3 == 0) LDA(v[g / 3], g < 12 ? l16b : l16k, mo, (g / 3 & 3) * 1024);
                if constexpr (g < 12) MF_VV(sc[g & 1], (g & 1 ? ka : kb)[f % 3], q[f % 3]);
                SCHED0();
                if (g < 6) { if (g & 1) { krd(f + 3, 1); krd(f + 3, 2); } else krd(f + 3, 0); }
                SCHED0();
            });
#pragma unroll
            for (int e = 0; e < 2; ++e) {
                const u32x2 ka = *(const u32x2*)(sKX + e * 12288), kb = *(const u32x2*)(sKX + (e + 2) * 12288);
                kx[e] = u32x4{ka[0], ka[1], kb[0], kb[1]};
            }
            sg += 1;
            qn = (sg & (TR - 1)) << 21;
            qn += ((qn >> 8) & 8192) ^ q0;
            qn = sg < NSEG_ ? qn : OOB;
            kn = qn + 32768;
            PIN_S(sg); PIN_S(qn); PIN_S(kn);
            LDA4(pvK, fK, fs1);
            PIN_V(kx[0]); PIN_V(kx[1]);
        }
        f32x4 acc[6];
        auto mf = [&](int i, int tt, const bf16x8 xb) {
            if (i == 0) { if (tt < 4) MF_AV0(acc[tt], wf[tt][0], xb); else MF_VV0(acc[tt], wf[tt][0], xb); }
            else if (20 * tt + i < 64) MF_AV(acc[tt], wf[tt][i], xb);
            else if (tt < 5 || i < 2) MF_VV(acc[tt], wf[tt][i], xb);
            else MF_VV(acc[5], wl[i % 3], xb);
        };
        auto take = [&](u64 live_, auto N) {
            unsigned m = 0;
            u64 bad;
            sfor<0, 22>([&](auto G) {
                constexpr int g = decltype(G)::value;
                if constexpr (g < 20 && g % 4 == 0) { WAITV(4 + decltype(N)::value - g / 4, v[g / 4]); *(u32x4*)(sg0 + g / 4 * 1024) = v[g / 4]; SCHED0(); }
                mf(g / 6, g % 6, asb8(v[g / 6]));
                SCHED0();
                if constexpr (g < 20 && g % 4 == 0) { m = mx4(m, v[g / 4]); if (g < 16) PIN_V(m); else { bad = __ballot(m == ~0u); PIN_S(bad); } }
                if constexpr (g == 1 || g == 2 || g == 6 || g == 13) { constexpr int e = g < 3 ? g - 1 : 2 + g / 8; kp[e >> 1][e & 1] = *(const u32x4*)(sKT + e * 12288 + w * 1024); }
                if constexpr (g == 5 || g == 10 || g == 18) wl[(g / 5 + 1) % 3] = *(const bf16x8*)(sW + (g / 5 + 1) * 1024);
                if (g == 18 && !decltype(N)::value) { bad &= live_; PIN_S(bad); }
                if (g == 20 && decltype(N)::value) WAITV(0, pvK);
                SCHED0();
            });
            return bad;
        };
        if (__builtin_expect(take(~0ull, IC<1>{}) != 0, 0)) {
            auto again = [&](u64 live_) { return take(live_, IC<0>{}); };
            GATHER(again);
            asm volatile("s_nop 7" : "+v"(acc[0]), "+v"(acc[1]), "+v"(acc[2]), "+v"(acc[3]), "+v"(acc[4]), "+v"(acc[5]));
        }
        unsigned xv, kx0;
        u64 nk;
        u32x4 ko, kz;
        {
            bf16x8 mm[3];
            u32x4 qp, qa, qb, qz;
            unsigned z0, z1;
            f32x4 s[2], sm;
            sfor<22, 128>([&](auto G) {
                constexpr int g = decltype(G)::value, kt[13] = {5, 5, 5, 3, 4, -1, -1, 3, -1, 4, -1, 3, 4}, ki[13] = {17, 18, 19, 17, 17, 0, 0, 18, 0, 18, 0, 19, 19};
                constexpr int i = g < 102 ? g / 6 : (g < 111 ? 17 + (g - 102) / 3 : (g < 124 ? ki[g < 111 || g > 123 ? 0 : g - 111] : 0));
                constexpr int tt = g < 102 ? g % 6 : (g < 111 ? (g - 102) % 3 : (g < 124 ? kt[g < 111 || g > 123 ? 0 : g - 111] : -1));
                if constexpr (tt >= 0) mf(i, tt, i < 5 ? asb8(v[i < 5 ? i : 0]) : mm[(i < 5 ? 0 : i - 5) % 3]);
                if constexpr (g == 116 || g == 117) MF_VV0(s[g & 1], kp[0][g & 1], qa);
                if constexpr (g == 119 || g == 124) MF_VV(s[~g & 1], kp[1][~g & 1], qb);
                if constexpr (g == 121 || g == 125) MF_VV(s[g == 125], kx[g == 125], qz);
                if constexpr (g == 126) MF_VV0(sm, kz, qz);
                if constexpr (g == 127) MF_VV(sm, ko, qp);
                SCHED0();
                if (g == 22) { LDS_BARRIER(); }
                if (g == 23) mm[0] = mrd(5);
                if (g == 24) wl[2] = *(const bf16x8*)(sW + 5120);
                if constexpr (g > 24 && g < 103 && g % 6 == (g < 102)) mm[(g / 6 - 3) % 3] = mrd(g / 6 + 2);
                if constexpr (g > 24 && g < 102 && g % 6 == 3) wl[(g / 6 + 2) % 3] = *(const bf16x8*)(sW + (g / 6 + 2) * 1024);
                if (g == 104) wl[1] = *(const bf16x8*)(sW + 19 * 1024);
                if (g == 110) PIN_V(wl[1]);
                if (g == 26) { nk = __ballot(pvK == 0); PIN_S(nk); dm0(0); }
                if (g == 30) asm volatile("s_cmp_eq_u64 %1, 0\n\ts_cselect_b32 %0, %2, %3" : "=s"(kx0) : "s"(nk), "s"(kn), "n"(OOB) : "scc");
                if constexpr (g >= G_K && g <= G_K + 5 * G_KS && (g - G_K) % G_KS == 0) dma((g - G_K) / G_KS, qn, kx0, false);
                if (g == G_K + 4 * G_KS - 2) dm0(4);
                if (g == 36) { ps = c0; pr = c2; PIN_S(ps); PIN_S(pr); }
                if (g == 42) { c2 = c0; c0 = c1; PIN_S(c2); PIN_S(c0); }
                if (g == 48) { c1 = pr; ts = 2 * ps; PIN_S(c1); PIN_S(ts); }
                if (g == 60) { mo = mo0 + 4 * c0; PIN_S(mo); }
                if (g == 66) { fs += 256; tz = fs & 8192; PIN_S(fs); PIN_S(tz); }
                if (g == 72) { fs += tz; tr = 2 * c0; PIN_S(fs); PIN_S(tr); }
                if (g == 84) { fk = fs1; fs1 += 1024; PIN_S(fk); PIN_S(fs1); }
                if (g == 90) { fs1 += fs1 & 8192; PIN_S(fs1); }
                if (g == 54) TIGHT(per, it, D_1);
                if (g == 78) { per = PER - per; PIN_S(per); }
                if (g == 96) { X += per; PIN_S(X); }
                if (g == 111) CVT2(qp[0], qp[1], acc[0]);
                if (g == 112) CVT2(qp[2], qp[3], acc[1]);
                if (g == 113) AND2(qa[0], qa[1], qp[0], qp[1], ma);
                if (g == 114) AND2(qa[2], qa[3], qp[2], qp[3], ma);
                if (g == 115) CVT2(z0, z1, acc[2]);
                if (g == 118) AND2(qz[0], qz[1], z0, z1, ma);
                if (g == 119) AND2(qz[2], qz[3], z0, z1, mb2);
                if (g == 116) AND2(qb[0], qb[1], qp[0], qp[1], mb2);
                if (g == 117) AND2(qb[2], qb[3], qp[2], qp[3], mb2);
                if (g == 120) CVT2(kz[0], kz[1], acc[5]);
                if (g == 121) asm volatile("v_cvt_pk_bf16_f32 %0, %2, %3\n\tv_cvt_pk_bf16_f32 %1, %4, %5" : "=&v"(kz[2]), "=&v"(kz[3]) : "v"(acc[5][0]), "v"(acc[5][1]), "v"(acc[5][2]), "v"(acc[5][3]));
                if (g == 123) *(f32x4*)sSw = s[0];
                if (g == 124) CVT2(ko[0], ko[1], acc[3]);
                if (g == 125) CVT2(ko[2], ko[3], acc[4]);
                if (g == 127) *(f32x4*)(sSw + 1040) = s[1];
                SCHED0();
            });
            asm volatile("s_nop 6" : "+v"(sm) :: "memory");
            *(f32x4*)(sSw + 2080) = sm;
        }
        LDS_BARRIER();
        constexpr float SC = 0.07216878364870322f * 1.4426950408889634f;
        {
            const f32x4 s0 = *(const f32x4*)sSr, s1 = *(const f32x4*)(sSr + SS_W), s2 = *(const f32x4*)(sSr + 2 * SS_W), s3 = *(const f32x4*)(sSr + 3 * SS_W);
            SCHED0();
            f32x4 sv = ((s0 + s1) + s2) + s3;
            u32x2 p;
            p[0] = __float_as_uint(fmaxf(fmaxf(fmaxf(sv[0], sv[1]), sv[2]), sv[3]));
            asm volatile(ROWRED("v_max_f32", "%0", "%1", "v_mul_f32 %2, %6, %2\n\tv_mul_f32 %3, %6, %3\n\t", "v_mul_f32 %4, %6, %4\n\tv_mul_f32 %5, %6, %5\n\t")
                         "v_fma_f32 %2, -%0, %6, %2\n\tv_fma_f32 %3, -%0, %6, %3\n\tv_fma_f32 %4, -%0, %6, %4\n\tv_fma_f32 %5, -%0, %6, %5\n\t"
                         "v_exp_f32 %2, %2\n\tv_exp_f32 %3, %3\n\tv_exp_f32 %4, %4\n\tv_exp_f32 %5, %5\n\t"
                         "v_add_f32 %0, %2, %3\n\tv_add_f32 %0, %0, %4\n\tv_add_f32 %0, %0, %5\n\t"
                         ROWRED("v_add_f32", "%0", "%1", "s_nop 1\n\t", "s_nop 1\n\t")
                         "v_rcp_f32 %0, %0\n\ts_nop 0\n\tv_mul_f32 %2, %0, %2\n\tv_mul_f32 %3, %0, %3\n\tv_mul_f32 %4, %0, %4\n\tv_mul_f32 %5, %0, %5\n\t"
                         "v_cvt_pk_bf16_f32 %0, %2, |%3|\n\tv_cvt_pk_bf16_f32 %1, %4, %5"
                         : "+v"(p[0]), "=&v"(p[1]), "+v"(sv[0]), "+v"(sv[1]), "+v"(sv[2]), "+v"(sv[3]) : "s"(SC));
            sst8(rx, ps, ppos, p);
        }
        SCHED0();
        {
            if (__builtin_expect(nk != 0, 0)) {
                wave_wait(rx, fK, fk, spins);
#pragma unroll
                for (int e = 0; e < 6; ++e) dma(e, qn, kn);
            }
            bf16x8 km[6], q[6];
            *(u32x4*)(sKM + w * 1024) = ko;
            *(u32x2*)(sKX + (SL_KM - SL_KT)) = u32x2{kz[0], kz[1]};
            SCHED0();
#pragma unroll
            for (int f = 0; f < 6; ++f) { q[f] = *(const bf16x8*)(sKT + w * 12288 + 6144 + f * 1024); SCHED0(); }
            LDA4(xv, fXv, fs);
            dm0(6);
            asm volatile("s_waitcnt lgkmcnt(5)\n\ts_barrier" ::: "memory");
#pragma unroll
            for (int f = 0; f < 6; ++f) { km[f] = *(const bf16x8*)(sKM + f * 1024); SCHED0(); }
            dma(6, qn, kn, false);
            BIAS(sc[2], mval, mval, mval, mval);
            PIN_V(q[5]);
            dma(7, qn, kn, false);
            float mx = max3r(max3r(sc[0][0], sc[0][1], sc[0][2]), max3r(sc[0][3], sc[1][0], sc[1][1]), max2r(sc[1][2], sc[1][3])), tm;
            PIN_V(mx);
#pragma unroll
            for (int f = 0; f < 6; ++f) { MF_VV(sc[2], km[f], q[f]); if (f & 1) dma(8 + f / 2, qn, kn, false); if (f == 4) dm0(10); SCHED0(); }
            asm volatile("s_nop 5" : "+v"(sc[2]));
            mx = max3r(max3r(mx, sc[2][0], sc[2][1]), sc[2][2], sc[2][3]);
            asm volatile("v_mov_b32 %1, %0\n\ts_nop 1\n\tv_permlane16_swap_b32 %0, %1\n\tv_max_f32 %0, %0, %1\n\tv_mov_b32 %1, %0\n\ts_nop 1\n\tv_permlane32_swap_b32 %0, %1\n\tv_max_f32 %0, %0, %1" : "+v"(mx), "=&v"(tm));
            dma(11, qn, kn);
            mx *= -SC;
#pragma unroll
            for (int e = 0; e < 3; ++e) {
#pragma unroll
                for (int r = 0; r < 4; ++r) sc[e][r] = __builtin_fmaf(sc[e][r], SC, mx);
                PIN_V(sc[e]);
            }
#pragma unroll
            for (int e = 0; e < 3; ++e) {
#pragma unroll
                for (int r = 0; r < 4; ++r) sc[e][r] = __builtin_amdgcn_exp2f(sc[e][r]);
            }
            const f32x4 t4 = (sc[0] + sc[1]) + sc[2];
            float sum = (t4[0] + t4[1]) + (t4[2] + t4[3]);
            PIN_V(sum);
            float inv = __builtin_amdgcn_rcpf(red_sum_q(sum));
            PIN_V(inv);
            unsigned u[6];
#pragma unroll
            for (int e = 0; e < 6; ++e) { u[e] = pk2(sc[e >> 1][2 * (e & 1)] * inv, sc[e >> 1][2 * (e & 1) + 1] * inv); if (!(e & 1)) u[e] = nosent(u[e]); }
            const auto a4 = __builtin_amdgcn_permlane16_swap(u[4], u[4], false, false), a5 = __builtin_amdgcn_permlane16_swap(u[5], u[5], false, false);
            sst16(rx, ts, pt2, u32x4{u[4], u[5], a4[1], a5[1]});
            u32x2 o0, o1;
            asm volatile("v_mov_b64 %0, -1\n\tv_mov_b64 %1, -1" : "=v"(o0), "=v"(o1));
            const u32x4 ones = {o0[0], o0[1], o1[0], o1[1]};
            if (rt) { sst16(rx, ts, pt1, u32x4{u[0], u[1], u[2], u[3]}); sst16(rx, tr, pt1, ones); } else { sst8(rx, ts, pt1, u32x2{u[2], u[3]}); sst8(rx, tr, pt1, u32x2{ones[0], ones[1]}); }
            sst8(rx, pr, ppos, u32x2{ones[0], ones[1]});
            sst16(rx, tr, pt2, ones);
            asm volatile("s_waitcnt vmcnt(11)\n\ts_barrier" : "+v"(xv) :: "memory");
            xv = __builtin_amdgcn_readfirstlane(xv);
            X = xv ? xv + per : X;
        }
    }
    SPIN_END;
}

constexpr int CL_R = 0, CL_C = CL_R + 16384, CL_END = CL_C + 4096;
__device__ __forceinline__ void c_role(const MegaArgs& A, char* smem, const int pool, const int ku, const int id) {
    CHAIN_IDS;
    const int cL = pool >> 2, j = 5 * (pool & 3) + ku;
    go.v = sld8(go.rh, go.so, l * 8);
    GO_ON(N_1);
    unsigned fXv = xm(FX_OFF + (pool & 3) * 64, cL);
    PIN_V(fXv);
    const int hi = (l >> 3) & 1, tk = l & 7;
    const unsigned pos = xm(XM_OFF + w * 32768 + (j / 5) * 8192 + (j % 5) * 1024 + (lq * 16 + cL * 8 + tk) * 16 + hi * 8, 0);
    const unsigned umo = xm(UM_OFF + (16 * j + 2 * w) * 1024, cL), po0 = xm(XP_OFF + w * 8192, cL), pto = xm(PT_OFF + w * 16384, cL), uto = xm(UT_OFF + (64 * j + 8 * w) * 1024, cL);
    const unsigned fU = lp(SG_FL + FL_U + (8 * w + tk) * 64 + (pool & 3) * 4);
    const unsigned xo = lp((2 * j + (w >> 1)) * 1024 + (lq * 16 + 8 * (w & 1) + tk) * 16 + hi * 8), sto = l < 8 ? lp(SG_ST + (4 * j + w) * 64 + l * 8) : OOB;
    u32x2 alt;
    { const u32x2 m = *(const u32x2*)(A.mem0 + tk * D_ + 32 * j + 16 * hi + 4 * lq); alt = u32x2{nosent(m[0]), m[1]}; }
    f32x4* sRw0 = (f32x4*)(smem + CL_R) + w * 256 + l;
    f32x4* sRw1 = (f32x4*)(smem + CL_R) + w * 256 + 128 + (l ^ 8);
    const f32x4* sRr = (const f32x4*)(smem + CL_R) + (hi * 2 + (w >> 1)) * 64 + lq * 16 + ((8 * (w & 1) + tk) ^ (8 * hi));
    f32x4* sC = (f32x4*)(smem + CL_C);
    int mb = 0;
    u32x4 ua[16], ub[16], um[4];
#pragma unroll
    for (int e = 0; e < 16; ++e) ua[e] = u32x4{0u, 0u, 0u, 0u};
#pragma unroll
    for (int e = 0; e < 4; ++e) um[e] = u32x4{0u, 0u, 0u, 0u};
    for (int s = 0; s < NTOUCH; ++s) sst8(rx, seg_base(cL, s) + lp(SG_X1), xo, u32x2{0u, 0u});
    unsigned X = rtime() - 2 * PER;
    auto seg_so = [&](int sg) { return (unsigned)sg < NSEG_ ? seg_base(cL, sg) : OOB; };
    unsigned dx = 0, sb0 = OOB, sb1 = seg_so(-cL * lag), pvU = sld4(rx, sb1, fU), ts = pto + 4 * PT_BUF, ts2 = ts + 16384;
    auto step = [&](const int it, auto LAST, u32x4 (&up)[16], u32x4 (&ut)[16]) {
        constexpr bool last = decltype(LAST)::value == 1, fin = decltype(LAST)::value == 2;
        const int nb = (mb == 2) ? 0 : mb + 1, pb = mb ? mb - 1 : 2, sg = it - cL * lag;
        if (!last) CHAIN_WAIT(pvU, fU, sb1, 0);
        u32x4 pt[16];
        u32x2 xr;
        auto pt_load = [&](int e) { LDA(pt[e], e < 4 || e >= 12 ? l16k : l16b, e < 8 ? ts2 : ts, (e & 3) * 1024); };
        pt_load(0);
        SCHED0();
        unsigned uo = sb1 != OOB ? uto + ut_slot(sg) : OOB, uo2 = uo + 65536;
        PIN_S(uo); PIN_S(uo2);
        auto ut_load = [&](int e) {
            if (!last) LDA(ut[e], (e & 4) ? l16k : l16b, e < 8 ? uo : uo2, (e & 3) * 1024);
        };
        pt_load(1);
        SCHED0();
        unsigned sbr = (sb0 ^ (cL << 13)) + lp(SG_XR), sbx = sb0 + lp(SG_X1), sb2 = seg_so(sg + 1);
        PIN_S(sbr); PIN_S(sbx); PIN_S(sb2);
        pt_load(2);
        SCHED0();
        unsigned fs = fx_step(it), cu = umo + mb * (2 * UM_BUF), cu2 = cu + 16384, cp = po0 + mb * (2 * XP_BUF), cr = pb * (2 * XM_BUF), cr2 = cr + 8192;
        PIN_S(fs); PIN_S(cu); PIN_S(cu2); PIN_S(cp); PIN_S(cr); PIN_S(cr2);
        pt_load(3);
        SCHED0();
        unsigned cs = sg < 0 ? OOB : nb * (2 * XM_BUF), cs2 = cs + 8192;
        X += PER - dx;
        PIN_S(cs); PIN_S(cs2); PIN_S(X);
#pragma unroll
        for (int e = 4; e < 13; ++e) { pt_load(e); if (e < 12) GAP(); }
        u32x2 st;
        {
            f32x4 d[2][2];
            auto check = [&](auto E0, auto N, auto W) {
                constexpr int e0 = decltype(E0)::value, n = decltype(N)::value;
                auto look = [&](auto WW) {
                    asm volatile("s_waitcnt vmcnt(%2)" : "+v"(pt[e0]), "+v"(pt[e0 + 1]) : "n"(decltype(WW)::value) : "memory");
                    if (n == 4) asm volatile("" : "+v"(pt[e0 + n - 2]), "+v"(pt[e0 + n - 1]));
                    unsigned m = 0;
#pragma unroll
                    for (int e = e0; e < e0 + n; ++e) m = mx4(m, pt[e]);
                    return m; };
                if (__builtin_expect(__ballot(look(W) == ~0u) != 0, 0)) {
#pragma unroll 1
                    for (;;) {
#pragma unroll
                        for (int e = e0; e < e0 + n; ++e) pt_load(e);
                        LIVE;
                        if (!BAD(look(IC<0>{}))) break;
                    }
                }
            };
            sfor<0, 40>([&](auto G) {
                constexpr int g = decltype(G)::value, n = g & 1;
                if constexpr (g == 0) check(IC<0>{}, IC<4>{}, IC<9>{});
                if constexpr (g == 8) check(IC<4>{}, IC<2>{}, IC<10>{});
                if constexpr (g == 16) check(IC<6>{}, IC<2>{}, IC<(last ? 9 : 11)>{});
                if constexpr (g == 24) check(IC<8>{}, IC<4>{}, IC<(last ? 5 : 10)>{});
                if constexpr (g == 32) check(IC<12>{}, IC<2>{}, IC<(last ? 3 : 11)>{});
                if constexpr (g == 36) { check(IC<14>{}, IC<2>{}, IC<(last ? 0 : 10)>{}); PIN_V(xr); }
                if constexpr (g < 4) MF_VV0(d[n][g >> 1], um[2 * n], pt[g >> 1]);
                else if constexpr (g < 8) MF_VV(d[n][(g >> 1) & 1], um[2 * n + 1], pt[g >> 1]);
                else if constexpr (g < 24) {
                    constexpr int hh = (g - 8) >> 1;
                    const u32x4 x = up[8 * n + hh], z = pt[4 + (hh >> 1)];
                    const u32x2 xl = {x[0], x[1]}, zl = {z[2 * (hh & 1)], z[2 * (hh & 1) + 1]};
                    MH_(d[n][0], "v", xl, "v", zl);
                } else MF_VV(d[n][1], up[8 * n + ((g - 24) >> 1)], pt[8 + ((g - 24) >> 1)]);
                constexpr int rq = g < 32 ? ((g & 7) == 2 ? 0 : ((g & 7) == 5 ? 1 : ((g & 7) == 7 ? 2 : -99))) + 3 * (g >> 3) : (g == 33 ? 12 : (g == 35 ? 13 : -99));
                if constexpr (rq >= 0 && rq < 3) pt_load(13 + rq); else if constexpr (rq == 3) LDA8(xr, xo, sbr, 0); else if constexpr (rq > 3) ut_load(rq - 4);
                if (g == 24) sRw0[0] = d[0][0];
                if (g == 27) sRw1[0] = d[1][0];
                SCHED0();
            });
            const f32x4 xf = {lo16(xr[0]), hi16(xr[0]), lo16(xr[1]), hi16(xr[1])};
            asm volatile("" : "+v"(d[0][1]) : "v"(xf) : "memory");
            sRw0[64] = d[0][1];
            ut_load(10);
            asm volatile("s_nop 2" : "+v"(d[1][1]) :: "memory");
            sRw1[64] = d[1][1];
            ut_load(11);
            LDS_BARRIER();
            const f32x4 s0 = sRr[0], s1 = sRr[256], s2 = sRr[512], s3 = sRr[768];
            ut_load(12);
            SCHED0();
            f32x4 s = (((xf + s0) + s1) + s2) + s3;
            sst8(rx, sbx, xo, u32x2{pk2(s[0], s[1]), pk2(s[2], s[3])});
            float sm = (s[0] + s[1]) + (s[2] + s[3]);
            sm += dpp_ror8(sm);
            PIN_V(sm);
            ut_load(13);
            sm = red_sum_q(sm) * (1.0f / 32);
            PIN_V(sm);
            ut_load(14);
            s -= sm;
            float sq = __builtin_fmaf(s[3], s[3], __builtin_fmaf(s[2], s[2], __builtin_fmaf(s[1], s[1], s[0] * s[0])));
            sq += dpp_ror8(sq);
            PIN_V(sq);
            ut_load(15);
            sq = red_sum_q(sq);
            st = u32x2{nosent(__float_as_uint(sm)), __float_as_uint(sq)};
            PIN_V(st);
        }
        SCHED0();
        if (last) { DRAIN(); sst8(rx, sb0, sto, st); return; }
        unsigned xv;
        rt_until(X + E_X);
        LDA4(xv, fXv, fs);
        ts = pto + mb * (2 * PT_BUF); ts2 = ts + 16384;
        PIN_S(ts); PIN_S(ts2);
        asm volatile("s_waitcnt vmcnt(4)" ::: "memory");
        sst8(rx, sb0, sto, st);
        sst8(rx, cr, pos, u32x2{~0u, ~0u});
        sst8(rx, cr2, pos, u32x2{~0u, ~0u});
        WAITV(3, xv);
        X = __builtin_amdgcn_readfirstlane(xv);
        PIN_S(X);
        if (__builtin_expect(X == 0, 0)) {
#pragma unroll 1
            for (;;) {
                LDA4(xv, fXv, fs);
                LIVE;
                WAITV(0, xv);
                X = __builtin_amdgcn_readfirstlane(xv);
                if (X || !live_) break;
            }
        }
        unsigned d2;
        TIGHT(dx, it, D_1); TIGHT(d2, it, D_2);
        if (fin) {
#pragma unroll 1
            for (;;) {
                rt_until(X + E_U - d2);
#pragma unroll
                for (int i = 0; i < 4; ++i) LDI(um[i], l16b, (i & 2) ? cu2 : cu, (i & 1) * 1024);
                LIVE;
                asm volatile("s_waitcnt vmcnt(0)" : "+v"(um[0]), "+v"(um[1]), "+v"(um[2]), "+v"(um[3]) :: "memory");
                if (!BAD(mx4(mx4(mx4(mx4(0u, um[0]), um[1]), um[2]), um[3]))) break;
            }
            rt_until(X + E_L);
            mb = nb; sb0 = sb1; sb1 = sb2;
            return;
        }
        u32x4 p[5];
        f32x4 a[2], b[2];
        auto take2 = [&](u64 live_) {
            unsigned m = 0;
            u64 bad;
#pragma unroll
            for (int i = 0; i < 4; ++i) { WAITV(9 - i, um[i]); m = mx4(m, um[i]); PIN_V(m); }
#pragma unroll
            for (int i = 0; i < 5; ++i) {
                WAITV(4 - i, p[i]);
#pragma unroll
                for (int e = 0; e < 4; ++e) {
                    f32x4& d = (e & 1) ? b[e >> 1] : a[e >> 1];
                    if (i == 0) MF_VV0(d, ut[8 * (e >> 1) + (e & 1)], p[0]); else if (i < 4) MF_VV(d, ut[8 * (e >> 1) + 2 * (i & 3) + (e & 1)], p[i]); else MF_VV(d, um[e], p[4]);
                    SCHED0();
                    if (e == 0) { m = mx4(m, p[i]); if (i < 4) PIN_V(m); else { bad = __ballot(m == ~0u); PIN_S(bad); } }
                    if (e == 1 && i == 4) { bad &= live_; PIN_S(bad); }
                    SCHED0();
                }
            }
            return bad;
        };
        {
            unsigned tP = X + E_P - d2, tU = X + E_U - d2, ask = 1;
            PIN_S(ask);
            rt_until(tU);
#pragma unroll 1
            for (;;) {
#pragma unroll
                for (int i = 0; i < 4; ++i) LDI(um[i], l16b, (i & 2) ? cu2 : cu, (i & 1) * 1024);
                LDA4(pvU, fU, sb2);
                LIVE;
                if (__builtin_expect(ask | !FASTR, 1)) rt_fine(tP);
                LD5(p, cp);
                ask = 0; PIN_S(ask);
                SCHED0();
                if (!take2(live_)) break;
            }
        }
        f32x4 x;
        asm volatile("v_add_f32_dpp %0, %8, %4 row_ror:8 row_mask:0xf bank_mask:0x3\n\tv_add_f32_dpp %1, %9, %5 row_ror:8 row_mask:0xf bank_mask:0x3\n\t"
                     "v_add_f32_dpp %2, %10, %6 row_ror:8 row_mask:0xf bank_mask:0x3\n\tv_add_f32_dpp %3, %11, %7 row_ror:8 row_mask:0xf bank_mask:0x3"
                     : "=&v"(x[0]), "=&v"(x[1]), "=&v"(x[2]), "=&v"(x[3]) : "v"(a[0][0]), "v"(a[0][1]), "v"(a[0][2]), "v"(a[0][3]), "v"(b[0][0]), "v"(b[0][1]), "v"(b[0][2]), "v"(b[0][3]));
        asm volatile("s_nop 0\n\tv_add_f32_dpp %0, %8, %4 row_ror:8 row_mask:0xf bank_mask:0xc\n\tv_add_f32_dpp %1, %9, %5 row_ror:8 row_mask:0xf bank_mask:0xc\n\t"
                     "v_add_f32_dpp %2, %10, %6 row_ror:8 row_mask:0xf bank_mask:0xc\n\tv_add_f32_dpp %3, %11, %7 row_ror:8 row_mask:0xf bank_mask:0xc"
                     : "+v"(x[0]), "+v"(x[1]), "+v"(x[2]), "+v"(x[3]) : "v"(b[1][0]), "v"(b[1][1]), "v"(b[1][2]), "v"(b[1][3]), "v"(a[1][0]), "v"(a[1][1]), "v"(a[1][2]), "v"(a[1][3]));
        sC[w * 64 + l] = x;
        LDS_BARRIER();
        const f32x4 s = ((sC[l] + sC[64 + l]) + sC[128 + l]) + sC[192 + l];
        const u32x2 r = {nosent(pk2(s[0], s[1])), pk2(s[2], s[3])};
        sst8(rx, cs, pos, r);
        sst8(rx, cs2, pos, r);
        SCHED0();
        if (__builtin_expect(cs == OOB, 0)) { sst8(rx, nb * (2 * XM_BUF), pos, alt); sst8(rx, nb * (2 * XM_BUF) + 8192, pos, alt); }
        PIN_V(pvU);
        mb = nb; sb0 = sb1; sb1 = sb2;
    };
    int it = 0;
    PIN_S(spins); PIN_V(pvU); PIN_V(alt);
#pragma unroll 1
    do { step(it, IC<0>{}, ua, ub); step(it + 1, IC<0>{}, ub, ua); it += 2; } while (it + 1 < itend);
    if (LAGV & 1) { step(it, IC<2>{}, ua, ub); step(it + 1, IC<1>{}, ub, ua); } else step(it, IC<1>{}, ua, ub);
    SPIN_END;
}

constexpr int VL_V = 20480, VL_W = VL_V + 6144, VL_END = VL_W + 81920;
__device__ __forceinline__ void vm_role(const MegaArgs& A, char* smem, const int h, const int id) {
    CHAIN_IDS;
    const int pool = h >> 2;
    bf16x8 wf[3][20], wo[10][4];
    char* sWo = smem + VL_W + w * 20480 + l * 16;
    {
        bf16x8 tmp[20];
        load_vo<5>(smem, w, l, go, A.Wv + h * DH_, A.Wo + (size_t)(h * DH_) * D_, wf, [&](int k, int ct, int ww) -> bf16x8& { return k < 4 ? wo[ct][(k - ww) & 3] : tmp[4 * (ct >> 1) + 2 * (k & 1) + (ct & 1)]; });
#pragma unroll
        for (int i = 0; i < 20; ++i) *(bf16x8*)(sWo + i * 1024) = tmp[i];
    }
    GO_ON(N_1);
    CHAIN_STAGE;
    unsigned upos[5];
#pragma unroll
    for (int m = 0; m < 5; ++m) upos[m] = xm(UM_OFF + ((10 * w + 2 * m + (lq & 1)) * 8 + (h >> 2)) * 1024 + ((h & 3) * 16 + l16) * 16, lq >> 1);
    char* sV = smem + VL_V + l * 16;
    const char* sV1 = sV + ((w + 1) & 3) * 1024; const char* sV2 = sV + ((w + 2) & 3) * 1024; const char* sV3 = sV + ((w + 3) & 3) * 1024;
    const unsigned xw = (h == 1 && t < 8) ? xm(FX_OFF + (t & 3) * 64, t >> 2) : OOB;
    int mb = 0;
    unsigned X = rtime() - 2 * PER, dx = 0;
    for (int it = 0; it < itend; ++it) {
        const int nb = (mb == 2) ? 0 : mb + 1, pb = mb ? mb - 1 : 2;
        unsigned mo = mo0 + mb * (2 * XM_BUF), fs = fx_step(it), us = mb * (2 * UM_BUF), ur = pb * (2 * UM_BUF);
        X += PER - dx;
        PIN_S(mo); PIN_S(fs); PIN_S(us); PIN_S(ur); PIN_S(X);
        rt_fine(X - E_G);
        f32x4 acc[3];
        u32x4 v[5];
        auto take = [&](u64 live_) {
            unsigned m = 0;
            u64 bad;
            sfor<0, 11>([&](auto G) {
                constexpr int g = decltype(G)::value, e = g < 9 && !(g & 1) ? g / 2 : -1;
                if constexpr (e >= 0) { WAITV(4 - e, v[e]); *(u32x4*)(sg0 + e * 1024) = v[e]; SCHED0(); }
                if (g < 3) MF_AV0(acc[g % 3], wf[g % 3][0], v[0]); else MF_AV(acc[g % 3], wf[g % 3][g / 3], v[g / 3]);
                SCHED0();
                if constexpr (e >= 0) { m = mx4(m, v[e]); if (e < 4) PIN_V(m); else { bad = __ballot(m == ~0u); PIN_S(bad); } }
                if (g == 9) { bad &= live_; PIN_S(bad); }
                SCHED0();
            });
            return bad;
        };
        GATHER(take);
        bf16x8 wl[4];
        u32x4 vf[6], o;
        f32x4 d[10];
        unsigned xv;
        {
            constexpr int MW = 5;
            bf16x8 mm[MW];
            u32x2 vz;
            u64 tb64;
            unsigned t0, t1;
            asm volatile("s_waitcnt lgkmcnt(0)\n\ts_barrier\n\ts_memrealtime %0" : "=s"(tb64) :: "memory");
            mm[0] = mrd(5);
            SCHED0();
            sfor<11, 60>([&](auto G) {
                constexpr int g = decltype(G)::value, i = g < 45 ? g / 3 : (g < 50 ? g - 30 : 15 + (g - 50) / 2), tt = g < 45 ? g % 3 : (g < 50 ? 2 : g & 1);
                const bf16x8 xb = i < 5 ? asb8(v[i < 5 ? i : 0]) : mm[(i < 5 ? 0 : i - 5) % MW];
                MF_AV(acc[tt], wf[tt][i], xb);
                SCHED0();
                if constexpr (g < 15) mm[g - 10] = mrd(g - 5);
                if constexpr (g >= 18 && g < 46 && g % 3 == 0) mm[(g / 3 - 6) % MW] = mrd(g / 3 + 4);
                if constexpr (g >= 54 && g < 58) wl[g - 54] = *(const bf16x8*)(sWo + (g - 54) * 1024);
                if (g == 17) asm volatile("s_waitcnt lgkmcnt(0)" : "+s"(tb64) :: "memory");
                if (g == 19) asm volatile("s_sub_u32 %0, %2, %3\n\ts_add_u32 %1, %2, %4" : "=&s"(t0), "=&s"(t1) : "s"((unsigned)tb64), "s"(X), "s"(E_S) : "scc");
                if (g == 22) asm volatile("s_cmp_gt_i32 %1, %3\n\ts_cselect_b32 %0, %2, %0" : "+s"(X) : "s"(t0), "s"(t1), "s"(E_T) : "scc");
                if (g == 23) asm volatile("v_or_b32 %0, 1, %1" : "=v"(xv) : "s"(X));
                if (g == 25) sst4(rx, fs, xw, xv);
                if (g == 52) CVT2(vz[0], vz[1], acc[2]);
                if (g == 53) *(u32x2*)(sV + 4096 + (w >> 1) * 1024 + (w & 1) * 8) = vz;
                SCHED0();
            });
        }
        asm volatile("s_nop 2");
        SCHED0();
        CVT2(vf[0][0], vf[0][1], acc[0]);
        SCHED0();
        asm volatile("s_nop 1");
        SCHED0();
        CVT2(vf[0][2], vf[0][3], acc[1]);
        *(u32x4*)(sV + w * 1024) = vf[0];
        SCHED0();
        asm volatile("s_nop 0");
        sfor<0, 67>([&](auto G) {
            constexpr int g = decltype(G)::value, m = g < 10 ? 0 : (g - 10) / 10, r = g < 10 ? 0 : (g - 10) % 10, kk = g < 10 ? 0 : 1 + r / 2, ct = g < 10 ? g : 2 * m + (r & 1);
            if constexpr (g < 60) {
                if constexpr (kk >= 4) MF_VV(d[ct], vf[kk], wl[2 * (kk & 1) + (r & 1)]);
                else if constexpr (ct == 0) { if (kk == 0) MF_VA0(d[0], vf[0], wo[0][0]); else MF_VA(d[0], vf[kk], wo[0][kk]); }
                else { if (kk == 0) MF_VV0(d[ct], vf[0], wo[ct][0]); else MF_VV(d[ct], vf[kk], wo[ct][kk]); }
            }
            SCHED0();
            if (g == 3) { LDS_BARRIER(); }
            if (g == 4) vf[1] = *(const u32x4*)sV1;
            if (g == 5) vf[2] = *(const u32x4*)sV2;
            if (g == 6) vf[3] = *(const u32x4*)sV3;
            if (g == 7) vf[4] = *(const u32x4*)(sV + 4096);
            if (g == 8) vf[5] = *(const u32x4*)(sV + 5120);
            if constexpr (g >= 17 && g < 51 && (g - 17) % 10 < 4) wl[(g - 17) % 10] = *(const bf16x8*)(sWo + ((g - 17) / 10 * 4 + 4 + (g - 17) % 10) * 1024);
            if (g == 12) LDA4(xv, fXv, fs);
            if constexpr (m > 0) {
                if (g == 60) asm volatile("s_nop 3");
                if (r == 1) CVT2(o[0], o[1], d[2 * m - 2]);
                SCHED0();
                if (g == 61) asm volatile("s_nop 1");
                if (r == 2) CVT2(o[2], o[3], d[2 * m - 1]);
                if (r == 3) { o[0] = nosent(o[0]); o[2] = nosent(o[2]); asm volatile("" : "+v"(o[0]), "+v"(o[2])); }
                if (r == 4) asm volatile("v_permlane16_swap_b32 %0, %1" : "+v"(o[1]), "+v"(o[3]));
                if (r == 5) asm volatile("v_permlane16_swap_b32 %0, %1" : "+v"(o[0]), "+v"(o[2]));
                if (r == 6) sst16(rx, us, upos[m - 1], o);
            }
            SCHED0();
        });
#pragma unroll
        for (int m = 0; m < 5; ++m) { __builtin_amdgcn_s_sleep(VSLP); sst16(rx, ur, upos[m], u32x4{~0u, ~0u, ~0u, ~0u}); SCHED0(); }
        WAITV(10, xv);
        xv = __builtin_amdgcn_readfirstlane(xv);
        X = xv ? xv : X;
        TIGHT(dx, it, D_1);
        mb = nb;
    }
    SPIN_END;
}
#define DBG_LD 0
#define WORK_IDS const int t = threadIdx.x, l = t & 63, l16 = l & 15, lq = l >> 4; const int w = __builtin_amdgcn_readfirstlane(t >> 6); \
    GO_DECL(id >> 5); const unsigned l16b = l * 16, dvo = w * 2048 + l * 16 + DBG_LD, lds0 = lds_addr(smem) + w * 2048; unsigned spins; SZERO(spins)
#define XSET(so, base, ld) asm volatile("s_mov_b32 m0, %2\n\ts_mov_b32 %0, %1" : "=s"(so) : "s"(base), "s"(ld) : "m0")
#define XADV(so) asm volatile("s_add_u32 m0, m0, 0x2000\n\ts_add_u32 %0, %0, 0x4000" : "+s"(so) :: "m0", "scc")
#define XDMA(so, rt) asm volatile("buffer_load_dwordx4 %0, %1, %2 offen offset:%3 sc1 lds" :: "v"(dvo), "s"(rx), "s"(so), "n"((rt) * 1024) : "memory")
#define XALL(so, base, ld) _Pragma("unroll") for (int e_ = 0; e_ < 10; ++e_) { if (e_ % 5 == 0) XSET(so, base, ld); else XADV(so); XDMA(so, e_ / 5); }
#define XALL2(so, base, ld) _Pragma("unroll") for (int e_ = 0; e_ < 5; ++e_) { if (e_ == 0) XSET(so, base, ld); else XADV(so); XDMA(so, 0); XDMA(so, 1); }
#define VMB(n) asm volatile("s_waitcnt vmcnt(%0)\n\ts_barrier" :: "n"(n) : "memory")
#define VMB_LATE(k) asm volatile("s_cmp_lg_u32 %0, %1\n\ts_cbranch_scc1 1f\n\ts_waitcnt vmcnt(%2)\n1:\n\ts_barrier" :: "s"(xs), "n"(OOB), "n"(k) : "memory", "scc")
#define VMW(n) asm volatile("s_waitcnt vmcnt(%0)" :: "n"(n) : "memory")
#define NEXT_DECL(asked) const unsigned fXo = l < 4 ? lp(SG_FL + FL_X + ((h & 12) + l) * 128) : OOB, fx1 = (((TR >> 2) & ~L & 1) ^ hside(h)) << 13; const int dl = __builtin_amdgcn_readfirstlane((L ^ 1) * TR), ld0 = __builtin_amdgcn_readfirstlane(L ? -K_W : K_0); \
    unsigned xb = seg_base(0, 0), xs, fb = seg_base(hside(h), 0), msk, so, pv, pend, fXv = xm(FX_OFF + ((h >> 2) & 3) * 64, hside(h)), xv, fs; PIN_V(fXv); if (L) wave_wait(rx, fXo, fb, spins); if (!(asked)) { XALL(so, xb, lds0); } VMB(5); int s; SZERO(s)
#define G_L1 150
#define G_E1 182
#define G_L2 210
#define NEXT_ADDR(a, st) \
    if (g == a) { s = __builtin_amdgcn_readfirstlane(s); PIN_S(s); xb = (s + 1) << 19; PIN_S(xb); } \
    if (g == a + st) { xs = (xb >> 8) & 8192; PIN_S(xs); } \
    if (g == a + 2 * st) { xb = (xb | xs) + 2 * SG_OFF; PIN_S(xb); } \
    if (g == a + 3 * st) { fb = (xb ^ fx1) - (dl << 19); PIN_S(fb); } \
    if (g == a + 5 * st) asm volatile("s_cmp_lt_i32 %1, %2\n\ts_cselect_b32 %0, 0, 15" : "=s"(msk) : "s"(s), "s"(dl - 1) : "scc"); \
    if (g == a + 6 * st) asm volatile("s_cmp_lt_i32 %1, 63\n\ts_cselect_b32 %0, %0, %2" : "+s"(fb) : "s"(s), "n"(OOB) : "scc"); \
    if (g == a + 7 * st) { PIN_S(s); fs = (s - ld0) << 8; PIN_S(fs); } \
    if (g == a + 8 * st) { fs += fs & ~8191u; PIN_S(fs); } \
    if (g == a + 9 * st) asm volatile("s_cmp_lt_i32 %0, 0\n\ts_cselect_b32 %0, %1, %0" : "+s"(fs) : "n"(OOB) : "scc")
#define NEXT_LOOK(n, ld) \
    if (g == G_L1 || g == G_L2) LDA4(pv, fXo, fb); \
    if (g == G_E1) { WAITV(n, pv); xs = (unsigned)__ballot(pv == 0); PIN_S(xs); } \
    if (g == G_E1 + 2) asm volatile("s_and_b32 %0, %0, %2\n\ts_cselect_b32 %0, %3, %1" : "+s"(xs) : "s"(xb), "s"(msk), "n"(OOB) : "scc")
#define NEXT_GO(n, ld, fl) \
    WAITV(n, pv); \
    if (xs != OOB) { asm volatile("s_barrier" ::: "memory"); prime(); SCHED0(); tail(); LDA4(xv, fXv, fs); WAITV(0, xv); sst4(rx, pend, fl, 1u); } \
    else { \
        tail(); \
        if ((unsigned)__ballot(pv == 0) & msk) { DRAIN(); sst4(rx, pend, fl, 1u); if (s == NSEG_ - 1) { SPIN_END; __builtin_amdgcn_endpgm(); } pend = OOB; wave_wait(rx, fXo, fb, spins); } \
        LDA4(xv, fXv, fs); \
        XALL(so, xb, ld); \
        WAITV(10, xv); sst4(rx, pend, fl, 1u); \
        VMB(6); prime(); \
    } \
    so = __builtin_amdgcn_readfirstlane(xv); \
    if (__builtin_expect(so == 0, 0) && fs != OOB) { \
        _Pragma("unroll 1") for (int k_ = 0; k_ < 8 && so == 0; ++k_) { __builtin_amdgcn_s_sleep(4); so = __builtin_amdgcn_readfirstlane(sld4(rx, fs, fXv)); MEMFENCE(); } \
    } \
    so += E_W; PIN_S(so); rt_until(so)

constexpr int QL_W = 81920;
__device__ __forceinline__ void qk_role(const MegaArgs& A, char* smem, const int L, const int h, const int id) {
    WORK_IDS;
    bf16x8 wf[6][20];
    char* sW = smem + QL_W + w * 20480 + l * 16;
    unsigned x0;
    load_qk<0, 11>(smem, w, l, go, A.Wq + h * DH_, A.Wk + h * DH_, [&](int m) { return 6 * w + m; }, wf, [&]() {
        const unsigned tag = (__builtin_amdgcn_readlane(go.v[0], 63) + 2) & ~1u, a0 = __builtin_amdgcn_readlane(go.v[0], HS_A / 8), a1 = __builtin_amdgcn_readlane(go.v[1], HS_A / 8);
        const rsrc_t rx = arena(A, a0 & 1);
        x0 = ((a0 ^ tag) | (a1 ^ tag) | 2 * L) > 1 ? OOB : seg_base(0, 0);
        PIN_S(x0);
        unsigned so;
        XALL(so, x0, lds0);
        GO_ASK;
    });
#pragma unroll
    for (int i = 0; i < 20; ++i) *(bf16x8*)(sW + i * 1024) = wf[5][i];
    GO_ON(N_1);
    const unsigned fI = l == 0 ? xm(FI_OFF + (h & 15) * 64 + (4 * L + w) * 4, hside(h)) : OOB;
    const unsigned po0 = 2 * QK_OFF + ((L * 16 + (h & 15)) * 4 + (w & 2)) * 16384 + (w & 1) * 3072 + hside(h) * 8192;
    const int it0 = L * A.lag;
    const unsigned l16s = l16b;
    NEXT_DECL(x0 != OOB);
    unsigned vx = l16b, ld = lds0, po;
    bf16x8 xw[3], wl[2];
    f32x4 acc[2][6];
    u32x4 o[2];
    auto prime = [&]() { xw[0] = *(const bf16x8*)(smem + vx); xw[1] = *(const bf16x8*)(smem + vx + 2048); };
    auto tail = [&]() {
#pragma unroll
        for (int f = 0; f < 3; ++f) { CVT2(o[f & 1][0], o[f & 1][1], acc[1][2 * f]); CVT2(o[f & 1][2], o[f & 1][3], acc[1][2 * f + 1]); sst16(rx, po, l16s + f * 1024, o[f & 1]); SCHED0(); }
    };
    prime();
    wl[0] = *(const bf16x8*)sW;
#pragma unroll 1
    for (;; ++s) {
        SCHED0();
        sfor<0, 240>([&](auto G) {
            constexpr int g = decltype(G)::value, i = g / 6, tt = g % 6, ks = i % 20;
            f32x4& d = acc[i / 20][tt];
            if constexpr (tt == 5) { if (ks == 0) MF_VV0(d, wl[i & 1], xw[i % 3]); else MF_VV(d, wl[i & 1], xw[i % 3]); }
            else if constexpr (20 * tt + ks < 64) { if (ks == 0) MF_AV0(d, wf[tt][ks], xw[i % 3]); else MF_AV(d, wf[tt][ks], xw[i % 3]); }
            else { if (ks == 0) MF_VV0(d, wf[tt][ks], xw[i % 3]); else MF_VV(d, wf[tt][ks], xw[i % 3]); }
            SCHED0();
            if constexpr (tt == 1 && i < 38) xw[(i + 2) % 3] = *(const bf16x8*)(smem + vx + (i + 2) % 20 * 2048 + (i + 2) / 20 * 1024);
            if constexpr (tt == 3) wl[~i & 1] = *(const bf16x8*)(sW + (i + 1) % 20 * 1024);
            NEXT_ADDR(2, 6);
            if (g == 4) { PIN_S(s); po = (s & (TR - 1)) << 21; PIN_S(po); }
            if (g == 10) { so = (po >> 8) & 8192; PIN_S(so); }
            if (g == 16) { po = (po0 ^ so) + po; PIN_S(po); }
            if (g == 58) { VMW(0); }
            if (g == 60) asm volatile("s_barrier" ::: "memory");
            if (g == 62) { PIN_S(s); pend = s + it0; so = pend & 7; PIN_S(pend); PIN_S(so); }
            if (g == 64) { pend = (pend >> 3) << 4; PIN_S(pend); }
            if (g == 66) { pend = (pend | so) << 10; PIN_S(pend); }
            if (g == 68) { ld = 2 * lds0 + 40960 - ld; PIN_S(ld); }
            if constexpr (g >= 122 && g < 140) {
                constexpr int f = (g - 122) / 6;
                if (tt == 2) CVT2(o[0][0], o[0][1], acc[0][2 * f]);
                if (tt == 4) CVT2(o[0][2], o[0][3], acc[0][2 * f + 1]);
                if (tt == 0) sst16(rx, po, l16s + f * 1024, o[0]);
            }
            if (g == 140) { po += 16384; PIN_S(po); }
            NEXT_LOOK(0, ld);
            {
                constexpr int a[10] = {186, 190, 194, 198, 202, 206, 214, 218, 222, 226};
#pragma unroll
                for (int e = 0; e < 10; ++e) {
                    if (g == a[e]) { if (e % 5 == 0) XSET(so, xs, ld); else XADV(so); }
                    if (g == a[e] + (e == 5 ? 6 : 2)) XDMA(so, e / 5);
                }
            }
            if (g == 236) { vx ^= 40960; PIN_V(vx); }
            SCHED0();
        });
        NEXT_GO(5, ld, fI);
    }
}

constexpr int OL_V = 40960, OL_W = OL_V + 12288;
__device__ __forceinline__ void vo_role(const MegaArgs& A, char* smem, const int L, const int h, const int id) {
    WORK_IDS;
    bf16x8 wf[3][20], wo[10][6];
    char* sW = smem + OL_W + w * 24576 + l * 16;
    {
        load_vo<0>(smem, w, l, go, A.Wv + h * DH_, A.Wo + (size_t)(h * DH_) * D_, wf, [&](int k, int ct, int) -> bf16x8& { return wo[ct][k]; });
#pragma unroll
        for (int i = 0; i < 24; ++i) *(bf16x8*)(sW + i * 1024) = wo[4 + i / 8 * 2 + (i & 1)][2 + (i >> 1 & 3)];
    }
    GO_ON(N_1);
    const unsigned fU = l == 0 ? lp(SG_FL + FL_U + h * 64 + w * 4) : OOB;
    const unsigned l16s = l16b;
    const unsigned ua = xm(UT_OFF + (320 * w + h) * 1024, L) + l16s, ub = ua ^ (w == 3 ? 8192 : 0);
    char* sV = smem + OL_V + l * 16;
    char* sVw = sV + w * 1024;
    char* sVh = sV + 4096 + (w >> 1) * 1024 + (w & 1) * 8;
    NEXT_DECL(false);
    unsigned uo;
    constexpr int XW = 4;
    bf16x8 xw[XW], wl[4];
    f32x4 d[4][2];
    u32x4 o[2];
    auto prime = [&]() {
#pragma unroll
        for (int i = 0; i < XW - 1; ++i) xw[i] = *(const bf16x8*)(smem + l16b + i * 2048);
    };
    auto tail = [&]() {
#pragma unroll
        for (int f = 0; f < 2; ++f) { CVT2(o[f][0], o[f][1], d[f][0]); CVT2(o[f][2], o[f][3], d[f][1]); sst16(rx, uo + f * 65536, ub, o[f]); SCHED0(); }
    };
    prime();
#pragma unroll 1
    for (;; ++s) {
        f32x4 acc[2][3];
        u32x4 vf[2][6];
        unsigned cur = xb;
        SCHED0();
        sfor<0, 240>([&](auto G) {
            constexpr int g = decltype(G)::value;
            constexpr int rt = g < 120 ? g / 60 : (g < 144 ? 0 : (g < 168 ? 1 : g & 1));
            constexpr int ct = g < 144 ? g & 3 : (g < 168 ? (g - 144) / 12 * 2 + (g & 1) : (g - 120) / 24 * 2 + (g >> 1 & 1));
            constexpr int kk = g < 144 ? (g + 24) % 24 / 4 : (g < 168 ? g % 12 / 2 : g % 24 / 4), wi = (g - 168) / 24 * 8 + (g % 24 - 8) / 2;
            if constexpr (g < 120) { if (g % 60 < 3) MF_AV0(acc[rt][g % 3], wf[g % 3][0], xw[g / 3 % XW]); else MF_AV(acc[rt][g % 3], wf[g % 3][g % 60 / 3], xw[g / 3 % XW]); }
            else if constexpr (ct >= 4 && kk >= 2) MF_VV(d[ct & 3][rt], vf[rt][kk], wl[wi & 3]);
            else if constexpr (ct == 0 && kk < 4) { if (kk == 0) MF_VA0(d[0][rt], vf[rt][0], wo[0][0]); else MF_VA(d[0][rt], vf[rt][kk], wo[0][kk]); }
            else { if (kk == 0) MF_VV0(d[ct & 3][rt], vf[rt][0], wo[ct][0]); else MF_VV(d[ct & 3][rt], vf[rt][kk], wo[ct][kk]); }
            SCHED0();
            if constexpr (g % 3 == 1 && g / 3 + XW - 1 < 40) { constexpr int i = g / 3 + XW - 1; xw[i % XW] = *(const bf16x8*)(smem + l16b + i % 20 * 2048 + i / 20 * 1024); }
            NEXT_ADDR(2, 3);
            if (g == 24) { PIN_S(s); uo = (s & (TR - 1)) << 22; PIN_S(uo); }
            if (g == 48) { VMW(0); }
            if (g == 50) asm volatile("s_barrier" ::: "memory");
            if (g == 51) { pend = cur ^ (L << 13); PIN_S(pend); }
            if constexpr (g >= 60 && g < 140 && g % 60 < 40) {
                constexpr int r = g / 60 - 1, q = r ? g - 120 : (g % 3 == 1 ? -1 : (g - 60) / 3 * 2 + g % 3 / 2);
                if (q == 1) CVT2(o[0][0], o[0][1], acc[r][0]);
                if (q == 2) CVT2(o[0][2], o[0][3], acc[r][1]);
                if (q == 3) CVT2(o[1][0], o[1][1], acc[r][2]);
                if (q == 4) *(u32x4*)(sVw + r * 6144) = o[0];
                if (q == 5) *(u32x2*)(sVh + r * 6144) = u32x2{o[1][0], o[1][1]};
                if (q == 11) asm volatile("s_waitcnt lgkmcnt(%0)\n\ts_barrier" :: "n"(r ? 0 : 3) : "memory");
                if constexpr (q >= 12 && q < 18) vf[r][q - 12] = *(const u32x4*)(sV + r * 6144 + (q - 12) * 1024);
            }
            if constexpr (g >= 168 && g % 24 < 18 && g % 24 != 8 && !(g & 1)) {
                constexpr int j = (g - 168) / 24 * 8 + g % 24 / 2 - (g % 24 > 8);
                wl[j & 3] = *(const bf16x8*)(sW + j * 1024);
            }
            if constexpr (g >= 156) {
                constexpr int p = g < 168 ? 0 : (g - 144) / 24, q = g < 168 ? g - 156 : g % 24, c0 = 2 * p & 3;
                if (q == 1) CVT2(o[0][0], o[0][1], d[c0][0]);
                if (q == 3) CVT2(o[0][2], o[0][3], d[c0][1]);
                if (q == 5) sst16(rx, uo, p ? ub : ua, o[0]);
                if (q == 7) CVT2(o[1][0], o[1][1], d[c0 + 1][0]);
                if (q == 8) { uo += 65536; PIN_S(uo); }
                if (q == 9) CVT2(o[1][2], o[1][3], d[c0 + 1][1]);
                if (q == 11) sst16(rx, uo, p ? ub : ua, o[1]);
            }
            if constexpr (g == 168 || g == 181 || g == 205 || g == 229) { uo += 65536; PIN_S(uo); }
            NEXT_LOOK(4, lds0);
            {
                constexpr int a[10] = {186, 189, 192, 200, 205, 209, 213, 216, 224, 231}, b[10] = {188, 191, 196, 204, 207, 212, 215, 220, 228, 233};
#pragma unroll
                for (int e = 0; e < 10; ++e) {
                    if (g == a[e]) { if (e % 5 == 0) XSET(so, xs, lds0); else XADV(so); }
                    if (g == b[e]) XDMA(so, e / 5);
                }
            }
            SCHED0();
        });
        NEXT_GO(7, lds0, fU);
    }
}

#define G_B 83
#define BSLP 6
constexpr int BL_Q = 40960, BL_P = BL_Q + 49152;
__device__ __forceinline__ void b_role(const MegaArgs& A, char* smem, const int L, const int hb, const int id) {
    WORK_IDS;
    const unsigned t0 = rtime();
    bf16x8 wf[4][20];
    unsigned dst[4];
    const int sw = (l16 & 6) << 3;
    {
        const void* const base[4] = {A.wqkv + 80 * hb, A.wqkv + 80 * hb, A.wqkv + 80 * hb, A.wqkv + 80 * hb};
        unsigned vo[4];
        int rp[4];
#pragma unroll
        for (int m = 0; m < 4; ++m) {
            const int idx = min((4 * m + w) * 64 + l, 959), c = idx % 30, T = min(4 * m + w, 14), e = 16 * (T % 5) + 4 * lq;
            vo[m] = (idx / 30) * 3840 + (c / 10) * (2 * D_) + (c % 10) * 16;
            rp[m] = (4 * lq + (l16 >> 2)) * 480 + 32 * T + (l & 3) * 8;
            dst[m] = lds_addr(smem) + BL_Q + (T / 5 * 4 + e / 20) * 4096 + l16 * 64 + ((e % 20 * 2) ^ sw);
        }
        while (rtime() - t0 < (L ? DLYB1 : DLYB0)) __builtin_amdgcn_s_sleep(8);
        load_coop<4, 0, 4>(smem, w, go, base, vo, 32 * 3840, rp, 7680, wf);
    }
    GO_ON(N_1);
    f32x4 cs[4];
    u32x4 one = {0x3f803f80u, 0x3f803f80u, 0x3f803f80u, 0x3f803f80u};
    {
        asm volatile("s_nop 1" : "+v"(one));
        sfor<0, 80>([&](auto G) { constexpr int g = decltype(G)::value, tt = g & 3, ks = g >> 2;
            if (ks == 0) MF_AV0(cs[tt], wf[tt][0], one); else if (tt < 3 || ks < 4) MF_AV(cs[tt], wf[tt][ks], one); else MF_VV(cs[tt], wf[tt][ks], one); });
        asm volatile("s_nop 7" : "+v"(cs[0]), "+v"(cs[1]), "+v"(cs[2]), "+v"(cs[3]));
    }
#pragma unroll
    for (int i = 0; i < 12; ++i) *(u32x4*)(smem + BL_Q + i * 4096 + t * 16) = u32x4{0u, 0u, 0u, 0u};
    const bool odd = l16 & 1;
    f32x4 wm[3];
#pragma unroll
    for (int e = 0; e < 8; ++e) { const int d = l16 + 16 - 16 * (e >> 2) - 4 * lq - (e & 3); wm[e >> 2][e & 3] = d >= 0 && d <= 14 && !(d & 1) ? 0.f : -INFINITY; }
    wm[2] = f32x4{-INFINITY, -INFINITY, -INFINITY, -INFINITY};
    const unsigned ps0 = odd ? 0x01000c0cu : 0x0c0c0100u, ps1 = odd ? 0x03020c0cu : 0x0c0c0302u;
    unsigned qa = lds_addr(smem) + BL_Q + w * 4096 + l16 * 64 + ((lq * 16) ^ sw), qb = qa ^ 2048, va[6];
    {
        const int row = 4 * lq + (l16 >> 2), vs = (row & 6) << 3;
        va[0] = va[2] = lds_addr(smem) + BL_Q + (8 + w) * 4096 + row * 64 + ((8 * (w & 1) + 8 * (l & 3)) ^ vs);
        va[1] = va[3] = lds_addr(smem) + BL_Q + (8 + w) * 4096 + row * 64 + ((32 * (~w & 1) + 8 * (l & 3)) ^ vs);
        va[4] = va[0] ^ 2048; va[5] = va[1] ^ 2048;
#pragma unroll
        for (int e = 0; e < 6; ++e) PIN_V(va[e]);
    }
    const int g0 = (20 * hb + 5 * w + (w & 1) + (lq & 2)) >> 1, g1 = (20 * hb + 5 * w + 4 * (~w & 1)) >> 1;
    const unsigned oo0 = lp(SG_OB + (g0 >> 2) * 2048 + (lq & 1) * 1024 + (g0 & 3) * 256 + l16 * 16);
    const unsigned oo1 = lq < 2 ? lp(SG_OB + (g1 >> 2) * 2048 + lq * 1024 + (g1 & 3) * 256 + l16 * 16 + (w & 1) * 8) : OOB;
    const unsigned fO = t < 4 ? lp(SG_FL + FL_O + (8 * t + hb) * 128) : OOB;
    const unsigned st0 = lp(SG_ST + (4 * (l >> 5) + w) * 256 + (l & 31) * 8), st2 = l < 32 ? st0 + 4096 : OOB;
    unsigned fXv = xm(FX_OFF + (hb >> 1) * 64, L), xv, X = rtime() - 2 * PER, xs = OOB;
    PIN_V(fXv);
    const float z2 = l < 32 ? 1.f : 0.f;
    f32v2* sP = (f32v2*)(smem + BL_P) + (l & 31);
    unsigned sK = lds_addr(smem) + BL_P + 1024, sKw = sK + 4 * w;
    PIN_V(sK); PIN_V(sKw);
    unsigned pend = OOB, sb = seg_base(L, 0), so, fs = fx_step(1 + L * A.lag);
    const int i0 = 1 + L * A.lag;
    for (int s = 0; s < NTOUCH; ++s) { sst16(rx, seg_base(L, s), oo0, u32x4{0u, 0u, 0u, 0u}); sst8(rx, seg_base(L, s), oo1, u32x2{0u, 0u}); }
    u32x2 st[3];
    auto look = [&](int e, unsigned b, unsigned f) { if (e == 0) LDA8(st[0], st0, b, 0); if (e == 1) LDA8(st[1], st0, b, 2048); if (e == 2) LDA8(st[2], st2, b, 0); if (e == 3) LDA4(xv, fXv, f); };
#define SEEN(n) asm volatile("s_waitcnt vmcnt(%4)" : "+v"(st[0]), "+v"(st[1]), "+v"(st[2]), "+v"(xv) : "n"(n) : "memory")
#define SWAP2(op, a, b) asm volatile(op " %0, %1" : "+v"(a), "+v"(b))
    int s;
    SZERO(s);
#pragma unroll 1
    do {
        if (xs == OOB) {
            bool e = (int)(rtime() - X - E_B) >= 0;
#pragma unroll
            for (int i = 0; i < 4; ++i) look(i, e ? sb : OOB, e ? fs : OOB);
            VMB(4);
            sst4(rx, pend, fO, 1u);
            pend = OOB;
            SEEN(1);
#pragma unroll 1
            for (;;) {
                xv = __builtin_amdgcn_readfirstlane(xv);
                X = xv ? xv : X;
                LIVE;
                if (e) { if (!BAD(umax3(st[0][0], st[1][0], st[2][0]))) break; __builtin_amdgcn_s_sleep(BSLP); }
                e = true;
                rt_until(X + E_B);
#pragma unroll
                for (int i = 0; i < 4; ++i) look(i, sb, fs);
                SEEN(0);
            }
            XSET(so, sb + lp(SG_X1), lds0); XDMA(so, 0); XDMA(so, 1); XADV(so); XDMA(so, 0); XDMA(so, 1);
        }
        VMB(2);
        f32x4 acc[4][2];
        unsigned cur;
        float rd[2];
        u32x2 y[2];
        bf16x8 kf[3], qf[2];
        u32x4 k4;
#define MUL2(x, i, r) do { asm volatile("v_mul_f32 %0, %1, %0" : "+v"(x[i]) : "v"(r)); x[i + 1] *= r; USE_V(x[i + 1]); } while (0)
#define FMA2(x, i, c, m) do { asm volatile("v_fma_f32 %0, -%2, %1, %0" : "+v"(x[i]) : "v"(c[i]), "v"(m)); x[i + 1] = __builtin_fmaf(-m, c[i + 1], x[i + 1]); USE_V(x[i + 1]); } while (0)
#define ADD1(d, x, z) asm volatile("v_add_f32 %0, %1, %2" : "=v"(d) : "v"(x), "v"(z))
        auto fin = [&](int e, int u) {
            if (u < 6 && u % 3 < 2) MUL2(acc[e][u / 3], 2 * (u % 3), rd[u / 3]);
            if (u % 3 == 2) CVT2(y[u / 3][0], y[u / 3][1], acc[e][u / 3]);
            if (u == 6) { LDS_AT(u32x2, dst[e]) = y[0]; LDS_AT(u32x2, dst[e] + 1024) = y[1]; dst[e] ^= 2048; USE_V(dst[e]); }
        };
        {
            bf16x8 xw[6][2];
#pragma unroll
            for (int e = 0; e < 4; ++e) xw[e >> 1][e & 1] = *(const bf16x8*)(smem + l16b + e * 1024);
            SCHED0();
            u64 now, bad;
            float a, b, c, d, mw, mb[2];
            unsigned lb, lf;
            f32v2 p0, p1, p2, p3;
            sfor<0, 160>([&](auto G) {
                constexpr int g = decltype(G)::value, ks = g < 112 ? g / 8 : (g < 124 ? 14 + (g - 112) / 6 : (g < 148 ? 16 + (g - 124) % 8 / 2 : 14 + (g - 148) / 2));
                constexpr int tt = g < 112 ? g % 8 / 2 : (g < 124 ? (g - 112) % 6 / 2 : (g < 148 ? (g - 124) / 8 : 3)), rt = g & 1;
                constexpr int q = g >= 112 || g % 8 == 1 || g % 8 == 3 || g % 8 == 7 ? -1 : g / 8 * 5 + (g % 8 < 4 ? g % 8 / 2 : g % 8 - 2);
                if (ks == 0) MF_AV0(acc[tt][rt], wf[tt][0], xw[0][rt]);
                else if (tt < 3 || ks < 4) MF_AV(acc[tt][rt], wf[tt][ks], xw[ks % 6][rt]); else MF_VV(acc[tt][rt], wf[tt][ks], xw[ks % 6][rt]);
                SCHED0();
                constexpr int rq = g < 112 ? (g % 8 == 1 || g % 8 == 3 ? 2 * ks + 4 + g % 8 / 2 : -1) : (g < 124 && g % 3 != 0 ? 32 + (g - 112) / 3 * 2 + (g - 112) % 3 : -1);
                if constexpr (rq >= 0) xw[rq / 2 % 6][rq & 1] = *(const bf16x8*)(smem + l16b + rq * 1024);
                if (q == 0 || q == 5 || q == 11) XADV(so);
                if (q == 1 || q == 6 || q == 13) XDMA(so, 0);
                if (q == 3 || q == 8 || q == 15) XDMA(so, 1);
                if (q == 10) VMB_LATE(4);
                if (q == 2) { ADD1(c, st[0][1], st[1][1]); a = __uint_as_float(st[0][0]) + __uint_as_float(st[1][0]); USE_V(a); }
                if (q == 4) { ADD1(b, a, st[2][0]); a += __uint_as_float(st[2][0]); USE_V(a); }
                if (q == 7) { SWAP2("v_permlane32_swap_b32", a, b); SCHED0(); c += __uint_as_float(st[2][1]); USE_V(c); }
                if (q == 9) { mw = (a + b) * 0.2f; USE_V(mw); }
                if (q == 12) { a = __uint_as_float(st[0][0]) - mw; b = __uint_as_float(st[1][0]) - mw; USE_V(a); USE_V(b); }
                if (q == 14) { d = (__uint_as_float(st[2][0]) - mw) * z2; USE_V(d); }
                if (q == 16) { a = __builtin_fmaf(b, b, a * a); USE_V(a); }
                if (q == 17) { c = __builtin_fmaf(32.f, __builtin_fmaf(d, d, a), c); USE_V(c); }
                if (q == 18) { b = c; PIN_V(b); }
                if (q == 19) SWAP2("v_permlane32_swap_b32", c, b);
                if (q == 21) { c += b; sP[w * 32] = f32v2{mw, c}; }
                if (q == 22) { cur = sb; sb = (s + 1) << 19; PIN_S(cur); PIN_S(sb); }
                if (q == 23) { lb = (sb >> 8) & 8192; PIN_S(lb); }
                if (q == 24) { sb = (sb | lb) + 2 * SG_OFF; PIN_S(sb); }
                if (q == 26) { sb ^= L << 13; X += PER; PIN_S(sb); PIN_S(X); }
                if (q == 27) { fs += 256; lb = fs & 8192; PIN_S(fs); PIN_S(lb); }
                if (q == 28) { fs += lb; PIN_S(fs); }
                if (q == 34) { TIGHT(lb, s + i0, D_1); X -= lb; PIN_S(X); }
                if (q == 35) { asm volatile("s_cmp_eq_u32 %1, %2\n\ts_cselect_b32 %0, %3, 0" : "=s"(lb) : "s"(s + i0), "n"(NSEG_ + LAGV - 1), "s"(E_Z) : "scc"); X -= lb; PIN_S(X); }
                if (q == 30) asm volatile("s_waitcnt vmcnt(4) lgkmcnt(2)\n\ts_barrier" ::: "memory");
                if (q == 31) sst4(rx, pend, fO, 1u);
                if (q == 32) { p0 = sP[0]; p1 = sP[32]; }
                if (q == 33) { p2 = sP[64]; p3 = sP[96]; }
                if (q == 36) ADD1(a, p0[0], p1[0]);
                if (q == 37) { b = p2[0] + p3[0]; USE_V(b); }
                if (q == 38) { mw = (a + b) * 0.25f; USE_V(mw); }
                if (q == 39) { a = p0[0] - mw; PIN_V(a); b = p1[0] - mw; PIN_V(b); }
                if (q == 40) { c = p2[0] - mw; PIN_V(c); }
                if (q == 41) { mb[0] = rb(mw); PIN_V(mb[0]); }
                if (q == 42) { d = p3[0] - mw; PIN_V(d); mb[1] = mb[0]; PIN_V(mb[1]); }
                if (q == 43) { a = __builtin_fmaf(b, b, a * a); PIN_V(a); }
                if (q == 44) { SWAP2("v_permlane16_swap_b32", mb[0], mb[1]); ADD1(b, p0[1], p1[1]); }
                if (q == 45) { a = __builtin_fmaf(c, c, a); PIN_V(a); }
                if (q == 50) VMB(1);
                if (q == 55) { a = __builtin_fmaf(d, d, a); PIN_V(a); }
                if (g == 73) ADD1(c, p2[1], p3[1]);
                if (g == 75) ADD1(b, b, c);
                if (g == 89) { a = __builtin_fmaf(160.f, a, b); PIN_V(a); }
                if (g == 91) { a *= 1.0f / D_; PIN_V(a); }
                if (q == 60) asm volatile("v_cvt_pk_bf16_f32 %0, 0, %0" : "+v"(a));
                if (g == 97) { a += 1e-5f; PIN_V(a); }
                if (g == 99) asm volatile("v_cvt_pk_bf16_f32 %0, 0, %0" : "+v"(a));
                if (q == 65) { a = __builtin_amdgcn_sqrtf(a); PIN_V(a); }
                if (g == 105) asm volatile("v_cvt_pk_bf16_f32 %0, 0, %0" : "+v"(a));
                if (q == 66) { rd[0] = __builtin_amdgcn_rcpf(a); PIN_V(rd[0]); }
                if (q == 67) { rd[1] = rd[0]; PIN_V(rd[1]); }
                if (q == 69) SWAP2("v_permlane16_swap_b32", rd[0], rd[1]);
                if constexpr (g >= 72 && g < 104 && q >= 0 && g % 8) {
                    constexpr int e = (g % 8 < 5 ? g % 8 + 2 : 2 * (g % 8) - 10) + (g >= 88), i = g >> 2 & 2;
                    FMA2(acc[e >> 1][e & 1], i, cs[e >> 1], mb[e & 1]);
                }
                if (g == G_B) asm volatile("s_memrealtime %0" : "=s"(now));
                if (g == G_B + 4) asm volatile("s_waitcnt lgkmcnt(0)" : "+s"(now));
                if (g == G_B + 12) { lf = (unsigned)now - X; PIN_S(lf); }
                if (g == G_B + 20) { lf -= E_B; PIN_S(lf); }
                if (g == G_B + 26) asm volatile("s_cmp_lt_i32 %1, 63\n\ts_cselect_b32 %0, %0, -1" : "+s"(lf) : "s"(s) : "scc");
                if (g == G_B + 28) asm volatile("s_cmp_ge_i32 %1, 0\n\ts_cselect_b32 %0, %2, %4\n\ts_cselect_b32 %1, %3, %4" : "=&s"(lb), "+s"(lf) : "s"(sb), "s"(fs), "n"(OOB) : "scc");
                if constexpr (g >= G_B + 29 && g < G_B + 33) look(g - G_B - 29, lb, lf);
                if constexpr (g >= 132 && g < 155 && (g - 132) % 8 < 7) fin((g - 132) / 8, (g - 132) % 8);
                if (g == 155) LDS_BARRIER();
                if (g == 156) { kf[0] = LDS_AT(bf16x8, qb + 17408); qf[0] = LDS_AT(bf16x8, qa); }
                if (g == 157) { kf[1] = LDS_AT(bf16x8, qa + 16384); qf[1] = LDS_AT(bf16x8, qa + 1024); }
                if (g == 158) { kf[2] = LDS_AT(bf16x8, qa + 17408); SEEN(0); }
                if (g == 159) { lf = umax3(st[0][0], st[1][0], st[2][0]); bad = __ballot(lf == ~0u); PIN_S(bad); }
                SCHED0();
            });
            xv = __builtin_amdgcn_readfirstlane(xv);
            asm volatile("s_cmp_eq_u64 %1, 0\n\ts_cselect_b32 %0, %0, %2" : "+s"(lb) : "s"(bad), "n"(OOB) : "scc");
            LDS_AT(unsigned, sKw) = lb;
            X = xv ? xv : X;
            PIN_S(X);
        }
        {
            constexpr float SC = 0.22360679774997896f * 1.4426950408889634f;
            bf16x8 vx[2][2];
            f32x4 sc[4];
            SCHED0();
            fin(3, 0); fin(3, 1);
            SCHED0();
            MF_VVC(sc[0], kf[0], qf[0], wm[2]); SCHED0(); fin(3, 2); SCHED0();
            MF_VVC(sc[1], kf[1], qf[0], wm[1]); SCHED0(); fin(3, 3); SCHED0();
            MF_VVC(sc[2], kf[1], qf[1], wm[0]); SCHED0(); fin(3, 4); SCHED0();
            MF_VVC(sc[3], kf[2], qf[1], wm[1]); SCHED0(); fin(3, 5); fin(3, 6);
            SCHED0();
            float sv[4][2];
            float m0, m1;
#define MAXQ(m, x, y, p, q) asm volatile("v_max_f32 %1, %5, %6\n\tv_max_f32 %2, %7, %8\n\tv_max_f32 %3, %9, %10\n\tv_max_f32 %4, %11, %12\n\tv_max3_f32 %0, %1, %2, %3\n\tv_max_f32 %0, %0, %4" \
    : "=&v"(m), "=&v"(x[0]), "=&v"(x[1]), "=&v"(y[0]), "=&v"(y[1]) : "v"(p[0]), "v"(p[1]), "v"(p[2]), "v"(p[3]), "v"(q[0]), "v"(q[1]), "v"(q[2]), "v"(q[3]))
            MAXQ(m0, sv[0], sv[1], sc[0], sc[1]);
            MAXQ(m1, sv[2], sv[3], sc[2], sc[3]);
            LDS_BARRIER();
            k4 = LDS_AT(u32x4, sK);
            SCHED0();
            auto tr = [](unsigned a) { return __builtin_amdgcn_ds_read_tr16_b64_v4i16((lds_b4*)a); };
#pragma unroll
            for (int dt = 0; dt < 2; ++dt) { vx[0][dt] = cat8(tr(va[4 + dt] + 1024), tr(va[dt])); vx[1][dt] = cat8(tr(va[2 + dt]), tr(va[dt] + 1024)); }
            SCHED0();
            red_max_q2(m0, m1);
            m0 *= -SC; m1 *= -SC;
            USE_V(m0); USE_V(m1);
            SCHED0();
            xs = __builtin_amdgcn_readfirstlane(max(umax3(k4[0], k4[1], k4[2]), k4[3]));
            u32x4 pt[2];
            f32x4 o[2][2], sm[2];
#pragma unroll
            for (int qt = 0; qt < 2; ++qt) {
#pragma unroll
                for (int e = 0; e < 4; ++e) sv[2 * qt + (e >> 1)][e & 1] = __builtin_amdgcn_exp2f(__builtin_fmaf(sv[2 * qt + (e >> 1)][e & 1], SC, qt ? m1 : m0));
                if (!qt) XSET(so, xs + lp(SG_X1), lds0);
#pragma unroll
                for (int e = 0; e < 2; ++e) {
                    const unsigned p = pk2(sv[2 * qt + e][0], sv[2 * qt + e][1]);
                    pt[qt][2 * e] = __builtin_amdgcn_perm(p, p, ps0);
                    pt[qt][2 * e + 1] = __builtin_amdgcn_perm(p, p, ps1);
                }
                USE_V(pt[qt]);
                SCHED0();
                XDMA(so, qt);
                asm volatile("s_nop 0");
                MF_VV0(sm[qt], one, pt[qt]);
                if (!qt) MF_VV0(o[0][0], vx[0][0], pt[0]);
                SCHED0();
            }
            XADV(so);
            MF_VV0(o[1][0], vx[1][0], pt[1]);
            PIN_V(sm[0]);
            const float s0 = __builtin_amdgcn_rcpf(sm[0][0]);
            USE_V(s0);
            XDMA(so, 0);
            MF_VV0(o[0][1], vx[0][1], pt[0]);
            SCHED0();
            PIN_V(o[0][0]);
            MUL2(o[0][0], 0, s0);
            SCHED0();
            MF_VV0(o[1][1], vx[1][1], pt[1]);
            SCHED0();
            PIN_V(sm[1]);
            const float s1 = __builtin_amdgcn_rcpf(sm[1][0]);
            USE_V(s1);
            MUL2(o[0][0], 2, s0);
            XDMA(so, 1);
            PIN_V(o[1][0]);
            MUL2(o[1][0], 0, s1); MUL2(o[1][0], 2, s1);
            {
                const auto a = __builtin_amdgcn_permlane16_swap(pk2(o[0][0][0], o[0][0][1]), pk2(o[1][0][0], o[1][0][1]), false, false);
                const auto b = __builtin_amdgcn_permlane16_swap(pk2(o[0][0][2], o[0][0][3]), pk2(o[1][0][2], o[1][0][3]), false, false);
                sst16(rx, cur, oo0, u32x4{a[0], b[0], a[1], b[1]});
            }
            SCHED0();
            asm volatile("" : "+v"(o[0][1]), "+v"(o[1][1]));
            o[0][1] *= s0; o[1][1] *= s1;
            {
                const auto a = __builtin_amdgcn_permlane16_swap(pk2(o[0][1][0], o[0][1][1]), pk2(o[1][1][0], o[1][1][1]), false, false);
                const auto b = __builtin_amdgcn_permlane16_swap(pk2(o[0][1][2], o[0][1][3]), pk2(o[1][1][2], o[1][1][3]), false, false);
                sst8(rx, cur, oo1, u32x2{a[0], b[0]});
            }
            SCHED0();
            wm[2] = wm[0];
            qa ^= 2048; qb ^= 2048;
            PIN_V(wm[2]); PIN_V(qa); PIN_V(qb);
#pragma unroll
            for (int e = 0; e < 6; ++e) { va[e] ^= 2048; PIN_V(va[e]); }
        }
        pend = cur;
    } while (++s < NSEG_);
    VMB(0);
    sst4(rx, pend, fO, 1u);
    SPIN_END;
}

__device__ __forceinline__ void bo_role(const MegaArgs& A, char* smem, const int L, const int j, const int id) {
    WORK_IDS;
    const unsigned t0 = rtime();
    const int rt = w & 1, hf = w >> 1;
    if (id == 31 && w == 0) probe(A, go, l, id);
    INIT_BEGIN(id >> 5);
    if (L) { INIT_END(56 + j); }
    bf16x8 wf[5][20];
    {
        const void* const base[3] = {A.wo + 160 * j, A.wo + 160 * j, A.wo + 160 * j};
        unsigned vo[3];
        int rp[5];
#pragma unroll
        for (int m = 0; m < 3; ++m) { const int idx = min((4 * m + w) * 64 + l, 639); vo[m] = (idx / 20) * (2 * D_) + (idx % 20) * 16; }
#pragma unroll
        for (int m = 0; m < 5; ++m) { rp[m] = (8 * lq + (l16 >> 2)) * 320 + 32 * (m < 4 ? 4 * hf + m : 8 + hf) + (l & 3) * 8; }
        while (rtime() - t0 < (L ? DLYB1 : DLYB0)) __builtin_amdgcn_s_sleep(8);
        load_coop<3, 0, 5>(smem, w, go, base, vo, 64 * D_, rp, 1280, wf);
    }
    INIT_SEEN;
    const unsigned x0 = lp((5 * j + 2 * hf) * 2048 + rt * 1024 + l16b), x1 = lp((5 * j + 2 * hf + 1) * 2048 + rt * 1024 + l16b), x2 = lp((5 * j + 4) * 2048 + rt * 1024 + l16b + hf * 8);
    const unsigned fO = l < 8 ? lp(SG_FL + FL_O + (8 * j + l) * 128) : OOB, fX = t < 8 ? lp(SG_FL + FL_X + (4 * (t & 3) + j) * 128) + (t & 4) * 2048 : OOB;
    const unsigned sr = lp(SG_ST + ST_2 + rt * 1024 + lq * 256 + l16 * 8), sw = l < 16 ? sr + j * 256 + hf * 128 : OOB;
    auto flag = [&](unsigned b) { sst4(rx, b & ~8192u, fX ^ (b & 8192u), 1u); };
    const rsrc_t ro = mk_rsrc(A.out, S_ * D_ * 2);
    const unsigned oo = ((16 * rt + l16) * D_ + 160 * j + 64 * hf + 4 * lq) * 2, oo2 = oo + (128 - 48 * hf) * 2;
    const char* sX = smem + rt * 1024 + l16b;
    unsigned pend = OOB, sb = seg_base(L, 0), so, xs = OOB;
    unsigned* sK = (unsigned*)(smem + 40960);
    auto first = [&](unsigned b) { XSET(so, b + lp(SG_OB), lds0); XDMA(so, 0); XDMA(so, 1); XADV(so); XDMA(so, 0); XDMA(so, 1); XADV(so); XDMA(so, 0); XDMA(so, 1); };
    int s;
    SZERO(s);
#pragma unroll 1
    do {
        if (xs == OOB) {
            unsigned pv;
            LDA4(pv, fO, sb);
            VMB(1);
            flag(pend);
            pend = OOB;
            WAITV(1, pv);
            if (__ballot(pv == 0) & 255) wave_wait(rx, fO, sb, spins);
            first(sb);
            VMB(4);
        } else VMB(5);
        f32x4 acc[5], xf[5];
        u32x4 r0, r1, k4;
        u32x2 r2;
        unsigned cur;
        {
            bf16x8 xw[3];
            unsigned sx, pv;
            xw[0] = *(const bf16x8*)sX; xw[1] = *(const bf16x8*)(sX + 2048);
            SCHED0();
            sfor<0, 100>([&](auto G) {
                constexpr int g = decltype(G)::value, ks = g / 5, tt = g % 5;
                if constexpr (20 * tt + ks < 64) { if (ks == 0) MF_AV0(acc[tt], wf[tt][0], xw[0]); else MF_AV(acc[tt], wf[tt][ks], xw[ks % 3]); }
                else { if (ks == 0) MF_VV0(acc[tt], wf[tt][0], xw[0]); else MF_VV(acc[tt], wf[tt][ks], xw[ks % 3]); }
                SCHED0();
                if (g == 0 || g == 7) XADV(so);
                if (g == 2 || g == 12) XDMA(so, 0);
                if (g == 5 || g == 15) XDMA(so, 1);
                if (g == 3) { cur = sb; sx = sb + lp(SG_X1); PIN_S(cur); PIN_S(sx); }
                if (g == 10) VMB_LATE(4);
                if (g == 18) LDA(r0, x0, sx, 0);
                if (g == 22) LDA(r1, x1, sx, 0);
                if (g == 25) LDA8(r2, x2, sx, 0);
                if (g == 30) VMB(7);
                if (g == 32) flag(pend);
                if (g == 35) { sb = (s + 1) << 19; PIN_S(sb); }
                if (g == 37) { sx = (sb >> 8) & 8192; PIN_S(sx); }
                if (g == 38) { sb = (sb | sx) + 2 * SG_OFF; PIN_S(sb); }
                if (g == 40) { sb ^= L << 13; PIN_S(sb); }
                if (g == 42) asm volatile("s_cmp_lt_i32 %1, 63\n\ts_cselect_b32 %0, %0, %2" : "+s"(sb) : "s"(s), "n"(OOB) : "scc");
                if (g == 50) VMB(4);
                if constexpr (tt == 1 && ks < 18) xw[(ks + 2) % 3] = *(const bf16x8*)(sX + (ks + 2) * 2048);
                if (g == 60) LDA4(pv, fO, sb);
                if (g == 63) asm volatile("s_waitcnt vmcnt(2)" : "+v"(r0), "+v"(r1), "+v"(r2) :: "memory");
                if constexpr (g >= 65 && g < 90 && (tt == 2 || tt == 3)) {
                    constexpr int e = ks - 13, k = tt - 2;
                    const unsigned u = e < 2 ? r0[2 * (e & 1) + k] : (e < 4 ? r1[2 * (e & 1) + k] : r2[k]);
                    xf[e][2 * k] = lo16(u); xf[e][2 * k + 1] = hi16(u);
                    asm volatile("" : "+v"(xf[e][2 * k]), "+v"(xf[e][2 * k + 1]));
                }
                if (g == 90) WAITV(0, pv);
                if (g == 91) { sx = (unsigned)__ballot(pv == 0); PIN_S(sx); }
                if (g == 92) asm volatile("s_and_b32 %0, %0, 255\n\ts_cselect_b32 %0, %2, %1" : "+s"(sx) : "s"(sb), "n"(OOB) : "scc");
                if (g == 93) sK[w] = sx;
                if (g == 97) LDS_BARRIER();
                if (g == 98) k4 = *(const u32x4*)sK;
                SCHED0();
            });
        }
#pragma unroll
        for (int e = 0; e < 5; ++e) { PIN_V(acc[e]); acc[e] += xf[e]; PIN_V(acc[e]); SCHED0(); }
        xs = __builtin_amdgcn_readfirstlane(max(umax3(k4[0], k4[1], k4[2]), k4[3]));
        if (L) {
            if (xs != OOB) first(xs);
            const unsigned so2 = s * (SEG_ * D_ * 2);
#pragma unroll
            for (int e = 0; e < 5; ++e) __builtin_amdgcn_raw_buffer_store_b64(u32x2{pk2(acc[e][0], acc[e][1]), pk2(acc[e][2], acc[e][3])}, ro, e < 4 ? oo + 32 * e : oo2, so2, OUTAUX);
        } else {
            f32x4 a = ((acc[0] + acc[1]) + (acc[2] + acc[3])) + acc[4];
            const float mw = red_sum_q((a[0] + a[1]) + (a[2] + a[3])) * (1.0f / 80);
#pragma unroll
            for (int e = 0; e < 5; ++e) { xf[e] = acc[e]; acc[e] -= mw; }
            a = acc[0] * acc[0];
#pragma unroll
            for (int e = 1; e < 5; ++e) a = __builtin_elementwise_fma(acc[e], acc[e], a);
            sst8(rx, cur, sw, u32x2{nosent(__float_as_uint(mw)), __float_as_uint(red_sum_q((a[0] + a[1]) + (a[2] + a[3])))});
            SCHED0();
            if (xs != OOB) first(xs);
            r0 = pk8(xf[0], xf[1]); r1 = pk8(xf[2], xf[3]); r2 = u32x2{pk2(xf[4][0], xf[4][1]), pk2(xf[4][2], xf[4][3])};
            const unsigned so2 = cur + lp(SG_XR);
            sst16(rx, so2, x0, r0); sst16(rx, so2, x1, r1); sst8(rx, so2, x2, r2);
            VMW(3);
            u32x2 p, q;
#pragma unroll 1
            for (;;) {
                LDA8(p, sr, cur, 0); LDA8(q, sr, cur, 128);
                LIVE;
                asm volatile("s_waitcnt vmcnt(0)" : "+v"(p), "+v"(q) :: "memory");
                if (!BAD(max(p[0], q[0]))) break;
            }
            const float m0 = __uint_as_float(p[0]), m1 = __uint_as_float(q[0]), mean = red_sum_q(m0 + m1) * 0.125f, e0 = m0 - mean, e1 = m1 - mean;
            const float var = red_sum_q((__uint_as_float(p[1]) + __uint_as_float(q[1])) + 80.f * (e0 * e0 + e1 * e1)) * (1.0f / D_);
            const float rd = __builtin_amdgcn_rcpf(rb(__builtin_amdgcn_sqrtf(rb(rb(var) + 1e-5f)))), c = (mw - rb(mean)) * rd;
#pragma unroll
            for (int e = 0; e < 5; ++e) acc[e] = acc[e] * rd + c;
            sst16(rx, cur, x0, pk8(acc[0], acc[1])); sst16(rx, cur, x1, pk8(acc[2], acc[3])); sst8(rx, cur, x2, u32x2{pk2(acc[4][0], acc[4][1]), pk2(acc[4][2], acc[4][3])});
            pend = cur;
        }
    } while (++s < NSEG_);
    if (id == 159) bst4(go.rh, t < 8 ? t * 1024 + HS_E : OOB, tags);
    VMB(0);
    flag(pend);
    SPIN_END;
}

#define CT0 1
#define CT1 0
constexpr unsigned CODE_SZ = 100 * 1024, CODE_BK = 0;
constexpr int MEGA_LDS = 163840;
__global__ void __launch_bounds__(256, 1) mega_kernel(MegaArgs A) {
    extern __shared__ __attribute__((aligned(16))) char smem[];
    int role = 32 * (blockIdx.x & 7) + (blockIdx.x >> 3);
    PIN_S(role);
    const int pool = role >> 5, k = role & 31;
    {
        const unsigned t0 = rtime();
        if (k >= 8 && k < 13) {
            const char* pc;
            asm volatile("s_getpc_b64 %0" : "=s"(pc));
            auto touch = [&]() { const unsigned x = __builtin_amdgcn_raw_buffer_load_b32(mk_rsrc(pc, CODE_SZ), ((k - 8) * 256 + threadIdx.x) * 128 - CODE_BK, 0, 0); asm volatile("" :: "v"(x)); };
            if (CT0) touch();
            init_area(A, 5 * pool + k - 8, role, t0);
            if (CT1) { while (rtime() - t0 < CT1) __builtin_amdgcn_s_sleep(8); touch(); }
        }
        if (k >= 17 && k < 29 && ((k - 13) & 4)) { first_rows(A, smem, 8 * ((k - 17) / 8 * 4 + ((k - 17) & 3)) + pool, role, t0); while (rtime() - t0 < DLY2) __builtin_amdgcn_s_sleep(8); }
    }
    if (k < 13) {
        if (!(A.roles & 1)) return;
        if (k < 4) s_role(A, smem, 4 * pool + k, role);
        else if (k < 8) vm_role(A, smem, 4 * pool + k - 4, role);
        else c_role(A, smem, pool, k - 8, role);
        return;
    }
    if (k < 21) { if (A.roles & 2) qk_role(A, smem, (k - 13) >> 2, 4 * pool + ((k - 13) & 3), role); }
    else if (k < 29) { if (A.roles & 4) vo_role(A, smem, (k - 21) >> 2, 4 * pool + ((k - 21) & 3), role); }
    else if (k < 31) { if (A.roles & 8) b_role(A, smem, pool >> 2, 2 * (pool & 3) + k - 29, role); }
    else if (A.roles & 16) bo_role(A, smem, pool >> 2, pool & 3, role);
}

#define ROLES 31
extern "C" void kernel_launch(void* const* d_in, const int* in_sizes, int n_in,
                              void* d_out, int out_size, void* d_ws, size_t ws_size,
                              hipStream_t stream) {
    MegaArgs A;
    char* ws = (char*)d_ws;
    A.hs = ws; ws += 262144;
    A.tmo = (unsigned*)ws; ws += 4096;
    A.ar = ws;
    A.x0 = (const bf16_t*)d_in[0]; A.mem0 = (const bf16_t*)d_in[1]; A.Wq = (const bf16_t*)d_in[2]; A.Wk = (const bf16_t*)d_in[3]; A.Wv = (const bf16_t*)d_in[4];
    A.Wo = (const bf16_t*)d_in[5]; A.wqkv = (const bf16_t*)d_in[8]; A.wo = (const bf16_t*)d_in[9];
    A.out = (bf16_t*)d_out; A.pad = 0;
    A.lag = LAGV; A.nit = NSEG_ + LAGV; A.roles = ROLES;

    (void)hipFuncSetAttribute((const void*)mega_kernel, hipFuncAttributeMaxDynamicSharedMemorySize, MEGA_LDS);

    mega_kernel<<<256, 256, MEGA_LDS, stream>>>(A);
}
```

```cpp
#define PER 288
#define E_U 123
#define E_P 139
#define A_UP 0
#define A_MAX 0
#include <hip/hip_runtime.h>
#include <hip/hip_bf16.h>
#include <stdint.h>

typedef unsigned short bf16_t;
typedef short bf16x8 __attribute__((ext_vector_type(8)));
typedef short bf16x4 __attribute__((ext_vector_type(4)));
typedef float f32x4 __attribute__((ext_vector_type(4)));
typedef unsigned u32x4 __attribute__((ext_vector_type(4)));
typedef unsigned u32x2 __attribute__((ext_vector_type(2)));
typedef unsigned long long u64;
typedef __bf16 bf16v2 __attribute__((ext_vector_type(2)));
typedef float f32v2 __attribute__((ext_vector_type(2)));

__device__ __forceinline__ float bf2f(bf16_t v) { return __uint_as_float(((unsigned)v) << 16); }
__device__ __forceinline__ unsigned pk2(float a, float b) {
    f32v2 f = {a, b};
    bf16v2 r = __builtin_convertvector(f, bf16v2);
    return __builtin_bit_cast(unsigned, r);
}
__device__ __forceinline__ float rb(float a) { return __uint_as_float((pk2(a, 0.f) & 0xffffu) << 16); }
__device__ __forceinline__ float lo16(unsigned u) { return __uint_as_float(u << 16); }
__device__ __forceinline__ float hi16(unsigned u) { return __uint_as_float(u & 0xffff0000u); }
__device__ __forceinline__ bf16x4 pk4(f32x4 v) {
    u32x2 u = {pk2(v[0], v[1]), pk2(v[2], v[3])};
    return __builtin_bit_cast(bf16x4, u);
}
__device__ __forceinline__ bf16x8 cat8(bf16x4 a, bf16x4 b) { return bf16x8{a[0], a[1], a[2], a[3], b[0], b[1], b[2], b[3]}; }
__device__ __forceinline__ bf16x8 asb8(u32x4 v) { return __builtin_bit_cast(bf16x8, v); }
__device__ __forceinline__ f32x4 asf4(u32x4 v) { return __builtin_bit_cast(f32x4, v); }
__device__ __forceinline__ f32x4 mfma32(bf16x8 a, bf16x8 b, f32x4 c) { return __builtin_amdgcn_mfma_f32_16x16x32_bf16(a, b, c, 0, 0, 0); }
__device__ __forceinline__ f32x4 mfma16(bf16x4 a, bf16x4 b, f32x4 c) { return __builtin_amdgcn_mfma_f32_16x16x16bf16_1k(a, b, c, 0, 0, 0); }

#define MF_(acc, ca, a, cb, b) asm volatile("v_mfma_f32_16x16x32_bf16 %0, %1, %2, %0" : "+v"(acc) : ca(a), cb(b))
#define MF0_(acc, ca, a, cb, b) asm volatile("v_mfma_f32_16x16x32_bf16 %0, %1, %2, 0" : "=&v"(acc) : ca(a), cb(b))
#define MF_AV(acc, a, b) MF_(acc, "a", a, "v", b)
#define MF_VA(acc, a, b) MF_(acc, "v", a, "a", b)
#define MF_VV(acc, a, b) MF_(acc, "v", a, "v", b)
#define MF_AV0(acc, a, b) MF0_(acc, "a", a, "v", b)
#define MF_VA0(acc, a, b) MF0_(acc, "v", a, "a", b)
#define MF_VV0(acc, a, b) MF0_(acc, "v", a, "v", b)
#define MF_VVC(d, a, b, c) asm volatile("v_mfma_f32_16x16x32_bf16 %0, %1, %2, %3" : "=&v"(d) : "v"(a), "v"(b), "v"(c))
#define MH_(acc, ca, a, cb, b) asm volatile("v_mfma_f32_16x16x16_bf16 %0, %1, %2, %0" : "+v"(acc) : ca(a), cb(b))
#define MH0_(acc, ca, a, cb, b) asm volatile("v_mfma_f32_16x16x16_bf16 %0, %1, %2, 0" : "=&v"(acc) : ca(a), cb(b))

constexpr int S_ = 2048, D_ = 640, H_ = 32, DH_ = 192, M_ = 8, SEG_ = 32, NSEG_ = 64;
constexpr int HD_ = H_ * DH_;
#define RLX_AGENT __ATOMIC_RELAXED, __HIP_MEMORY_SCOPE_AGENT
#define AUX_SC1 16
typedef __amdgpu_buffer_rsrc_t rsrc_t;
__device__ __forceinline__ rsrc_t mk_rsrc(const void* p, unsigned bytes) { return __builtin_amdgcn_make_buffer_rsrc((void*)p, 0, (int)bytes, 0x00020000); }
__device__ __forceinline__ u32x4 bld16(rsrc_t r, unsigned off) { return __builtin_amdgcn_raw_buffer_load_b128(r, off, 0, AUX_SC1); }
__device__ __forceinline__ u32x2 bld8(rsrc_t r, unsigned off) { return __builtin_amdgcn_raw_buffer_load_b64(r, off, 0, AUX_SC1); }
__device__ __forceinline__ unsigned bld4(rsrc_t r, unsigned off) { return __builtin_amdgcn_raw_buffer_load_b32(r, off, 0, AUX_SC1); }
__device__ __forceinline__ void bst16(rsrc_t r, unsigned off, u32x4 v) { __builtin_amdgcn_raw_buffer_store_b128(v, r, off, 0, AUX_SC1); }
__device__ __forceinline__ void bst8(rsrc_t r, unsigned off, u32x2 v) { __builtin_amdgcn_raw_buffer_store_b64(v, r, off, 0, AUX_SC1); }
__device__ __forceinline__ void bst4(rsrc_t r, unsigned off, unsigned v) { __builtin_amdgcn_raw_buffer_store_b32(v, r, off, 0, AUX_SC1); }
#define LDS_BARRIER() asm volatile("s_waitcnt lgkmcnt(0)\n\ts_barrier" ::: "memory")
#define DRAIN() asm volatile("s_waitcnt vmcnt(0)" ::: "memory")
#define MEMFENCE() asm volatile("" ::: "memory")
#define SCHED0() __builtin_amdgcn_sched_barrier(0)
__device__ __forceinline__ float red_max_q(float x) {
    auto a = __builtin_amdgcn_permlane16_swap(__float_as_uint(x), __float_as_uint(x), false, false);
    x = fmaxf(__uint_as_float(a[0]), __uint_as_float(a[1]));
    auto c = __builtin_amdgcn_permlane32_swap(__float_as_uint(x), __float_as_uint(x), false, false);
    return fmaxf(__uint_as_float(c[0]), __uint_as_float(c[1]));
}
__device__ __forceinline__ float max3r(float a, float b, float c) { float r; asm("v_max3_f32 %0, %1, %2, %3" : "=v"(r) : "v"(a), "v"(b), "v"(c)); return r; }
__device__ __forceinline__ float max2r(float a, float b) { float r; asm("v_max_f32 %0, %1, %2" : "=v"(r) : "v"(a), "v"(b)); return r; }
__device__ __forceinline__ void red_max_q2(float& x, float& y) {
    float s, t;
    asm("v_mov_b32 %2, %0\n\tv_mov_b32 %3, %1\n\ts_nop 0\n\tv_permlane16_swap_b32 %0, %2\n\tv_permlane16_swap_b32 %1, %3\n\tv_max_f32 %0, %0, %2\n\tv_max_f32 %1, %1, %3\n\t"
        "v_mov_b32 %2, %0\n\tv_mov_b32 %3, %1\n\ts_nop 0\n\tv_permlane32_swap_b32 %0, %2\n\tv_permlane32_swap_b32 %1, %3\n\tv_max_f32 %0, %0, %2\n\tv_max_f32 %1, %1, %3"
        : "+v"(x), "+v"(y), "=&v"(s), "=&v"(t));
}
__device__ __forceinline__ void red_sum_q2(float& x, float& y) {
    float s, t;
    asm("v_mov_b32 %2, %0\n\tv_mov_b32 %3, %1\n\ts_nop 0\n\tv_permlane16_swap_b32 %0, %2\n\tv_permlane16_swap_b32 %1, %3\n\tv_add_f32 %0, %0, %2\n\tv_add_f32 %1, %1, %3\n\t"
        "v_mov_b32 %2, %0\n\tv_mov_b32 %3, %1\n\ts_nop 0\n\tv_permlane32_swap_b32 %0, %2\n\tv_permlane32_swap_b32 %1, %3\n\tv_add_f32 %0, %0, %2\n\tv_add_f32 %1, %1, %3"
        : "+v"(x), "+v"(y), "=&v"(s), "=&v"(t));
}
__device__ __forceinline__ float red_sum_q(float x) {
    auto a = __builtin_amdgcn_permlane16_swap(__float_as_uint(x), __float_as_uint(x), false, false);
    x = __uint_as_float(a[0]) + __uint_as_float(a[1]);
    auto c = __builtin_amdgcn_permlane32_swap(__float_as_uint(x), __float_as_uint(x), false, false);
    return __uint_as_float(c[0]) + __uint_as_float(c[1]);
}
__device__ __forceinline__ u32x4 pk8(f32x4 a, f32x4 b) { return u32x4{pk2(a[0], a[1]), pk2(a[2], a[3]), pk2(b[0], b[1]), pk2(b[2], b[3])}; }
__device__ __forceinline__ u32x4 tag4(f32x4 v, unsigned tg) {
    return u32x4{(__float_as_uint(v[0]) & ~3u) | tg, (__float_as_uint(v[1]) & ~3u) | tg, (__float_as_uint(v[2]) & ~3u) | tg, (__float_as_uint(v[3]) & ~3u) | tg};
}
__device__ __forceinline__ unsigned tagbad(u32x4 v, unsigned tg) { return ((v[0] ^ tg) | (v[1] ^ tg) | (v[2] ^ tg) | (v[3] ^ tg)) & 3u; }

__device__ __host__ __forceinline__ constexpr unsigned xm(unsigned o, unsigned side) { return ((o >> 13) << 14) | (o & 8191u) | ((((o >> 20) ^ side) & 1u) << 13); }
__device__ __host__ __forceinline__ constexpr unsigned lp(unsigned o) { return ((o >> 13) << 14) | (o & 8191u); }
__device__ __host__ __forceinline__ constexpr int hside(int h) { return h >> 4; }
constexpr unsigned XM_OFF = 24576, XM_BUF = 4 * 32768;
constexpr unsigned XP_OFF = XM_OFF + 3 * XM_BUF, XP_BUF = 32768;
constexpr unsigned PT_OFF = XP_OFF + 3 * XP_BUF, PT_BUF = 65536;
constexpr unsigned FI_OFF = PT_OFF + 3 * PT_BUF, FI_N = 80;
constexpr unsigned FX_OFF = FI_OFF + FI_N * 1024;
constexpr unsigned A_END = FX_OFF + FI_N * 256;
constexpr unsigned UM_OFF = 1u << 20, UM_BUF = 40 * 8192;
#define TR 8
constexpr unsigned UT_OFF = 2u << 20;
constexpr unsigned QK_OFF = UT_OFF + TR * (2u << 20);
constexpr unsigned SG_OFF = QK_OFF + TR * (1u << 20);
enum { SG_XL = 0, SG_XR = 40960, SG_X1 = 81920, SG_OB = 122880, SG_ST = 163840, SG_FL = 172032 };
enum { FL_U = 0, FL_X = 2048, FL_O = 4096, ST_2 = 6144 };
constexpr unsigned ARENA = 2 * (SG_OFF + 64 * 262144);
static_assert(A_END <= UM_OFF && UM_OFF + 3 * UM_BUF <= UT_OFF && QK_OFF % (1u << 21) == 0 && SG_OFF % (1u << 21) == 0, "map");
static_assert(ARENA + (1u << 23) < (1u << 28), "workspace");
__device__ __forceinline__ unsigned seg_base(int layer, int seg) { return 2 * SG_OFF + (unsigned)seg * 524288 + ((((seg >> 2) ^ layer) & 1) << 13); }
__device__ __forceinline__ unsigned ut_slot(int seg) { return (unsigned)(seg & (TR - 1)) << 22; }
__device__ __forceinline__ unsigned qk_base(int seg, int side) { return 2 * QK_OFF + ((unsigned)(seg & (TR - 1)) << 21) + (((seg ^ side) & 1) << 13); }
__device__ __forceinline__ unsigned fi_step(int it) { return ((it >> 3) << 14) | ((it & 7) << 10); }
__device__ __forceinline__ unsigned fx_step(int it) { return ((it >> 5) << 14) | ((it & 31) << 8); }
static_assert(FI_OFF % 8192 == 0 && FX_OFF % 8192 == 0, "steps");
__device__ __forceinline__ u32x4 sld16(rsrc_t r, unsigned so, unsigned lo) { return __builtin_amdgcn_raw_buffer_load_b128(r, lo, so, AUX_SC1); }
__device__ __forceinline__ u32x2 sld8(rsrc_t r, unsigned so, unsigned lo) { return __builtin_amdgcn_raw_buffer_load_b64(r, lo, so, AUX_SC1); }
__device__ __forceinline__ unsigned sld4(rsrc_t r, unsigned so, unsigned lo) { return __builtin_amdgcn_raw_buffer_load_b32(r, lo, so, AUX_SC1); }
__device__ __forceinline__ void sst4(rsrc_t r, unsigned so, unsigned lo, unsigned v) { __builtin_amdgcn_raw_buffer_store_b32(v, r, lo, so, AUX_SC1); }
__device__ __forceinline__ void sst16(rsrc_t r, unsigned so, unsigned lo, u32x4 v) { __builtin_amdgcn_raw_buffer_store_b128(v, r, lo, so, AUX_SC1); }
__device__ __forceinline__ void sst8(rsrc_t r, unsigned so, unsigned lo, u32x2 v) { __builtin_amdgcn_raw_buffer_store_b64(v, r, lo, so, AUX_SC1); }
__device__ __forceinline__ unsigned umax3(unsigned a, unsigned b, unsigned c) { return max(max(a, b), c); }
__device__ __forceinline__ unsigned mx4(unsigned m, u32x4 v) { return umax3(m, v[0], v[2]); }
__device__ __forceinline__ unsigned nosent(unsigned x) { return min(x, 0xfffffffeu); }
constexpr unsigned OOB = 0x7f000000u;
constexpr unsigned kSpinCap = 1u << 18;

__device__ __forceinline__ char* arena_base(char* p) { return (char*)(((size_t)p + 0x3fffff) & ~(size_t)0x3fffff); }

typedef __attribute__((address_space(3))) bf16x4 lds_b4;
typedef __attribute__((address_space(3))) volatile int lds_int;
template <int V> struct IC { static constexpr int value = V; };
template <int B, int E, class F> __device__ __forceinline__ void sfor(F&& f) { if constexpr (B < E) { f(IC<B>{}); sfor<B + 1, E>(f); } }
#define WNT ""
#define DMA_NEXT(x) asm volatile("global_load_lds_dwordx4 %0, %1" x "\n\ts_add_u32 m0, m0, %2\n\tv_add_u32 %0, %3, %0" : "+v"(vo) : "s"(base), "s"(step), "s"(adv) : "memory", "m0", "scc")
template <bool NT> __device__ __forceinline__ void dma_next(const void* base, unsigned& vo, unsigned step, unsigned adv) { if (NT) DMA_NEXT(WNT); else DMA_NEXT(""); }
__device__ __forceinline__ void set_m0(unsigned x) { asm volatile("s_mov_b32 m0, %0\n\ts_nop 0" :: "s"(x) : "memory", "m0"); }
__device__ __forceinline__ bf16x8 tr_read(const char* p, int o0, int o1) { return cat8(__builtin_amdgcn_ds_read_tr16_b64_v4i16((lds_b4*)(p + o0)), __builtin_amdgcn_ds_read_tr16_b64_v4i16((lds_b4*)(p + o1))); }
struct Go { rsrc_t rh; unsigned so; u32x2 v; };
#define G_EB 2
#define GO_ASK asm volatile("buffer_load_dwordx2 %0, %1, %2, %3 offen sc1" : "=&v"(go.v) : "v"((threadIdx.x & 63) * 8), "s"(go.rh), "s"(go.so) : "memory")
struct Nop { __device__ void operator()() const {} };
template <int N, int N1, int NI, int NJ, int NS, int SL, bool V, int NE = 0, class ISSUE, class TAKE, class EARLY = Nop>
__device__ __forceinline__ void load_ring(char* smem, const int w, Go& go, ISSUE issue_, TAKE take, EARLY early = Nop{}) {
    constexpr int AH = NS - 1, SH = NE ? (NS - N % NS) % NS : 0, GE = N - 1 - AH - (NE ? G_EB : 0), HE = GE + AH + 1;
    static_assert(HE >= N - NS + 2, "free slots");
    const unsigned sta = (unsigned)(size_t)(__attribute__((address_space(3))) char*)smem + w * 1024;
    auto issue = [&](int st) {
        if ((st + SH) % NS == 0) set_m0(sta); else if (st == 0) set_m0(sta + SH * SL);
        const int n = st < N1 ? NI : NJ;
#pragma unroll
        for (int m = 0; m < n; ++m) issue_(st, m, m + 1 < n ? 4096u : SL - 4096u * (n - 1));
    };
#pragma unroll
    for (int st = 0; st < AH; ++st) issue(st);
    auto run = [&](auto WW) {
        sfor<0, N + 1>([&](auto ST) {
            constexpr int st = decltype(ST)::value, e = st + AH < N ? st + AH : N, a = e < N1 ? e : N1, n1 = a > st + 1 ? a - st - 1 : 0, n = st < N ? n1 * NI + (e - st - 1 - n1) * NJ + (st > GE && st <= GE + AH) + (st > HE ? NE : 0) : 0;
            asm volatile("s_waitcnt vmcnt(%0) lgkmcnt(0)\n\ts_barrier" :: "n"(n) : "memory");
            if constexpr (st > 0) take(IC<st - 1>{}, WW, smem, -1);
            if constexpr (st + AH < N) issue(st + AH);
            if constexpr (st == GE) GO_ASK;
            if constexpr (st == HE || st == N) asm volatile("" : "+v"(go.v));
            constexpr int sl = (st + SH) % NS, hi = sl / (65536 / SL) * (65536 / SL);
            if constexpr (st < N) take(ST, WW, smem + hi * SL, (sl - hi) * SL);
            if constexpr (NE && st == HE) early();
        });
    };
    if (!V || w == 0) run(IC<0>{}); else if (w == 1) run(IC<1>{}); else if (w == 2) run(IC<2>{}); else run(IC<3>{});
}
template <int R, int NTT> __device__ __forceinline__ void take_k(int ks, int ww, const char* p, int o, const int (&rp)[NTT], int hf, bf16x8 (&wf)[NTT][20]) {
#pragma unroll
    for (int tt = 0; tt < NTT; ++tt) {
        const int i = (ks + 20 - R * ww) % 20;
        if (o >= 0) wf[tt][i] = tr_read(p, rp[tt] + o, rp[tt] + hf + o);
        else if (20 * tt + i < 64) asm volatile("" : "+a"(wf[tt][i]));
        else asm volatile("" : "+v"(wf[tt][i]));
    }
}
#define SWP (R != 0)
#define ORD(st) (SWP ? (st & 1 ? 3u : -1u) : 1u)
#define BNT 4
template <int NI, int R, int NTT, int NE = 0, class EARLY = Nop>
__device__ __forceinline__ void load_coop(char* smem, const int w, Go& go, const void* const (&base)[NI], unsigned (&vo)[NI], const unsigned adv, const int (&rp)[NTT], const int hf, bf16x8 (&wf)[NTT][20], EARLY early = Nop{}) {
    load_ring<20, 20, NI, NI, (NI > 4 ? 6 : 8), (NI > 4 ? 24576 : 16384), R != 0, NE>(smem, w, go, [&](int st, int m, unsigned step) { dma_next<(NI > BNT)>(base[m], vo[m], step, ORD(st) * adv); },
        [&](auto ST, auto WW, const char* p, int o) { take_k<R, NTT>(decltype(ST)::value ^ SWP, decltype(WW)::value, p, o, rp, hf, wf); }, early);
}
__device__ __forceinline__ unsigned hd_src(int idx) { const int r = idx / 24, u = idx % 24; return r * (2 * HD_) + ((u * 16) ^ ((r & 6) << 4)); }
__device__ __forceinline__ int hd_pos(int l, int tile) { const int r = l >> 2; return r * 384 + ((32 * tile) ^ ((r & 6) << 4)) + (l & 3) * 8; }
__device__ __forceinline__ unsigned row_src(int idx) { return (idx * 16) ^ ((idx / 80 & 3) << 6); }
__device__ __forceinline__ int row_pos(int r, int piece) { return r * 1280 + ((piece ^ (r & 3)) << 6); }
template <int R, int NE = 0, class TILE, class EARLY = Nop> __device__ __forceinline__ void load_qk(char* smem, int w, int l, Go& go, const bf16_t* Wq, const bf16_t* Wk, TILE tile, bf16x8 (&wf)[6][20], EARLY early = Nop{}) {
    const void* const base[6] = {Wq, Wq, Wq, Wk, Wk, Wk};
    unsigned vo[6];
    int rp[6];
#pragma unroll
    for (int m = 0; m < 6; ++m) { vo[m] = hd_src((4 * (m % 3) + w) * 64 + l) + SWP * 64 * HD_; rp[m] = tile(m) / 12 * 12288 + hd_pos(l, tile(m) % 12); }
    load_coop<6, R, 6, NE>(smem, w, go, base, vo, 64 * HD_, rp, 6144, wf, early);
}
template <int R, class DST> __device__ __forceinline__ void load_vo(char* smem, int w, int l, Go& go, const bf16_t* Wv, const bf16_t* Wo, bf16x8 (&wf)[3][20], DST dst) {
    unsigned vo[3], v2[5];
    int r2[5];
    bf16x4 lo[10];
#pragma unroll
    for (int m = 0; m < 3; ++m) vo[m] = hd_src((4 * m + w) * 64 + l) + SWP * 64 * HD_;
#pragma unroll
    for (int m = 0; m < 5; ++m) v2[m] = row_src((4 * m + w) * 64 + l) + SWP * 20480;
#pragma unroll
    for (int m = 0; m < 5; ++m) r2[m] = row_pos(l >> 2, 5 * w + m) + (l & 3) * 8;
    const int rp[3] = {hd_pos(l, 2 * w), hd_pos(l, 2 * w + 1), hd_pos(l, 8 + w)};
    load_ring<32, 20, 3, 5, 8, 20480, R != 0>(smem, w, go, [&](int st, int m, unsigned step) { if (st < 20) dma_next<true>(Wv, vo[m % 3], step, ORD(st) * (64 * HD_)); else dma_next<true>(Wo, v2[m], step, ORD(st) * 20480u); },
        [&](auto ST, auto WW, const char* p, int o) {
            constexpr int st = decltype(ST)::value;
            if constexpr (st < 20) take_k<R, 3>(st ^ SWP, decltype(WW)::value, p, o, rp, 6144, wf);
            else {
#pragma unroll
                for (int t = 0; t < 10; ++t) {
                    if (o < 0) {
                        if (st & 1) { bf16x8& d = dst(st / 2 - 10, t, decltype(WW)::value); if (t == 0 && st < 28) asm volatile("" : "+a"(d)); else asm volatile("" : "+v"(d)); }
                        continue;
                    }
                    const bf16x4 x = __builtin_amdgcn_ds_read_tr16_b64_v4i16((lds_b4*)(p + r2[t / 2] + o + (t & 1) * 32));
                    if (st & 1) dst(st / 2 - 10, t, decltype(WW)::value) = SWP ? cat8(x, lo[t]) : cat8(lo[t], x); else lo[t] = x;
                }
            }
        });
}

struct MegaArgs {
    const bf16_t* x0; const bf16_t* mem0; const bf16_t* Wq; const bf16_t* Wk; const bf16_t* Wv; const bf16_t* Wo; const bf16_t* wqkv; const bf16_t* wo;
    char* ar; unsigned* tmo; char* hs; bf16_t* out;
    int nit, lag, roles, pad;
};

#define WSLP 2
#define LIVE u64 live_; asm volatile("s_add_u32 %0, %0, 1\n\ts_cmp_gt_u32 %0, %2\n\ts_cselect_b64 %1, 0, -1" : "+s"(spins), "=s"(live_) : "n"(kSpinCap) : "scc")
#define BAD(m) (__ballot(m == ~0u) & live_)
#define SPIN_END if (spins > kSpinCap) __hip_atomic_store(A.tmo, 1u + id, RLX_AGENT)
__device__ __forceinline__ void wave_wait(rsrc_t rx, unsigned off, unsigned so, unsigned& spins) {
#pragma unroll 1
    for (;;) {
        const unsigned v = sld4(rx, so, off);
        LIVE;
        if (!(__ballot(v == 0 && off != OOB) & live_)) break;
        __builtin_amdgcn_s_sleep(WSLP);
        MEMFENCE();
    }
}
__device__ __forceinline__ unsigned rtime() { return (unsigned)__builtin_amdgcn_s_memrealtime(); }
__device__ __forceinline__ void rt_until(unsigned target) {
    asm volatile("1:\n\ts_memrealtime vcc\n\ts_waitcnt lgkmcnt(0)\n\ts_sub_u32 vcc_lo, %0, vcc_lo\n\ts_cmpk_lt_u32 vcc_lo, 600\n\ts_cbranch_scc1 1b" :: "s"(target - 1) : "memory", "scc", "vcc");
}
#define RT_K 26
__device__ __forceinline__ void rt_fine(unsigned target) {
    u64 a, b, c;
    unsigned n;
    asm volatile("1:\n\ts_memrealtime vcc\n\ts_waitcnt lgkmcnt(0)\n\ts_sub_u32 vcc_lo, %4, vcc_lo\n\ts_cmpk_lt_u32 vcc_lo, 600\n\ts_cbranch_scc1 1b\n\t"
                 "s_add_u32 vcc_lo, vcc_lo, 8\n\ts_cbranch_scc0 3f\n\t"
                 "s_memrealtime %0\n\ts_nop 4\n\ts_memrealtime %1\n\ts_nop 4\n\ts_memrealtime %2\n\ts_lshl_b32 %3, %4, 2\n\ts_addk_i32 %3, %5\n\ts_nop 2\n\ts_memrealtime vcc\n\ts_waitcnt lgkmcnt(0)\n\t"
                 "s_sub_u32 %3, %3, vcc_lo\n\ts_mov_b64 vcc, %0\n\ts_sub_u32 %3, %3, vcc_lo\n\ts_mov_b64 vcc, %1\n\ts_sub_u32 %3, %3, vcc_lo\n\ts_mov_b64 vcc, %2\n\ts_sub_u32 %3, %3, vcc_lo\n\t"
                 "s_max_i32 %3, %3, 0\n\ts_min_i32 %3, %3, 64\n\t"
                 "s_branch 2f\n\t"
                 "3:\n\ts_add_u32 %3, %4, 13\n\t"
                 "4:\n\ts_memrealtime vcc\n\ts_waitcnt lgkmcnt(0)\n\ts_sub_u32 vcc_lo, %3, vcc_lo\n\ts_cmpk_lt_u32 vcc_lo, 16\n\ts_cbranch_scc1 4b\n\t"
                 "s_mov_b32 %3, 0\n\t"
                 "2:\n\ts_sub_u32 %3, %3, 5\n\ts_cbranch_scc0 2b"
                 : "=&s"(a), "=&s"(b), "=&s"(c), "=&s"(n) : "s"(target - 14), "n"(56 - RT_K) : "memory", "scc", "vcc");
}
#define LDA(dst, vo, so, imm) asm volatile("buffer_load_dwordx4 %0, %1, %2, %3 offen offset:%4 sc1" : "=v"(dst) : "v"(vo), "s"(rx), "s"(so), "n"(imm) : "memory")
#define LDI(dst, vo, so, imm) asm volatile("buffer_load_dwordx4 %0, %1, %2, %3 offen offset:%4 sc1" : "+v"(dst) : "v"(vo), "s"(rx), "s"(so), "n"(imm) : "memory")
#define LDA8(dst, vo, so, imm) asm volatile("buffer_load_dwordx2 %0, %1, %2, %3 offen offset:%4 sc1" : "=v"(dst) : "v"(vo), "s"(rx), "s"(so), "n"(imm) : "memory")
#define LDA4(dst, vo, so) asm volatile("buffer_load_dword %0, %1, %2, %3 offen sc1" : "=v"(dst) : "v"(vo), "s"(rx), "s"(so) : "memory")
#define WAITV(n, x) asm volatile("s_waitcnt vmcnt(%1)" : "+v"(x) : "n"(n) : "memory")
#define PIN_S(x) asm volatile("" : "+s"(x))
#define SZERO(x) asm volatile("s_mov_b32 %0, 0" : "=s"(x))
#define PIN_V(x) asm volatile("" : "+v"(x))
#define USE_V(x) asm volatile("" :: "v"(x))
#define LDS_AT(T, a) (*(__attribute__((address_space(3))) T*)(a))
__device__ __forceinline__ float dpp_ror8(float x) { return __uint_as_float(__builtin_amdgcn_mov_dpp(__float_as_uint(x), 0x128, 0xf, 0xf, true)); }
__device__ __forceinline__ unsigned lds_addr(const void* p) { return (unsigned)(size_t)(__attribute__((address_space(3))) const char*)p; }
#define DMA1(vo, so, dst, imm) asm volatile("s_mov_b32 m0, %3\n\ts_nop 0\n\tbuffer_load_dwordx4 %0, %1, %2 offen offset:%4 sc1 lds" :: "v"(vo), "s"(rx), "s"(so), "s"(dst), "n"(imm) : "memory", "m0")

enum { HS_A = 432, HS_E = 504, HS_F = 512, N_1 = 60, N_INIT = 108 };
__device__ __forceinline__ unsigned hs_tags(const Go& go, int l) { return (bld4(go.rh, l < 8 ? l * 1024 + HS_E : OOB) + 2) & ~1u; }
__device__ __forceinline__ rsrc_t arena(const MegaArgs& A, unsigned flip) { return mk_rsrc(arena_base(A.ar) + (flip << 21), ARENA); }
__device__ __forceinline__ unsigned hs_wait(Go& go, int l, int n, unsigned off, bool known, unsigned tag, unsigned& spins) {
#pragma unroll 1
    for (;;) {
        if (!known) tag = (__builtin_amdgcn_readlane(go.v[0], 63) + 2) & ~1u;
        LIVE;
        if (!(__ballot(l < n && ((go.v[0] ^ tag) | (go.v[1] ^ tag)) > 1) & live_)) break;
        __builtin_amdgcn_s_sleep(8);
        go.v = sld8(go.rh, go.so, off);
    }
    return __builtin_amdgcn_readfirstlane(go.v[0]) & 1;
}
#define GO_DECL(pool) Go go; go.rh = mk_rsrc(A.hs, 8192); go.so = (pool) * 1024
#define GO_ON(n) const rsrc_t rx = arena(A, hs_wait(go, l, n / 2, l * 8, false, 0, spins))
#define INIT_BEGIN(pool) const unsigned tags = hs_tags(go, l); go.v = sld8(go.rh, go.so, HS_F); INIT_FLIP(pool)
#define INIT_FLIP(pool) const unsigned tag = __builtin_amdgcn_readlane(tags, pool), flip = hs_wait(go, l, 1, HS_F, true, tag, spins); \
    const rsrc_t rx = arena(A, flip)
#define INIT_END(ii) asm volatile("s_waitcnt vmcnt(0)\n\ts_barrier" ::: "memory"); bst4(go.rh, t < 8 ? t * 1024 + (ii) * 4 : OOB, tags | flip)
#define INIT_SEEN hs_wait(go, l, N_INIT / 2, l * 8, true, tag, spins)
__device__ __forceinline__ void probe(const MegaArgs& A, const Go& go, int l, int id) {
    const rsrc_t rf = arena(A, 0);
    const unsigned tags = hs_tags(go, l);
    unsigned tm[2] = {~0u, ~0u};
    bst4(rf, l < 2 ? l * 8192 : OOB, 0u);
    DRAIN();
#pragma unroll
    for (int k = 0; k < 4; ++k) {
        const unsigned long long t0 = __builtin_amdgcn_s_memtime();
        bst4(rf, l ? OOB : (k & 1) * 8192, 0u);
        DRAIN();
        const unsigned d = (unsigned)(__builtin_amdgcn_s_memtime() - t0);
        tm[k & 1] = d < tm[k & 1] ? d : tm[k & 1];
    }
    bst8(go.rh, l < 8 ? l * 1024 + HS_F : OOB, u32x2{tags | (tm[1] < tm[0]), tags});
}
#define OUTAUX 0
#define DLYB0 0u
#define DLYB1 2000u
#define DLY1 0
#define DLY2 900
#define START_IDS const int t = threadIdx.x, l = t & 63, w = __builtin_amdgcn_readfirstlane(t >> 6); const unsigned l16b = l * 16; GO_DECL(id >> 5); unsigned spins; SZERO(spins)
__device__ __forceinline__ void init_area(const MegaArgs& A, const int ii, const int id, const unsigned t0) {
    START_IDS;
    INIT_BEGIN(id >> 5);
    const int i = 4 * ii + w;
    const bf16_t* mp = A.mem0 + (l & 7) * D_ + 32 * (i % 20) + 4 * (l >> 4);
    const u32x2 x = *(const u32x2*)mp, y = *(const u32x2*)(mp + 16);
    while (rtime() - t0 < DLY1) __builtin_amdgcn_s_sleep(4);
    sst16(rx, xm(XM_OFF + (i / 5 & 15) * 8192 + i % 5 * 1024, i / 80), l16b, u32x4{nosent(x[0]), x[1], nosent(y[0]), y[1]});
    auto fill = [&](unsigned o, unsigned n, unsigned f) {
#pragma unroll 2
        for (unsigned u = i * 1024; u < 2 * n; u += 163840) sst16(rx, 2 * o + u, l16b, u32x4{f, f, f, f});
    };
    fill(XM_OFF + XM_BUF, FI_OFF - PT_BUF - XM_OFF - XM_BUF, ~0u);
    fill(FI_OFF - PT_BUF, PT_BUF, 0u);
    fill(UM_OFF, 3 * UM_BUF, ~0u);
    for (unsigned u = i; u < 208; u += 160) {
        const unsigned f = u < 160 && (u / 16 * 8 + (u & 7) - (l & 1) * A.lag) >= NSEG_;
        sst16(rx, 2 * FI_OFF + u * 1024, l16b, u32x4{f, f, f, f});
    }
    INIT_END(16 + ii);
}
static_assert(FI_N == 80 && FX_OFF + 24576 <= UM_OFF && SG_FL == SG_ST + 8192 && SG_ST % 8192 == 0, "init");
__device__ __forceinline__ void first_rows(const MegaArgs& A, char* smem, const int sg, const int id, const unsigned t0) {
    START_IDS;
    set_m0(lds_addr(smem) + w * 1024);
    {
        unsigned vo[5];
#pragma unroll
        for (int m = 0; m < 5; ++m) vo[m] = row_src((4 * m + w) * 64 + l);
#pragma unroll
        for (int m = 0; m < 10; ++m) dma_next<true>(A.x0 + sg * (SEG_ * D_), vo[m % 5], 4096u, 20480u);
    }
    asm volatile("s_waitcnt vmcnt(0)\n\ts_barrier" ::: "memory");
    const unsigned tags = hs_tags(go, l);
    u32x4 xr[2][5], xl[2][5];
#pragma unroll 1
    for (int rt = 0; rt < 2; ++rt) {
        f32x4 v[10], a;
#pragma unroll
        for (int e = 0; e < 5; ++e) { xr[0][e] = xr[1][e]; xl[0][e] = xl[1][e]; }
#pragma unroll
        for (int e = 0; e < 10; ++e) {
            const u32x2 x = *(const u32x2*)(smem + rt * 20480 + row_pos(l & 15, 5 * w + e / 2) + (e & 1) * 32 + (l >> 4) * 8);
            xr[1][e / 2][2 * (e & 1)] = x[0]; xr[1][e / 2][2 * (e & 1) + 1] = x[1];
            v[e] = f32x4{lo16(x[0]), hi16(x[0]), lo16(x[1]), hi16(x[1])};
            a = e ? a + v[e] : v[0];
        }
        const float mw = red_sum_q((a[0] + a[1]) + (a[2] + a[3])) * (1.0f / 160);
#pragma unroll
        for (int e = 0; e < 10; ++e) { const f32x4 d = v[e] - mw; a = e ? __builtin_elementwise_fma(d, d, a) : d * d; }
        f32v2* sp = (f32v2*)(smem + 49152) + rt * 64 + (l & 15);
        sp[16 * w] = f32v2{mw, red_sum_q((a[0] + a[1]) + (a[2] + a[3]))};
        LDS_BARRIER();
        go.v = sld8(go.rh, go.so, HS_F);
        const f32v2 p = sp[l & 48];
        const float mean = red_sum_q(p[0]) * 0.25f, var = red_sum_q(p[1] + 160.f * (p[0] - mean) * (p[0] - mean)) * (1.0f / D_);
        const float mb = rb(mean), rd = 1.0f / rb(sqrtf(rb(rb(var) + 1e-5f)));
#pragma unroll
        for (int e = 0; e < 10; ++e) {
            const unsigned x = pk2(v[e][0] - mb, v[e][1] - mb), y = pk2(v[e][2] - mb, v[e][3] - mb);
            v[e] = f32x4{lo16(x), hi16(x), lo16(y), hi16(y)} * rd;
            if (e & 1) xl[1][e / 2] = pk8(v[e - 1], v[e]);
        }
    }
    INIT_FLIP(id >> 5);
    while (rtime() - t0 < (sg < 8 ? 0 : (sg < 16 ? DLY1 : DLY2))) __builtin_amdgcn_s_sleep(4);
#pragma unroll
    for (int e = 0; e < 20; ++e) {
        sst16(rx, seg_base(0, sg) + lp((e < 10 ? SG_XL : SG_XR) + (5 * w + e % 10 / 2) * 2048), l16b + (e & 1) * 1024, (e < 10 ? xl : xr)[e & 1][e % 10 / 2]);
        if (e == 9 && sg == 0) { asm volatile("s_waitcnt vmcnt(0)\n\ts_barrier" ::: "memory"); bst8(go.rh, t < 8 ? t * 1024 + HS_A : OOB, u32x2{tags | flip, tags}); }
    }
    const unsigned f = (w >> 1) - 1;
#pragma unroll
    for (int e = 0; e < 8; ++e) sst16(rx, 2 * (SG_OFF + SG_ST) + sg * 524288 + w * 8192 + e / 4 * 4096, l16b + (e & 3) * 1024, u32x4{f, f, f, f});
    INIT_END(sg + (sg < 16 ? 0 : 44));
}
#define LAGV 7
static_assert(LAGV < TR, "rings");
#define NTOUCH 0
#define VSLP 1
#define E_W 137
#define K_W 5
#define K_0 2
#define E_X 40
#define E_B 95
#define E_S 16
#define E_T 10
#define E_G 54
#define IT_1 61
#define D_1 16
#define D_2 8
#define E_L 160
#define E_Z 60
#define E_C 0
#define E_V 150
#define E_M 4
#define FASTR 1
#define A_0 0
#define A_K 7
__device__ __forceinline__ bool adapt(int& mg, unsigned xv, unsigned xp, int up, int mx) {
    const int e = xv - xp;
    const bool late = (unsigned)(e + 16) >= 36u;
    mg = min(max(mg + (xv ? (late ? up : e) : 0), 0), mx);
    PIN_S(mg);
    return late;
}
#define TIGHT(x, it, d) asm volatile("s_cmp_ge_i32 %1, %2\n\ts_cselect_b32 %0, %3, 0" : "=s"(x) : "s"(it), "s"(IT_1), "s"(d) : "scc")
#define CHAIN_WAIT(pv, off, so, code) if (__builtin_expect(__ballot(pv == 0) != 0, 0)) { if (so != OOB) { wave_wait(rx, off, so, spins); } }
#define GAPN 11
#define GAP() asm volatile("s_nop %0" :: "n"(GAPN))
#define LD5(v, so) do { LDA(v[0], l16b, so, 0); GAP(); LDA(v[1], l16b, so, 1024); GAP(); LDA(v[2], l16b, so, 2048); GAP(); LDA(v[3], l16b, so, 3072); GAP(); LDA(v[4], l16k, so, 0); } while (0)
#define CHAIN_IDS const int t = threadIdx.x, l = t & 63, l16 = l & 15, lq = l >> 4; const int w = __builtin_amdgcn_readfirstlane(t >> 6); \
    GO_DECL(id >> 5); const int lag = A.lag, itend = A.nit; const unsigned l16b = l * 16, l16k = l * 16 + 4096; unsigned spins = 0
#define CHAIN_STAGE const char* sg1 = smem + ((w + 1) & 3) * 5120 + l * 16; const char* sg2 = smem + ((w + 2) & 3) * 5120 + l * 16; const char* sg3 = smem + ((w + 3) & 3) * 5120 + l * 16; \
    char* sg0 = smem + w * 5120 + l * 16; auto mrd = [&](int i) { return *(const bf16x8*)((i < 10 ? sg1 : (i < 15 ? sg2 : sg3)) + (i % 5) * 1024); }; \
    const unsigned mo0 = xm(XM_OFF + (pool & 3) * 32768 + w * 8192, pool >> 2), fX = xm(FX_OFF + (pool & 3) * 64, pool >> 2); unsigned fXv = fX; PIN_V(fXv)
#define GATHER(take) do { _Pragma("unroll 1") for (;;) { LD5(v, mo); LIVE; if (!take(live_)) break; } } while (0)
#define CVT2(o0, o1, x) do { o0 = pk2(x[0], x[1]); o1 = pk2(x[2], x[3]); asm volatile("" : "+v"(o0), "+v"(o1)); } while (0)
#define RR_(op, d, x, y, n) op "_dpp " d ", " x ", " y " row_ror:" n " row_mask:0xf bank_mask:0xf\n\t"
#define ROWRED(op, x, y, f0, f1) f0 RR_(op, y, x, x, "1") RR_(op, y, x, y, "2") RR_(op, y, x, y, "3") f1 RR_(op, x, y, y, "4") RR_(op, x, y, x, "8") RR_(op, x, y, x, "12")
#define BIAS(x, a, b, c, d) asm volatile("v_mov_b32 %0, 0xff800000\n\tv_cndmask_b32 %1, %0, 0, %5\n\tv_cndmask_b32 %2, %0, 0, %6\n\tv_cndmask_b32 %3, %0, 0, %7\n\tv_cndmask_b32 %0, %0, 0, %4" \
    : "=&v"(x[0]), "=&v"(x[1]), "=&v"(x[2]), "=&v"(x[3]) : "s"(a), "s"(b), "s"(c), "s"(d))
#define AND2(o0, o1, a, b, m) do { o0 = a & m; o1 = b & m; asm volatile("" : "+v"(o0), "+v"(o1)); } while (0)

constexpr int SL_S = 20480, SS_W = 3136, SL_KM = SL_S + 4 * SS_W, SL_KT = SL_KM + 6144, SL_W = SL_KT + 49152, SL_END = SL_W + 4 * 18432;
static_assert(SL_END <= 163840, "LDS");
#define G_LK 100
#define G_K 32
#define G_KS 6
__device__ __forceinline__ void s_role(const MegaArgs& A, char* smem, const int h, const int id) {
    CHAIN_IDS;
    const int pool = h >> 2, hs = hside(h);
    bf16x8 wf[6][20];
    const char* sW = smem + SL_W + w * 18432 + l * 16 - 2048;
    load_qk<5>(smem, w, l, go, A.Wq + h * DH_, A.Wk + h * DH_, [&](int m) { return m / 3 * 12 + (m % 3 < 2 ? 2 * w + m % 3 : 8 + w); }, wf);
#pragma unroll
    for (int i = 2; i < 20; ++i) *(bf16x8*)(sW + i * 1024) = wf[5][i];
    GO_ON(N_1);
    *(f32x4*)(smem + SL_S + w * SS_W + 3120) = f32x4{-INFINITY, -INFINITY, -INFINITY, -INFINITY};
#pragma unroll
    for (int i = 0; i < 12; ++i) *(u32x4*)(smem + SL_KT + w * 12288 + i * 1024 + l * 16) = u32x4{0u, 0u, 0u, 0u};
    asm volatile("s_waitcnt lgkmcnt(0)" ::: "memory");
    CHAIN_STAGE;
    char* sSw = smem + SL_S + w * SS_W + lq * 256 + ((l16 + 4 * lq) & 15) * 16;
    const int kq = l16, sL = w >> 1, rt = w & 1, row8 = (4 * w + lq) & 7;
    const bool kval = kq < 8 || (kq >> 1) == 4 + sL;
    const char* sSr = smem + SL_S + (kval ? (kq >> 2) * 1040 + (kq & 3) * 256 + ((4 * w + lq + 4 * kq) & 15) * 16 : 3120);
    const unsigned ppos = !kval ? OOB : xm(XP_OFF + (h >> 3) * 8192 +
        (kq < 8 ? ((h & 7) >> 1) * 1024 + ((kq & 3) * 16 + (h & 1) * 8 + row8) * 16 + (kq >> 2) * 8 : 4096 + ((h & 3) * 16 + ((h >> 2) & 1) * 8 + row8) * 16 + (kq & 1) * 8), sL);
    const unsigned pt1 = xm(PT_OFF + (h >> 3) * 16384 + (rt ? (h & 7) * 1024 : 8192 + ((h & 7) >> 1) * 1024 + (h & 1) * 8) + l * 16, sL);
    const unsigned pt2 = lq == 2 * sL ? xm(PT_OFF + (h >> 3) * 16384 + 12288 + (((h >> 2) & 1) * 2 + rt) * 1024 + ((h & 3) * 16 + l16) * 16, sL) : OOB;
    const u64 cm0 = __ballot(4 * lq <= l16), cm1 = __ballot(4 * lq + 1 <= l16), cm2 = __ballot(4 * lq + 2 <= l16), cm3 = __ballot(4 * lq + 3 <= l16), mval = __ballot((lq >> 1) == sL), oval = __ballot(rt);
    const unsigned ma = l16 < 8 ? ~0u : 0u, mb2 = ~ma;
    const unsigned fK = xm(FI_OFF + (h & 15) * 64 + (4 * sL + (l & 3)) * 4, hs);
    const unsigned qo = ((sL * 16 + (h & 15)) * 4 + rt) * 16384, ktl = lds_addr(smem + SL_KT) + w * 12288;
    const char* sKT = smem + SL_KT + l * 16;
    char* sKM = smem + SL_KM + l * 16;
    char* sKX = smem + SL_KT + l * 16 + 4096 + (w >> 1) * 1024 + (w & 1) * 8;
    unsigned X = rtime() - PER;
    int mg = A_0;
    auto qk_so = [&](int it) { const int sg = it - sL * lag; return (unsigned)sg < NSEG_ ? qk_base(sg, hs) + qo : OOB; };
    auto dm0 = [&](int e) {
        if (e == 0) asm volatile("s_mov_b32 m0, %0" :: "s"(ktl) : "m0");
        if (e == 4 || e == 6 || e == 10) asm volatile("s_add_u32 m0, m0, %0" :: "n"(e == 6 ? 2048 : 4096) : "m0", "scc");
    };
    auto dma = [&](int e, unsigned sq, unsigned sk, bool set = true) {
        if (set && (e == 0 || e == 4 || e == 6 || e == 10)) { dm0(e); asm volatile("s_nop 0"); }
        asm volatile("buffer_load_dwordx4 %0, %1, %2 offen offset:%3 sc1 lds" :: "v"(e % 6 < 4 ? l16b : l16k), "s"(rx), "s"(e < 6 ? sk : sq), "n"((e % 6 & 3) * 1024) : "memory");
    };
    {
        const unsigned pv0 = bld4(rx, fK), z = 0, so = qk_so(0);
        CHAIN_WAIT(pv0, fK, z, 1);
#pragma unroll
        for (int e = 0; e < 12; ++e) dma(e, so, so + 32768);
        asm volatile("s_waitcnt vmcnt(0)\n\ts_barrier" ::: "memory");
    }

    unsigned c0 = 0, c1 = 65536, c2 = 131072, mo = mo0, fs = -256, fs1 = 1024, sg = -sL * lag, q0 = 2 * QK_OFF + qo + hs * 8192;
    static_assert(2 * XP_BUF == 65536 && PT_BUF == 65536 && XM_BUF == 131072, "strides");
    PIN_S(c0); PIN_S(c1); PIN_S(c2); PIN_S(fs); PIN_S(fs1); PIN_S(sg); PIN_S(q0);
    for (int it = 0; it < itend; ++it) {
        unsigned ps, pr, ts, tr, qn, kn, tz, fk, per, xp;
        bf16x8 wl[3];
        f32x4 sc[3];
        u32x4 v[5], kp[2][2], kx[2];
        unsigned pvK;
        {
            bf16x8 ka[3], kb[3], q[3];
            auto krd = [&](int f, int e) {
                if (e == 0) kb[f % 3] = *(const bf16x8*)(sKT + (w & 2) * 12288 + f * 1024);
                if (e == 1) q[f % 3] = *(const bf16x8*)(sKT + w * 12288 + 6144 + f * 1024);
                if (e == 2) ka[f % 3] = *(const bf16x8*)(sKT + w * 12288 + f * 1024);
            };
            BIAS(sc[0], oval, oval, oval, oval); BIAS(sc[1], cm0, cm1, cm2, cm3);
            asm volatile("s_waitcnt vmcnt(%0)" :: "n"(5) : "memory");
#pragma unroll
            for (int e = 0; e < 9; ++e) { krd(e / 3, e % 3); SCHED0(); }
            PIN_V(ka[2]);
            rt_fine(X - E_G);
            sfor<0, 13>([&](auto G) {
                constexpr int g = decltype(G)::value, f = g / 2;
                if constexpr (g % 3 == 0) LDA(v[g / 3], g < 12 ? l16b : l16k, mo, (g / 3 & 3) * 1024);
                if constexpr (g < 12) MF_VV(sc[g & 1], (g & 1 ? ka : kb)[f % 3], q[f % 3]);
                SCHED0();
                if (g < 6) { if (g & 1) { krd(f + 3, 1); krd(f + 3, 2); } else krd(f + 3, 0); }
                SCHED0();
            });
#pragma unroll
            for (int e = 0; e < 2; ++e) {
                const u32x2 ka = *(const u32x2*)(sKX + e * 12288), kb = *(const u32x2*)(sKX + (e + 2) * 12288);
                kx[e] = u32x4{ka[0], ka[1], kb[0], kb[1]};
            }
            sg += 1;
            qn = (sg & (TR - 1)) << 21;
            qn += ((qn >> 8) & 8192) ^ q0;
            qn = sg < NSEG_ ? qn : OOB;
            kn = qn + 32768;
            PIN_S(sg); PIN_S(qn); PIN_S(kn);
            if (!G_LK) LDA4(pvK, fK, fs1);
            PIN_V(kx[0]); PIN_V(kx[1]);
        }
        f32x4 acc[6];
        auto mf = [&](int i, int tt, const bf16x8 xb) {
            if (i == 0) { if (tt < 4) MF_AV0(acc[tt], wf[tt][0], xb); else MF_VV0(acc[tt], wf[tt][0], xb); }
            else if (20 * tt + i < 64) MF_AV(acc[tt], wf[tt][i], xb);
            else if (tt < 5 || i < 2) MF_VV(acc[tt], wf[tt][i], xb);
            else MF_VV(acc[5], wl[i % 3], xb);
        };
        auto take = [&](u64 live_, auto N) {
            unsigned m = 0;
            u64 bad;
            sfor<0, 22>([&](auto G) {
                constexpr int g = decltype(G)::value;
                if constexpr (g < 20 && g % 4 == 0) { WAITV(4 + (decltype(N)::value && !G_LK) - g / 4, v[g / 4]); *(u32x4*)(sg0 + g / 4 * 1024) = v[g / 4]; SCHED0(); }
                mf(g / 6, g % 6, asb8(v[g / 6]));
                SCHED0();
                if constexpr (g < 20 && g % 4 == 0) { m = mx4(m, v[g / 4]); if (g < 16) PIN_V(m); else { bad = __ballot(m == ~0u); PIN_S(bad); } }
                if constexpr (g == 1 || g == 2 || g == 6 || g == 13) { constexpr int e = g < 3 ? g - 1 : 2 + g / 8; kp[e >> 1][e & 1] = *(const u32x4*)(sKT + e * 12288 + w * 1024); }
                if constexpr (g == 5 || g == 10 || g == 18) wl[(g / 5 + 1) % 3] = *(const bf16x8*)(sW + (g / 5 + 1) * 1024);
                if (g == 18 && !decltype(N)::value) { bad &= live_; PIN_S(bad); }
                if (g == 20 && decltype(N)::value && !G_LK) WAITV(0, pvK);
                SCHED0();
            });
            return bad;
        };
        if (__builtin_expect(take(~0ull, IC<1>{}) != 0, 0)) {
            auto again = [&](u64 live_) { return take(live_, IC<0>{}); };
            GATHER(again);
            asm volatile("s_nop 7" : "+v"(acc[0]), "+v"(acc[1]), "+v"(acc[2]), "+v"(acc[3]), "+v"(acc[4]), "+v"(acc[5]));
        }
        unsigned xv, kx0;
        u64 nk;
        u32x4 ko, kz;
        {
            bf16x8 mm[3];
            u32x4 qp, qa, qb, qz;
            unsigned z0, z1;
            f32x4 s[2], sm;
            sfor<22, 128>([&](auto G) {
                constexpr int g = decltype(G)::value, kt[13] = {5, 5, 5, 3, 4, -1, -1, 3, -1, 4, -1, 3, 4}, ki[13] = {17, 18, 19, 17, 17, 0, 0, 18, 0, 18, 0, 19, 19};
                constexpr int i = g < 102 ? g / 6 : (g < 111 ? 17 + (g - 102) / 3 : (g < 124 ? ki[g < 111 || g > 123 ? 0 : g - 111] : 0));
                constexpr int tt = g < 102 ? g % 6 : (g < 111 ? (g - 102) % 3 : (g < 124 ? kt[g < 111 || g > 123 ? 0 : g - 111] : -1));
                if constexpr (tt >= 0) mf(i, tt, i < 5 ? asb8(v[i < 5 ? i : 0]) : mm[(i < 5 ? 0 : i - 5) % 3]);
                if constexpr (g == 116 || g == 117) MF_VV0(s[g & 1], kp[0][g & 1], qa);
                if constexpr (g == 119 || g == 124) MF_VV(s[~g & 1], kp[1][~g & 1], qb);
                if constexpr (g == 121 || g == 125) MF_VV(s[g == 125], kx[g == 125], qz);
                if constexpr (g == 126) MF_VV0(sm, kz, qz);
                if constexpr (g == 127) MF_VV(sm, ko, qp);
                SCHED0();
                if (g == 22) { LDS_BARRIER(); }
                if (g == 23) mm[0] = mrd(5);
                if (g == 24) wl[2] = *(const bf16x8*)(sW + 5120);
                if constexpr (g > 24 && g < 103 && g % 6 == (g < 102)) mm[(g / 6 - 3) % 3] = mrd(g / 6 + 2);
                if constexpr (g > 24 && g < 102 && g % 6 == 3) wl[(g / 6 + 2) % 3] = *(const bf16x8*)(sW + (g / 6 + 2) * 1024);
                if (g == 104) wl[1] = *(const bf16x8*)(sW + 19 * 1024);
                if (g == 110) PIN_V(wl[1]);
                if (g == G_LK) LDA4(pvK, fK, fk);
                if (g == 26 && !G_LK) { nk = __ballot(pvK == 0); PIN_S(nk); dm0(0); }
                if (g == 30 && !G_LK) asm volatile("s_cmp_eq_u64 %1, 0\n\ts_cselect_b32 %0, %2, %3" : "=s"(kx0) : "s"(nk), "s"(kn), "n"(OOB) : "scc");
                if constexpr (!G_LK && g >= G_K && g <= G_K + 5 * G_KS && (g - G_K) % G_KS == 0) dma((g - G_K) / G_KS, qn, kx0, false);
                if (g == G_K + 4 * G_KS - 2 && !G_LK) dm0(4);
                if (g == 36) { ps = c0; pr = c2; PIN_S(ps); PIN_S(pr); }
                if (g == 42) { c2 = c0; c0 = c1; PIN_S(c2); PIN_S(c0); }
                if (g == 48) { c1 = pr; ts = 2 * ps; PIN_S(c1); PIN_S(ts); }
                if (g == 60) { mo = mo0 + 4 * c0; PIN_S(mo); }
                if (g == 66) { fs += 256; tz = fs & 8192; PIN_S(fs); PIN_S(tz); }
                if (g == 72) { fs += tz; tr = 2 * c0; PIN_S(fs); PIN_S(tr); }
                if (g == 84) { fk = fs1; fs1 += 1024; PIN_S(fk); PIN_S(fs1); }
                if (g == 90) { fs1 += fs1 & 8192; PIN_S(fs1); }
                if (g == 54) TIGHT(per, it, D_1);
                if (g == 78) { per = PER - per; PIN_S(per); }
                if (g == 96) { xp = X; X += per + mg; PIN_S(xp); PIN_S(X); }
                if (g == 111) CVT2(qp[0], qp[1], acc[0]);
                if (g == 112) CVT2(qp[2], qp[3], acc[1]);
                if (g == 113) AND2(qa[0], qa[1], qp[0], qp[1], ma);
                if (g == 114) AND2(qa[2], qa[3], qp[2], qp[3], ma);
                if (g == 115) CVT2(z0, z1, acc[2]);
                if (g == 118) AND2(qz[0], qz[1], z0, z1, ma);
                if (g == 119) AND2(qz[2], qz[3], z0, z1, mb2);
                if (g == 116) AND2(qb[0], qb[1], qp[0], qp[1], mb2);
                if (g == 117) AND2(qb[2], qb[3], qp[2], qp[3], mb2);
                if (g == 120) CVT2(kz[0], kz[1], acc[5]);
                if (g == 121) asm volatile("v_cvt_pk_bf16_f32 %0, %2, %3\n\tv_cvt_pk_bf16_f32 %1, %4, %5" : "=&v"(kz[2]), "=&v"(kz[3]) : "v"(acc[5][0]), "v"(acc[5][1]), "v"(acc[5][2]), "v"(acc[5][3]));
                if (g == 123) *(f32x4*)sSw = s[0];
                if (g == 124) CVT2(ko[0], ko[1], acc[3]);
                if (g == 125) CVT2(ko[2], ko[3], acc[4]);
                if (g == 127) *(f32x4*)(sSw + 1040) = s[1];
                SCHED0();
            });
            asm volatile("s_nop 6" : "+v"(sm) :: "memory");
            *(f32x4*)(sSw + 2080) = sm;
        }
        LDS_BARRIER();
        constexpr float SC = 0.07216878364870322f * 1.4426950408889634f;
        {
            const f32x4 s0 = *(const f32x4*)sSr, s1 = *(const f32x4*)(sSr + SS_W), s2 = *(const f32x4*)(sSr + 2 * SS_W), s3 = *(const f32x4*)(sSr + 3 * SS_W);
            SCHED0();
            f32x4 sv = ((s0 + s1) + s2) + s3;
            u32x2 p;
            p[0] = __float_as_uint(fmaxf(fmaxf(fmaxf(sv[0], sv[1]), sv[2]), sv[3]));
            asm volatile(ROWRED("v_max_f32", "%0", "%1", "v_mul_f32 %2, %6, %2\n\tv_mul_f32 %3, %6, %3\n\t", "v_mul_f32 %4, %6, %4\n\tv_mul_f32 %5, %6, %5\n\t")
                         "v_fma_f32 %2, -%0, %6, %2\n\tv_fma_f32 %3, -%0, %6, %3\n\tv_fma_f32 %4, -%0, %6, %4\n\tv_fma_f32 %5, -%0, %6, %5\n\t"
                         "v_exp_f32 %2, %2\n\tv_exp_f32 %3, %3\n\tv_exp_f32 %4, %4\n\tv_exp_f32 %5, %5\n\t"
                         "v_add_f32 %0, %2, %3\n\tv_add_f32 %0, %0, %4\n\tv_add_f32 %0, %0, %5\n\t"
                         ROWRED("v_add_f32", "%0", "%1", "s_nop 1\n\t", "s_nop 1\n\t")
                         "v_rcp_f32 %0, %0\n\ts_nop 0\n\tv_mul_f32 %2, %0, %2\n\tv_mul_f32 %3, %0, %3\n\tv_mul_f32 %4, %0, %4\n\tv_mul_f32 %5, %0, %5\n\t"
                         "v_cvt_pk_bf16_f32 %0, %2, |%3|\n\tv_cvt_pk_bf16_f32 %1, %4, %5"
                         : "+v"(p[0]), "=&v"(p[1]), "+v"(sv[0]), "+v"(sv[1]), "+v"(sv[2]), "+v"(sv[3]) : "s"(SC));
            sst8(rx, ps, ppos, p);
        }
        SCHED0();
        {
            if (G_LK) { WAITV(1, pvK); nk = __ballot(pvK == 0); }
            if (__builtin_expect(nk != 0, 0)) {
                wave_wait(rx, fK, fk, spins);
#pragma unroll
                for (int e = 0; e < 6; ++e) if (!G_LK) dma(e, qn, kn);
            }
            bf16x8 km[6], q[6];
            *(u32x4*)(sKM + w * 1024) = ko;
            *(u32x2*)(sKX + (SL_KM - SL_KT)) = u32x2{kz[0], kz[1]};
            SCHED0();
#pragma unroll
            for (int f = 0; f < 6; ++f) { q[f] = *(const bf16x8*)(sKT + w * 12288 + 6144 + f * 1024); SCHED0(); if (G_LK) dma(f, qn, kn); SCHED0(); }
            LDA4(xv, fXv, fs);
            dm0(6);
            asm volatile("s_waitcnt lgkmcnt(5)\n\ts_barrier" ::: "memory");
#pragma unroll
            for (int f = 0; f < 6; ++f) { km[f] = *(const bf16x8*)(sKM + f * 1024); SCHED0(); }
            dma(6, qn, kn, false);
            BIAS(sc[2], mval, mval, mval, mval);
            PIN_V(q[5]);
            dma(7, qn, kn, false);
            float mx = max3r(max3r(sc[0][0], sc[0][1], sc[0][2]), max3r(sc[0][3], sc[1][0], sc[1][1]), max2r(sc[1][2], sc[1][3])), tm;
            PIN_V(mx);
#pragma unroll
            for (int f = 0; f < 6; ++f) { MF_VV(sc[2], km[f], q[f]); if (f & 1) dma(8 + f / 2, qn, kn, false); if (f == 4) dm0(10); SCHED0(); }
            asm volatile("s_nop 5" : "+v"(sc[2]));
            mx = max3r(max3r(mx, sc[2][0], sc[2][1]), sc[2][2], sc[2][3]);
            asm volatile("v_mov_b32 %1, %0\n\ts_nop 1\n\tv_permlane16_swap_b32 %0, %1\n\tv_max_f32 %0, %0, %1\n\tv_mov_b32 %1, %0\n\ts_nop 1\n\tv_permlane32_swap_b32 %0, %1\n\tv_max_f32 %0, %0, %1" : "+v"(mx), "=&v"(tm));
            dma(11, qn, kn);
            mx *= -SC;
#pragma unroll
            for (int e = 0; e < 3; ++e) {
#pragma unroll
                for (int r = 0; r < 4; ++r) sc[e][r] = __builtin_fmaf(sc[e][r], SC, mx);
                PIN_V(sc[e]);
            }
#pragma unroll
            for (int e = 0; e < 3; ++e) {
#pragma unroll
                for (int r = 0; r < 4; ++r) sc[e][r] = __builtin_amdgcn_exp2f(sc[e][r]);
            }
            const f32x4 t4 = (sc[0] + sc[1]) + sc[2];
            float sum = (t4[0] + t4[1]) + (t4[2] + t4[3]);
            PIN_V(sum);
            float inv = __builtin_amdgcn_rcpf(red_sum_q(sum));
            PIN_V(inv);
            unsigned u[6];
#pragma unroll
            for (int e = 0; e < 6; ++e) { u[e] = pk2(sc[e >> 1][2 * (e & 1)] * inv, sc[e >> 1][2 * (e & 1) + 1] * inv); if (!(e & 1)) u[e] = nosent(u[e]); }
            const auto a4 = __builtin_amdgcn_permlane16_swap(u[4], u[4], false, false), a5 = __builtin_amdgcn_permlane16_swap(u[5], u[5], false, false);
            sst16(rx, ts, pt2, u32x4{u[4], u[5], a4[1], a5[1]});
            u32x2 o0, o1;
            asm volatile("v_mov_b64 %0, -1\n\tv_mov_b64 %1, -1" : "=v"(o0), "=v"(o1));
            const u32x4 ones = {o0[0], o0[1], o1[0], o1[1]};
            if (rt) { sst16(rx, ts, pt1, u32x4{u[0], u[1], u[2], u[3]}); sst16(rx, tr, pt1, ones); } else { sst8(rx, ts, pt1, u32x2{u[2], u[3]}); sst8(rx, tr, pt1, u32x2{ones[0], ones[1]}); }
            sst8(rx, pr, ppos, u32x2{ones[0], ones[1]});
            sst16(rx, tr, pt2, ones);
            asm volatile("s_waitcnt vmcnt(11)\n\ts_barrier" : "+v"(xv) :: "memory");
            xv = __builtin_amdgcn_readfirstlane(xv);
            adapt(mg, xv, xp, A_UP, A_MAX);
            X = xv ? xv + per + mg : X;
        }
    }
    SPIN_END;
}

constexpr int CL_R = 0, CL_C = CL_R + 16384, CL_END = CL_C + 4096;
__device__ __forceinline__ void c_role(const MegaArgs& A, char* smem, const int pool, const int ku, const int id) {
    CHAIN_IDS;
    const int cL = pool >> 2, j = 5 * (pool & 3) + ku;
    go.v = sld8(go.rh, go.so, l * 8);
    GO_ON(N_1);
    unsigned fXv = xm(FX_OFF + (pool & 3) * 64, cL);
    PIN_V(fXv);
    const int hi = (l >> 3) & 1, tk = l & 7;
    const unsigned pos = xm(XM_OFF + w * 32768 + (j / 5) * 8192 + (j % 5) * 1024 + (lq * 16 + cL * 8 + tk) * 16 + hi * 8, 0);
    const unsigned umo = xm(UM_OFF + (16 * j + 2 * w) * 1024, cL), po0 = xm(XP_OFF + w * 8192, cL), pto = xm(PT_OFF + w * 16384, cL), uto = xm(UT_OFF + (64 * j + 8 * w) * 1024, cL);
    const unsigned fU = lp(SG_FL + FL_U + (8 * w + tk) * 64 + (pool & 3) * 4);
    const unsigned xo = lp((2 * j + (w >> 1)) * 1024 + (lq * 16 + 8 * (w & 1) + tk) * 16 + hi * 8), sto = l < 8 ? lp(SG_ST + (4 * j + w) * 64 + l * 8) : OOB;
    u32x2 alt;
    { const u32x2 m = *(const u32x2*)(A.mem0 + tk * D_ + 32 * j + 16 * hi + 4 * lq); alt = u32x2{nosent(m[0]), m[1]}; }
    f32x4* sRw0 = (f32x4*)(smem + CL_R) + w * 256 + l;
    f32x4* sRw1 = (f32x4*)(smem + CL_R) + w * 256 + 128 + (l ^ 8);
    const f32x4* sRr = (const f32x4*)(smem + CL_R) + (hi * 2 + (w >> 1)) * 64 + lq * 16 + ((8 * (w & 1) + tk) ^ (8 * hi));
    f32x4* sC = (f32x4*)(smem + CL_C);
    int mb = 0;
    u32x4 ua[16], ub[16], um[4];
#pragma unroll
    for (int e = 0; e < 16; ++e) ua[e] = u32x4{0u, 0u, 0u, 0u};
#pragma unroll
    for (int e = 0; e < 4; ++e) um[e] = u32x4{0u, 0u, 0u, 0u};
    for (int s = 0; s < NTOUCH; ++s) sst8(rx, seg_base(cL, s) + lp(SG_X1), xo, u32x2{0u, 0u});
    unsigned X = rtime() - 2 * PER;
    auto seg_so = [&](int sg) { return (unsigned)sg < NSEG_ ? seg_base(cL, sg) : OOB; };
    int mg = A_0;
    unsigned dx = 0, sb0 = OOB, sb1 = seg_so(-cL * lag), pvU = sld4(rx, sb1, fU), ts = pto + 4 * PT_BUF, ts2 = ts + 16384;
    auto step = [&](const int it, auto LAST, u32x4 (&up)[16], u32x4 (&ut)[16]) {
        constexpr bool last = decltype(LAST)::value == 1, fin = decltype(LAST)::value == 2;
        const int nb = (mb == 2) ? 0 : mb + 1, pb = mb ? mb - 1 : 2, sg = it - cL * lag;
        if (!last) CHAIN_WAIT(pvU, fU, sb1, 0);
        u32x4 pt[16];
        u32x2 xr;
        auto pt_load = [&](int e) { LDA(pt[e], e < 4 || e >= 12 ? l16k : l16b, e < 8 ? ts2 : ts, (e & 3) * 1024); };
        if (!last && E_C) rt_until(X + E_C - dx);
        pt_load(0);
        SCHED0();
        unsigned uo = sb1 != OOB ? uto + ut_slot(sg) : OOB, uo2 = uo + 65536;
        PIN_S(uo); PIN_S(uo2);
        auto ut_load = [&](int e) {
            if (!last) LDA(ut[e], (e & 4) ? l16k : l16b, e < 8 ? uo : uo2, (e & 3) * 1024);
        };
        pt_load(1);
        SCHED0();
        unsigned sbr = (sb0 ^ (cL << 13)) + lp(SG_XR), sbx = sb0 + lp(SG_X1), sb2 = seg_so(sg + 1);
        PIN_S(sbr); PIN_S(sbx); PIN_S(sb2);
        pt_load(2);
        SCHED0();
        unsigned fs = fx_step(it), cu = umo + mb * (2 * UM_BUF), cu2 = cu + 16384, cp = po0 + mb * (2 * XP_BUF), cr = pb * (2 * XM_BUF), cr2 = cr + 8192;
        PIN_S(fs); PIN_S(cu); PIN_S(cu2); PIN_S(cp); PIN_S(cr); PIN_S(cr2);
        pt_load(3);
        SCHED0();
        unsigned cs = sg < 0 ? OOB : nb * (2 * XM_BUF), cs2 = cs + 8192;
        X += PER - dx + mg;
        PIN_S(cs); PIN_S(cs2); PIN_S(X);
#pragma unroll
        for (int e = 4; e < 13; ++e) { pt_load(e); if (e < 12) GAP(); }
        u32x2 st;
        {
            f32x4 d[2][2];
            auto check = [&](auto E0, auto N, auto W) {
                constexpr int e0 = decltype(E0)::value, n = decltype(N)::value;
                auto look = [&](auto WW) {
                    asm volatile("s_waitcnt vmcnt(%2)" : "+v"(pt[e0]), "+v"(pt[e0 + 1]) : "n"(decltype(WW)::value) : "memory");
                    if (n == 4) asm volatile("" : "+v"(pt[e0 + n - 2]), "+v"(pt[e0 + n - 1]));
                    unsigned m = 0;
#pragma unroll
                    for (int e = e0; e < e0 + n; ++e) m = mx4(m, pt[e]);
                    return m; };
                if (__builtin_expect(__ballot(look(W) == ~0u) != 0, 0)) {
#pragma unroll 1
                    for (;;) {
#pragma unroll
                        for (int e = e0; e < e0 + n; ++e) pt_load(e);
                        LIVE;
                        if (!BAD(look(IC<0>{}))) break;
                    }
                }
            };
            sfor<0, 40>([&](auto G) {
                constexpr int g = decltype(G)::value, n = g & 1;
                if constexpr (g == 0) check(IC<0>{}, IC<4>{}, IC<9>{});
                if constexpr (g == 8) check(IC<4>{}, IC<2>{}, IC<10>{});
                if constexpr (g == 16) check(IC<6>{}, IC<2>{}, IC<(last ? 9 : 11)>{});
                if constexpr (g == 24) check(IC<8>{}, IC<4>{}, IC<(last ? 5 : 10)>{});
                if constexpr (g == 32) check(IC<12>{}, IC<2>{}, IC<(last ? 3 : 11)>{});
                if constexpr (g == 36) { check(IC<14>{}, IC<2>{}, IC<(last ? 0 : 10)>{}); PIN_V(xr); }
                if constexpr (g < 4) MF_VV0(d[n][g >> 1], um[2 * n], pt[g >> 1]);
                else if constexpr (g < 8) MF_VV(d[n][(g >> 1) & 1], um[2 * n + 1], pt[g >> 1]);
                else if constexpr (g < 24) {
                    constexpr int hh = (g - 8) >> 1;
                    const u32x4 x = up[8 * n + hh], z = pt[4 + (hh >> 1)];
                    const u32x2 xl = {x[0], x[1]}, zl = {z[2 * (hh & 1)], z[2 * (hh & 1) + 1]};
                    MH_(d[n][0], "v", xl, "v", zl);
                } else MF_VV(d[n][1], up[8 * n + ((g - 24) >> 1)], pt[8 + ((g - 24) >> 1)]);
                constexpr int rq = g < 32 ? ((g & 7) == 2 ? 0 : ((g & 7) == 5 ? 1 : ((g & 7) == 7 ? 2 : -99))) + 3 * (g >> 3) : (g == 33 ? 12 : (g == 35 ? 13 : -99));
                if constexpr (rq >= 0 && rq < 3) pt_load(13 + rq); else if constexpr (rq == 3) LDA8(xr, xo, sbr, 0); else if constexpr (rq > 3) ut_load(rq - 4);
                if (g == 24) sRw0[0] = d[0][0];
                if (g == 27) sRw1[0] = d[1][0];
                SCHED0();
            });
            const f32x4 xf = {lo16(xr[0]), hi16(xr[0]), lo16(xr[1]), hi16(xr[1])};
            asm volatile("" : "+v"(d[0][1]) : "v"(xf) : "memory");
            sRw0[64] = d[0][1];
            ut_load(10);
            asm volatile("s_nop 2" : "+v"(d[1][1]) :: "memory");
            sRw1[64] = d[1][1];
            ut_load(11);
            LDS_BARRIER();
            const f32x4 s0 = sRr[0], s1 = sRr[256], s2 = sRr[512], s3 = sRr[768];
            ut_load(12);
            SCHED0();
            f32x4 s = (((xf + s0) + s1) + s2) + s3;
            sst8(rx, sbx, xo, u32x2{pk2(s[0], s[1]), pk2(s[2], s[3])});
            float sm = (s[0] + s[1]) + (s[2] + s[3]);
            sm += dpp_ror8(sm);
            PIN_V(sm);
            ut_load(13);
            sm = red_sum_q(sm) * (1.0f / 32);
            PIN_V(sm);
            ut_load(14);
            s -= sm;
            float sq = __builtin_fmaf(s[3], s[3], __builtin_fmaf(s[2], s[2], __builtin_fmaf(s[1], s[1], s[0] * s[0])));
            sq += dpp_ror8(sq);
            PIN_V(sq);
            ut_load(15);
            sq = red_sum_q(sq);
            st = u32x2{nosent(__float_as_uint(sm)), __float_as_uint(sq)};
            PIN_V(st);
        }
        SCHED0();
        if (last) { DRAIN(); sst8(rx, sb0, sto, st); return; }
        unsigned xv;
        rt_until(X + E_X);
        LDA4(xv, fXv, fs);
        ts = pto + mb * (2 * PT_BUF); ts2 = ts + 16384;
        PIN_S(ts); PIN_S(ts2);
        asm volatile("s_waitcnt vmcnt(4)" ::: "memory");
        sst8(rx, sb0, sto, st);
        sst8(rx, cr, pos, u32x2{~0u, ~0u});
        sst8(rx, cr2, pos, u32x2{~0u, ~0u});
        WAITV(3, xv);
        const unsigned xp = X;
        X = __builtin_amdgcn_readfirstlane(xv);
        PIN_S(X);
        if (__builtin_expect(X == 0, 0)) {
#pragma unroll 1
            for (;;) {
                LDA4(xv, fXv, fs);
                LIVE;
                WAITV(0, xv);
                X = __builtin_amdgcn_readfirstlane(xv);
                if (X || !live_) break;
            }
        }
        unsigned d2;
        TIGHT(dx, it, D_1); TIGHT(d2, it, D_2);
        adapt(mg, X, xp, A_UP, A_MAX);
        d2 -= mg >> 1;
        if (fin) {
#pragma unroll 1
            for (;;) {
                rt_until(X + E_U - d2);
#pragma unroll
                for (int i = 0; i < 4; ++i) LDI(um[i], l16b, (i & 2) ? cu2 : cu, (i & 1) * 1024);
                LIVE;
                asm volatile("s_waitcnt vmcnt(0)" : "+v"(um[0]), "+v"(um[1]), "+v"(um[2]), "+v"(um[3]) :: "memory");
                if (!BAD(mx4(mx4(mx4(mx4(0u, um[0]), um[1]), um[2]), um[3]))) break;
            }
            rt_until(X + E_L);
            mb = nb; sb0 = sb1; sb1 = sb2;
            return;
        }
        u32x4 p[5];
        f32x4 a[2], b[2];
        auto take2 = [&](u64 live_) {
            unsigned m = 0;
            u64 bad;
#pragma unroll
            for (int i = 0; i < 4; ++i) { WAITV(9 - i, um[i]); m = mx4(m, um[i]); PIN_V(m); }
#pragma unroll
            for (int i = 0; i < 5; ++i) {
                WAITV(4 - i, p[i]);
#pragma unroll
                for (int e = 0; e < 4; ++e) {
                    f32x4& d = (e & 1) ? b[e >> 1] : a[e >> 1];
                    if (i == 0) MF_VV0(d, ut[8 * (e >> 1) + (e & 1)], p[0]); else if (i < 4) MF_VV(d, ut[8 * (e >> 1) + 2 * (i & 3) + (e & 1)], p[i]); else MF_VV(d, um[e], p[4]);
                    SCHED0();
                    if (e == 0) { m = mx4(m, p[i]); if (i < 4) PIN_V(m); else { bad = __ballot(m == ~0u); PIN_S(bad); } }
                    if (e == 1 && i == 4) { bad &= live_; PIN_S(bad); }
                    SCHED0();
                }
            }
            return bad;
        };
        {
            unsigned tP = X + E_P - d2, tU = X + E_U - d2 - E_M * (4 - ku), ask = 1;
            PIN_S(ask);
            rt_until(tU);
#pragma unroll 1
            for (;;) {
#pragma unroll
                for (int i = 0; i < 4; ++i) LDI(um[i], l16b, (i & 2) ? cu2 : cu, (i & 1) * 1024);
                LDA4(pvU, fU, sb2);
                LIVE;
                if (__builtin_expect(ask | !FASTR, 1)) rt_fine(tP);
                LD5(p, cp);
                ask = 0; PIN_S(ask);
                SCHED0();
                if (!take2(live_)) break;
            }
        }
        f32x4 x;
        asm volatile("v_add_f32_dpp %0, %8, %4 row_ror:8 row_mask:0xf bank_mask:0x3\n\tv_add_f32_dpp %1, %9, %5 row_ror:8 row_mask:0xf bank_mask:0x3\n\t"
                     "v_add_f32_dpp %2, %10, %6 row_ror:8 row_mask:0xf bank_mask:0x3\n\tv_add_f32_dpp %3, %11, %7 row_ror:8 row_mask:0xf bank_mask:0x3"
                     : "=&v"(x[0]), "=&v"(x[1]), "=&v"(x[2]), "=&v"(x[3]) : "v"(a[0][0]), "v"(a[0][1]), "v"(a[0][2]), "v"(a[0][3]), "v"(b[0][0]), "v"(b[0][1]), "v"(b[0][2]), "v"(b[0][3]));
        asm volatile("s_nop 0\n\tv_add_f32_dpp %0, %8, %4 row_ror:8 row_mask:0xf bank_mask:0xc\n\tv_add_f32_dpp %1, %9, %5 row_ror:8 row_mask:0xf bank_mask:0xc\n\t"
                     "v_add_f32_dpp %2, %10, %6 row_ror:8 row_mask:0xf bank_mask:0xc\n\tv_add_f32_dpp %3, %11, %7 row_ror:8 row_mask:0xf bank_mask:0xc"
                     : "+v"(x[0]), "+v"(x[1]), "+v"(x[2]), "+v"(x[3]) : "v"(b[1][0]), "v"(b[1][1]), "v"(b[1][2]), "v"(b[1][3]), "v"(a[1][0]), "v"(a[1][1]), "v"(a[1][2]), "v"(a[1][3]));
        sC[w * 64 + l] = x;
        LDS_BARRIER();
        const f32x4 s = ((sC[l] + sC[64 + l]) + sC[128 + l]) + sC[192 + l];
        const u32x2 r = {nosent(pk2(s[0], s[1])), pk2(s[2], s[3])};
        sst8(rx, cs, pos, r);
        sst8(rx, cs2, pos, r);
        SCHED0();
        if (__builtin_expect(cs == OOB, 0)) { sst8(rx, nb * (2 * XM_BUF), pos, alt); sst8(rx, nb * (2 * XM_BUF) + 8192, pos, alt); }
        PIN_V(pvU);
        mb = nb; sb0 = sb1; sb1 = sb2;
    };
    int it = 0;
    PIN_S(spins); PIN_V(pvU); PIN_V(alt);
#pragma unroll 1
    do { step(it, IC<0>{}, ua, ub); step(it + 1, IC<0>{}, ub, ua); it += 2; } while (it + 2 < itend);
    if (LAGV & 1) { step(it, IC<2>{}, ua, ub); step(it + 1, IC<1>{}, ub, ua); } else { step(it, IC<0>{}, ua, ub); step(it + 1, IC<2>{}, ub, ua); step(it + 2, IC<1>{}, ua, ub); }
    SPIN_END;
}

constexpr int VL_V = 20480, VL_W = VL_V + 6144, VL_END = VL_W + 81920;
__device__ __forceinline__ void vm_role(const MegaArgs& A, char* smem, const int h, const int id) {
    CHAIN_IDS;
    const int pool = h >> 2;
    bf16x8 wf[3][20], wo[10][4];
    char* sWo = smem + VL_W + w * 20480 + l * 16;
    {
        bf16x8 tmp[20];
        load_vo<5>(smem, w, l, go, A.Wv + h * DH_, A.Wo + (size_t)(h * DH_) * D_, wf, [&](int k, int ct, int ww) -> bf16x8& { return k < 4 ? wo[ct][(k - ww) & 3] : tmp[4 * (ct >> 1) + 2 * (k & 1) + (ct & 1)]; });
#pragma unroll
        for (int i = 0; i < 20; ++i) *(bf16x8*)(sWo + i * 1024) = tmp[i];
    }
    GO_ON(N_1);
    CHAIN_STAGE;
    unsigned upos[5];
#pragma unroll
    for (int m = 0; m < 5; ++m) upos[m] = xm(UM_OFF + ((10 * w + 2 * m + (lq & 1)) * 8 + (h >> 2)) * 1024 + ((h & 3) * 16 + l16) * 16, lq >> 1);
    char* sV = smem + VL_V + l * 16;
    const char* sV1 = sV + ((w + 1) & 3) * 1024; const char* sV2 = sV + ((w + 2) & 3) * 1024; const char* sV3 = sV + ((w + 3) & 3) * 1024;
    const unsigned xw = (h == 1 && t < 8) ? xm(FX_OFF + (t & 3) * 64, t >> 2) : OOB;
    int mb = 0;
    unsigned X = rtime() - 2 * PER, dx = 0;
    int mg = A_0, cnt = 0;
    for (int it = 0; it < itend; ++it) {
        const int nb = (mb == 2) ? 0 : mb + 1, pb = mb ? mb - 1 : 2;
        unsigned mo = mo0 + mb * (2 * XM_BUF), fs = fx_step(it), us = mb * (2 * UM_BUF), ur = pb * (2 * UM_BUF);
        X += PER - dx + mg;
        const unsigned xp = X;
        PIN_S(mo); PIN_S(fs); PIN_S(us); PIN_S(ur); PIN_S(X);
        rt_fine(X - E_G);
        f32x4 acc[3];
        u32x4 v[5];
        auto take = [&](u64 live_) {
            unsigned m = 0;
            u64 bad;
            sfor<0, 11>([&](auto G) {
                constexpr int g = decltype(G)::value, e = g < 9 && !(g & 1) ? g / 2 : -1;
                if constexpr (e >= 0) { WAITV(4 - e, v[e]); *(u32x4*)(sg0 + e * 1024) = v[e]; SCHED0(); }
                if (g < 3) MF_AV0(acc[g % 3], wf[g % 3][0], v[0]); else MF_AV(acc[g % 3], wf[g % 3][g / 3], v[g / 3]);
                SCHED0();
                if constexpr (e >= 0) { m = mx4(m, v[e]); if (e < 4) PIN_V(m); else { bad = __ballot(m == ~0u); PIN_S(bad); } }
                if (g == 9) { bad &= live_; PIN_S(bad); }
                SCHED0();
            });
            return bad;
        };
        GATHER(take);
        bf16x8 wl[4];
        u32x4 vf[6], o;
        f32x4 d[10];
        unsigned xv;
        {
            constexpr int MW = 5;
            bf16x8 mm[MW];
            u32x2 vz;
            u64 tb64;
            unsigned t0, t1;
            asm volatile("s_waitcnt lgkmcnt(0)\n\ts_barrier\n\ts_memrealtime %0" : "=s"(tb64) :: "memory");
            mm[0] = mrd(5);
            SCHED0();
            sfor<11, 60>([&](auto G) {
                constexpr int g = decltype(G)::value, i = g < 45 ? g / 3 : (g < 50 ? g - 30 : 15 + (g - 50) / 2), tt = g < 45 ? g % 3 : (g < 50 ? 2 : g & 1);
                const bf16x8 xb = i < 5 ? asb8(v[i < 5 ? i : 0]) : mm[(i < 5 ? 0 : i - 5) % MW];
                MF_AV(acc[tt], wf[tt][i], xb);
                SCHED0();
                if constexpr (g < 15) mm[g - 10] = mrd(g - 5);
                if constexpr (g >= 18 && g < 46 && g % 3 == 0) mm[(g / 3 - 6) % MW] = mrd(g / 3 + 4);
                if constexpr (g >= 54 && g < 58) wl[g - 54] = *(const bf16x8*)(sWo + (g - 54) * 1024);
                if (g == 17) asm volatile("s_waitcnt lgkmcnt(0)" : "+s"(tb64) :: "memory");
                if (g == 19) asm volatile("s_sub_u32 %0, %2, %3\n\ts_add_u32 %1, %2, %4" : "=&s"(t0), "=&s"(t1) : "s"((unsigned)tb64), "s"(X), "s"(E_S) : "scc");
                if (g == 22) asm volatile("s_cmp_gt_i32 %1, %3\n\ts_cselect_b32 %0, %2, %0" : "+s"(X) : "s"(t0), "s"(t1), "s"(E_T) : "scc");
                if (g == 23) asm volatile("v_or_b32 %0, 1, %1" : "=v"(xv) : "s"(X));
                if (g == 25) sst4(rx, fs, xw, xv);
                if (g == 52) CVT2(vz[0], vz[1], acc[2]);
                if (g == 53) *(u32x2*)(sV + 4096 + (w >> 1) * 1024 + (w & 1) * 8) = vz;
                SCHED0();
            });
        }
        asm volatile("s_nop 2");
        SCHED0();
        CVT2(vf[0][0], vf[0][1], acc[0]);
        SCHED0();
        asm volatile("s_nop 1");
        SCHED0();
        CVT2(vf[0][2], vf[0][3], acc[1]);
        *(u32x4*)(sV + w * 1024) = vf[0];
        SCHED0();
        asm volatile("s_nop 0");
        sfor<0, 67>([&](auto G) {
            constexpr int g = decltype(G)::value, m = g < 10 ? 0 : (g - 10) / 10, r = g < 10 ? 0 : (g - 10) % 10, kk = g < 10 ? 0 : 1 + r / 2, ct = g < 10 ? g : 2 * m + (r & 1);
            if constexpr (g < 60) {
                if constexpr (kk >= 4) MF_VV(d[ct], vf[kk], wl[2 * (kk & 1) + (r & 1)]);
                else if constexpr (ct == 0) { if (kk == 0) MF_VA0(d[0], vf[0], wo[0][0]); else MF_VA(d[0], vf[kk], wo[0][kk]); }
                else { if (kk == 0) MF_VV0(d[ct], vf[0], wo[ct][0]); else MF_VV(d[ct], vf[kk], wo[ct][kk]); }
            }
            SCHED0();
            if (g == 3) { LDS_BARRIER(); }
            if (g == 4) vf[1] = *(const u32x4*)sV1;
            if (g == 5) vf[2] = *(const u32x4*)sV2;
            if (g == 6) vf[3] = *(const u32x4*)sV3;
            if (g == 7) vf[4] = *(const u32x4*)(sV + 4096);
            if (g == 8) vf[5] = *(const u32x4*)(sV + 5120);
            if constexpr (g >= 17 && g < 51 && (g - 17) % 10 < 4) wl[(g - 17) % 10] = *(const bf16x8*)(sWo + ((g - 17) / 10 * 4 + 4 + (g - 17) % 10) * 1024);
            if (g == 12) LDA4(xv, fXv, fs);
            if constexpr (m > 0) {
                if (g == 60) asm volatile("s_nop 3");
                if (r == 1) CVT2(o[0], o[1], d[2 * m - 2]);
                SCHED0();
                if (g == 61) asm volatile("s_nop 1");
                if (r == 2) CVT2(o[2], o[3], d[2 * m - 1]);
                if (r == 3) { o[0] = nosent(o[0]); o[2] = nosent(o[2]); asm volatile("" : "+v"(o[0]), "+v"(o[2])); }
                if (r == 4) asm volatile("v_permlane16_swap_b32 %0, %1" : "+v"(o[1]), "+v"(o[3]));
                if (r == 5) asm volatile("v_permlane16_swap_b32 %0, %1" : "+v"(o[0]), "+v"(o[2]));
                if (r == 6) sst16(rx, us, upos[m - 1], o);
            }
            SCHED0();
        });
        if (E_V) rt_until(X + E_V);
#pragma unroll
        for (int m = 0; m < 5; ++m) { __builtin_amdgcn_s_sleep(VSLP); sst16(rx, ur, upos[m], u32x4{~0u, ~0u, ~0u, ~0u}); SCHED0(); }
        WAITV(10, xv);
        xv = __builtin_amdgcn_readfirstlane(xv);
        X = xv ? xv : X;
        cnt = adapt(mg, X | 1, xp, A_UP, A_MAX) ? 0 : cnt + 1;
        if (cnt == A_K) { mg = max(mg - 2, 0); cnt = 0; }
        PIN_S(mg); PIN_S(cnt);
        TIGHT(dx, it, D_1);
        mb = nb;
    }
    SPIN_END;
}
#define DBG_LD 0
#define WORK_IDS const int t = threadIdx.x, l = t & 63, l16 = l & 15, lq = l >> 4; const int w = __builtin_amdgcn_readfirstlane(t >> 6); \
    GO_DECL(id >> 5); const unsigned l16b = l * 16, dvo = w * 2048 + l * 16 + DBG_LD, lds0 = lds_addr(smem) + w * 2048; unsigned spins; SZERO(spins)
#define XSET(so, base, ld) asm volatile("s_mov_b32 m0, %2\n\ts_mov_b32 %0, %1" : "=s"(so) : "s"(base), "s"(ld) : "m0")
#define XADV(so) asm volatile("s_add_u32 m0, m0, 0x2000\n\ts_add_u32 %0, %0, 0x4000" : "+s"(so) :: "m0", "scc")
#define XDMA(so, rt) asm volatile("buffer_load_dwordx4 %0, %1, %2 offen offset:%3 sc1 lds" :: "v"(dvo), "s"(rx), "s"(so), "n"((rt) * 1024) : "memory")
#define XALL(so, base, ld) _Pragma("unroll") for (int e_ = 0; e_ < 10; ++e_) { if (e_ % 5 == 0) XSET(so, base, ld); else XADV(so); XDMA(so, e_ / 5); }
#define XALL2(so, base, ld) _Pragma("unroll") for (int e_ = 0; e_ < 5; ++e_) { if (e_ == 0) XSET(so, base, ld); else XADV(so); XDMA(so, 0); XDMA(so, 1); }
#define VMB(n) asm volatile("s_waitcnt vmcnt(%0)\n\ts_barrier" :: "n"(n) : "memory")
#define VMB_LATE(k) asm volatile("s_cmp_lg_u32 %0, %1\n\ts_cbranch_scc1 1f\n\ts_waitcnt vmcnt(%2)\n1:\n\ts_barrier" :: "s"(xs), "n"(OOB), "n"(k) : "memory", "scc")
#define VMW(n) asm volatile("s_waitcnt vmcnt(%0)" :: "n"(n) : "memory")
#define NEXT_DECL(asked) const unsigned fXo = l < 4 ? lp(SG_FL + FL_X + ((h & 12) + l) * 128) : OOB, fx1 = (((TR >> 2) & ~L & 1) ^ hside(h)) << 13; const int dl = __builtin_amdgcn_readfirstlane((L ^ 1) * TR), ld0 = __builtin_amdgcn_readfirstlane(L ? -K_W : K_0); \
    unsigned xb = seg_base(0, 0), xs, fb = seg_base(hside(h), 0), msk, so, pv, pend, fXv = xm(FX_OFF + ((h >> 2) & 3) * 64, hside(h)), xv, fs; PIN_V(fXv); if (L) wave_wait(rx, fXo, fb, spins); if (!(asked)) { XALL(so, xb, lds0); } VMB(5); int s; SZERO(s)
#define G_L1 150
#define G_E1 182
#define G_L2 210
#define NEXT_ADDR(a, st) \
    if (g == a) { s = __builtin_amdgcn_readfirstlane(s); PIN_S(s); xb = (s + 1) << 19; PIN_S(xb); } \
    if (g == a + st) { xs = (xb >> 8) & 8192; PIN_S(xs); } \
    if (g == a + 2 * st) { xb = (xb | xs) + 2 * SG_OFF; PIN_S(xb); } \
    if (g == a + 3 * st) { fb = (xb ^ fx1) - (dl << 19); PIN_S(fb); } \
    if (g == a + 5 * st) asm volatile("s_cmp_lt_i32 %1, %2\n\ts_cselect_b32 %0, 0, 15" : "=s"(msk) : "s"(s), "s"(dl - 1) : "scc"); \
    if (g == a + 6 * st) asm volatile("s_cmp_lt_i32 %1, 63\n\ts_cselect_b32 %0, %0, %2" : "+s"(fb) : "s"(s), "n"(OOB) : "scc"); \
    if (g == a + 7 * st) { PIN_S(s); fs = (s - ld0) << 8; PIN_S(fs); } \
    if (g == a + 8 * st) { fs += fs & ~8191u; PIN_S(fs); } \
    if (g == a + 9 * st) asm volatile("s_cmp_lt_i32 %0, 0\n\ts_cselect_b32 %0, %1, %0" : "+s"(fs) : "n"(OOB) : "scc")
#define NEXT_LOOK(n, ld) \
    if (g == G_L1 || g == G_L2) LDA4(pv, fXo, fb); \
    if (g == G_E1) { WAITV(n, pv); xs = (unsigned)__ballot(pv == 0); PIN_S(xs); } \
    if (g == G_E1 + 2) asm volatile("s_and_b32 %0, %0, %2\n\ts_cselect_b32 %0, %3, %1" : "+s"(xs) : "s"(xb), "s"(msk), "n"(OOB) : "scc")
#define NEXT_GO(n, ld, fl) \
    WAITV(n, pv); \
    if (xs != OOB) { asm volatile("s_barrier" ::: "memory"); prime(); SCHED0(); tail(); LDA4(xv, fXv, fs); WAITV(0, xv); sst4(rx, pend, fl, 1u); } \
    else { \
        tail(); \
        if ((unsigned)__ballot(pv == 0) & msk) { DRAIN(); sst4(rx, pend, fl, 1u); if (s == NSEG_ - 1) { SPIN_END; __builtin_amdgcn_endpgm(); } pend = OOB; wave_wait(rx, fXo, fb, spins); } \
        LDA4(xv, fXv, fs); \
        XALL(so, xb, ld); \
        WAITV(10, xv); sst4(rx, pend, fl, 1u); \
        VMB(6); prime(); \
    } \
    so = __builtin_amdgcn_readfirstlane(xv); \
    if (__builtin_expect(so == 0, 0) && fs != OOB) { \
        _Pragma("unroll 1") for (int k_ = 0; k_ < 8 && so == 0; ++k_) { __builtin_amdgcn_s_sleep(4); so = __builtin_amdgcn_readfirstlane(sld4(rx, fs, fXv)); MEMFENCE(); } \
    } \
    so += E_W; PIN_S(so); rt_until(so)

constexpr int QL_W = 81920;
__device__ __forceinline__ void qk_role(const MegaArgs& A, char* smem, const int L, const int h, const int id) {
    WORK_IDS;
    bf16x8 wf[6][20];
    char* sW = smem + QL_W + w * 20480 + l * 16;
    unsigned x0;
    load_qk<0, 11>(smem, w, l, go, A.Wq + h * DH_, A.Wk + h * DH_, [&](int m) { return 6 * w + m; }, wf, [&]() {
        const unsigned tag = (__builtin_amdgcn_readlane(go.v[0], 63) + 2) & ~1u, a0 = __builtin_amdgcn_readlane(go.v[0], HS_A / 8), a1 = __builtin_amdgcn_readlane(go.v[1], HS_A / 8);
        const rsrc_t rx = arena(A, a0 & 1);
        x0 = ((a0 ^ tag) | (a1 ^ tag) | 2 * L) > 1 ? OOB : seg_base(0, 0);
        PIN_S(x0);
        unsigned so;
        XALL(so, x0, lds0);
        GO_ASK;
    });
#pragma unroll
    for (int i = 0; i < 20; ++i) *(bf16x8*)(sW + i * 1024) = wf[5][i];
    GO_ON(N_1);
    const unsigned fI = l == 0 ? xm(FI_OFF + (h & 15) * 64 + (4 * L + w) * 4, hside(h)) : OOB;
    const unsigned po0 = 2 * QK_OFF + ((L * 16 + (h & 15)) * 4 + (w & 2)) * 16384 + (w & 1) * 3072 + hside(h) * 8192;
    const int it0 = L * A.lag;
    const unsigned l16s = l16b;
    NEXT_DECL(x0 != OOB);
    unsigned vx = l16b, ld = lds0, po;
    bf16x8 xw[3], wl[2];
    f32x4 acc[2][6];
    u32x4 o[2];
    auto prime = [&]() { xw[0] = *(const bf16x8*)(smem + vx); xw[1] = *(const bf16x8*)(smem + vx + 2048); };
    auto tail = [&]() {
#pragma unroll
        for (int f = 0; f < 3; ++f) { CVT2(o[f & 1][0], o[f & 1][1], acc[1][2 * f]); CVT2(o[f & 1][2], o[f & 1][3], acc[1][2 * f + 1]); sst16(rx, po, l16s + f * 1024, o[f & 1]); SCHED0(); }
    };
    prime();
    wl[0] = *(const bf16x8*)sW;
#pragma unroll 1
    for (;; ++s) {
        SCHED0();
        sfor<0, 240>([&](auto G) {
            constexpr int g = decltype(G)::value, i = g / 6, tt = g % 6, ks = i % 20;
            f32x4& d = acc[i / 20][tt];
            if constexpr (tt == 5) { if (ks == 0) MF_VV0(d, wl[i & 1], xw[i % 3]); else MF_VV(d, wl[i & 1], xw[i % 3]); }
            else if constexpr (20 * tt + ks < 64) { if (ks == 0) MF_AV0(d, wf[tt][ks], xw[i % 3]); else MF_AV(d, wf[tt][ks], xw[i % 3]); }
            else { if (ks == 0) MF_VV0(d, wf[tt][ks], xw[i % 3]); else MF_VV(d, wf[tt][ks], xw[i % 3]); }
            SCHED0();
            if constexpr (tt == 1 && i < 38) xw[(i + 2) % 3] = *(const bf16x8*)(smem + vx + (i + 2) % 20 * 2048 + (i + 2) / 20 * 1024);
            if constexpr (tt == 3) wl[~i & 1] = *(const bf16x8*)(sW + (i + 1) % 20 * 1024);
            NEXT_ADDR(2, 6);
            if (g == 4) { PIN_S(s); po = (s & (TR - 1)) << 21; PIN_S(po); }
            if (g == 10) { so = (po >> 8) & 8192; PIN_S(so); }
            if (g == 16) { po = (po0 ^ so) + po; PIN_S(po); }
            if (g == 58) { VMW(0); }
            if (g == 60) asm volatile("s_barrier" ::: "memory");
            if (g == 62) { PIN_S(s); pend = s + it0; so = pend & 7; PIN_S(pend); PIN_S(so); }
            if (g == 64) { pend = (pend >> 3) << 4; PIN_S(pend); }
            if (g == 66) { pend = (pend | so) << 10; PIN_S(pend); }
            if (g == 68) { ld = 2 * lds0 + 40960 - ld; PIN_S(ld); }
            if constexpr (g >= 122 && g < 140) {
                constexpr int f = (g - 122) / 6;
                if (tt == 2) CVT2(o[0][0], o[0][1], acc[0][2 * f]);
                if (tt == 4) CVT2(o[0][2], o[0][3], acc[0][2 * f + 1]);
                if (tt == 0) sst16(rx, po, l16s + f * 1024, o[0]);
            }
            if (g == 140) { po += 16384; PIN_S(po); }
            NEXT_LOOK(0, ld);
            {
                constexpr int a[10] = {186, 190, 194, 198, 202, 206, 214, 218, 222, 226};
#pragma unroll
                for (int e = 0; e < 10; ++e) {
                    if (g == a[e]) { if (e % 5 == 0) XSET(so, xs, ld); else XADV(so); }
                    if (g == a[e] + (e == 5 ? 6 : 2)) XDMA(so, e / 5);
                }
            }
            if (g == 236) { vx ^= 40960; PIN_V(vx); }
            SCHED0();
        });
        NEXT_GO(5, ld, fI);
    }
}

constexpr int OL_V = 40960, OL_W = OL_V + 12288;
__device__ __forceinline__ void vo_role(const MegaArgs& A, char* smem, const int L, const int h, const int id) {
    WORK_IDS;
    bf16x8 wf[3][20], wo[10][6];
    char* sW = smem + OL_W + w * 24576 + l * 16;
    {
        load_vo<0>(smem, w, l, go, A.Wv + h * DH_, A.Wo + (size_t)(h * DH_) * D_, wf, [&](int k, int ct, int) -> bf16x8& { return wo[ct][k]; });
#pragma unroll
        for (int i = 0; i < 24; ++i) *(bf16x8*)(sW + i * 1024) = wo[4 + i / 8 * 2 + (i & 1)][2 + (i >> 1 & 3)];
    }
    GO_ON(N_1);
    const unsigned fU = l == 0 ? lp(SG_FL + FL_U + h * 64 + w * 4) : OOB;
    const unsigned l16s = l16b;
    const unsigned ua = xm(UT_OFF + (320 * w + h) * 1024, L) + l16s, ub = ua ^ (w == 3 ? 8192 : 0);
    char* sV = smem + OL_V + l * 16;
    char* sVw = sV + w * 1024;
    char* sVh = sV + 4096 + (w >> 1) * 1024 + (w & 1) * 8;
    NEXT_DECL(false);
    unsigned uo;
    constexpr int XW = 4;
    bf16x8 xw[XW], wl[4];
    f32x4 d[4][2];
    u32x4 o[2];
    auto prime = [&]() {
#pragma unroll
        for (int i = 0; i < XW - 1; ++i) xw[i] = *(const bf16x8*)(smem + l16b + i * 2048);
    };
    auto tail = [&]() {
#pragma unroll
        for (int f = 0; f < 2; ++f) { CVT2(o[f][0], o[f][1], d[f][0]); CVT2(o[f][2], o[f][3], d[f][1]); sst16(rx, uo + f * 65536, ub, o[f]); SCHED0(); }
    };
    prime();
#pragma unroll 1
    for (;; ++s) {
        f32x4 acc[2][3];
        u32x4 vf[2][6];
        unsigned cur = xb;
        SCHED0();
        sfor<0, 240>([&](auto G) {
            constexpr int g = decltype(G)::value;
            constexpr int rt = g < 120 ? g / 60 : (g < 144 ? 0 : (g < 168 ? 1 : g & 1));
            constexpr int ct = g < 144 ? g & 3 : (g < 168 ? (g - 144) / 12 * 2 + (g & 1) : (g - 120) / 24 * 2 + (g >> 1 & 1));
            constexpr int kk = g < 144 ? (g + 24) % 24 / 4 : (g < 168 ? g % 12 / 2 : g % 24 / 4), wi = (g - 168) / 24 * 8 + (g % 24 - 8) / 2;
            if constexpr (g < 120) { if (g % 60 < 3) MF_AV0(acc[rt][g % 3], wf[g % 3][0], xw[g / 3 % XW]); else MF_AV(acc[rt][g % 3], wf[g % 3][g % 60 / 3], xw[g / 3 % XW]); }
            else if constexpr (ct >= 4 && kk >= 2) MF_VV(d[ct & 3][rt], vf[rt][kk], wl[wi & 3]);
            else if constexpr (ct == 0 && kk < 4) { if (kk == 0) MF_VA0(d[0][rt], vf[rt][0], wo[0][0]); else MF_VA(d[0][rt], vf[rt][kk], wo[0][kk]); }
            else { if (kk == 0) MF_VV0(d[ct & 3][rt], vf[rt][0], wo[ct][0]); else MF_VV(d[ct & 3][rt], vf[rt][kk], wo[ct][kk]); }
            SCHED0();
            if constexpr (g % 3 == 1 && g / 3 + XW - 1 < 40) { constexpr int i = g / 3 + XW - 1; xw[i % XW] = *(const bf16x8*)(smem + l16b + i % 20 * 2048 + i / 20 * 1024); }
            NEXT_ADDR(2, 3);
            if (g == 24) { PIN_S(s); uo = (s & (TR - 1)) << 22; PIN_S(uo); }
            if (g == 48) { VMW(0); }
            if (g == 50) asm volatile("s_barrier" ::: "memory");
            if (g == 51) { pend = cur ^ (L << 13); PIN_S(pend); }
            if constexpr (g >= 60 && g < 140 && g % 60 < 40) {
                constexpr int r = g / 60 - 1, q = r ? g - 120 : (g % 3 == 1 ? -1 : (g - 60) / 3 * 2 + g % 3 / 2);
                if (q == 1) CVT2(o[0][0], o[0][1], acc[r][0]);
                if (q == 2) CVT2(o[0][2], o[0][3], acc[r][1]);
                if (q == 3) CVT2(o[1][0], o[1][1], acc[r][2]);
                if (q == 4) *(u32x4*)(sVw + r * 6144) = o[0];
                if (q == 5) *(u32x2*)(sVh + r * 6144) = u32x2{o[1][0], o[1][1]};
                if (q == 11) asm volatile("s_waitcnt lgkmcnt(%0)\n\ts_barrier" :: "n"(r ? 0 : 3) : "memory");
                if constexpr (q >= 12 && q < 18) vf[r][q - 12] = *(const u32x4*)(sV + r * 6144 + (q - 12) * 1024);
            }
            if constexpr (g >= 168 && g % 24 < 18 && g % 24 != 8 && !(g & 1)) {
                constexpr int j = (g - 168) / 24 * 8 + g % 24 / 2 - (g % 24 > 8);
                wl[j & 3] = *(const bf16x8*)(sW + j * 1024);
            }
            if constexpr (g >= 156) {
                constexpr int p = g < 168 ? 0 : (g - 144) / 24, q = g < 168 ? g - 156 : g % 24, c0 = 2 * p & 3;
                if (q == 1) CVT2(o[0][0], o[0][1], d[c0][0]);
                if (q == 3) CVT2(o[0][2], o[0][3], d[c0][1]);
                if (q == 5) sst16(rx, uo, p ? ub : ua, o[0]);
                if (q == 7) CVT2(o[1][0], o[1][1], d[c0 + 1][0]);
                if (q == 8) { uo += 65536; PIN_S(uo); }
                if (q == 9) CVT2(o[1][2], o[1][3], d[c0 + 1][1]);
                if (q == 11) sst16(rx, uo, p ? ub : ua, o[1]);
            }
            if constexpr (g == 168 || g == 181 || g == 205 || g == 229) { uo += 65536; PIN_S(uo); }
            NEXT_LOOK(4, lds0);
            {
                constexpr int a[10] = {186, 189, 192, 200, 205, 209, 213, 216, 224, 231}, b[10] = {188, 191, 196, 204, 207, 212, 215, 220, 228, 233};
#pragma unroll
                for (int e = 0; e < 10; ++e) {
                    if (g == a[e]) { if (e % 5 == 0) XSET(so, xs, lds0); else XADV(so); }
                    if (g == b[e]) XDMA(so, e / 5);
                }
            }
            SCHED0();
        });
        NEXT_GO(7, lds0, fU);
    }
}

#define G_B 83
#define BSLP 6
constexpr int BL_Q = 40960, BL_P = BL_Q + 49152;
__device__ __forceinline__ void b_role(const MegaArgs& A, char* smem, const int L, const int hb, const int id) {
    WORK_IDS;
    const unsigned t0 = rtime();
    bf16x8 wf[4][20];
    unsigned dst[4];
    const int sw = (l16 & 6) << 3;
    {
        const void* const base[4] = {A.wqkv + 80 * hb, A.wqkv + 80 * hb, A.wqkv + 80 * hb, A.wqkv + 80 * hb};
        unsigned vo[4];
        int rp[4];
#pragma unroll
        for (int m = 0; m < 4; ++m) {
            const int idx = min((4 * m + w) * 64 + l, 959), c = idx % 30, T = min(4 * m + w, 14), e = 16 * (T % 5) + 4 * lq;
            vo[m] = (idx / 30) * 3840 + (c / 10) * (2 * D_) + (c % 10) * 16;
            rp[m] = (4 * lq + (l16 >> 2)) * 480 + 32 * T + (l & 3) * 8;
            dst[m] = lds_addr(smem) + BL_Q + (T / 5 * 4 + e / 20) * 4096 + l16 * 64 + ((e % 20 * 2) ^ sw);
        }
        while (rtime() - t0 < (L ? DLYB1 : DLYB0)) __builtin_amdgcn_s_sleep(8);
        load_coop<4, 0, 4>(smem, w, go, base, vo, 32 * 3840, rp, 7680, wf);
    }
    GO_ON(N_1);
    f32x4 cs[4];
    u32x4 one = {0x3f803f80u, 0x3f803f80u, 0x3f803f80u, 0x3f803f80u};
    {
        asm volatile("s_nop 1" : "+v"(one));
        sfor<0, 80>([&](auto G) { constexpr int g = decltype(G)::value, tt = g & 3, ks = g >> 2;
            if (ks == 0) MF_AV0(cs[tt], wf[tt][0], one); else if (tt < 3 || ks < 4) MF_AV(cs[tt], wf[tt][ks], one); else MF_VV(cs[tt], wf[tt][ks], one); });
        asm volatile("s_nop 7" : "+v"(cs[0]), "+v"(cs[1]), "+v"(cs[2]), "+v"(cs[3]));
    }
#pragma unroll
    for (int i = 0; i < 12; ++i) *(u32x4*)(smem + BL_Q + i * 4096 + t * 16) = u32x4{0u, 0u, 0u, 0u};
    const bool odd = l16 & 1;
    f32x4 wm[3];
#pragma unroll
    for (int e = 0; e < 8; ++e) { const int d = l16 + 16 - 16 * (e >> 2) - 4 * lq - (e & 3); wm[e >> 2][e & 3] = d >= 0 && d <= 14 && !(d & 1) ? 0.f : -INFINITY; }
    wm[2] = f32x4{-INFINITY, -INFINITY, -INFINITY, -INFINITY};
    const unsigned ps0 = odd ? 0x01000c0cu : 0x0c0c0100u, ps1 = odd ? 0x03020c0cu : 0x0c0c0302u;
    unsigned qa = lds_addr(smem) + BL_Q + w * 4096 + l16 * 64 + ((lq * 16) ^ sw), qb = qa ^ 2048, va[6];
    {
        const int row = 4 * lq + (l16 >> 2), vs = (row & 6) << 3;
        va[0] = va[2] = lds_addr(smem) + BL_Q + (8 + w) * 4096 + row * 64 + ((8 * (w & 1) + 8 * (l & 3)) ^ vs);
        va[1] = va[3] = lds_addr(smem) + BL_Q + (8 + w) * 4096 + row * 64 + ((32 * (~w & 1) + 8 * (l & 3)) ^ vs);
        va[4] = va[0] ^ 2048; va[5] = va[1] ^ 2048;
#pragma unroll
        for (int e = 0; e < 6; ++e) PIN_V(va[e]);
    }
    const int g0 = (20 * hb + 5 * w + (w & 1) + (lq & 2)) >> 1, g1 = (20 * hb + 5 * w + 4 * (~w & 1)) >> 1;
    const unsigned oo0 = lp(SG_OB + (g0 >> 2) * 2048 + (lq & 1) * 1024 + (g0 & 3) * 256 + l16 * 16);
    const unsigned oo1 = lq < 2 ? lp(SG_OB + (g1 >> 2) * 2048 + lq * 1024 + (g1 & 3) * 256 + l16 * 16 + (w & 1) * 8) : OOB;
    const unsigned fO = t < 4 ? lp(SG_FL + FL_O + (8 * t + hb) * 128) : OOB;
    const unsigned st0 = lp(SG_ST + (4 * (l >> 5) + w) * 256 + (l & 31) * 8), st2 = l < 32 ? st0 + 4096 : OOB;
    unsigned fXv = xm(FX_OFF + (hb >> 1) * 64, L), xv, X = rtime() - 2 * PER, xs = OOB;
    PIN_V(fXv);
    const float z2 = l < 32 ? 1.f : 0.f;
    f32v2* sP = (f32v2*)(smem + BL_P) + (l & 31);
    unsigned sK = lds_addr(smem) + BL_P + 1024, sKw = sK + 4 * w;
    PIN_V(sK); PIN_V(sKw);
    unsigned pend = OOB, sb = seg_base(L, 0), so, fs = fx_step(1 + L * A.lag);
    const int i0 = 1 + L * A.lag;
    for (int s = 0; s < NTOUCH; ++s) { sst16(rx, seg_base(L, s), oo0, u32x4{0u, 0u, 0u, 0u}); sst8(rx, seg_base(L, s), oo1, u32x2{0u, 0u}); }
    u32x2 st[3];
    auto look = [&](int e, unsigned b, unsigned f) { if (e == 0) LDA8(st[0], st0, b, 0); if (e == 1) LDA8(st[1], st0, b, 2048); if (e == 2) LDA8(st[2], st2, b, 0); if (e == 3) LDA4(xv, fXv, f); };
#define SEEN(n) asm volatile("s_waitcnt vmcnt(%4)" : "+v"(st[0]), "+v"(st[1]), "+v"(st[2]), "+v"(xv) : "n"(n) : "memory")
#define SWAP2(op, a, b) asm volatile(op " %0, %1" : "+v"(a), "+v"(b))
    int s;
    SZERO(s);
#pragma unroll 1
    do {
        if (xs == OOB) {
            bool e = (int)(rtime() - X - E_B) >= 0;
#pragma unroll
            for (int i = 0; i < 4; ++i) look(i, e ? sb : OOB, e ? fs : OOB);
            VMB(4);
            sst4(rx, pend, fO, 1u);
            pend = OOB;
            SEEN(1);
#pragma unroll 1
            for (;;) {
                xv = __builtin_amdgcn_readfirstlane(xv);
                X = xv ? xv : X;
                LIVE;
                if (e) { if (!BAD(umax3(st[0][0], st[1][0], st[2][0]))) break; __builtin_amdgcn_s_sleep(BSLP); }
                e = true;
                rt_until(X + E_B);
#pragma unroll
                for (int i = 0; i < 4; ++i) look(i, sb, fs);
                SEEN(0);
            }
            XSET(so, sb + lp(SG_X1), lds0); XDMA(so, 0); XDMA(so, 1); XADV(so); XDMA(so, 0); XDMA(so, 1);
        }
        VMB(2);
        f32x4 acc[4][2];
        unsigned cur;
        float rd[2];
        u32x2 y[2];
        bf16x8 kf[3], qf[2];
        u32x4 k4;
#define MUL2(x, i, r) do { asm volatile("v_mul_f32 %0, %1, %0" : "+v"(x[i]) : "v"(r)); x[i + 1] *= r; USE_V(x[i + 1]); } while (0)
#define FMA2(x, i, c, m) do { asm volatile("v_fma_f32 %0, -%2, %1, %0" : "+v"(x[i]) : "v"(c[i]), "v"(m)); x[i + 1] = __builtin_fmaf(-m, c[i + 1], x[i + 1]); USE_V(x[i + 1]); } while (0)
#define ADD1(d, x, z) asm volatile("v_add_f32 %0, %1, %2" : "=v"(d) : "v"(x), "v"(z))
        auto fin = [&](int e, int u) {
            if (u < 6 && u % 3 < 2) MUL2(acc[e][u / 3], 2 * (u % 3), rd[u / 3]);
            if (u % 3 == 2) CVT2(y[u / 3][0], y[u / 3][1], acc[e][u / 3]);
            if (u == 6) { LDS_AT(u32x2, dst[e]) = y[0]; LDS_AT(u32x2, dst[e] + 1024) = y[1]; dst[e] ^= 2048; USE_V(dst[e]); }
        };
        {
            bf16x8 xw[6][2];
#pragma unroll
            for (int e = 0; e < 4; ++e) xw[e >> 1][e & 1] = *(const bf16x8*)(smem + l16b + e * 1024);
            SCHED0();
            u64 now, bad;
            float a, b, c, d, mw, mb[2];
            unsigned lb, lf;
            f32v2 p0, p1, p2, p3;
            sfor<0, 160>([&](auto G) {
                constexpr int g = decltype(G)::value, ks = g < 112 ? g / 8 : (g < 124 ? 14 + (g - 112) / 6 : (g < 148 ? 16 + (g - 124) % 8 / 2 : 14 + (g - 148) / 2));
                constexpr int tt = g < 112 ? g % 8 / 2 : (g < 124 ? (g - 112) % 6 / 2 : (g < 148 ? (g - 124) / 8 : 3)), rt = g & 1;
                constexpr int q = g >= 112 || g % 8 == 1 || g % 8 == 3 || g % 8 == 7 ? -1 : g / 8 * 5 + (g % 8 < 4 ? g % 8 / 2 : g % 8 - 2);
                if (ks == 0) MF_AV0(acc[tt][rt], wf[tt][0], xw[0][rt]);
                else if (tt < 3 || ks < 4) MF_AV(acc[tt][rt], wf[tt][ks], xw[ks % 6][rt]); else MF_VV(acc[tt][rt], wf[tt][ks], xw[ks % 6][rt]);
                SCHED0();
                constexpr int rq = g < 112 ? (g % 8 == 1 || g % 8 == 3 ? 2 * ks + 4 + g % 8 / 2 : -1) : (g < 124 && g % 3 != 0 ? 32 + (g - 112) / 3 * 2 + (g - 112) % 3 : -1);
                if constexpr (rq >= 0) xw[rq / 2 % 6][rq & 1] = *(const bf16x8*)(smem + l16b + rq * 1024);
                if (q == 0 || q == 5 || q == 11) XADV(so);
                if (q == 1 || q == 6 || q == 13) XDMA(so, 0);
                if (q == 3 || q == 8 || q == 15) XDMA(so, 1);
                if (q == 10) VMB_LATE(4);
                if (q == 2) { ADD1(c, st[0][1], st[1][1]); a = __uint_as_float(st[0][0]) + __uint_as_float(st[1][0]); USE_V(a); }
                if (q == 4) { ADD1(b, a, st[2][0]); a += __uint_as_float(st[2][0]); USE_V(a); }
                if (q == 7) { SWAP2("v_permlane32_swap_b32", a, b); SCHED0(); c += __uint_as_float(st[2][1]); USE_V(c); }
                if (q == 9) { mw = (a + b) * 0.2f; USE_V(mw); }
                if (q == 12) { a = __uint_as_float(st[0][0]) - mw; b = __uint_as_float(st[1][0]) - mw; USE_V(a); USE_V(b); }
                if (q == 14) { d = (__uint_as_float(st[2][0]) - mw) * z2; USE_V(d); }
                if (q == 16) { a = __builtin_fmaf(b, b, a * a); USE_V(a); }
                if (q == 17) { c = __builtin_fmaf(32.f, __builtin_fmaf(d, d, a), c); USE_V(c); }
                if (q == 18) { b = c; PIN_V(b); }
                if (q == 19) SWAP2("v_permlane32_swap_b32", c, b);
                if (q == 21) { c += b; sP[w * 32] = f32v2{mw, c}; }
                if (q == 22) { cur = sb; sb = (s + 1) << 19; PIN_S(cur); PIN_S(sb); }
                if (q == 23) { lb = (sb >> 8) & 8192; PIN_S(lb); }
                if (q == 24) { sb = (sb | lb) + 2 * SG_OFF; PIN_S(sb); }
                if (q == 26) { sb ^= L << 13; X += PER + A_MAX / 2; PIN_S(sb); PIN_S(X); }
                if (q == 27) { fs += 256; lb = fs & 8192; PIN_S(fs); PIN_S(lb); }
                if (q == 28) { fs += lb; PIN_S(fs); }
                if (q == 34) { TIGHT(lb, s + i0, D_1); X -= lb; PIN_S(X); }
                if (q == 35) { asm volatile("s_cmp_eq_u32 %1, %2\n\ts_cselect_b32 %0, %3, 0" : "=s"(lb) : "s"(s + i0), "n"(NSEG_ + LAGV - 1), "s"(E_Z) : "scc"); X -= lb; PIN_S(X); }
                if (q == 30) asm volatile("s_waitcnt vmcnt(4) lgkmcnt(2)\n\ts_barrier" ::: "memory");
                if (q == 31) sst4(rx, pend, fO, 1u);
                if (q == 32) { p0 = sP[0]; p1 = sP[32]; }
                if (q == 33) { p2 = sP[64]; p3 = sP[96]; }
                if (q == 36) ADD1(a, p0[0], p1[0]);
                if (q == 37) { b = p2[0] + p3[0]; USE_V(b); }
                if (q == 38) { mw = (a + b) * 0.25f; USE_V(mw); }
                if (q == 39) { a = p0[0] - mw; PIN_V(a); b = p1[0] - mw; PIN_V(b); }
                if (q == 40) { c = p2[0] - mw; PIN_V(c); }
                if (q == 41) { mb[0] = rb(mw); PIN_V(mb[0]); }
                if (q == 42) { d = p3[0] - mw; PIN_V(d); mb[1] = mb[0]; PIN_V(mb[1]); }
                if (q == 43) { a = __builtin_fmaf(b, b, a * a); PIN_V(a); }
                if (q == 44) { SWAP2("v_permlane16_swap_b32", mb[0], mb[1]); ADD1(b, p0[1], p1[1]); }
                if (q == 45) { a = __builtin_fmaf(c, c, a); PIN_V(a); }
                if (q == 50) VMB(1);
                if (q == 55) { a = __builtin_fmaf(d, d, a); PIN_V(a); }
                if (g == 73) ADD1(c, p2[1], p3[1]);
                if (g == 75) ADD1(b, b, c);
                if (g == 89) { a = __builtin_fmaf(160.f, a, b); PIN_V(a); }
                if (g == 91) { a *= 1.0f / D_; PIN_V(a); }
                if (q == 60) asm volatile("v_cvt_pk_bf16_f32 %0, 0, %0" : "+v"(a));
                if (g == 97) { a += 1e-5f; PIN_V(a); }
                if (g == 99) asm volatile("v_cvt_pk_bf16_f32 %0, 0, %0" : "+v"(a));
                if (q == 65) { a = __builtin_amdgcn_sqrtf(a); PIN_V(a); }
                if (g == 105) asm volatile("v_cvt_pk_bf16_f32 %0, 0, %0" : "+v"(a));
                if (q == 66) { rd[0] = __builtin_amdgcn_rcpf(a); PIN_V(rd[0]); }
                if (q == 67) { rd[1] = rd[0]; PIN_V(rd[1]); }
                if (q == 69) SWAP2("v_permlane16_swap_b32", rd[0], rd[1]);
                if constexpr (g >= 72 && g < 104 && q >= 0 && g % 8) {
                    constexpr int e = (g % 8 < 5 ? g % 8 + 2 : 2 * (g % 8) - 10) + (g >= 88), i = g >> 2 & 2;
                    FMA2(acc[e >> 1][e & 1], i, cs[e >> 1], mb[e & 1]);
                }
                if (g == G_B) asm volatile("s_memrealtime %0" : "=s"(now));
                if (g == G_B + 4) asm volatile("s_waitcnt lgkmcnt(0)" : "+s"(now));
                if (g == G_B + 12) { lf = (unsigned)now - X; PIN_S(lf); }
                if (g == G_B + 20) { lf -= E_B; PIN_S(lf); }
                if (g == G_B + 26) asm volatile("s_cmp_lt_i32 %1, 63\n\ts_cselect_b32 %0, %0, -1" : "+s"(lf) : "s"(s) : "scc");
                if (g == G_B + 28) asm volatile("s_cmp_ge_i32 %1, 0\n\ts_cselect_b32 %0, %2, %4\n\ts_cselect_b32 %1, %3, %4" : "=&s"(lb), "+s"(lf) : "s"(sb), "s"(fs), "n"(OOB) : "scc");
                if constexpr (g >= G_B + 29 && g < G_B + 33) look(g - G_B - 29, lb, lf);
                if constexpr (g >= 132 && g < 155 && (g - 132) % 8 < 7) fin((g - 132) / 8, (g - 132) % 8);
                if (g == 155) LDS_BARRIER();
                if (g == 156) { kf[0] = LDS_AT(bf16x8, qb + 17408); qf[0] = LDS_AT(bf16x8, qa); }
                if (g == 157) { kf[1] = LDS_AT(bf16x8, qa + 16384); qf[1] = LDS_AT(bf16x8, qa + 1024); }
                if (g == 158) { kf[2] = LDS_AT(bf16x8, qa + 17408); SEEN(0); }
                if (g == 159) { lf = umax3(st[0][0], st[1][0], st[2][0]); bad = __ballot(lf == ~0u); PIN_S(bad); }
                SCHED0();
            });
            xv = __builtin_amdgcn_readfirstlane(xv);
            asm volatile("s_cmp_eq_u64 %1, 0\n\ts_cselect_b32 %0, %0, %2" : "+s"(lb) : "s"(bad), "n"(OOB) : "scc");
            LDS_AT(unsigned, sKw) = lb;
            X = xv ? xv : X;
            PIN_S(X);
        }
        {
            constexpr float SC = 0.22360679774997896f * 1.4426950408889634f;
            bf16x8 vx[2][2];
            f32x4 sc[4];
            SCHED0();
            fin(3, 0); fin(3, 1);
            SCHED0();
            MF_VVC(sc[0], kf[0], qf[0], wm[2]); SCHED0(); fin(3, 2); SCHED0();
            MF_VVC(sc[1], kf[1], qf[0], wm[1]); SCHED0(); fin(3, 3); SCHED0();
            MF_VVC(sc[2], kf[1], qf[1], wm[0]); SCHED0(); fin(3, 4); SCHED0();
            MF_VVC(sc[3], kf[2], qf[1], wm[1]); SCHED0(); fin(3, 5); fin(3, 6);
            SCHED0();
            float sv[4][2];
            float m0, m1;
#define MAXQ(m, x, y, p, q) asm volatile("v_max_f32 %1, %5, %6\n\tv_max_f32 %2, %7, %8\n\tv_max_f32 %3, %9, %10\n\tv_max_f32 %4, %11, %12\n\tv_max3_f32 %0, %1, %2, %3\n\tv_max_f32 %0, %0, %4" \
    : "=&v"(m), "=&v"(x[0]), "=&v"(x[1]), "=&v"(y[0]), "=&v"(y[1]) : "v"(p[0]), "v"(p[1]), "v"(p[2]), "v"(p[3]), "v"(q[0]), "v"(q[1]), "v"(q[2]), "v"(q[3]))
            MAXQ(m0, sv[0], sv[1], sc[0], sc[1]);
            MAXQ(m1, sv[2], sv[3], sc[2], sc[3]);
            LDS_BARRIER();
            k4 = LDS_AT(u32x4, sK);
            SCHED0();
            auto tr = [](unsigned a) { return __builtin_amdgcn_ds_read_tr16_b64_v4i16((lds_b4*)a); };
#pragma unroll
            for (int dt = 0; dt < 2; ++dt) { vx[0][dt] = cat8(tr(va[4 + dt] + 1024), tr(va[dt])); vx[1][dt] = cat8(tr(va[2 + dt]), tr(va[dt] + 1024)); }
            SCHED0();
            red_max_q2(m0, m1);
            m0 *= -SC; m1 *= -SC;
            USE_V(m0); USE_V(m1);
            SCHED0();
            xs = __builtin_amdgcn_readfirstlane(max(umax3(k4[0], k4[1], k4[2]), k4[3]));
            u32x4 pt[2];
            f32x4 o[2][2], sm[2];
#pragma unroll
            for (int qt = 0; qt < 2; ++qt) {
#pragma unroll
                for (int e = 0; e < 4; ++e) sv[2 * qt + (e >> 1)][e & 1] = __builtin_amdgcn_exp2f(__builtin_fmaf(sv[2 * qt + (e >> 1)][e & 1], SC, qt ? m1 : m0));
                if (!qt) XSET(so, xs + lp(SG_X1), lds0);
#pragma unroll
                for (int e = 0; e < 2; ++e) {
                    const unsigned p = pk2(sv[2 * qt + e][0], sv[2 * qt + e][1]);
                    pt[qt][2 * e] = __builtin_amdgcn_perm(p, p, ps0);
                    pt[qt][2 * e + 1] = __builtin_amdgcn_perm(p, p, ps1);
                }
                USE_V(pt[qt]);
                SCHED0();
                XDMA(so, qt);
                asm volatile("s_nop 0");
                MF_VV0(sm[qt], one, pt[qt]);
                if (!qt) MF_VV0(o[0][0], vx[0][0], pt[0]);
                SCHED0();
            }
            XADV(so);
            MF_VV0(o[1][0], vx[1][0], pt[1]);
            PIN_V(sm[0]);
            const float s0 = __builtin_amdgcn_rcpf(sm[0][0]);
            USE_V(s0);
            XDMA(so, 0);
            MF_VV0(o[0][1], vx[0][1], pt[0]);
            SCHED0();
            PIN_V(o[0][0]);
            MUL2(o[0][0], 0, s0);
            SCHED0();
            MF_VV0(o[1][1], vx[1][1], pt[1]);
            SCHED0();
            PIN_V(sm[1]);
            const float s1 = __builtin_amdgcn_rcpf(sm[1][0]);
            USE_V(s1);
            MUL2(o[0][0], 2, s0);
            XDMA(so, 1);
            PIN_V(o[1][0]);
            MUL2(o[1][0], 0, s1); MUL2(o[1][0], 2, s1);
            {
                const auto a = __builtin_amdgcn_permlane16_swap(pk2(o[0][0][0], o[0][0][1]), pk2(o[1][0][0], o[1][0][1]), false, false);
                const auto b = __builtin_amdgcn_permlane16_swap(pk2(o[0][0][2], o[0][0][3]), pk2(o[1][0][2], o[1][0][3]), false, false);
                sst16(rx, cur, oo0, u32x4{a[0], b[0], a[1], b[1]});
            }
            SCHED0();
            asm volatile("" : "+v"(o[0][1]), "+v"(o[1][1]));
            o[0][1] *= s0; o[1][1] *= s1;
            {
                const auto a = __builtin_amdgcn_permlane16_swap(pk2(o[0][1][0], o[0][1][1]), pk2(o[1][1][0], o[1][1][1]), false, false);
                const auto b = __builtin_amdgcn_permlane16_swap(pk2(o[0][1][2], o[0][1][3]), pk2(o[1][1][2], o[1][1][3]), false, false);
                sst8(rx, cur, oo1, u32x2{a[0], b[0]});
            }
            SCHED0();
            wm[2] = wm[0];
            qa ^= 2048; qb ^= 2048;
            PIN_V(wm[2]); PIN_V(qa); PIN_V(qb);
#pragma unroll
            for (int e = 0; e < 6; ++e) { va[e] ^= 2048; PIN_V(va[e]); }
        }
        pend = cur;
    } while (++s < NSEG_);
    VMB(0);
    sst4(rx, pend, fO, 1u);
    SPIN_END;
}

__device__ __forceinline__ void bo_role(const MegaArgs& A, char* smem, const int L, const int j, const int id) {
    WORK_IDS;
    const unsigned t0 = rtime();
    const int rt = w & 1, hf = w >> 1;
    if (id == 31 && w == 0) probe(A, go, l, id);
    INIT_BEGIN(id >> 5);
    if (L) { INIT_END(56 + j); }
    bf16x8 wf[5][20];
    {
        const void* const base[3] = {A.wo + 160 * j, A.wo + 160 * j, A.wo + 160 * j};
        unsigned vo[3];
        int rp[5];
#pragma unroll
        for (int m = 0; m < 3; ++m) { const int idx = min((4 * m + w) * 64 + l, 639); vo[m] = (idx / 20) * (2 * D_) + (idx % 20) * 16; }
#pragma unroll
        for (int m = 0; m < 5; ++m) { rp[m] = (8 * lq + (l16 >> 2)) * 320 + 32 * (m < 4 ? 4 * hf + m : 8 + hf) + (l & 3) * 8; }
        while (rtime() - t0 < (L ? DLYB1 : DLYB0)) __builtin_amdgcn_s_sleep(8);
        load_coop<3, 0, 5>(smem, w, go, base, vo, 64 * D_, rp, 1280, wf);
    }
    INIT_SEEN;
    const unsigned x0 = lp((5 * j + 2 * hf) * 2048 + rt * 1024 + l16b), x1 = lp((5 * j + 2 * hf + 1) * 2048 + rt * 1024 + l16b), x2 = lp((5 * j + 4) * 2048 + rt * 1024 + l16b + hf * 8);
    const unsigned fO = l < 8 ? lp(SG_FL + FL_O + (8 * j + l) * 128) : OOB, fX = t < 8 ? lp(SG_FL + FL_X + (4 * (t & 3) + j) * 128) + (t & 4) * 2048 : OOB;
    const unsigned sr = lp(SG_ST + ST_2 + rt * 1024 + lq * 256 + l16 * 8), sw = l < 16 ? sr + j * 256 + hf * 128 : OOB;
    auto flag = [&](unsigned b) { sst4(rx, b & ~8192u, fX ^ (b & 8192u), 1u); };
    const rsrc_t ro = mk_rsrc(A.out, S_ * D_ * 2);
    const unsigned oo = ((16 * rt + l16) * D_ + 160 * j + 64 * hf + 4 * lq) * 2, oo2 = oo + (128 - 48 * hf) * 2;
    const char* sX = smem + rt * 1024 + l16b;
    unsigned pend = OOB, sb = seg_base(L, 0), so, xs = OOB;
    unsigned* sK = (unsigned*)(smem + 40960);
    auto first = [&](unsigned b) { XSET(so, b + lp(SG_OB), lds0); XDMA(so, 0); XDMA(so, 1); XADV(so); XDMA(so, 0); XDMA(so, 1); XADV(so); XDMA(so, 0); XDMA(so, 1); };
    int s;
    SZERO(s);
#pragma unroll 1
    do {
        if (xs == OOB) {
            unsigned pv;
            LDA4(pv, fO, sb);
            VMB(1);
            flag(pend);
            pend = OOB;
            WAITV(1, pv);
            if (__ballot(pv == 0) & 255) wave_wait(rx, fO, sb, spins);
            first(sb);
            VMB(4);
        } else VMB(5);
        f32x4 acc[5], xf[5];
        u32x4 r0, r1, k4;
        u32x2 r2;
        unsigned cur;
        {
            bf16x8 xw[3];
            unsigned sx, pv;
            xw[0] = *(const bf16x8*)sX; xw[1] = *(const bf16x8*)(sX + 2048);
            SCHED0();
            sfor<0, 100>([&](auto G) {
                constexpr int g = decltype(G)::value, ks = g / 5, tt = g % 5;
                if constexpr (20 * tt + ks < 64) { if (ks == 0) MF_AV0(acc[tt], wf[tt][0], xw[0]); else MF_AV(acc[tt], wf[tt][ks], xw[ks % 3]); }
                else { if (ks == 0) MF_VV0(acc[tt], wf[tt][0], xw[0]); else MF_VV(acc[tt], wf[tt][ks], xw[ks % 3]); }
                SCHED0();
                if (g == 0 || g == 7) XADV(so);
                if (g == 2 || g == 12) XDMA(so, 0);
                if (g == 5 || g == 15) XDMA(so, 1);
                if (g == 3) { cur = sb; sx = sb + lp(SG_X1); PIN_S(cur); PIN_S(sx); }
                if (g == 10) VMB_LATE(4);
                if (g == 18) LDA(r0, x0, sx, 0);
                if (g == 22) LDA(r1, x1, sx, 0);
                if (g == 25) LDA8(r2, x2, sx, 0);
                if (g == 30) VMB(7);
                if (g == 32) flag(pend);
                if (g == 35) { sb = (s + 1) << 19; PIN_S(sb); }
                if (g == 37) { sx = (sb >> 8) & 8192; PIN_S(sx); }
                if (g == 38) { sb = (sb | sx) + 2 * SG_OFF; PIN_S(sb); }
                if (g == 40) { sb ^= L << 13; PIN_S(sb); }
                if (g == 42) asm volatile("s_cmp_lt_i32 %1, 63\n\ts_cselect_b32 %0, %0, %2" : "+s"(sb) : "s"(s), "n"(OOB) : "scc");
                if (g == 50) VMB(4);
                if constexpr (tt == 1 && ks < 18) xw[(ks + 2) % 3] = *(const bf16x8*)(sX + (ks + 2) * 2048);
                if (g == 60) LDA4(pv, fO, sb);
                if (g == 63) asm volatile("s_waitcnt vmcnt(2)" : "+v"(r0), "+v"(r1), "+v"(r2) :: "memory");
                if constexpr (g >= 65 && g < 90 && (tt == 2 || tt == 3)) {
                    constexpr int e = ks - 13, k = tt - 2;
                    const unsigned u = e < 2 ? r0[2 * (e & 1) + k] : (e < 4 ? r1[2 * (e & 1) + k] : r2[k]);
                    xf[e][2 * k] = lo16(u); xf[e][2 * k + 1] = hi16(u);
                    asm volatile("" : "+v"(xf[e][2 * k]), "+v"(xf[e][2 * k + 1]));
                }
                if (g == 90) WAITV(0, pv);
                if (g == 91) { sx = (unsigned)__ballot(pv == 0); PIN_S(sx); }
                if (g == 92) asm volatile("s_and_b32 %0, %0, 255\n\ts_cselect_b32 %0, %2, %1" : "+s"(sx) : "s"(sb), "n"(OOB) : "scc");
                if (g == 93) sK[w] = sx;
                if (g == 97) LDS_BARRIER();
                if (g == 98) k4 = *(const u32x4*)sK;
                SCHED0();
            });
        }
#pragma unroll
        for (int e = 0; e < 5; ++e) { PIN_V(acc[e]); acc[e] += xf[e]; PIN_V(acc[e]); SCHED0(); }
        xs = __builtin_amdgcn_readfirstlane(max(umax3(k4[0], k4[1], k4[2]), k4[3]));
        if (L) {
            if (xs != OOB) first(xs);
            const unsigned so2 = s * (SEG_ * D_ * 2);
#pragma unroll
            for (int e = 0; e < 5; ++e) __builtin_amdgcn_raw_buffer_store_b64(u32x2{pk2(acc[e][0], acc[e][1]), pk2(acc[e][2], acc[e][3])}, ro, e < 4 ? oo + 32 * e : oo2, so2, OUTAUX);
        } else {
            f32x4 a = ((acc[0] + acc[1]) + (acc[2] + acc[3])) + acc[4];
            const float mw = red_sum_q((a[0] + a[1]) + (a[2] + a[3])) * (1.0f / 80);
#pragma unroll
            for (int e = 0; e < 5; ++e) { xf[e] = acc[e]; acc[e] -= mw; }
            a = acc[0] * acc[0];
#pragma unroll
            for (int e = 1; e < 5; ++e) a = __builtin_elementwise_fma(acc[e], acc[e], a);
            sst8(rx, cur, sw, u32x2{nosent(__float_as_uint(mw)), __float_as_uint(red_sum_q((a[0] + a[1]) + (a[2] + a[3])))});
            SCHED0();
            if (xs != OOB) first(xs);
            r0 = pk8(xf[0], xf[1]); r1 = pk8(xf[2], xf[3]); r2 = u32x2{pk2(xf[4][0], xf[4][1]), pk2(xf[4][2], xf[4][3])};
            const unsigned so2 = cur + lp(SG_XR);
            sst16(rx, so2, x0, r0); sst16(rx, so2, x1, r1); sst8(rx, so2, x2, r2);
            VMW(3);
            u32x2 p, q;
#pragma unroll 1
            for (;;) {
                LDA8(p, sr, cur, 0); LDA8(q, sr, cur, 128);
                LIVE;
                asm volatile("s_waitcnt vmcnt(0)" : "+v"(p), "+v"(q) :: "memory");
                if (!BAD(max(p[0], q[0]))) break;
            }
            const float m0 = __uint_as_float(p[0]), m1 = __uint_as_float(q[0]), mean = red_sum_q(m0 + m1) * 0.125f, e0 = m0 - mean, e1 = m1 - mean;
            const float var = red_sum_q((__uint_as_float(p[1]) + __uint_as_float(q[1])) + 80.f * (e0 * e0 + e1 * e1)) * (1.0f / D_);
            const float rd = __builtin_amdgcn_rcpf(rb(__builtin_amdgcn_sqrtf(rb(rb(var) + 1e-5f)))), c = (mw - rb(mean)) * rd;
#pragma unroll
            for (int e = 0; e < 5; ++e) acc[e] = acc[e] * rd + c;
            sst16(rx, cur, x0, pk8(acc[0], acc[1])); sst16(rx, cur, x1, pk8(acc[2], acc[3])); sst8(rx, cur, x2, u32x2{pk2(acc[4][0], acc[4][1]), pk2(acc[4][2], acc[4][3])});
            pend = cur;
        }
    } while (++s < NSEG_);
    if (id == 159) bst4(go.rh, t < 8 ? t * 1024 + HS_E : OOB, tags);
    VMB(0);
    flag(pend);
    SPIN_END;
}

#define CT0 1
#define CT1 0
constexpr unsigned CODE_SZ = 100 * 1024, CODE_BK = 0;
constexpr int MEGA_LDS = 163840;
__global__ void __launch_bounds__(256, 1) mega_kernel(MegaArgs A) {
    extern __shared__ __attribute__((aligned(16))) char smem[];
    int role = 32 * (blockIdx.x & 7) + (blockIdx.x >> 3);
    PIN_S(role);
    const int pool = role >> 5, k = role & 31;
    {
        const unsigned t0 = rtime();
        if (k >= 8 && k < 13) {
            const char* pc;
            asm volatile("s_getpc_b64 %0" : "=s"(pc));
            auto touch = [&]() { const unsigned x = __builtin_amdgcn_raw_buffer_load_b32(mk_rsrc(pc, CODE_SZ), ((k - 8) * 256 + threadIdx.x) * 128 - CODE_BK, 0, 0); asm volatile("" :: "v"(x)); };
            if (CT0) touch();
            init_area(A, 5 * pool + k - 8, role, t0);
            if (CT1) { while (rtime() - t0 < CT1) __builtin_amdgcn_s_sleep(8); touch(); }
        }
        if (k >= 17 && k < 29 && ((k - 13) & 4)) { first_rows(A, smem, 8 * ((k - 17) / 8 * 4 + ((k - 17) & 3)) + pool, role, t0); while (rtime() - t0 < DLY2) __builtin_amdgcn_s_sleep(8); }
    }
    if (k < 13) {
        if (!(A.roles & 1)) return;
        if (k < 4) s_role(A, smem, 4 * pool + k, role);
        else if (k < 8) vm_role(A, smem, 4 * pool + k - 4, role);
        else c_role(A, smem, pool, k - 8, role);
        return;
    }
    if (k < 21) { if (A.roles & 2) qk_role(A, smem, (k - 13) >> 2, 4 * pool + ((k - 13) & 3), role); }
    else if (k < 29) { if (A.roles & 4) vo_role(A, smem, (k - 21) >> 2, 4 * pool + ((k - 21) & 3), role); }
    else if (k < 31) { if (A.roles & 8) b_role(A, smem, pool >> 2, 2 * (pool & 3) + k - 29, role); }
    else if (A.roles & 16) bo_role(A, smem, pool >> 2, pool & 3, role);
}

#define ROLES 31
extern "C" void kernel_launch(void* const* d_in, const int* in_sizes, int n_in,
                              void* d_out, int out_size, void* d_ws, size_t ws_size,
                              hipStream_t stream) {
    MegaArgs A;
    char* ws = (char*)d_ws;
    A.hs = ws; ws += 262144;
    A.tmo = (unsigned*)ws; ws += 4096;
    A.ar = ws;
    A.x0 = (const bf16_t*)d_in[0]; A.mem0 = (const bf16_t*)d_in[1]; A.Wq = (const bf16_t*)d_in[2]; A.Wk = (const bf16_t*)d_in[3]; A.Wv = (const bf16_t*)d_in[4];
    A.Wo = (const bf16_t*)d_in[5]; A.wqkv = (const bf16_t*)d_in[8]; A.wo = (const bf16_t*)d_in[9];
    A.out = (bf16_t*)d_out; A.pad = 0;
    A.lag = LAGV; A.nit = NSEG_ + LAGV; A.roles = ROLES;

    (void)hipFuncSetAttribute((const void*)mega_kernel, hipFuncAttributeMaxDynamicSharedMemorySize, MEGA_LDS);

    mega_kernel<<<256, 256, MEGA_LDS, stream>>>(A);
}
```

```cpp
#define A_MAX 8
#include <hip/hip_runtime.h>
#include <hip/hip_bf16.h>
#include <stdint.h>

typedef unsigned short bf16_t;
typedef short bf16x8 __attribute__((ext_vector_type(8)));
typedef short bf16x4 __attribute__((ext_vector_type(4)));
typedef float f32x4 __attribute__((ext_vector_type(4)));
typedef unsigned u32x4 __attribute__((ext_vector_type(4)));
typedef unsigned u32x2 __attribute__((ext_vector_type(2)));
typedef unsigned long long u64;
typedef __bf16 bf16v2 __attribute__((ext_vector_type(2)));
typedef float f32v2 __attribute__((ext_vector_type(2)));

__device__ __forceinline__ float bf2f(bf16_t v) { return __uint_as_float(((unsigned)v) << 16); }
__device__ __forceinline__ unsigned pk2(float a, float b) {
    f32v2 f = {a, b};
    bf16v2 r = __builtin_convertvector(f, bf16v2);
    return __builtin_bit_cast(unsigned, r);
}
__device__ __forceinline__ float rb(float a) { return __uint_as_float((pk2(a, 0.f) & 0xffffu) << 16); }
__device__ __forceinline__ float lo16(unsigned u) { return __uint_as_float(u << 16); }
__device__ __forceinline__ float hi16(unsigned u) { return __uint_as_float(u & 0xffff0000u); }
__device__ __forceinline__ bf16x4 pk4(f32x4 v) {
    u32x2 u = {pk2(v[0], v[1]), pk2(v[2], v[3])};
    return __builtin_bit_cast(bf16x4, u);
}
__device__ __forceinline__ bf16x8 cat8(bf16x4 a, bf16x4 b) { return bf16x8{a[0], a[1], a[2], a[3], b[0], b[1], b[2], b[3]}; }
__device__ __forceinline__ bf16x8 asb8(u32x4 v) { return __builtin_bit_cast(bf16x8, v); }
__device__ __forceinline__ f32x4 asf4(u32x4 v) { return __builtin_bit_cast(f32x4, v); }
__device__ __forceinline__ f32x4 mfma32(bf16x8 a, bf16x8 b, f32x4 c) { return __builtin_amdgcn_mfma_f32_16x16x32_bf16(a, b, c, 0, 0, 0); }
__device__ __forceinline__ f32x4 mfma16(bf16x4 a, bf16x4 b, f32x4 c) { return __builtin_amdgcn_mfma_f32_16x16x16bf16_1k(a, b, c, 0, 0, 0); }

#define MF_(acc, ca, a, cb, b) asm volatile("v_mfma_f32_16x16x32_bf16 %0, %1, %2, %0" : "+v"(acc) : ca(a), cb(b))
#define MF0_(acc, ca, a, cb, b) asm volatile("v_mfma_f32_16x16x32_bf16 %0, %1, %2, 0" : "=&v"(acc) : ca(a), cb(b))
#define MF_AV(acc, a, b) MF_(acc, "a", a, "v", b)
#define MF_VA(acc, a, b) MF_(acc, "v", a, "a", b)
#define MF_VV(acc, a, b) MF_(acc, "v", a, "v", b)
#define MF_AV0(acc, a, b) MF0_(acc, "a", a, "v", b)
#define MF_VA0(acc, a, b) MF0_(acc, "v", a, "a", b)
#define MF_VV0(acc, a, b) MF0_(acc, "v", a, "v", b)
#define MF_VVC(d, a, b, c) asm volatile("v_mfma_f32_16x16x32_bf16 %0, %1, %2, %3" : "=&v"(d) : "v"(a), "v"(b), "v"(c))
#define MH_(acc, ca, a, cb, b) asm volatile("v_mfma_f32_16x16x16_bf16 %0, %1, %2, %0" : "+v"(acc) : ca(a), cb(b))
#define MH0_(acc, ca, a, cb, b) asm volatile("v_mfma_f32_16x16x16_bf16 %0, %1, %2, 0" : "=&v"(acc) : ca(a), cb(b))

constexpr int S_ = 2048, D_ = 640, H_ = 32, DH_ = 192, M_ = 8, SEG_ = 32, NSEG_ = 64;
constexpr int HD_ = H_ * DH_;
#define RLX_AGENT __ATOMIC_RELAXED, __HIP_MEMORY_SCOPE_AGENT
#define AUX_SC1 16
typedef __amdgpu_buffer_rsrc_t rsrc_t;
__device__ __forceinline__ rsrc_t mk_rsrc(const void* p, unsigned bytes) { return __builtin_amdgcn_make_buffer_rsrc((void*)p, 0, (int)bytes, 0x00020000); }
__device__ __forceinline__ u32x4 bld16(rsrc_t r, unsigned off) { return __builtin_amdgcn_raw_buffer_load_b128(r, off, 0, AUX_SC1); }
__device__ __forceinline__ u32x2 bld8(rsrc_t r, unsigned off) { return __builtin_amdgcn_raw_buffer_load_b64(r, off, 0, AUX_SC1); }
__device__ __forceinline__ unsigned bld4(rsrc_t r, unsigned off) { return __builtin_amdgcn_raw_buffer_load_b32(r, off, 0, AUX_SC1); }
__device__ __forceinline__ void bst16(rsrc_t r, unsigned off, u32x4 v) { __builtin_amdgcn_raw_buffer_store_b128(v, r, off, 0, AUX_SC1); }
__device__ __forceinline__ void bst8(rsrc_t r, unsigned off, u32x2 v) { __builtin_amdgcn_raw_buffer_store_b64(v, r, off, 0, AUX_SC1); }
__device__ __forceinline__ void bst4(rsrc_t r, unsigned off, unsigned v) { __builtin_amdgcn_raw_buffer_store_b32(v, r, off, 0, AUX_SC1); }
#define LDS_BARRIER() asm volatile("s_waitcnt lgkmcnt(0)\n\ts_barrier" ::: "memory")
#define DRAIN() asm volatile("s_waitcnt vmcnt(0)" ::: "memory")
#define MEMFENCE() asm volatile("" ::: "memory")
#define SCHED0() __builtin_amdgcn_sched_barrier(0)
__device__ __forceinline__ float red_max_q(float x) {
    auto a = __builtin_amdgcn_permlane16_swap(__float_as_uint(x), __float_as_uint(x), false, false);
    x = fmaxf(__uint_as_float(a[0]), __uint_as_float(a[1]));
    auto c = __builtin_amdgcn_permlane32_swap(__float_as_uint(x), __float_as_uint(x), false, false);
    return fmaxf(__uint_as_float(c[0]), __uint_as_float(c[1]));
}
__device__ __forceinline__ float max3r(float a, float b, float c) { float r; asm("v_max3_f32 %0, %1, %2, %3" : "=v"(r) : "v"(a), "v"(b), "v"(c)); return r; }
__device__ __forceinline__ float max2r(float a, float b) { float r; asm("v_max_f32 %0, %1, %2" : "=v"(r) : "v"(a), "v"(b)); return r; }
__device__ __forceinline__ void red_max_q2(float& x, float& y) {
    float s, t;
    asm("v_mov_b32 %2, %0\n\tv_mov_b32 %3, %1\n\ts_nop 0\n\tv_permlane16_swap_b32 %0, %2\n\tv_permlane16_swap_b32 %1, %3\n\tv_max_f32 %0, %0, %2\n\tv_max_f32 %1, %1, %3\n\t"
        "v_mov_b32 %2, %0\n\tv_mov_b32 %3, %1\n\ts_nop 0\n\tv_permlane32_swap_b32 %0, %2\n\tv_permlane32_swap_b32 %1, %3\n\tv_max_f32 %0, %0, %2\n\tv_max_f32 %1, %1, %3"
        : "+v"(x), "+v"(y), "=&v"(s), "=&v"(t));
}
__device__ __forceinline__ void red_sum_q2(float& x, float& y) {
    float s, t;
    asm("v_mov_b32 %2, %0\n\tv_mov_b32 %3, %1\n\ts_nop 0\n\tv_permlane16_swap_b32 %0, %2\n\tv_permlane16_swap_b32 %1, %3\n\tv_add_f32 %0, %0, %2\n\tv_add_f32 %1, %1, %3\n\t"
        "v_mov_b32 %2, %0\n\tv_mov_b32 %3, %1\n\ts_nop 0\n\tv_permlane32_swap_b32 %0, %2\n\tv_permlane32_swap_b32 %1, %3\n\tv_add_f32 %0, %0, %2\n\tv_add_f32 %1, %1, %3"
        : "+v"(x), "+v"(y), "=&v"(s), "=&v"(t));
}
__device__ __forceinline__ float red_sum_q(float x) {
    auto a = __builtin_amdgcn_permlane16_swap(__float_as_uint(x), __float_as_uint(x), false, false);
    x = __uint_as_float(a[0]) + __uint_as_float(a[1]);
    auto c = __builtin_amdgcn_permlane32_swap(__float_as_uint(x), __float_as_uint(x), false, false);
    return __uint_as_float(c[0]) + __uint_as_float(c[1]);
}
__device__ __forceinline__ u32x4 pk8(f32x4 a, f32x4 b) { return u32x4{pk2(a[0], a[1]), pk2(a[2], a[3]), pk2(b[0], b[1]), pk2(b[2], b[3])}; }
__device__ __forceinline__ u32x4 tag4(f32x4 v, unsigned tg) {
    return u32x4{(__float_as_uint(v[0]) & ~3u) | tg, (__float_as_uint(v[1]) & ~3u) | tg, (__float_as_uint(v[2]) & ~3u) | tg, (__float_as_uint(v[3]) & ~3u) | tg};
}
__device__ __forceinline__ unsigned tagbad(u32x4 v, unsigned tg) { return ((v[0] ^ tg) | (v[1] ^ tg) | (v[2] ^ tg) | (v[3] ^ tg)) & 3u; }

__device__ __host__ __forceinline__ constexpr unsigned xm(unsigned o, unsigned side) { return ((o >> 13) << 14) | (o & 8191u) | ((((o >> 20) ^ side) & 1u) << 13); }
__device__ __host__ __forceinline__ constexpr unsigned lp(unsigned o) { return ((o >> 13) << 14) | (o & 8191u); }
__device__ __host__ __forceinline__ constexpr int hside(int h) { return h >> 4; }
constexpr unsigned XM_OFF = 24576, XM_BUF = 4 * 32768;
constexpr unsigned XP_OFF = XM_OFF + 3 * XM_BUF, XP_BUF = 32768;
constexpr unsigned PT_OFF = XP_OFF + 3 * XP_BUF, PT_BUF = 65536;
constexpr unsigned FI_OFF = PT_OFF + 3 * PT_BUF, FI_N = 80;
constexpr unsigned FX_OFF = FI_OFF + FI_N * 1024;
constexpr unsigned A_END = FX_OFF + FI_N * 256;
constexpr unsigned UM_OFF = 1u << 20, UM_BUF = 40 * 8192;
#define TR 8
constexpr unsigned UT_OFF = 2u << 20;
constexpr unsigned QK_OFF = UT_OFF + TR * (2u << 20);
constexpr unsigned SG_OFF = QK_OFF + TR * (1u << 20);
enum { SG_XL = 0, SG_XR = 40960, SG_X1 = 81920, SG_OB = 122880, SG_ST = 163840, SG_FL = 172032 };
enum { FL_U = 0, FL_X = 2048, FL_O = 4096, ST_2 = 6144 };
constexpr unsigned ARENA = 2 * (SG_OFF + 64 * 262144);
static_assert(A_END <= UM_OFF && UM_OFF + 3 * UM_BUF <= UT_OFF && QK_OFF % (1u << 21) == 0 && SG_OFF % (1u << 21) == 0, "map");
static_assert(ARENA + (1u << 23) < (1u << 28), "workspace");
__device__ __forceinline__ unsigned seg_base(int layer, int seg) { return 2 * SG_OFF + (unsigned)seg * 524288 + ((((seg >> 2) ^ layer) & 1) << 13); }
__device__ __forceinline__ unsigned ut_slot(int seg) { return (unsigned)(seg & (TR - 1)) << 22; }
__device__ __forceinline__ unsigned qk_base(int seg, int side) { return 2 * QK_OFF + ((unsigned)(seg & (TR - 1)) << 21) + (((seg ^ side) & 1) << 13); }
__device__ __forceinline__ unsigned fi_step(int it) { return ((it >> 3) << 14) | ((it & 7) << 10); }
__device__ __forceinline__ unsigned fx_step(int it) { return ((it >> 5) << 14) | ((it & 31) << 8); }
static_assert(FI_OFF % 8192 == 0 && FX_OFF % 8192 == 0, "steps");
__device__ __forceinline__ u32x4 sld16(rsrc_t r, unsigned so, unsigned lo) { return __builtin_amdgcn_raw_buffer_load_b128(r, lo, so, AUX_SC1); }
__device__ __forceinline__ u32x2 sld8(rsrc_t r, unsigned so, unsigned lo) { return __builtin_amdgcn_raw_buffer_load_b64(r, lo, so, AUX_SC1); }
__device__ __forceinline__ unsigned sld4(rsrc_t r, unsigned so, unsigned lo) { return __builtin_amdgcn_raw_buffer_load_b32(r, lo, so, AUX_SC1); }
__device__ __forceinline__ void sst4(rsrc_t r, unsigned so, unsigned lo, unsigned v) { __builtin_amdgcn_raw_buffer_store_b32(v, r, lo, so, AUX_SC1); }
__device__ __forceinline__ void sst16(rsrc_t r, unsigned so, unsigned lo, u32x4 v) { __builtin_amdgcn_raw_buffer_store_b128(v, r, lo, so, AUX_SC1); }
__device__ __forceinline__ void sst8(rsrc_t r, unsigned so, unsigned lo, u32x2 v) { __builtin_amdgcn_raw_buffer_store_b64(v, r, lo, so, AUX_SC1); }
__device__ __forceinline__ unsigned umax3(unsigned a, unsigned b, unsigned c) { return max(max(a, b), c); }
__device__ __forceinline__ unsigned mx4(unsigned m, u32x4 v) { return umax3(m, v[0], v[2]); }
__device__ __forceinline__ unsigned nosent(unsigned x) { return min(x, 0xfffffffeu); }
constexpr unsigned OOB = 0x7f000000u;
constexpr unsigned kSpinCap = 1u << 18;

__device__ __forceinline__ char* arena_base(char* p) { return (char*)(((size_t)p + 0x3fffff) & ~(size_t)0x3fffff); }

typedef __attribute__((address_space(3))) bf16x4 lds_b4;
typedef __attribute__((address_space(3))) volatile int lds_int;
template <int V> struct IC { static constexpr int value = V; };
template <int B, int E, class F> __device__ __forceinline__ void sfor(F&& f) { if constexpr (B < E) { f(IC<B>{}); sfor<B + 1, E>(f); } }
#define WNT ""
#define DMA_NEXT(x) asm volatile("global_load_lds_dwordx4 %0, %1" x "\n\ts_add_u32 m0, m0, %2\n\tv_add_u32 %0, %3, %0" : "+v"(vo) : "s"(base), "s"(step), "s"(adv) : "memory", "m0", "scc")
template <bool NT> __device__ __forceinline__ void dma_next(const void* base, unsigned& vo, unsigned step, unsigned adv) { if (NT) DMA_NEXT(WNT); else DMA_NEXT(""); }
__device__ __forceinline__ void set_m0(unsigned x) { asm volatile("s_mov_b32 m0, %0\n\ts_nop 0" :: "s"(x) : "memory", "m0"); }
__device__ __forceinline__ bf16x8 tr_read(const char* p, int o0, int o1) { return cat8(__builtin_amdgcn_ds_read_tr16_b64_v4i16((lds_b4*)(p + o0)), __builtin_amdgcn_ds_read_tr16_b64_v4i16((lds_b4*)(p + o1))); }
struct Go { rsrc_t rh; unsigned so; u32x2 v; };
#define G_EB 2
#define GO_ASK asm volatile("buffer_load_dwordx2 %0, %1, %2, %3 offen sc1" : "=&v"(go.v) : "v"((threadIdx.x & 63) * 8), "s"(go.rh), "s"(go.so) : "memory")
struct Nop { __device__ void operator()() const {} };
template <int N, int N1, int NI, int NJ, int NS, int SL, bool V, int NE = 0, class ISSUE, class TAKE, class EARLY = Nop>
__device__ __forceinline__ void load_ring(char* smem, const int w, Go& go, ISSUE issue_, TAKE take, EARLY early = Nop{}) {
    constexpr int AH = NS - 1, SH = NE ? (NS - N % NS) % NS : 0, GE = N - 1 - AH - (NE ? G_EB : 0), HE = GE + AH + 1;
    static_assert(HE >= N - NS + 2, "free slots");
    const unsigned sta = (unsigned)(size_t)(__attribute__((address_space(3))) char*)smem + w * 1024;
    auto issue = [&](int st) {
        if ((st + SH) % NS == 0) set_m0(sta); else if (st == 0) set_m0(sta + SH * SL);
        const int n = st < N1 ? NI : NJ;
#pragma unroll
        for (int m = 0; m < n; ++m) issue_(st, m, m + 1 < n ? 4096u : SL - 4096u * (n - 1));
    };
#pragma unroll
    for (int st = 0; st < AH; ++st) issue(st);
    auto run = [&](auto WW) {
        sfor<0, N + 1>([&](auto ST) {
            constexpr int st = decltype(ST)::value, e = st + AH < N ? st + AH : N, a = e < N1 ? e : N1, n1 = a > st + 1 ? a - st - 1 : 0, n = st < N ? n1 * NI + (e - st - 1 - n1) * NJ + (st > GE && st <= GE + AH) + (st > HE ? NE : 0) : 0;
            asm volatile("s_waitcnt vmcnt(%0) lgkmcnt(0)\n\ts_barrier" :: "n"(n) : "memory");
            if constexpr (st > 0) take(IC<st - 1>{}, WW, smem, -1);
            if constexpr (st + AH < N) issue(st + AH);
            if constexpr (st == GE) GO_ASK;
            if constexpr (st == HE || st == N) asm volatile("" : "+v"(go.v));
            constexpr int sl = (st + SH) % NS, hi = sl / (65536 / SL) * (65536 / SL);
            if constexpr (st < N) take(ST, WW, smem + hi * SL, (sl - hi) * SL);
            if constexpr (NE && st == HE) early();
        });
    };
    if (!V || w == 0) run(IC<0>{}); else if (w == 1) run(IC<1>{}); else if (w == 2) run(IC<2>{}); else run(IC<3>{});
}
template <int R, int NTT> __device__ __forceinline__ void take_k(int ks, int ww, const char* p, int o, const int (&rp)[NTT], int hf, bf16x8 (&wf)[NTT][20]) {
#pragma unroll
    for (int tt = 0; tt < NTT; ++tt) {
        const int i = (ks + 20 - R * ww) % 20;
        if (o >= 0) wf[tt][i] = tr_read(p, rp[tt] + o, rp[tt] + hf + o);
        else if (20 * tt + i < 64) asm volatile("" : "+a"(wf[tt][i]));
        else asm volatile("" : "+v"(wf[tt][i]));
    }
}
#define SWP (R != 0)
#define ORD(st) (SWP ? (st & 1 ? 3u : -1u) : 1u)
#define BNT 4
template <int NI, int R, int NTT, int NE = 0, class EARLY = Nop>
__device__ __forceinline__ void load_coop(char* smem, const int w, Go& go, const void* const (&base)[NI], unsigned (&vo)[NI], const unsigned adv, const int (&rp)[NTT], const int hf, bf16x8 (&wf)[NTT][20], EARLY early = Nop{}) {
    load_ring<20, 20, NI, NI, (NI > 4 ? 6 : 8), (NI > 4 ? 24576 : 16384), R != 0, NE>(smem, w, go, [&](int st, int m, unsigned step) { dma_next<(NI > BNT)>(base[m], vo[m], step, ORD(st) * adv); },
        [&](auto ST, auto WW, const char* p, int o) { take_k<R, NTT>(decltype(ST)::value ^ SWP, decltype(WW)::value, p, o, rp, hf, wf); }, early);
}
__device__ __forceinline__ unsigned hd_src(int idx) { const int r = idx / 24, u = idx % 24; return r * (2 * HD_) + ((u * 16) ^ ((r & 6) << 4)); }
__device__ __forceinline__ int hd_pos(int l, int tile) { const int r = l >> 2; return r * 384 + ((32 * tile) ^ ((r & 6) << 4)) + (l & 3) * 8; }
__device__ __forceinline__ unsigned row_src(int idx) { return (idx * 16) ^ ((idx / 80 & 3) << 6); }
__device__ __forceinline__ int row_pos(int r, int piece) { return r * 1280 + ((piece ^ (r & 3)) << 6); }
template <int R, int NE = 0, class TILE, class EARLY = Nop> __device__ __forceinline__ void load_qk(char* smem, int w, int l, Go& go, const bf16_t* Wq, const bf16_t* Wk, TILE tile, bf16x8 (&wf)[6][20], EARLY early = Nop{}) {
    const void* const base[6] = {Wq, Wq, Wq, Wk, Wk, Wk};
    unsigned vo[6];
    int rp[6];
#pragma unroll
    for (int m = 0; m < 6; ++m) { vo[m] = hd_src((4 * (m % 3) + w) * 64 + l) + SWP * 64 * HD_; rp[m] = tile(m) / 12 * 12288 + hd_pos(l, tile(m) % 12); }
    load_coop<6, R, 6, NE>(smem, w, go, base, vo, 64 * HD_, rp, 6144, wf, early);
}
template <int R, class DST> __device__ __forceinline__ void load_vo(char* smem, int w, int l, Go& go, const bf16_t* Wv, const bf16_t* Wo, bf16x8 (&wf)[3][20], DST dst) {
    unsigned vo[3], v2[5];
    int r2[5];
    bf16x4 lo[10];
#pragma unroll
    for (int m = 0; m < 3; ++m) vo[m] = hd_src((4 * m + w) * 64 + l) + SWP * 64 * HD_;
#pragma unroll
    for (int m = 0; m < 5; ++m) v2[m] = row_src((4 * m + w) * 64 + l) + SWP * 20480;
#pragma unroll
    for (int m = 0; m < 5; ++m) r2[m] = row_pos(l >> 2, 5 * w + m) + (l & 3) * 8;
    const int rp[3] = {hd_pos(l, 2 * w), hd_pos(l, 2 * w + 1), hd_pos(l, 8 + w)};
    load_ring<32, 20, 3, 5, 8, 20480, R != 0>(smem, w, go, [&](int st, int m, unsigned step) { if (st < 20) dma_next<true>(Wv, vo[m % 3], step, ORD(st) * (64 * HD_)); else dma_next<true>(Wo, v2[m], step, ORD(st) * 20480u); },
        [&](auto ST, auto WW, const char* p, int o) {
            constexpr int st = decltype(ST)::value;
            if constexpr (st < 20) take_k<R, 3>(st ^ SWP, decltype(WW)::value, p, o, rp, 6144, wf);
            else {
#pragma unroll
                for (int t = 0; t < 10; ++t) {
                    if (o < 0) {
                        if (st & 1) { bf16x8& d = dst(st / 2 - 10, t, decltype(WW)::value); if (t == 0 && st < 28) asm volatile("" : "+a"(d)); else asm volatile("" : "+v"(d)); }
                        continue;
                    }
                    const bf16x4 x = __builtin_amdgcn_ds_read_tr16_b64_v4i16((lds_b4*)(p + r2[t / 2] + o + (t & 1) * 32));
                    if (st & 1) dst(st / 2 - 10, t, decltype(WW)::value) = SWP ? cat8(x, lo[t]) : cat8(lo[t], x); else lo[t] = x;
                }
            }
        });
}

struct MegaArgs {
    const bf16_t* x0; const bf16_t* mem0; const bf16_t* Wq; const bf16_t* Wk; const bf16_t* Wv; const bf16_t* Wo; const bf16_t* wqkv; const bf16_t* wo;
    char* ar; unsigned* tmo; char* hs; bf16_t* out;
    int nit, lag, roles, pad;
};

#define WSLP 2
#define LIVE u64 live_; asm volatile("s_add_u32 %0, %0, 1\n\ts_cmp_gt_u32 %0, %2\n\ts_cselect_b64 %1, 0, -1" : "+s"(spins), "=s"(live_) : "n"(kSpinCap) : "scc")
#define BAD(m) (__ballot(m == ~0u) & live_)
#define SPIN_END if (spins > kSpinCap) __hip_atomic_store(A.tmo, 1u + id, RLX_AGENT)
__device__ __forceinline__ void wave_wait(rsrc_t rx, unsigned off, unsigned so, unsigned& spins) {
#pragma unroll 1
    for (;;) {
        const unsigned v = sld4(rx, so, off);
        LIVE;
        if (!(__ballot(v == 0 && off != OOB) & live_)) break;
        __builtin_amdgcn_s_sleep(WSLP);
        MEMFENCE();
    }
}
__device__ __forceinline__ unsigned rtime() { return (unsigned)__builtin_amdgcn_s_memrealtime(); }
__device__ __forceinline__ void rt_until(unsigned target) {
    asm volatile("1:\n\ts_memrealtime vcc\n\ts_waitcnt lgkmcnt(0)\n\ts_sub_u32 vcc_lo, %0, vcc_lo\n\ts_cmpk_lt_u32 vcc_lo, 600\n\ts_cbranch_scc1 1b" :: "s"(target - 1) : "memory", "scc", "vcc");
}
#define RT_K 26
__device__ __forceinline__ void rt_fine(unsigned target) {
    u64 a, b, c;
    unsigned n;
    asm volatile("1:\n\ts_memrealtime vcc\n\ts_waitcnt lgkmcnt(0)\n\ts_sub_u32 vcc_lo, %4, vcc_lo\n\ts_cmpk_lt_u32 vcc_lo, 600\n\ts_cbranch_scc1 1b\n\t"
                 "s_add_u32 vcc_lo, vcc_lo, 8\n\ts_cbranch_scc0 3f\n\t"
                 "s_memrealtime %0\n\ts_nop 4\n\ts_memrealtime %1\n\ts_nop 4\n\ts_memrealtime %2\n\ts_lshl_b32 %3, %4, 2\n\ts_addk_i32 %3, %5\n\ts_nop 2\n\ts_memrealtime vcc\n\ts_waitcnt lgkmcnt(0)\n\t"
                 "s_sub_u32 %3, %3, vcc_lo\n\ts_mov_b64 vcc, %0\n\ts_sub_u32 %3, %3, vcc_lo\n\ts_mov_b64 vcc, %1\n\ts_sub_u32 %3, %3, vcc_lo\n\ts_mov_b64 vcc, %2\n\ts_sub_u32 %3, %3, vcc_lo\n\t"
                 "s_max_i32 %3, %3, 0\n\ts_min_i32 %3, %3, 64\n\t"
                 "s_branch 2f\n\t"
                 "3:\n\ts_add_u32 %3, %4, 13\n\t"
                 "4:\n\ts_memrealtime vcc\n\ts_waitcnt lgkmcnt(0)\n\ts_sub_u32 vcc_lo, %3, vcc_lo\n\ts_cmpk_lt_u32 vcc_lo, 16\n\ts_cbranch_scc1 4b\n\t"
                 "s_mov_b32 %3, 0\n\t"
                 "2:\n\ts_sub_u32 %3, %3, 5\n\ts_cbranch_scc0 2b"
                 : "=&s"(a), "=&s"(b), "=&s"(c), "=&s"(n) : "s"(target - 14), "n"(56 - RT_K) : "memory", "scc", "vcc");
}
#define LDA(dst, vo, so, imm) asm volatile("buffer_load_dwordx4 %0, %1, %2, %3 offen offset:%4 sc1" : "=v"(dst) : "v"(vo), "s"(rx), "s"(so), "n"(imm) : "memory")
#define LDI(dst, vo, so, imm) asm volatile("buffer_load_dwordx4 %0, %1, %2, %3 offen offset:%4 sc1" : "+v"(dst) : "v"(vo), "s"(rx), "s"(so), "n"(imm) : "memory")
#define LDA8(dst, vo, so, imm) asm volatile("buffer_load_dwordx2 %0, %1, %2, %3 offen offset:%4 sc1" : "=v"(dst) : "v"(vo), "s"(rx), "s"(so), "n"(imm) : "memory")
#define LDA4(dst, vo, so) asm volatile("buffer_load_dword %0, %1, %2, %3 offen sc1" : "=v"(dst) : "v"(vo), "s"(rx), "s"(so) : "memory")
#define WAITV(n, x) asm volatile("s_waitcnt vmcnt(%1)" : "+v"(x) : "n"(n) : "memory")
#define PIN_S(x) asm volatile("" : "+s"(x))
#define SZERO(x) asm volatile("s_mov_b32 %0, 0" : "=s"(x))
#define PIN_V(x) asm volatile("" : "+v"(x))
#define USE_V(x) asm volatile("" :: "v"(x))
#define LDS_AT(T, a) (*(__attribute__((address_space(3))) T*)(a))
__device__ __forceinline__ float dpp_ror8(float x) { return __uint_as_float(__builtin_amdgcn_mov_dpp(__float_as_uint(x), 0x128, 0xf, 0xf, true)); }
__device__ __forceinline__ unsigned lds_addr(const void* p) { return (unsigned)(size_t)(__attribute__((address_space(3))) const char*)p; }
#define DMA1(vo, so, dst, imm) asm volatile("s_mov_b32 m0, %3\n\ts_nop 0\n\tbuffer_load_dwordx4 %0, %1, %2 offen offset:%4 sc1 lds" :: "v"(vo), "s"(rx), "s"(so), "s"(dst), "n"(imm) : "memory", "m0")

enum { HS_A = 432, HS_E = 504, HS_F = 512, N_1 = 60, N_INIT = 108 };
__device__ __forceinline__ unsigned hs_tags(const Go& go, int l) { return (bld4(go.rh, l < 8 ? l * 1024 + HS_E : OOB) + 2) & ~1u; }
__device__ __forceinline__ rsrc_t arena(const MegaArgs& A, unsigned flip) { return mk_rsrc(arena_base(A.ar) + (flip << 21), ARENA); }
__device__ __forceinline__ unsigned hs_wait(Go& go, int l, int n, unsigned off, bool known, unsigned tag, unsigned& spins) {
#pragma unroll 1
    for (;;) {
        if (!known) tag = (__builtin_amdgcn_readlane(go.v[0], 63) + 2) & ~1u;
        LIVE;
        if (!(__ballot(l < n && ((go.v[0] ^ tag) | (go.v[1] ^ tag)) > 1) & live_)) break;
        __builtin_amdgcn_s_sleep(8);
        go.v = sld8(go.rh, go.so, off);
    }
    return __builtin_amdgcn_readfirstlane(go.v[0]) & 1;
}
#define GO_DECL(pool) Go go; go.rh = mk_rsrc(A.hs, 8192); go.so = (pool) * 1024
#define GO_ON(n) const rsrc_t rx = arena(A, hs_wait(go, l, n / 2, l * 8, false, 0, spins))
#define INIT_BEGIN(pool) const unsigned tags = hs_tags(go, l); go.v = sld8(go.rh, go.so, HS_F); INIT_FLIP(pool)
#define INIT_FLIP(pool) const unsigned tag = __builtin_amdgcn_readlane(tags, pool), flip = hs_wait(go, l, 1, HS_F, true, tag, spins); \
    const rsrc_t rx = arena(A, flip)
#define INIT_END(ii) asm volatile("s_waitcnt vmcnt(0)\n\ts_barrier" ::: "memory"); bst4(go.rh, t < 8 ? t * 1024 + (ii) * 4 : OOB, tags | flip)
#define INIT_SEEN hs_wait(go, l, N_INIT / 2, l * 8, true, tag, spins)
__device__ __forceinline__ void probe(const MegaArgs& A, const Go& go, int l, int id) {
    const rsrc_t rf = arena(A, 0);
    const unsigned tags = hs_tags(go, l);
    unsigned tm[2] = {~0u, ~0u};
    bst4(rf, l < 2 ? l * 8192 : OOB, 0u);
    DRAIN();
#pragma unroll
    for (int k = 0; k < 4; ++k) {
        const unsigned long long t0 = __builtin_amdgcn_s_memtime();
        bst4(rf, l ? OOB : (k & 1) * 8192, 0u);
        DRAIN();
        const unsigned d = (unsigned)(__builtin_amdgcn_s_memtime() - t0);
        tm[k & 1] = d < tm[k & 1] ? d : tm[k & 1];
    }
    bst8(go.rh, l < 8 ? l * 1024 + HS_F : OOB, u32x2{tags | (tm[1] < tm[0]), tags});
}
#define OUTAUX 0
#define DLYB0 900u
#define DLYB1 2400u
#define DLY1 0
#define DLY2 900
#define START_IDS const int t = threadIdx.x, l = t & 63, w = __builtin_amdgcn_readfirstlane(t >> 6); const unsigned l16b = l * 16; GO_DECL(id >> 5); unsigned spins; SZERO(spins)
__device__ __forceinline__ void init_area(const MegaArgs& A, const int ii, const int id, const unsigned t0) {
    START_IDS;
    INIT_BEGIN(id >> 5);
    const int i = 4 * ii + w;
    const bf16_t* mp = A.mem0 + (l & 7) * D_ + 32 * (i % 20) + 4 * (l >> 4);
    const u32x2 x = *(const u32x2*)mp, y = *(const u32x2*)(mp + 16);
    while (rtime() - t0 < DLY1) __builtin_amdgcn_s_sleep(4);
    sst16(rx, xm(XM_OFF + (i / 5 & 15) * 8192 + i % 5 * 1024, i / 80), l16b, u32x4{nosent(x[0]), x[1], nosent(y[0]), y[1]});
    auto fill = [&](unsigned o, unsigned n, unsigned f) {
#pragma unroll 2
        for (unsigned u = i * 1024; u < 2 * n; u += 163840) sst16(rx, 2 * o + u, l16b, u32x4{f, f, f, f});
    };
    fill(XM_OFF + XM_BUF, FI_OFF - PT_BUF - XM_OFF - XM_BUF, ~0u);
    fill(FI_OFF - PT_BUF, PT_BUF, 0u);
    fill(UM_OFF, 3 * UM_BUF, ~0u);
    for (unsigned u = i; u < 208; u += 160) {
        const unsigned f = u < 160 && (u / 16 * 8 + (u & 7) - (l & 1) * A.lag) >= NSEG_;
        sst16(rx, 2 * FI_OFF + u * 1024, l16b, u32x4{f, f, f, f});
    }
    INIT_END(16 + ii);
}
static_assert(FI_N == 80 && FX_OFF + 24576 <= UM_OFF && SG_FL == SG_ST + 8192 && SG_ST % 8192 == 0, "init");
__device__ __forceinline__ void first_rows(const MegaArgs& A, char* smem, const int sg, const int id, const unsigned t0) {
    START_IDS;
    set_m0(lds_addr(smem) + w * 1024);
    {
        unsigned vo[5];
#pragma unroll
        for (int m = 0; m < 5; ++m) vo[m] = row_src((4 * m + w) * 64 + l);
#pragma unroll
        for (int m = 0; m < 10; ++m) dma_next<true>(A.x0 + sg * (SEG_ * D_), vo[m % 5], 4096u, 20480u);
    }
    asm volatile("s_waitcnt vmcnt(0)\n\ts_barrier" ::: "memory");
    const unsigned tags = hs_tags(go, l);
    u32x4 xr[2][5], xl[2][5];
#pragma unroll 1
    for (int rt = 0; rt < 2; ++rt) {
        f32x4 v[10], a;
#pragma unroll
        for (int e = 0; e < 5; ++e) { xr[0][e] = xr[1][e]; xl[0][e] = xl[1][e]; }
#pragma unroll
        for (int e = 0; e < 10; ++e) {
            const u32x2 x = *(const u32x2*)(smem + rt * 20480 + row_pos(l & 15, 5 * w + e / 2) + (e & 1) * 32 + (l >> 4) * 8);
            xr[1][e / 2][2 * (e & 1)] = x[0]; xr[1][e / 2][2 * (e & 1) + 1] = x[1];
            v[e] = f32x4{lo16(x[0]), hi16(x[0]), lo16(x[1]), hi16(x[1])};
            a = e ? a + v[e] : v[0];
        }
        const float mw = red_sum_q((a[0] + a[1]) + (a[2] + a[3])) * (1.0f / 160);
#pragma unroll
        for (int e = 0; e < 10; ++e) { const f32x4 d = v[e] - mw; a = e ? __builtin_elementwise_fma(d, d, a) : d * d; }
        f32v2* sp = (f32v2*)(smem + 49152) + rt * 64 + (l & 15);
        sp[16 * w] = f32v2{mw, red_sum_q((a[0] + a[1]) + (a[2] + a[3]))};
        LDS_BARRIER();
        go.v = sld8(go.rh, go.so, HS_F);
        const f32v2 p = sp[l & 48];
        const float mean = red_sum_q(p[0]) * 0.25f, var = red_sum_q(p[1] + 160.f * (p[0] - mean) * (p[0] - mean)) * (1.0f / D_);
        const float mb = rb(mean), rd = 1.0f / rb(sqrtf(rb(rb(var) + 1e-5f)));
#pragma unroll
        for (int e = 0; e < 10; ++e) {
            const unsigned x = pk2(v[e][0] - mb, v[e][1] - mb), y = pk2(v[e][2] - mb, v[e][3] - mb);
            v[e] = f32x4{lo16(x), hi16(x), lo16(y), hi16(y)} * rd;
            if (e & 1) xl[1][e / 2] = pk8(v[e - 1], v[e]);
        }
    }
    INIT_FLIP(id >> 5);
    while (rtime() - t0 < (sg < 8 ? 0 : (sg < 16 ? DLY1 : DLY2))) __builtin_amdgcn_s_sleep(4);
#pragma unroll
    for (int e = 0; e < 20; ++e) {
        sst16(rx, seg_base(0, sg) + lp((e < 10 ? SG_XL : SG_XR) + (5 * w + e % 10 / 2) * 2048), l16b + (e & 1) * 1024, (e < 10 ? xl : xr)[e & 1][e % 10 / 2]);
        if (e == 9 && sg == 0) { asm volatile("s_waitcnt vmcnt(0)\n\ts_barrier" ::: "memory"); bst8(go.rh, t < 8 ? t * 1024 + HS_A : OOB, u32x2{tags | flip, tags}); }
    }
    const unsigned f = (w >> 1) - 1;
#pragma unroll
    for (int e = 0; e < 8; ++e) sst16(rx, 2 * (SG_OFF + SG_ST) + sg * 524288 + w * 8192 + e / 4 * 4096, l16b + (e & 3) * 1024, u32x4{f, f, f, f});
    INIT_END(sg + (sg < 16 ? 0 : 44));
}
#define LAGV 7
static_assert(LAGV < TR, "rings");
#define NTOUCH 0
#define VSLP 1
#define PER 286
#define E_U 122
#define E_P 138
#define E_W 137
#define K_W 5
#define K_0 2
#define E_X 40
#define E_B 95
#define E_S 16
#define E_T 10
#define E_G 54
#define IT_1 61
#define D_1 16
#define D_2 8
#define E_L 160
#define E_Z 60
#define E_C 0
#define E_V 150
#define PVU_E 1
#define XV_L 1
#define E_M 4
#define FASTR 1
#define A_0 0
#define A_Q 6
#define A_UP 3
#define A_K 8
__device__ __forceinline__ bool adapt(int& mg, unsigned xv, unsigned xp, int mx) {
    const int e = xv - xp;
    const bool late = (unsigned)(e + 16) >= 36u;
    mg = max(mg + (xv && !late ? e : 0), 0);
    PIN_S(mg);
    mg = min(mg, mx);
    PIN_S(mg);
    return late;
}
#define TIGHT(x, it, d) asm volatile("s_cmp_ge_i32 %1, %2\n\ts_cselect_b32 %0, %3, 0" : "=s"(x) : "s"(it), "s"(IT_1), "s"(d) : "scc")
#define CHAIN_WAIT(pv, off, so, code) if (__builtin_expect(__ballot(pv == 0) != 0, 0)) { if (so != OOB) { wave_wait(rx, off, so, spins); } }
#define GAPN 11
#define GAP() asm volatile("s_nop %0" :: "n"(GAPN))
#define LD5(v, so) do { LDA(v[0], l16b, so, 0); GAP(); LDA(v[1], l16b, so, 1024); GAP(); LDA(v[2], l16b, so, 2048); GAP(); LDA(v[3], l16b, so, 3072); GAP(); LDA(v[4], l16k, so, 0); } while (0)
#define CHAIN_IDS const int t = threadIdx.x, l = t & 63, l16 = l & 15, lq = l >> 4; const int w = __builtin_amdgcn_readfirstlane(t >> 6); \
    GO_DECL(id >> 5); const int lag = A.lag, itend = A.nit; const unsigned l16b = l * 16, l16k = l * 16 + 4096; unsigned spins = 0
#define CHAIN_STAGE const char* sg1 = smem + ((w + 1) & 3) * 5120 + l * 16; const char* sg2 = smem + ((w + 2) & 3) * 5120 + l * 16; const char* sg3 = smem + ((w + 3) & 3) * 5120 + l * 16; \
    char* sg0 = smem + w * 5120 + l * 16; auto mrd = [&](int i) { return *(const bf16x8*)((i < 10 ? sg1 : (i < 15 ? sg2 : sg3)) + (i % 5) * 1024); }; \
    const unsigned mo0 = xm(XM_OFF + (pool & 3) * 32768 + w * 8192, pool >> 2), fX = xm(FX_OFF + (pool & 3) * 64, pool >> 2); unsigned fXv = fX; PIN_V(fXv)
#define GATHER(take) do { _Pragma("unroll 1") for (;;) { LD5(v, mo); LIVE; if (!take(live_)) break; } } while (0)
#define CVT2(o0, o1, x) do { o0 = pk2(x[0], x[1]); o1 = pk2(x[2], x[3]); asm volatile("" : "+v"(o0), "+v"(o1)); } while (0)
#define RR_(op, d, x, y, n) op "_dpp " d ", " x ", " y " row_ror:" n " row_mask:0xf bank_mask:0xf\n\t"
#define ROWRED(op, x, y, f0, f1) f0 RR_(op, y, x, x, "1") RR_(op, y, x, y, "2") RR_(op, y, x, y, "3") f1 RR_(op, x, y, y, "4") RR_(op, x, y, x, "8") RR_(op, x, y, x, "12")
#define BIAS(x, a, b, c, d) asm volatile("v_mov_b32 %0, 0xff800000\n\tv_cndmask_b32 %1, %0, 0, %5\n\tv_cndmask_b32 %2, %0, 0, %6\n\tv_cndmask_b32 %3, %0, 0, %7\n\tv_cndmask_b32 %0, %0, 0, %4" \
    : "=&v"(x[0]), "=&v"(x[1]), "=&v"(x[2]), "=&v"(x[3]) : "s"(a), "s"(b), "s"(c), "s"(d))
#define AND2(o0, o1, a, b, m) do { o0 = a & m; o1 = b & m; asm volatile("" : "+v"(o0), "+v"(o1)); } while (0)

constexpr int SL_S = 20480, SS_W = 3136, SL_KM = SL_S + 4 * SS_W, SL_KT = SL_KM + 6144, SL_W = SL_KT + 49152, SL_END = SL_W + 4 * 18432;
static_assert(SL_END <= 163840, "LDS");
#define G_LK 100
#define G_K 32
#define G_KS 6
__device__ __forceinline__ void s_role(const MegaArgs& A, char* smem, const int h, const int id) {
    CHAIN_IDS;
    const int pool = h >> 2, hs = hside(h);
    bf16x8 wf[6][20];
    const char* sW = smem + SL_W + w * 18432 + l * 16 - 2048;
    load_qk<5>(smem, w, l, go, A.Wq + h * DH_, A.Wk + h * DH_, [&](int m) { return m / 3 * 12 + (m % 3 < 2 ? 2 * w + m % 3 : 8 + w); }, wf);
#pragma unroll
    for (int i = 2; i < 20; ++i) *(bf16x8*)(sW + i * 1024) = wf[5][i];
    GO_ON(N_1);
    *(f32x4*)(smem + SL_S + w * SS_W + 3120) = f32x4{-INFINITY, -INFINITY, -INFINITY, -INFINITY};
#pragma unroll
    for (int i = 0; i < 12; ++i) *(u32x4*)(smem + SL_KT + w * 12288 + i * 1024 + l * 16) = u32x4{0u, 0u, 0u, 0u};
    asm volatile("s_waitcnt lgkmcnt(0)" ::: "memory");
    CHAIN_STAGE;
    char* sSw = smem + SL_S + w * SS_W + lq * 256 + ((l16 + 4 * lq) & 15) * 16;
    const int kq = l16, sL = w >> 1, rt = w & 1, row8 = (4 * w + lq) & 7;
    const bool kval = kq < 8 || (kq >> 1) == 4 + sL;
    const char* sSr = smem + SL_S + (kval ? (kq >> 2) * 1040 + (kq & 3) * 256 + ((4 * w + lq + 4 * kq) & 15) * 16 : 3120);
    const unsigned ppos = !kval ? OOB : xm(XP_OFF + (h >> 3) * 8192 +
        (kq < 8 ? ((h & 7) >> 1) * 1024 + ((kq & 3) * 16 + (h & 1) * 8 + row8) * 16 + (kq >> 2) * 8 : 4096 + ((h & 3) * 16 + ((h >> 2) & 1) * 8 + row8) * 16 + (kq & 1) * 8), sL);
    const unsigned pt1 = xm(PT_OFF + (h >> 3) * 16384 + (rt ? (h & 7) * 1024 : 8192 + ((h & 7) >> 1) * 1024 + (h & 1) * 8) + l * 16, sL);
    const unsigned pt2 = lq == 2 * sL ? xm(PT_OFF + (h >> 3) * 16384 + 12288 + (((h >> 2) & 1) * 2 + rt) * 1024 + ((h & 3) * 16 + l16) * 16, sL) : OOB;
    const u64 cm0 = __ballot(4 * lq <= l16), cm1 = __ballot(4 * lq + 1 <= l16), cm2 = __ballot(4 * lq + 2 <= l16), cm3 = __ballot(4 * lq + 3 <= l16), mval = __ballot((lq >> 1) == sL), oval = __ballot(rt);
    const unsigned ma = l16 < 8 ? ~0u : 0u, mb2 = ~ma;
    const unsigned fK = xm(FI_OFF + (h & 15) * 64 + (4 * sL + (l & 3)) * 4, hs);
    const unsigned qo = ((sL * 16 + (h & 15)) * 4 + rt) * 16384, ktl = lds_addr(smem + SL_KT) + w * 12288;
    const char* sKT = smem + SL_KT + l * 16;
    char* sKM = smem + SL_KM + l * 16;
    char* sKX = smem + SL_KT + l * 16 + 4096 + (w >> 1) * 1024 + (w & 1) * 8;
    unsigned X = rtime() - PER;
    int mg = A_0;
    auto qk_so = [&](int it) { const int sg = it - sL * lag; return (unsigned)sg < NSEG_ ? qk_base(sg, hs) + qo : OOB; };
    auto dm0 = [&](int e) {
        if (e == 0) asm volatile("s_mov_b32 m0, %0" :: "s"(ktl) : "m0");
        if (e == 4 || e == 6 || e == 10) asm volatile("s_add_u32 m0, m0, %0" :: "n"(e == 6 ? 2048 : 4096) : "m0", "scc");
    };
    auto dma = [&](int e, unsigned sq, unsigned sk, bool set = true) {
        if (set && (e == 0 || e == 4 || e == 6 || e == 10)) { dm0(e); asm volatile("s_nop 0"); }
        asm volatile("buffer_load_dwordx4 %0, %1, %2 offen offset:%3 sc1 lds" :: "v"(e % 6 < 4 ? l16b : l16k), "s"(rx), "s"(e < 6 ? sk : sq), "n"((e % 6 & 3) * 1024) : "memory");
    };
    {
        const unsigned pv0 = bld4(rx, fK), z = 0, so = qk_so(0);
        CHAIN_WAIT(pv0, fK, z, 1);
#pragma unroll
        for (int e = 0; e < 12; ++e) dma(e, so, so + 32768);
        asm volatile("s_waitcnt vmcnt(0)\n\ts_barrier" ::: "memory");
    }

    unsigned c0 = 0, c1 = 65536, c2 = 131072, mo = mo0, fs = -256, fs1 = 1024, sg = -sL * lag, q0 = 2 * QK_OFF + qo + hs * 8192;
    static_assert(2 * XP_BUF == 65536 && PT_BUF == 65536 && XM_BUF == 131072, "strides");
    PIN_S(c0); PIN_S(c1); PIN_S(c2); PIN_S(fs); PIN_S(fs1); PIN_S(sg); PIN_S(q0);
    for (int it = 0; it < itend; ++it) {
        unsigned ps, pr, ts, tr, qn, kn, tz, fk, per, xp;
        bf16x8 wl[3];
        f32x4 sc[3];
        u32x4 v[5], kp[2][2], kx[2];
        unsigned pvK;
        {
            bf16x8 ka[3], kb[3], q[3];
            auto krd = [&](int f, int e) {
                if (e == 0) kb[f % 3] = *(const bf16x8*)(sKT + (w & 2) * 12288 + f * 1024);
                if (e == 1) q[f % 3] = *(const bf16x8*)(sKT + w * 12288 + 6144 + f * 1024);
                if (e == 2) ka[f % 3] = *(const bf16x8*)(sKT + w * 12288 + f * 1024);
            };
            BIAS(sc[0], oval, oval, oval, oval); BIAS(sc[1], cm0, cm1, cm2, cm3);
            asm volatile("s_waitcnt vmcnt(%0)" :: "n"(5) : "memory");
#pragma unroll
            for (int e = 0; e < 9; ++e) { krd(e / 3, e % 3); SCHED0(); }
            PIN_V(ka[2]);
            rt_fine(X - E_G);
            sfor<0, 13>([&](auto G) {
                constexpr int g = decltype(G)::value, f = g / 2;
                if constexpr (g % 3 == 0) LDA(v[g / 3], g < 12 ? l16b : l16k, mo, (g / 3 & 3) * 1024);
                if constexpr (g < 12) MF_VV(sc[g & 1], (g & 1 ? ka : kb)[f % 3], q[f % 3]);
                SCHED0();
                if (g < 6) { if (g & 1) { krd(f + 3, 1); krd(f + 3, 2); } else krd(f + 3, 0); }
                SCHED0();
            });
#pragma unroll
            for (int e = 0; e < 2; ++e) {
                const u32x2 ka = *(const u32x2*)(sKX + e * 12288), kb = *(const u32x2*)(sKX + (e + 2) * 12288);
                kx[e] = u32x4{ka[0], ka[1], kb[0], kb[1]};
            }
            sg += 1;
            qn = (sg & (TR - 1)) << 21;
            qn += ((qn >> 8) & 8192) ^ q0;
            qn = sg < NSEG_ ? qn : OOB;
            kn = qn + 32768;
            PIN_S(sg); PIN_S(qn); PIN_S(kn);
            if (!G_LK) LDA4(pvK, fK, fs1);
            PIN_V(kx[0]); PIN_V(kx[1]);
        }
        f32x4 acc[6];
        auto mf = [&](int i, int tt, const bf16x8 xb) {
            if (i == 0) { if (tt < 4) MF_AV0(acc[tt], wf[tt][0], xb); else MF_VV0(acc[tt], wf[tt][0], xb); }
            else if (20 * tt + i < 64) MF_AV(acc[tt], wf[tt][i], xb);
            else if (tt < 5 || i < 2) MF_VV(acc[tt], wf[tt][i], xb);
            else MF_VV(acc[5], wl[i % 3], xb);
        };
        auto take = [&](u64 live_, auto N) {
            unsigned m = 0;
            u64 bad;
            sfor<0, 22>([&](auto G) {
                constexpr int g = decltype(G)::value;
                if constexpr (g < 20 && g % 4 == 0) { WAITV(4 + (decltype(N)::value && !G_LK) - g / 4, v[g / 4]); *(u32x4*)(sg0 + g / 4 * 1024) = v[g / 4]; SCHED0(); }
                mf(g / 6, g % 6, asb8(v[g / 6]));
                SCHED0();
                if constexpr (g < 20 && g % 4 == 0) { m = mx4(m, v[g / 4]); if (g < 16) PIN_V(m); else { bad = __ballot(m == ~0u); PIN_S(bad); } }
                if constexpr (g == 1 || g == 2 || g == 6 || g == 13) { constexpr int e = g < 3 ? g - 1 : 2 + g / 8; kp[e >> 1][e & 1] = *(const u32x4*)(sKT + e * 12288 + w * 1024); }
                if constexpr (g == 5 || g == 10 || g == 18) wl[(g / 5 + 1) % 3] = *(const bf16x8*)(sW + (g / 5 + 1) * 1024);
                if (g == 18 && !decltype(N)::value) { bad &= live_; PIN_S(bad); }
                if (g == 20 && decltype(N)::value && !G_LK) WAITV(0, pvK);
                SCHED0();
            });
            return bad;
        };
        if (__builtin_expect(take(~0ull, IC<1>{}) != 0, 0)) {
            auto again = [&](u64 live_) { return take(live_, IC<0>{}); };
            GATHER(again);
            asm volatile("s_nop 7" : "+v"(acc[0]), "+v"(acc[1]), "+v"(acc[2]), "+v"(acc[3]), "+v"(acc[4]), "+v"(acc[5]));
        }
        unsigned xv, kx0;
        u64 nk;
        u32x4 ko, kz;
        {
            bf16x8 mm[3];
            u32x4 qp, qa, qb, qz;
            unsigned z0, z1;
            f32x4 s[2], sm;
            sfor<22, 128>([&](auto G) {
                constexpr int g = decltype(G)::value, kt[13] = {5, 5, 5, 3, 4, -1, -1, 3, -1, 4, -1, 3, 4}, ki[13] = {17, 18, 19, 17, 17, 0, 0, 18, 0, 18, 0, 19, 19};
                constexpr int i = g < 102 ? g / 6 : (g < 111 ? 17 + (g - 102) / 3 : (g < 124 ? ki[g < 111 || g > 123 ? 0 : g - 111] : 0));
                constexpr int tt = g < 102 ? g % 6 : (g < 111 ? (g - 102) % 3 : (g < 124 ? kt[g < 111 || g > 123 ? 0 : g - 111] : -1));
                if constexpr (tt >= 0) mf(i, tt, i < 5 ? asb8(v[i < 5 ? i : 0]) : mm[(i < 5 ? 0 : i - 5) % 3]);
                if constexpr (g == 116 || g == 117) MF_VV0(s[g & 1], kp[0][g & 1], qa);
                if constexpr (g == 119 || g == 124) MF_VV(s[~g & 1], kp[1][~g & 1], qb);
                if constexpr (g == 121 || g == 125) MF_VV(s[g == 125], kx[g == 125], qz);
                if constexpr (g == 126) MF_VV0(sm, kz, qz);
                if constexpr (g == 127) MF_VV(sm, ko, qp);
                SCHED0();
                if (g == 22) { LDS_BARRIER(); }
                if (g == 23) mm[0] = mrd(5);
                if (g == 24) wl[2] = *(const bf16x8*)(sW + 5120);
                if constexpr (g > 24 && g < 103 && g % 6 == (g < 102)) mm[(g / 6 - 3) % 3] = mrd(g / 6 + 2);
                if constexpr (g > 24 && g < 102 && g % 6 == 3) wl[(g / 6 + 2) % 3] = *(const bf16x8*)(sW + (g / 6 + 2) * 1024);
                if (g == 104) wl[1] = *(const bf16x8*)(sW + 19 * 1024);
                if (g == 110) PIN_V(wl[1]);
                if (g == G_LK) LDA4(pvK, fK, fk);
                if (g == 26 && !G_LK) { nk = __ballot(pvK == 0); PIN_S(nk); dm0(0); }
                if (g == 30 && !G_LK) asm volatile("s_cmp_eq_u64 %1, 0\n\ts_cselect_b32 %0, %2, %3" : "=s"(kx0) : "s"(nk), "s"(kn), "n"(OOB) : "scc");
                if constexpr (!G_LK && g >= G_K && g <= G_K + 5 * G_KS && (g - G_K) % G_KS == 0) dma((g - G_K) / G_KS, qn, kx0, false);
                if (g == G_K + 4 * G_KS - 2 && !G_LK) dm0(4);
                if (g == 36) { ps = c0; pr = c2; PIN_S(ps); PIN_S(pr); }
                if (g == 42) { c2 = c0; c0 = c1; PIN_S(c2); PIN_S(c0); }
                if (g == 48) { c1 = pr; ts = 2 * ps; PIN_S(c1); PIN_S(ts); }
                if (g == 60) { mo = mo0 + 4 * c0; PIN_S(mo); }
                if (g == 66) { fs += 256; tz = fs & 8192; PIN_S(fs); PIN_S(tz); }
                if (g == 72) { fs += tz; tr = 2 * c0; PIN_S(fs); PIN_S(tr); }
                if (g == 84) { fk = fs1; fs1 += 1024; PIN_S(fk); PIN_S(fs1); }
                if (g == 90) { fs1 += fs1 & 8192; PIN_S(fs1); }
                if (g == 54) TIGHT(per, it, D_1);
                if (g == 78) { per = PER - per; PIN_S(per); }
                if (g == 96) { xp = X; X += per + mg; PIN_S(xp); PIN_S(X); }
                if (g == 111) CVT2(qp[0], qp[1], acc[0]);
                if (g == 112) CVT2(qp[2], qp[3], acc[1]);
                if (g == 113) AND2(qa[0], qa[1], qp[0], qp[1], ma);
                if (g == 114) AND2(qa[2], qa[3], qp[2], qp[3], ma);
                if (g == 115) CVT2(z0, z1, acc[2]);
                if (g == 118) AND2(qz[0], qz[1], z0, z1, ma);
                if (g == 119) AND2(qz[2], qz[3], z0, z1, mb2);
                if (g == 116) AND2(qb[0], qb[1], qp[0], qp[1], mb2);
                if (g == 117) AND2(qb[2], qb[3], qp[2], qp[3], mb2);
                if (g == 120) CVT2(kz[0], kz[1], acc[5]);
                if (g == 121) asm volatile("v_cvt_pk_bf16_f32 %0, %2, %3\n\tv_cvt_pk_bf16_f32 %1, %4, %5" : "=&v"(kz[2]), "=&v"(kz[3]) : "v"(acc[5][0]), "v"(acc[5][1]), "v"(acc[5][2]), "v"(acc[5][3]));
                if (g == 123) *(f32x4*)sSw = s[0];
                if (g == 124) CVT2(ko[0], ko[1], acc[3]);
                if (g == 125) CVT2(ko[2], ko[3], acc[4]);
                if (g == 127) *(f32x4*)(sSw + 1040) = s[1];
                SCHED0();
            });
            asm volatile("s_nop 6" : "+v"(sm) :: "memory");
            *(f32x4*)(sSw + 2080) = sm;
        }
        LDS_BARRIER();
        constexpr float SC = 0.07216878364870322f * 1.4426950408889634f;
        {
            const f32x4 s0 = *(const f32x4*)sSr, s1 = *(const f32x4*)(sSr + SS_W), s2 = *(const f32x4*)(sSr + 2 * SS_W), s3 = *(const f32x4*)(sSr + 3 * SS_W);
            SCHED0();
            f32x4 sv = ((s0 + s1) + s2) + s3;
            u32x2 p;
            p[0] = __float_as_uint(fmaxf(fmaxf(fmaxf(sv[0], sv[1]), sv[2]), sv[3]));
            asm volatile(ROWRED("v_max_f32", "%0", "%1", "v_mul_f32 %2, %6, %2\n\tv_mul_f32 %3, %6, %3\n\t", "v_mul_f32 %4, %6, %4\n\tv_mul_f32 %5, %6, %5\n\t")
                         "v_fma_f32 %2, -%0, %6, %2\n\tv_fma_f32 %3, -%0, %6, %3\n\tv_fma_f32 %4, -%0, %6, %4\n\tv_fma_f32 %5, -%0, %6, %5\n\t"
                         "v_exp_f32 %2, %2\n\tv_exp_f32 %3, %3\n\tv_exp_f32 %4, %4\n\tv_exp_f32 %5, %5\n\t"
                         "v_add_f32 %0, %2, %3\n\tv_add_f32 %0, %0, %4\n\tv_add_f32 %0, %0, %5\n\t"
                         ROWRED("v_add_f32", "%0", "%1", "s_nop 1\n\t", "s_nop 1\n\t")
                         "v_rcp_f32 %0, %0\n\ts_nop 0\n\tv_mul_f32 %2, %0, %2\n\tv_mul_f32 %3, %0, %3\n\tv_mul_f32 %4, %0, %4\n\tv_mul_f32 %5, %0, %5\n\t"
                         "v_cvt_pk_bf16_f32 %0, %2, |%3|\n\tv_cvt_pk_bf16_f32 %1, %4, %5"
                         : "+v"(p[0]), "=&v"(p[1]), "+v"(sv[0]), "+v"(sv[1]), "+v"(sv[2]), "+v"(sv[3]) : "s"(SC));
            sst8(rx, ps, ppos, p);
        }
        SCHED0();
        {
            if (G_LK) { WAITV(1, pvK); nk = __ballot(pvK == 0); }
            if (__builtin_expect(nk != 0, 0)) {
                wave_wait(rx, fK, fk, spins);
#pragma unroll
                for (int e = 0; e < 6; ++e) if (!G_LK) dma(e, qn, kn);
            }
            bf16x8 km[6], q[6];
            *(u32x4*)(sKM + w * 1024) = ko;
            *(u32x2*)(sKX + (SL_KM - SL_KT)) = u32x2{kz[0], kz[1]};
            SCHED0();
#pragma unroll
            for (int f = 0; f < 6; ++f) { q[f] = *(const bf16x8*)(sKT + w * 12288 + 6144 + f * 1024); SCHED0(); if (G_LK) dma(f, qn, kn); SCHED0(); }
            LDA4(xv, fXv, fs);
            dm0(6);
            asm volatile("s_waitcnt lgkmcnt(5)\n\ts_barrier" ::: "memory");
#pragma unroll
            for (int f = 0; f < 6; ++f) { km[f] = *(const bf16x8*)(sKM + f * 1024); SCHED0(); }
            dma(6, qn, kn, false);
            BIAS(sc[2], mval, mval, mval, mval);
            PIN_V(q[5]);
            dma(7, qn, kn, false);
            float mx = max3r(max3r(sc[0][0], sc[0][1], sc[0][2]), max3r(sc[0][3], sc[1][0], sc[1][1]), max2r(sc[1][2], sc[1][3])), tm;
            PIN_V(mx);
#pragma unroll
            for (int f = 0; f < 6; ++f) { MF_VV(sc[2], km[f], q[f]); if (f & 1) dma(8 + f / 2, qn, kn, false); if (f == 4) dm0(10); SCHED0(); }
            asm volatile("s_nop 5" : "+v"(sc[2]));
            mx = max3r(max3r(mx, sc[2][0], sc[2][1]), sc[2][2], sc[2][3]);
            asm volatile("v_mov_b32 %1, %0\n\ts_nop 1\n\tv_permlane16_swap_b32 %0, %1\n\tv_max_f32 %0, %0, %1\n\tv_mov_b32 %1, %0\n\ts_nop 1\n\tv_permlane32_swap_b32 %0, %1\n\tv_max_f32 %0, %0, %1" : "+v"(mx), "=&v"(tm));
            dma(11, qn, kn);
            mx *= -SC;
#pragma unroll
            for (int e = 0; e < 3; ++e) {
#pragma unroll
                for (int r = 0; r < 4; ++r) sc[e][r] = __builtin_fmaf(sc[e][r], SC, mx);
                PIN_V(sc[e]);
            }
#pragma unroll
            for (int e = 0; e < 3; ++e) {
#pragma unroll
                for (int r = 0; r < 4; ++r) sc[e][r] = __builtin_amdgcn_exp2f(sc[e][r]);
            }
            const f32x4 t4 = (sc[0] + sc[1]) + sc[2];
            float sum = (t4[0] + t4[1]) + (t4[2] + t4[3]);
            PIN_V(sum);
            float inv = __builtin_amdgcn_rcpf(red_sum_q(sum));
            PIN_V(inv);
            unsigned u[6];
#pragma unroll
            for (int e = 0; e < 6; ++e) { u[e] = pk2(sc[e >> 1][2 * (e & 1)] * inv, sc[e >> 1][2 * (e & 1) + 1] * inv); if (!(e & 1)) u[e] = nosent(u[e]); }
            const auto a4 = __builtin_amdgcn_permlane16_swap(u[4], u[4], false, false), a5 = __builtin_amdgcn_permlane16_swap(u[5], u[5], false, false);
            sst16(rx, ts, pt2, u32x4{u[4], u[5], a4[1], a5[1]});
            u32x2 o0, o1;
            asm volatile("v_mov_b64 %0, -1\n\tv_mov_b64 %1, -1" : "=v"(o0), "=v"(o1));
            const u32x4 ones = {o0[0], o0[1], o1[0], o1[1]};
            if (rt) { sst16(rx, ts, pt1, u32x4{u[0], u[1], u[2], u[3]}); sst16(rx, tr, pt1, ones); } else { sst8(rx, ts, pt1, u32x2{u[2], u[3]}); sst8(rx, tr, pt1, u32x2{ones[0], ones[1]}); }
            sst8(rx, pr, ppos, u32x2{ones[0], ones[1]});
            sst16(rx, tr, pt2, ones);
            asm volatile("s_waitcnt vmcnt(11)\n\ts_barrier" : "+v"(xv) :: "memory");
            xv = __builtin_amdgcn_readfirstlane(xv);
            adapt(mg, xv, xp, A_MAX);
            X = xv ? xv + per + mg : X;
        }
    }
    SPIN_END;
}

constexpr int CL_R = 0, CL_C = CL_R + 16384, CL_END = CL_C + 4096;
__device__ __forceinline__ void c_role(const MegaArgs& A, char* smem, const int pool, const int ku, const int id) {
    CHAIN_IDS;
    const int cL = pool >> 2, j = 5 * (pool & 3) + ku;
    go.v = sld8(go.rh, go.so, l * 8);
    GO_ON(N_1);
    unsigned fXv = xm(FX_OFF + (pool & 3) * 64, cL);
    PIN_V(fXv);
    const int hi = (l >> 3) & 1, tk = l & 7;
    const unsigned pos = xm(XM_OFF + w * 32768 + (j / 5) * 8192 + (j % 5) * 1024 + (lq * 16 + cL * 8 + tk) * 16 + hi * 8, 0);
    const unsigned umo = xm(UM_OFF + (16 * j + 2 * w) * 1024, cL), po0 = xm(XP_OFF + w * 8192, cL), pto = xm(PT_OFF + w * 16384, cL), uto = xm(UT_OFF + (64 * j + 8 * w) * 1024, cL);
    const unsigned fU = lp(SG_FL + FL_U + (8 * w + tk) * 64 + (pool & 3) * 4);
    const unsigned xo = lp((2 * j + (w >> 1)) * 1024 + (lq * 16 + 8 * (w & 1) + tk) * 16 + hi * 8), sto = l < 8 ? lp(SG_ST + (4 * j + w) * 64 + l * 8) : OOB;
    u32x2 alt;
    { const u32x2 m = *(const u32x2*)(A.mem0 + tk * D_ + 32 * j + 16 * hi + 4 * lq); alt = u32x2{nosent(m[0]), m[1]}; }
    f32x4* sRw0 = (f32x4*)(smem + CL_R) + w * 256 + l;
    f32x4* sRw1 = (f32x4*)(smem + CL_R) + w * 256 + 128 + (l ^ 8);
    const f32x4* sRr = (const f32x4*)(smem + CL_R) + (hi * 2 + (w >> 1)) * 64 + lq * 16 + ((8 * (w & 1) + tk) ^ (8 * hi));
    f32x4* sC = (f32x4*)(smem + CL_C);
    int mb = 0;
    u32x4 ua[16], ub[16], um[4];
#pragma unroll
    for (int e = 0; e < 16; ++e) ua[e] = u32x4{0u, 0u, 0u, 0u};
#pragma unroll
    for (int e = 0; e < 4; ++e) um[e] = u32x4{0u, 0u, 0u, 0u};
    for (int s = 0; s < NTOUCH; ++s) sst8(rx, seg_base(cL, s) + lp(SG_X1), xo, u32x2{0u, 0u});
    unsigned X = rtime() - 2 * PER;
    auto seg_so = [&](int sg) { return (unsigned)sg < NSEG_ ? seg_base(cL, sg) : OOB; };
    int mg = A_0;
    unsigned dx = 0, sb0 = OOB, sb1 = seg_so(-cL * lag), pvU = sld4(rx, sb1, fU), ts = pto + 4 * PT_BUF, ts2 = ts + 16384;
    auto step = [&](const int it, auto LAST, u32x4 (&up)[16], u32x4 (&ut)[16]) {
        constexpr bool last = decltype(LAST)::value == 1, fin = decltype(LAST)::value == 2;
        const int nb = (mb == 2) ? 0 : mb + 1, pb = mb ? mb - 1 : 2, sg = it - cL * lag;
        if (!last) CHAIN_WAIT(pvU, fU, sb1, 0);
        u32x4 pt[16];
        u32x2 xr;
        auto pt_load = [&](int e) { LDA(pt[e], e < 4 || e >= 12 ? l16k : l16b, e < 8 ? ts2 : ts, (e & 3) * 1024); };
        if (!last && E_C) rt_until(X + E_C - dx);
        pt_load(0);
        SCHED0();
        unsigned uo = sb1 != OOB ? uto + ut_slot(sg) : OOB, uo2 = uo + 65536;
        PIN_S(uo); PIN_S(uo2);
        auto ut_load = [&](int e) {
            if (!last) LDA(ut[e], (e & 4) ? l16k : l16b, e < 8 ? uo : uo2, (e & 3) * 1024);
        };
        pt_load(1);
        SCHED0();
        unsigned sbr = (sb0 ^ (cL << 13)) + lp(SG_XR), sbx = sb0 + lp(SG_X1), sb2 = seg_so(sg + 1);
        PIN_S(sbr); PIN_S(sbx); PIN_S(sb2);
        pt_load(2);
        SCHED0();
        unsigned fs = fx_step(it), cu = umo + mb * (2 * UM_BUF), cu2 = cu + 16384, cp = po0 + mb * (2 * XP_BUF), cr = pb * (2 * XM_BUF), cr2 = cr + 8192;
        PIN_S(fs); PIN_S(cu); PIN_S(cu2); PIN_S(cp); PIN_S(cr); PIN_S(cr2);
        pt_load(3);
        SCHED0();
        unsigned cs = sg < 0 ? OOB : nb * (2 * XM_BUF), cs2 = cs + 8192;
        X += PER - dx + mg;
        PIN_S(cs); PIN_S(cs2); PIN_S(X);
#pragma unroll
        for (int e = 4; e < 13; ++e) { pt_load(e); if (e < 12) GAP(); }
        u32x2 st;
        {
            f32x4 d[2][2];
            auto check = [&](auto E0, auto N, auto W) {
                constexpr int e0 = decltype(E0)::value, n = decltype(N)::value;
                auto look = [&](auto WW) {
                    asm volatile("s_waitcnt vmcnt(%2)" : "+v"(pt[e0]), "+v"(pt[e0 + 1]) : "n"(decltype(WW)::value) : "memory");
                    if (n == 4) asm volatile("" : "+v"(pt[e0 + n - 2]), "+v"(pt[e0 + n - 1]));
                    unsigned m = 0;
#pragma unroll
                    for (int e = e0; e < e0 + n; ++e) m = mx4(m, pt[e]);
                    return m; };
                if (__builtin_expect(__ballot(look(W) == ~0u) != 0, 0)) {
#pragma unroll 1
                    for (;;) {
#pragma unroll
                        for (int e = e0; e < e0 + n; ++e) pt_load(e);
                        LIVE;
                        if (!BAD(look(IC<0>{}))) break;
                    }
                }
            };
            sfor<0, 40>([&](auto G) {
                constexpr int g = decltype(G)::value, n = g & 1;
                if constexpr (g == 0) check(IC<0>{}, IC<4>{}, IC<9>{});
                if constexpr (g == 8) check(IC<4>{}, IC<2>{}, IC<10>{});
                if constexpr (g == 16) check(IC<6>{}, IC<2>{}, IC<(last ? 9 : 11)>{});
                if constexpr (g == 24) check(IC<8>{}, IC<4>{}, IC<(last ? 5 : 10)>{});
                if constexpr (g == 32) check(IC<12>{}, IC<2>{}, IC<(last ? 3 : 11)>{});
                if constexpr (g == 36) { check(IC<14>{}, IC<2>{}, IC<(last ? 0 : 10)>{}); PIN_V(xr); }
                if constexpr (g < 4) MF_VV0(d[n][g >> 1], um[2 * n], pt[g >> 1]);
                else if constexpr (g < 8) MF_VV(d[n][(g >> 1) & 1], um[2 * n + 1], pt[g >> 1]);
                else if constexpr (g < 24) {
                    constexpr int hh = (g - 8) >> 1;
                    const u32x4 x = up[8 * n + hh], z = pt[4 + (hh >> 1)];
                    const u32x2 xl = {x[0], x[1]}, zl = {z[2 * (hh & 1)], z[2 * (hh & 1) + 1]};
                    MH_(d[n][0], "v", xl, "v", zl);
                } else MF_VV(d[n][1], up[8 * n + ((g - 24) >> 1)], pt[8 + ((g - 24) >> 1)]);
                constexpr int rq = g < 32 ? ((g & 7) == 2 ? 0 : ((g & 7) == 5 ? 1 : ((g & 7) == 7 ? 2 : -99))) + 3 * (g >> 3) : (g == 33 ? 12 : (g == 35 ? 13 : -99));
                if constexpr (rq >= 0 && rq < 3) pt_load(13 + rq); else if constexpr (rq == 3) LDA8(xr, xo, sbr, 0); else if constexpr (rq > 3) ut_load(rq - 4);
                if (g == 24) sRw0[0] = d[0][0];
                if (g == 27) sRw1[0] = d[1][0];
                SCHED0();
            });
            const f32x4 xf = {lo16(xr[0]), hi16(xr[0]), lo16(xr[1]), hi16(xr[1])};
            asm volatile("" : "+v"(d[0][1]) : "v"(xf) : "memory");
            sRw0[64] = d[0][1];
            ut_load(10);
            asm volatile("s_nop 2" : "+v"(d[1][1]) :: "memory");
            sRw1[64] = d[1][1];
            ut_load(11);
            LDS_BARRIER();
            const f32x4 s0 = sRr[0], s1 = sRr[256], s2 = sRr[512], s3 = sRr[768];
            ut_load(12);
            SCHED0();
            f32x4 s = (((xf + s0) + s1) + s2) + s3;
            sst8(rx, sbx, xo, u32x2{pk2(s[0], s[1]), pk2(s[2], s[3])});
            float sm = (s[0] + s[1]) + (s[2] + s[3]);
            sm += dpp_ror8(sm);
            PIN_V(sm);
            ut_load(13);
            sm = red_sum_q(sm) * (1.0f / 32);
            PIN_V(sm);
            ut_load(14);
            s -= sm;
            float sq = __builtin_fmaf(s[3], s[3], __builtin_fmaf(s[2], s[2], __builtin_fmaf(s[1], s[1], s[0] * s[0])));
            sq += dpp_ror8(sq);
            PIN_V(sq);
            ut_load(15);
            sq = red_sum_q(sq);
            st = u32x2{nosent(__float_as_uint(sm)), __float_as_uint(sq)};
            PIN_V(st);
        }
        SCHED0();
        if (last) { DRAIN(); sst8(rx, sb0, sto, st); return; }
        unsigned xv;
        rt_until(X + E_X);
        LDA4(xv, fXv, fs);
        ts = pto + mb * (2 * PT_BUF); ts2 = ts + 16384;
        PIN_S(ts); PIN_S(ts2);
        asm volatile("s_waitcnt vmcnt(4)" ::: "memory");
        sst8(rx, sb0, sto, st);
        sst8(rx, cr, pos, u32x2{~0u, ~0u});
        sst8(rx, cr2, pos, u32x2{~0u, ~0u});
        WAITV(3, xv);
        const unsigned xp = X;
        X = __builtin_amdgcn_readfirstlane(xv);
        PIN_S(X);
        if (__builtin_expect(X == 0, 0)) {
#pragma unroll 1
            for (;;) {
                LDA4(xv, fXv, fs);
                LIVE;
                WAITV(0, xv);
                X = __builtin_amdgcn_readfirstlane(xv);
                if (X || !live_) break;
            }
        }
        unsigned d2;
        TIGHT(dx, it, D_1); TIGHT(d2, it, D_2);
        adapt(mg, X, xp, A_MAX);
        d2 -= mg >> 1;
        if (fin) {
#pragma unroll 1
            for (;;) {
                rt_until(X + E_U - d2);
#pragma unroll
                for (int i = 0; i < 4; ++i) LDI(um[i], l16b, (i & 2) ? cu2 : cu, (i & 1) * 1024);
                LIVE;
                asm volatile("s_waitcnt vmcnt(0)" : "+v"(um[0]), "+v"(um[1]), "+v"(um[2]), "+v"(um[3]) :: "memory");
                if (!BAD(mx4(mx4(mx4(mx4(0u, um[0]), um[1]), um[2]), um[3]))) break;
            }
            rt_until(X + E_L);
            mb = nb; sb0 = sb1; sb1 = sb2;
            return;
        }
        u32x4 p[5];
        f32x4 a[2], b[2];
        auto take2 = [&](u64 live_) {
            unsigned m = 0;
            u64 bad;
#pragma unroll
            for (int i = 0; i < 4; ++i) { WAITV(9 - PVU_E - i, um[i]); m = mx4(m, um[i]); PIN_V(m); }
#pragma unroll
            for (int i = 0; i < 5; ++i) {
                WAITV(4 - i, p[i]);
#pragma unroll
                for (int e = 0; e < 4; ++e) {
                    f32x4& d = (e & 1) ? b[e >> 1] : a[e >> 1];
                    if (i == 0) MF_VV0(d, ut[8 * (e >> 1) + (e & 1)], p[0]); else if (i < 4) MF_VV(d, ut[8 * (e >> 1) + 2 * (i & 3) + (e & 1)], p[i]); else MF_VV(d, um[e], p[4]);
                    SCHED0();
                    if (e == 0) { m = mx4(m, p[i]); if (i < 4) PIN_V(m); else { bad = __ballot(m == ~0u); PIN_S(bad); } }
                    if (e == 1 && i == 4) { bad &= live_; PIN_S(bad); }
                    SCHED0();
                }
            }
            return bad;
        };
        {
            unsigned tP = X + E_P - d2, tU = X + E_U - d2 - E_M * (4 - ku), ask = 1;
            PIN_S(ask);
            if (PVU_E) LDA4(pvU, fU, sb2);
            rt_until(tU);
#pragma unroll 1
            for (;;) {
#pragma unroll
                for (int i = 0; i < 4; ++i) LDI(um[i], l16b, (i & 2) ? cu2 : cu, (i & 1) * 1024);
                if (!PVU_E) LDA4(pvU, fU, sb2);
                LIVE;
                if (__builtin_expect(ask | !FASTR, 1)) rt_fine(tP);
                LD5(p, cp);
                ask = 0; PIN_S(ask);
                SCHED0();
                if (!take2(live_)) break;
            }
        }
        f32x4 x;
        asm volatile("v_add_f32_dpp %0, %8, %4 row_ror:8 row_mask:0xf bank_mask:0x3\n\tv_add_f32_dpp %1, %9, %5 row_ror:8 row_mask:0xf bank_mask:0x3\n\t"
                     "v_add_f32_dpp %2, %10, %6 row_ror:8 row_mask:0xf bank_mask:0x3\n\tv_add_f32_dpp %3, %11, %7 row_ror:8 row_mask:0xf bank_mask:0x3"
                     : "=&v"(x[0]), "=&v"(x[1]), "=&v"(x[2]), "=&v"(x[3]) : "v"(a[0][0]), "v"(a[0][1]), "v"(a[0][2]), "v"(a[0][3]), "v"(b[0][0]), "v"(b[0][1]), "v"(b[0][2]), "v"(b[0][3]));
        asm volatile("s_nop 0\n\tv_add_f32_dpp %0, %8, %4 row_ror:8 row_mask:0xf bank_mask:0xc\n\tv_add_f32_dpp %1, %9, %5 row_ror:8 row_mask:0xf bank_mask:0xc\n\t"
                     "v_add_f32_dpp %2, %10, %6 row_ror:8 row_mask:0xf bank_mask:0xc\n\tv_add_f32_dpp %3, %11, %7 row_ror:8 row_mask:0xf bank_mask:0xc"
                     : "+v"(x[0]), "+v"(x[1]), "+v"(x[2]), "+v"(x[3]) : "v"(b[1][0]), "v"(b[1][1]), "v"(b[1][2]), "v"(b[1][3]), "v"(a[1][0]), "v"(a[1][1]), "v"(a[1][2]), "v"(a[1][3]));
        sC[w * 64 + l] = x;
        LDS_BARRIER();
        const f32x4 s = ((sC[l] + sC[64 + l]) + sC[128 + l]) + sC[192 + l];
        const u32x2 r = {nosent(pk2(s[0], s[1])), pk2(s[2], s[3])};
        sst8(rx, cs, pos, r);
        sst8(rx, cs2, pos, r);
        SCHED0();
        if (__builtin_expect(cs == OOB, 0)) { sst8(rx, nb * (2 * XM_BUF), pos, alt); sst8(rx, nb * (2 * XM_BUF) + 8192, pos, alt); }
        PIN_V(pvU);
        mb = nb; sb0 = sb1; sb1 = sb2;
    };
    int it = 0;
    PIN_S(spins); PIN_V(pvU); PIN_V(alt);
#pragma unroll 1
    do { step(it, IC<0>{}, ua, ub); step(it + 1, IC<0>{}, ub, ua); it += 2; } while (it + 2 < itend);
    if (LAGV & 1) { step(it, IC<2>{}, ua, ub); step(it + 1, IC<1>{}, ub, ua); } else { step(it, IC<0>{}, ua, ub); step(it + 1, IC<2>{}, ub, ua); step(it + 2, IC<1>{}, ua, ub); }
    SPIN_END;
}

constexpr int VL_V = 20480, VL_W = VL_V + 6144, VL_END = VL_W + 81920;
__device__ __forceinline__ void vm_role(const MegaArgs& A, char* smem, const int h, const int id) {
    CHAIN_IDS;
    const int pool = h >> 2;
    bf16x8 wf[3][20], wo[10][4];
    char* sWo = smem + VL_W + w * 20480 + l * 16;
    {
        bf16x8 tmp[20];
        load_vo<5>(smem, w, l, go, A.Wv + h * DH_, A.Wo + (size_t)(h * DH_) * D_, wf, [&](int k, int ct, int ww) -> bf16x8& { return k < 4 ? wo[ct][(k - ww) & 3] : tmp[4 * (ct >> 1) + 2 * (k & 1) + (ct & 1)]; });
#pragma unroll
        for (int i = 0; i < 20; ++i) *(bf16x8*)(sWo + i * 1024) = tmp[i];
    }
    GO_ON(N_1);
    CHAIN_STAGE;
    unsigned upos[5];
#pragma unroll
    for (int m = 0; m < 5; ++m) upos[m] = xm(UM_OFF + ((10 * w + 2 * m + (lq & 1)) * 8 + (h >> 2)) * 1024 + ((h & 3) * 16 + l16) * 16, lq >> 1);
    char* sV = smem + VL_V + l * 16;
    const char* sV1 = sV + ((w + 1) & 3) * 1024; const char* sV2 = sV + ((w + 2) & 3) * 1024; const char* sV3 = sV + ((w + 3) & 3) * 1024;
    const unsigned xw = (h == 1 && t < 8) ? xm(FX_OFF + (t & 3) * 64, t >> 2) : OOB;
    int mb = 0;
    unsigned X = rtime() - 2 * PER, dx = 0;
    int mg = A_0, cnt = 0;
    for (int it = 0; it < itend; ++it) {
        const int nb = (mb == 2) ? 0 : mb + 1, pb = mb ? mb - 1 : 2;
        unsigned mo = mo0 + mb * (2 * XM_BUF), fs = fx_step(it), us = mb * (2 * UM_BUF), ur = pb * (2 * UM_BUF);
        X += PER - dx + mg;
        const unsigned xp = X;
        PIN_S(mo); PIN_S(fs); PIN_S(us); PIN_S(ur); PIN_S(X);
        rt_fine(X - E_G);
        f32x4 acc[3];
        u32x4 v[5];
        auto take = [&](u64 live_) {
            unsigned m = 0;
            u64 bad;
            sfor<0, 11>([&](auto G) {
                constexpr int g = decltype(G)::value, e = g < 9 && !(g & 1) ? g / 2 : -1;
                if constexpr (e >= 0) { WAITV(4 - e, v[e]); *(u32x4*)(sg0 + e * 1024) = v[e]; SCHED0(); }
                if (g < 3) MF_AV0(acc[g % 3], wf[g % 3][0], v[0]); else MF_AV(acc[g % 3], wf[g % 3][g / 3], v[g / 3]);
                SCHED0();
                if constexpr (e >= 0) { m = mx4(m, v[e]); if (e < 4) PIN_V(m); else { bad = __ballot(m == ~0u); PIN_S(bad); } }
                if (g == 9) { bad &= live_; PIN_S(bad); }
                SCHED0();
            });
            return bad;
        };
        GATHER(take);
        bf16x8 wl[4];
        u32x4 vf[6], o;
        f32x4 d[10];
        unsigned xv;
        {
            constexpr int MW = 5;
            bf16x8 mm[MW];
            u32x2 vz;
            u64 tb64;
            unsigned t0, t1;
            asm volatile("s_waitcnt lgkmcnt(0)\n\ts_barrier\n\ts_memrealtime %0" : "=s"(tb64) :: "memory");
            mm[0] = mrd(5);
            SCHED0();
            sfor<11, 60>([&](auto G) {
                constexpr int g = decltype(G)::value, i = g < 45 ? g / 3 : (g < 50 ? g - 30 : 15 + (g - 50) / 2), tt = g < 45 ? g % 3 : (g < 50 ? 2 : g & 1);
                const bf16x8 xb = i < 5 ? asb8(v[i < 5 ? i : 0]) : mm[(i < 5 ? 0 : i - 5) % MW];
                MF_AV(acc[tt], wf[tt][i], xb);
                SCHED0();
                if constexpr (g < 15) mm[g - 10] = mrd(g - 5);
                if constexpr (g >= 18 && g < 46 && g % 3 == 0) mm[(g / 3 - 6) % MW] = mrd(g / 3 + 4);
                if constexpr (g >= 54 && g < 58) wl[g - 54] = *(const bf16x8*)(sWo + (g - 54) * 1024);
                if (g == 17) asm volatile("s_waitcnt lgkmcnt(0)" : "+s"(tb64) :: "memory");
                if (g == 19) asm volatile("s_sub_u32 %0, %2, %3\n\ts_add_u32 %1, %2, %4" : "=&s"(t0), "=&s"(t1) : "s"((unsigned)tb64), "s"(X), "s"(E_S) : "scc");
                if (g == 22) asm volatile("s_cmp_gt_i32 %1, %3\n\ts_cselect_b32 %0, %2, %0" : "+s"(X) : "s"(t0), "s"(t1), "s"(E_T) : "scc");
                if (g == 23) asm volatile("v_or_b32 %0, 1, %1" : "=v"(xv) : "s"(X));
                if (g == 25) sst4(rx, fs, xw, xv);
                if (g == 52) CVT2(vz[0], vz[1], acc[2]);
                if (g == 53) *(u32x2*)(sV + 4096 + (w >> 1) * 1024 + (w & 1) * 8) = vz;
                SCHED0();
            });
        }
        asm volatile("s_nop 2");
        SCHED0();
        CVT2(vf[0][0], vf[0][1], acc[0]);
        SCHED0();
        asm volatile("s_nop 1");
        SCHED0();
        CVT2(vf[0][2], vf[0][3], acc[1]);
        *(u32x4*)(sV + w * 1024) = vf[0];
        SCHED0();
        asm volatile("s_nop 0");
        sfor<0, 67>([&](auto G) {
            constexpr int g = decltype(G)::value, m = g < 10 ? 0 : (g - 10) / 10, r = g < 10 ? 0 : (g - 10) % 10, kk = g < 10 ? 0 : 1 + r / 2, ct = g < 10 ? g : 2 * m + (r & 1);
            if constexpr (g < 60) {
                if constexpr (kk >= 4) MF_VV(d[ct], vf[kk], wl[2 * (kk & 1) + (r & 1)]);
                else if constexpr (ct == 0) { if (kk == 0) MF_VA0(d[0], vf[0], wo[0][0]); else MF_VA(d[0], vf[kk], wo[0][kk]); }
                else { if (kk == 0) MF_VV0(d[ct], vf[0], wo[ct][0]); else MF_VV(d[ct], vf[kk], wo[ct][kk]); }
            }
            SCHED0();
            if (g == 3) { LDS_BARRIER(); }
            if (g == 4) vf[1] = *(const u32x4*)sV1;
            if (g == 5) vf[2] = *(const u32x4*)sV2;
            if (g == 6) vf[3] = *(const u32x4*)sV3;
            if (g == 7) vf[4] = *(const u32x4*)(sV + 4096);
            if (g == 8) vf[5] = *(const u32x4*)(sV + 5120);
            if constexpr (g >= 17 && g < 51 && (g - 17) % 10 < 4) wl[(g - 17) % 10] = *(const bf16x8*)(sWo + ((g - 17) / 10 * 4 + 4 + (g - 17) % 10) * 1024);
            if (g == (XV_L ? 66 : 12)) { if (XV_L) sst16(rx, us, upos[4], o); LDA4(xv, fXv, fs); }
            if constexpr (m > 0) {
                if (g == 60) asm volatile("s_nop 3");
                if (r == 1) CVT2(o[0], o[1], d[2 * m - 2]);
                SCHED0();
                if (g == 61) asm volatile("s_nop 1");
                if (r == 2) CVT2(o[2], o[3], d[2 * m - 1]);
                if (r == 3) { o[0] = nosent(o[0]); o[2] = nosent(o[2]); asm volatile("" : "+v"(o[0]), "+v"(o[2])); }
                if (r == 4) asm volatile("v_permlane16_swap_b32 %0, %1" : "+v"(o[1]), "+v"(o[3]));
                if (r == 5) asm volatile("v_permlane16_swap_b32 %0, %1" : "+v"(o[0]), "+v"(o[2]));
                if (r == 6 && (m < 5 || !XV_L)) sst16(rx, us, upos[m - 1], o);
            }
            SCHED0();
        });
        if (E_V) rt_until(X + E_V);
#pragma unroll
        for (int m = 0; m < 5; ++m) { __builtin_amdgcn_s_sleep(VSLP); sst16(rx, ur, upos[m], u32x4{~0u, ~0u, ~0u, ~0u}); SCHED0(); }
        WAITV(XV_L ? 5 : 10, xv);
        xv = __builtin_amdgcn_readfirstlane(xv);
        X = xv ? xv : X;
        cnt = adapt(mg, X | 1, xp, A_MAX) && it > 3 ? max(cnt, 0) + A_UP : cnt - 1;
        PIN_S(cnt);
        if (cnt >= A_K) { mg = min(mg + 2, A_MAX); cnt -= A_UP; }
        PIN_S(mg); PIN_S(cnt);
        if (cnt <= -A_Q) { mg = max(mg - 2, 0); cnt = 0; }
        PIN_S(mg); PIN_S(cnt);
        TIGHT(dx, it, D_1);
        mb = nb;
    }
    SPIN_END;
}
#define DBG_LD 0
#define WORK_IDS const int t = threadIdx.x, l = t & 63, l16 = l & 15, lq = l >> 4; const int w = __builtin_amdgcn_readfirstlane(t >> 6); \
    GO_DECL(id >> 5); const unsigned l16b = l * 16, dvo = w * 2048 + l * 16 + DBG_LD, lds0 = lds_addr(smem) + w * 2048; unsigned spins; SZERO(spins)
#define XSET(so, base, ld) asm volatile("s_mov_b32 m0, %2\n\ts_mov_b32 %0, %1" : "=s"(so) : "s"(base), "s"(ld) : "m0")
#define XADV(so) asm volatile("s_add_u32 m0, m0, 0x2000\n\ts_add_u32 %0, %0, 0x4000" : "+s"(so) :: "m0", "scc")
#define XDMA(so, rt) asm volatile("buffer_load_dwordx4 %0, %1, %2 offen offset:%3 sc1 lds" :: "v"(dvo), "s"(rx), "s"(so), "n"((rt) * 1024) : "memory")
#define XALL(so, base, ld) _Pragma("unroll") for (int e_ = 0; e_ < 10; ++e_) { if (e_ % 5 == 0) XSET(so, base, ld); else XADV(so); XDMA(so, e_ / 5); }
#define XALL2(so, base, ld) _Pragma("unroll") for (int e_ = 0; e_ < 5; ++e_) { if (e_ == 0) XSET(so, base, ld); else XADV(so); XDMA(so, 0); XDMA(so, 1); }
#define VMB(n) asm volatile("s_waitcnt vmcnt(%0)\n\ts_barrier" :: "n"(n) : "memory")
#define VMB_LATE(k) asm volatile("s_cmp_lg_u32 %0, %1\n\ts_cbranch_scc1 1f\n\ts_waitcnt vmcnt(%2)\n1:\n\ts_barrier" :: "s"(xs), "n"(OOB), "n"(k) : "memory", "scc")
#define VMW(n) asm volatile("s_waitcnt vmcnt(%0)" :: "n"(n) : "memory")
#define NEXT_DECL(asked) const unsigned fXo = l < 4 ? lp(SG_FL + FL_X + ((h & 12) + l) * 128) : OOB, fx1 = (((TR >> 2) & ~L & 1) ^ hside(h)) << 13; const int dl = __builtin_amdgcn_readfirstlane((L ^ 1) * TR), ld0 = __builtin_amdgcn_readfirstlane(L ? -K_W : K_0); \
    unsigned xb = seg_base(0, 0), xs, fb = seg_base(hside(h), 0), msk, so, pv, pend, fXv = xm(FX_OFF + ((h >> 2) & 3) * 64, hside(h)), xv, fs; PIN_V(fXv); if (L) wave_wait(rx, fXo, fb, spins); if (!(asked)) { XALL(so, xb, lds0); } VMB(5); int s; SZERO(s)
#define G_L1 150
#define G_E1 182
#define G_L2 210
#define NEXT_ADDR(a, st) \
    if (g == a) { s = __builtin_amdgcn_readfirstlane(s); PIN_S(s); xb = (s + 1) << 19; PIN_S(xb); } \
    if (g == a + st) { xs = (xb >> 8) & 8192; PIN_S(xs); } \
    if (g == a + 2 * st) { xb = (xb | xs) + 2 * SG_OFF; PIN_S(xb); } \
    if (g == a + 3 * st) { fb = (xb ^ fx1) - (dl << 19); PIN_S(fb); } \
    if (g == a + 5 * st) asm volatile("s_cmp_lt_i32 %1, %2\n\ts_cselect_b32 %0, 0, 15" : "=s"(msk) : "s"(s), "s"(dl - 1) : "scc"); \
    if (g == a + 6 * st) asm volatile("s_cmp_lt_i32 %1, 63\n\ts_cselect_b32 %0, %0, %2" : "+s"(fb) : "s"(s), "n"(OOB) : "scc"); \
    if (g == a + 7 * st) { PIN_S(s); fs = (s - ld0) << 8; PIN_S(fs); } \
    if (g == a + 8 * st) { fs += fs & ~8191u; PIN_S(fs); } \
    if (g == a + 9 * st) asm volatile("s_cmp_lt_i32 %0, 0\n\ts_cselect_b32 %0, %1, %0" : "+s"(fs) : "n"(OOB) : "scc")
#define NEXT_LOOK(n, ld) \
    if (g == G_L1 || g == G_L2) LDA4(pv, fXo, fb); \
    if (g == G_E1) { WAITV(n, pv); xs = (unsigned)__ballot(pv == 0); PIN_S(xs); } \
    if (g == G_E1 + 2) asm volatile("s_and_b32 %0, %0, %2\n\ts_cselect_b32 %0, %3, %1" : "+s"(xs) : "s"(xb), "s"(msk), "n"(OOB) : "scc")
#define NEXT_GO(n, ld, fl) \
    WAITV(n, pv); \
    if (xs != OOB) { asm volatile("s_barrier" ::: "memory"); prime(); SCHED0(); tail(); LDA4(xv, fXv, fs); WAITV(0, xv); sst4(rx, pend, fl, 1u); } \
    else { \
        tail(); \
        if ((unsigned)__ballot(pv == 0) & msk) { DRAIN(); sst4(rx, pend, fl, 1u); if (s == NSEG_ - 1) { SPIN_END; __builtin_amdgcn_endpgm(); } pend = OOB; wave_wait(rx, fXo, fb, spins); } \
        LDA4(xv, fXv, fs); \
        XALL(so, xb, ld); \
        WAITV(10, xv); sst4(rx, pend, fl, 1u); \
        VMB(6); prime(); \
    } \
    so = __builtin_amdgcn_readfirstlane(xv); \
    if (__builtin_expect(so == 0, 0) && fs != OOB) { \
        _Pragma("unroll 1") for (int k_ = 0; k_ < 8 && so == 0; ++k_) { __builtin_amdgcn_s_sleep(4); so = __builtin_amdgcn_readfirstlane(sld4(rx, fs, fXv)); MEMFENCE(); } \
    } \
    so += E_W; PIN_S(so); rt_until(so)

constexpr int QL_W = 81920;
__device__ __forceinline__ void qk_role(const MegaArgs& A, char* smem, const int L, const int h, const int id) {
    WORK_IDS;
    bf16x8 wf[6][20];
    char* sW = smem + QL_W + w * 20480 + l * 16;
    unsigned x0;
    load_qk<0, 11>(smem, w, l, go, A.Wq + h * DH_, A.Wk + h * DH_, [&](int m) { return 6 * w + m; }, wf, [&]() {
        const unsigned tag = (__builtin_amdgcn_readlane(go.v[0], 63) + 2) & ~1u, a0 = __builtin_amdgcn_readlane(go.v[0], HS_A / 8), a1 = __builtin_amdgcn_readlane(go.v[1], HS_A / 8);
        const rsrc_t rx = arena(A, a0 & 1);
        x0 = ((a0 ^ tag) | (a1 ^ tag) | 2 * L) > 1 ? OOB : seg_base(0, 0);
        PIN_S(x0);
        unsigned so;
        XALL(so, x0, lds0);
        GO_ASK;
    });
#pragma unroll
    for (int i = 0; i < 20; ++i) *(bf16x8*)(sW + i * 1024) = wf[5][i];
    GO_ON(N_1);
    const unsigned fI = l == 0 ? xm(FI_OFF + (h & 15) * 64 + (4 * L + w) * 4, hside(h)) : OOB;
    const unsigned po0 = 2 * QK_OFF + ((L * 16 + (h & 15)) * 4 + (w & 2)) * 16384 + (w & 1) * 3072 + hside(h) * 8192;
    const int it0 = L * A.lag;
    const unsigned l16s = l16b;
    NEXT_DECL(x0 != OOB);
    unsigned vx = l16b, ld = lds0, po;
    bf16x8 xw[3], wl[2];
    f32x4 acc[2][6];
    u32x4 o[2];
    auto prime = [&]() { xw[0] = *(const bf16x8*)(smem + vx); xw[1] = *(const bf16x8*)(smem + vx + 2048); };
    auto tail = [&]() {
#pragma unroll
        for (int f = 0; f < 3; ++f) { CVT2(o[f & 1][0], o[f & 1][1], acc[1][2 * f]); CVT2(o[f & 1][2], o[f & 1][3], acc[1][2 * f + 1]); sst16(rx, po, l16s + f * 1024, o[f & 1]); SCHED0(); }
    };
    prime();
    wl[0] = *(const bf16x8*)sW;
#pragma unroll 1
    for (;; ++s) {
        SCHED0();
        sfor<0, 240>([&](auto G) {
            constexpr int g = decltype(G)::value, i = g / 6, tt = g % 6, ks = i % 20;
            f32x4& d = acc[i / 20][tt];
            if constexpr (tt == 5) { if (ks == 0) MF_VV0(d, wl[i & 1], xw[i % 3]); else MF_VV(d, wl[i & 1], xw[i % 3]); }
            else if constexpr (20 * tt + ks < 64) { if (ks == 0) MF_AV0(d, wf[tt][ks], xw[i % 3]); else MF_AV(d, wf[tt][ks], xw[i % 3]); }
            else { if (ks == 0) MF_VV0(d, wf[tt][ks], xw[i % 3]); else MF_VV(d, wf[tt][ks], xw[i % 3]); }
            SCHED0();
            if constexpr (tt == 1 && i < 38) xw[(i + 2) % 3] = *(const bf16x8*)(smem + vx + (i + 2) % 20 * 2048 + (i + 2) / 20 * 1024);
            if constexpr (tt == 3) wl[~i & 1] = *(const bf16x8*)(sW + (i + 1) % 20 * 1024);
            NEXT_ADDR(2, 6);
            if (g == 4) { PIN_S(s); po = (s & (TR - 1)) << 21; PIN_S(po); }
            if (g == 10) { so = (po >> 8) & 8192; PIN_S(so); }
            if (g == 16) { po = (po0 ^ so) + po; PIN_S(po); }
            if (g == 58) { VMW(0); }
            if (g == 60) asm volatile("s_barrier" ::: "memory");
            if (g == 62) { PIN_S(s); pend = s + it0; so = pend & 7; PIN_S(pend); PIN_S(so); }
            if (g == 64) { pend = (pend >> 3) << 4; PIN_S(pend); }
            if (g == 66) { pend = (pend | so) << 10; PIN_S(pend); }
            if (g == 68) { ld = 2 * lds0 + 40960 - ld; PIN_S(ld); }
            if constexpr (g >= 122 && g < 140) {
                constexpr int f = (g - 122) / 6;
                if (tt == 2) CVT2(o[0][0], o[0][1], acc[0][2 * f]);
                if (tt == 4) CVT2(o[0][2], o[0][3], acc[0][2 * f + 1]);
                if (tt == 0) sst16(rx, po, l16s + f * 1024, o[0]);
            }
            if (g == 140) { po += 16384; PIN_S(po); }
            NEXT_LOOK(0, ld);
            {
                constexpr int a[10] = {186, 190, 194, 198, 202, 206, 214, 218, 222, 226};
#pragma unroll
                for (int e = 0; e < 10; ++e) {
                    if (g == a[e]) { if (e % 5 == 0) XSET(so, xs, ld); else XADV(so); }
                    if (g == a[e] + (e == 5 ? 6 : 2)) XDMA(so, e / 5);
                }
            }
            if (g == 236) { vx ^= 40960; PIN_V(vx); }
            SCHED0();
        });
        NEXT_GO(5, ld, fI);
    }
}

constexpr int OL_V = 40960, OL_W = OL_V + 12288;
__device__ __forceinline__ void vo_role(const MegaArgs& A, char* smem, const int L, const int h, const int id) {
    WORK_IDS;
    bf16x8 wf[3][20], wo[10][6];
    char* sW = smem + OL_W + w * 24576 + l * 16;
    {
        load_vo<0>(smem, w, l, go, A.Wv + h * DH_, A.Wo + (size_t)(h * DH_) * D_, wf, [&](int k, int ct, int) -> bf16x8& { return wo[ct][k]; });
#pragma unroll
        for (int i = 0; i < 24; ++i) *(bf16x8*)(sW + i * 1024) = wo[4 + i / 8 * 2 + (i & 1)][2 + (i >> 1 & 3)];
    }
    GO_ON(N_1);
    const unsigned fU = l == 0 ? lp(SG_FL + FL_U + h * 64 + w * 4) : OOB;
    const unsigned l16s = l16b;
    const unsigned ua = xm(UT_OFF + (320 * w + h) * 1024, L) + l16s, ub = ua ^ (w == 3 ? 8192 : 0);
    char* sV = smem + OL_V + l * 16;
    char* sVw = sV + w * 1024;
    char* sVh = sV + 4096 + (w >> 1) * 1024 + (w & 1) * 8;
    NEXT_DECL(false);
    unsigned uo;
    constexpr int XW = 4;
    bf16x8 xw[XW], wl[4];
    f32x4 d[4][2];
    u32x4 o[2];
    auto prime = [&]() {
#pragma unroll
        for (int i = 0; i < XW - 1; ++i) xw[i] = *(const bf16x8*)(smem + l16b + i * 2048);
    };
    auto tail = [&]() {
#pragma unroll
        for (int f = 0; f < 2; ++f) { CVT2(o[f][0], o[f][1], d[f][0]); CVT2(o[f][2], o[f][3], d[f][1]); sst16(rx, uo + f * 65536, ub, o[f]); SCHED0(); }
    };
    prime();
#pragma unroll 1
    for (;; ++s) {
        f32x4 acc[2][3];
        u32x4 vf[2][6];
        unsigned cur = xb;
        SCHED0();
        sfor<0, 240>([&](auto G) {
            constexpr int g = decltype(G)::value;
            constexpr int rt = g < 120 ? g / 60 : (g < 144 ? 0 : (g < 168 ? 1 : g & 1));
            constexpr int ct = g < 144 ? g & 3 : (g < 168 ? (g - 144) / 12 * 2 + (g & 1) : (g - 120) / 24 * 2 + (g >> 1 & 1));
            constexpr int kk = g < 144 ? (g + 24) % 24 / 4 : (g < 168 ? g % 12 / 2 : g % 24 / 4), wi = (g - 168) / 24 * 8 + (g % 24 - 8) / 2;
            if constexpr (g < 120) { if (g % 60 < 3) MF_AV0(acc[rt][g % 3], wf[g % 3][0], xw[g / 3 % XW]); else MF_AV(acc[rt][g % 3], wf[g % 3][g % 60 / 3], xw[g / 3 % XW]); }
            else if constexpr (ct >= 4 && kk >= 2) MF_VV(d[ct & 3][rt], vf[rt][kk], wl[wi & 3]);
            else if constexpr (ct == 0 && kk < 4) { if (kk == 0) MF_VA0(d[0][rt], vf[rt][0], wo[0][0]); else MF_VA(d[0][rt], vf[rt][kk], wo[0][kk]); }
            else { if (kk == 0) MF_VV0(d[ct & 3][rt], vf[rt][0], wo[ct][0]); else MF_VV(d[ct & 3][rt], vf[rt][kk], wo[ct][kk]); }
            SCHED0();
            if constexpr (g % 3 == 1 && g / 3 + XW - 1 < 40) { constexpr int i = g / 3 + XW - 1; xw[i % XW] = *(const bf16x8*)(smem + l16b + i % 20 * 2048 + i / 20 * 1024); }
            NEXT_ADDR(2, 3);
            if (g == 24) { PIN_S(s); uo = (s & (TR - 1)) << 22; PIN_S(uo); }
            if (g == 48) { VMW(0); }
            if (g == 50) asm volatile("s_barrier" ::: "memory");
            if (g == 51) { pend = cur ^ (L << 13); PIN_S(pend); }
            if constexpr (g >= 60 && g < 140 && g % 60 < 40) {
                constexpr int r = g / 60 - 1, q = r ? g - 120 : (g % 3 == 1 ? -1 : (g - 60) / 3 * 2 + g % 3 / 2);
                if (q == 1) CVT2(o[0][0], o[0][1], acc[r][0]);
                if (q == 2) CVT2(o[0][2], o[0][3], acc[r][1]);
                if (q == 3) CVT2(o[1][0], o[1][1], acc[r][2]);
                if (q == 4) *(u32x4*)(sVw + r * 6144) = o[0];
                if (q == 5) *(u32x2*)(sVh + r * 6144) = u32x2{o[1][0], o[1][1]};
                if (q == 11) asm volatile("s_waitcnt lgkmcnt(%0)\n\ts_barrier" :: "n"(r ? 0 : 3) : "memory");
                if constexpr (q >= 12 && q < 18) vf[r][q - 12] = *(const u32x4*)(sV + r * 6144 + (q - 12) * 1024);
            }
            if constexpr (g >= 168 && g % 24 < 18 && g % 24 != 8 && !(g & 1)) {
                constexpr int j = (g - 168) / 24 * 8 + g % 24 / 2 - (g % 24 > 8);
                wl[j & 3] = *(const bf16x8*)(sW + j * 1024);
            }
            if constexpr (g >= 156) {
                constexpr int p = g < 168 ? 0 : (g - 144) / 24, q = g < 168 ? g - 156 : g % 24, c0 = 2 * p & 3;
                if (q == 1) CVT2(o[0][0], o[0][1], d[c0][0]);
                if (q == 3) CVT2(o[0][2], o[0][3], d[c0][1]);
                if (q == 5) sst16(rx, uo, p ? ub : ua, o[0]);
                if (q == 7) CVT2(o[1][0], o[1][1], d[c0 + 1][0]);
                if (q == 8) { uo += 65536; PIN_S(uo); }
                if (q == 9) CVT2(o[1][2], o[1][3], d[c0 + 1][1]);
                if (q == 11) sst16(rx, uo, p ? ub : ua, o[1]);
            }
            if constexpr (g == 168 || g == 181 || g == 205 || g == 229) { uo += 65536; PIN_S(uo); }
            NEXT_LOOK(4, lds0);
            {
                constexpr int a[10] = {186, 189, 192, 200, 205, 209, 213, 216, 224, 231}, b[10] = {188, 191, 196, 204, 207, 212, 215, 220, 228, 233};
#pragma unroll
                for (int e = 0; e < 10; ++e) {
                    if (g == a[e]) { if (e % 5 == 0) XSET(so, xs, lds0); else XADV(so); }
                    if (g == b[e]) XDMA(so, e / 5);
                }
            }
            SCHED0();
        });
        NEXT_GO(7, lds0, fU);
    }
}

#define G_B 83
#define BSLP 6
constexpr int BL_Q = 40960, BL_P = BL_Q + 49152;
__device__ __forceinline__ void b_role(const MegaArgs& A, char* smem, const int L, const int hb, const int id) {
    WORK_IDS;
    const unsigned t0 = rtime();
    bf16x8 wf[4][20];
    unsigned dst[4];
    const int sw = (l16 & 6) << 3;
    {
        const void* const base[4] = {A.wqkv + 80 * hb, A.wqkv + 80 * hb, A.wqkv + 80 * hb, A.wqkv + 80 * hb};
        unsigned vo[4];
        int rp[4];
#pragma unroll
        for (int m = 0; m < 4; ++m) {
            const int idx = min((4 * m + w) * 64 + l, 959), c = idx % 30, T = min(4 * m + w, 14), e = 16 * (T % 5) + 4 * lq;
            vo[m] = (idx / 30) * 3840 + (c / 10) * (2 * D_) + (c % 10) * 16;
            rp[m] = (4 * lq + (l16 >> 2)) * 480 + 32 * T + (l & 3) * 8;
            dst[m] = lds_addr(smem) + BL_Q + (T / 5 * 4 + e / 20) * 4096 + l16 * 64 + ((e % 20 * 2) ^ sw);
        }
        while (rtime() - t0 < (L ? DLYB1 : DLYB0)) __builtin_amdgcn_s_sleep(8);
        load_coop<4, 0, 4>(smem, w, go, base, vo, 32 * 3840, rp, 7680, wf);
    }
    GO_ON(N_1);
    f32x4 cs[4];
    u32x4 one = {0x3f803f80u, 0x3f803f80u, 0x3f803f80u, 0x3f803f80u};
    {
        asm volatile("s_nop 1" : "+v"(one));
        sfor<0, 80>([&](auto G) { constexpr int g = decltype(G)::value, tt = g & 3, ks = g >> 2;
            if (ks == 0) MF_AV0(cs[tt], wf[tt][0], one); else if (tt < 3 || ks < 4) MF_AV(cs[tt], wf[tt][ks], one); else MF_VV(cs[tt], wf[tt][ks], one); });
        asm volatile("s_nop 7" : "+v"(cs[0]), "+v"(cs[1]), "+v"(cs[2]), "+v"(cs[3]));
    }
#pragma unroll
    for (int i = 0; i < 12; ++i) *(u32x4*)(smem + BL_Q + i * 4096 + t * 16) = u32x4{0u, 0u, 0u, 0u};
    const bool odd = l16 & 1;
    f32x4 wm[3];
#pragma unroll
    for (int e = 0; e < 8; ++e) { const int d = l16 + 16 - 16 * (e >> 2) - 4 * lq - (e & 3); wm[e >> 2][e & 3] = d >= 0 && d <= 14 && !(d & 1) ? 0.f : -INFINITY; }
    wm[2] = f32x4{-INFINITY, -INFINITY, -INFINITY, -INFINITY};
    const unsigned ps0 = odd ? 0x01000c0cu : 0x0c0c0100u, ps1 = odd ? 0x03020c0cu : 0x0c0c0302u;
    unsigned qa = lds_addr(smem) + BL_Q + w * 4096 + l16 * 64 + ((lq * 16) ^ sw), qb = qa ^ 2048, va[6];
    {
        const int row = 4 * lq + (l16 >> 2), vs = (row & 6) << 3;
        va[0] = va[2] = lds_addr(smem) + BL_Q + (8 + w) * 4096 + row * 64 + ((8 * (w & 1) + 8 * (l & 3)) ^ vs);
        va[1] = va[3] = lds_addr(smem) + BL_Q + (8 + w) * 4096 + row * 64 + ((32 * (~w & 1) + 8 * (l & 3)) ^ vs);
        va[4] = va[0] ^ 2048; va[5] = va[1] ^ 2048;
#pragma unroll
        for (int e = 0; e < 6; ++e) PIN_V(va[e]);
    }
    const int g0 = (20 * hb + 5 * w + (w & 1) + (lq & 2)) >> 1, g1 = (20 * hb + 5 * w + 4 * (~w & 1)) >> 1;
    const unsigned oo0 = lp(SG_OB + (g0 >> 2) * 2048 + (lq & 1) * 1024 + (g0 & 3) * 256 + l16 * 16);
    const unsigned oo1 = lq < 2 ? lp(SG_OB + (g1 >> 2) * 2048 + lq * 1024 + (g1 & 3) * 256 + l16 * 16 + (w & 1) * 8) : OOB;
    const unsigned fO = t < 4 ? lp(SG_FL + FL_O + (8 * t + hb) * 128) : OOB;
    const unsigned st0 = lp(SG_ST + (4 * (l >> 5) + w) * 256 + (l & 31) * 8), st2 = l < 32 ? st0 + 4096 : OOB;
    unsigned fXv = xm(FX_OFF + (hb >> 1) * 64, L), xv, X = rtime() - 2 * PER, xs = OOB;
    PIN_V(fXv);
    const float z2 = l < 32 ? 1.f : 0.f;
    f32v2* sP = (f32v2*)(smem + BL_P) + (l & 31);
    unsigned sK = lds_addr(smem) + BL_P + 1024, sKw = sK + 4 * w;
    PIN_V(sK); PIN_V(sKw);
    unsigned pend = OOB, sb = seg_base(L, 0), so, fs = fx_step(1 + L * A.lag);
    const int i0 = 1 + L * A.lag;
    for (int s = 0; s < NTOUCH; ++s) { sst16(rx, seg_base(L, s), oo0, u32x4{0u, 0u, 0u, 0u}); sst8(rx, seg_base(L, s), oo1, u32x2{0u, 0u}); }
    u32x2 st[3];
    auto look = [&](int e, unsigned b, unsigned f) { if (e == 0) LDA8(st[0], st0, b, 0); if (e == 1) LDA8(st[1], st0, b, 2048); if (e == 2) LDA8(st[2], st2, b, 0); if (e == 3) LDA4(xv, fXv, f); };
#define SEEN(n) asm volatile("s_waitcnt vmcnt(%4)" : "+v"(st[0]), "+v"(st[1]), "+v"(st[2]), "+v"(xv) : "n"(n) : "memory")
#define SWAP2(op, a, b) asm volatile(op " %0, %1" : "+v"(a), "+v"(b))
    int s;
    SZERO(s);
#pragma unroll 1
    do {
        if (xs == OOB) {
            bool e = (int)(rtime() - X - E_B) >= 0;
#pragma unroll
            for (int i = 0; i < 4; ++i) look(i, e ? sb : OOB, e ? fs : OOB);
            VMB(4);
            sst4(rx, pend, fO, 1u);
            pend = OOB;
            SEEN(1);
#pragma unroll 1
            for (;;) {
                xv = __builtin_amdgcn_readfirstlane(xv);
                X = xv ? xv : X;
                LIVE;
                if (e) { if (!BAD(umax3(st[0][0], st[1][0], st[2][0]))) break; __builtin_amdgcn_s_sleep(BSLP); }
                e = true;
                rt_until(X + E_B);
#pragma unroll
                for (int i = 0; i < 4; ++i) look(i, sb, fs);
                SEEN(0);
            }
            XSET(so, sb + lp(SG_X1), lds0); XDMA(so, 0); XDMA(so, 1); XADV(so); XDMA(so, 0); XDMA(so, 1);
        }
        VMB(2);
        f32x4 acc[4][2];
        unsigned cur;
        float rd[2];
        u32x2 y[2];
        bf16x8 kf[3], qf[2];
        u32x4 k4;
#define MUL2(x, i, r) do { asm volatile("v_mul_f32 %0, %1, %0" : "+v"(x[i]) : "v"(r)); x[i + 1] *= r; USE_V(x[i + 1]); } while (0)
#define FMA2(x, i, c, m) do { asm volatile("v_fma_f32 %0, -%2, %1, %0" : "+v"(x[i]) : "v"(c[i]), "v"(m)); x[i + 1] = __builtin_fmaf(-m, c[i + 1], x[i + 1]); USE_V(x[i + 1]); } while (0)
#define ADD1(d, x, z) asm volatile("v_add_f32 %0, %1, %2" : "=v"(d) : "v"(x), "v"(z))
        auto fin = [&](int e, int u) {
            if (u < 6 && u % 3 < 2) MUL2(acc[e][u / 3], 2 * (u % 3), rd[u / 3]);
            if (u % 3 == 2) CVT2(y[u / 3][0], y[u / 3][1], acc[e][u / 3]);
            if (u == 6) { LDS_AT(u32x2, dst[e]) = y[0]; LDS_AT(u32x2, dst[e] + 1024) = y[1]; dst[e] ^= 2048; USE_V(dst[e]); }
        };
        {
            bf16x8 xw[6][2];
#pragma unroll
            for (int e = 0; e < 4; ++e) xw[e >> 1][e & 1] = *(const bf16x8*)(smem + l16b + e * 1024);
            SCHED0();
            u64 now, bad;
            float a, b, c, d, mw, mb[2];
            unsigned lb, lf;
            f32v2 p0, p1, p2, p3;
            sfor<0, 160>([&](auto G) {
                constexpr int g = decltype(G)::value, ks = g < 112 ? g / 8 : (g < 124 ? 14 + (g - 112) / 6 : (g < 148 ? 16 + (g - 124) % 8 / 2 : 14 + (g - 148) / 2));
                constexpr int tt = g < 112 ? g % 8 / 2 : (g < 124 ? (g - 112) % 6 / 2 : (g < 148 ? (g - 124) / 8 : 3)), rt = g & 1;
                constexpr int q = g >= 112 || g % 8 == 1 || g % 8 == 3 || g % 8 == 7 ? -1 : g / 8 * 5 + (g % 8 < 4 ? g % 8 / 2 : g % 8 - 2);
                if (ks == 0) MF_AV0(acc[tt][rt], wf[tt][0], xw[0][rt]);
                else if (tt < 3 || ks < 4) MF_AV(acc[tt][rt], wf[tt][ks], xw[ks % 6][rt]); else MF_VV(acc[tt][rt], wf[tt][ks], xw[ks % 6][rt]);
                SCHED0();
                constexpr int rq = g < 112 ? (g % 8 == 1 || g % 8 == 3 ? 2 * ks + 4 + g % 8 / 2 : -1) : (g < 124 && g % 3 != 0 ? 32 + (g - 112) / 3 * 2 + (g - 112) % 3 : -1);
                if constexpr (rq >= 0) xw[rq / 2 % 6][rq & 1] = *(const bf16x8*)(smem + l16b + rq * 1024);
                if (q == 0 || q == 5 || q == 11) XADV(so);
                if (q == 1 || q == 6 || q == 13) XDMA(so, 0);
                if (q == 3 || q == 8 || q == 15) XDMA(so, 1);
                if (q == 10) VMB_LATE(4);
                if (q == 2) { ADD1(c, st[0][1], st[1][1]); a = __uint_as_float(st[0][0]) + __uint_as_float(st[1][0]); USE_V(a); }
                if (q == 4) { ADD1(b, a, st[2][0]); a += __uint_as_float(st[2][0]); USE_V(a); }
                if (q == 7) { SWAP2("v_permlane32_swap_b32", a, b); SCHED0(); c += __uint_as_float(st[2][1]); USE_V(c); }
                if (q == 9) { mw = (a + b) * 0.2f; USE_V(mw); }
                if (q == 12) { a = __uint_as_float(st[0][0]) - mw; b = __uint_as_float(st[1][0]) - mw; USE_V(a); USE_V(b); }
                if (q == 14) { d = (__uint_as_float(st[2][0]) - mw) * z2; USE_V(d); }
                if (q == 16) { a = __builtin_fmaf(b, b, a * a); USE_V(a); }
                if (q == 17) { c = __builtin_fmaf(32.f, __builtin_fmaf(d, d, a), c); USE_V(c); }
                if (q == 18) { b = c; PIN_V(b); }
                if (q == 19) SWAP2("v_permlane32_swap_b32", c, b);
                if (q == 21) { c += b; sP[w * 32] = f32v2{mw, c}; }
                if (q == 22) { cur = sb; sb = (s + 1) << 19; PIN_S(cur); PIN_S(sb); }
                if (q == 23) { lb = (sb >> 8) & 8192; PIN_S(lb); }
                if (q == 24) { sb = (sb | lb) + 2 * SG_OFF; PIN_S(sb); }
                if (q == 26) { sb ^= L << 13; X += PER; PIN_S(sb); PIN_S(X); }
                if (q == 27) { fs += 256; lb = fs & 8192; PIN_S(fs); PIN_S(lb); }
                if (q == 28) { fs += lb; PIN_S(fs); }
                if (q == 34) { TIGHT(lb, s + i0, D_1); X -= lb; PIN_S(X); }
                if (q == 35) { asm volatile("s_cmp_eq_u32 %1, %2\n\ts_cselect_b32 %0, %3, 0" : "=s"(lb) : "s"(s + i0), "n"(NSEG_ + LAGV - 1), "s"(E_Z) : "scc"); X -= lb; PIN_S(X); }
                if (q == 30) asm volatile("s_waitcnt vmcnt(4) lgkmcnt(2)\n\ts_barrier" ::: "memory");
                if (q == 31) sst4(rx, pend, fO, 1u);
                if (q == 32) { p0 = sP[0]; p1 = sP[32]; }
                if (q == 33) { p2 = sP[64]; p3 = sP[96]; }
                if (q == 36) ADD1(a, p0[0], p1[0]);
                if (q == 37) { b = p2[0] + p3[0]; USE_V(b); }
                if (q == 38) { mw = (a + b) * 0.25f; USE_V(mw); }
                if (q == 39) { a = p0[0] - mw; PIN_V(a); b = p1[0] - mw; PIN_V(b); }
                if (q == 40) { c = p2[0] - mw; PIN_V(c); }
                if (q == 41) { mb[0] = rb(mw); PIN_V(mb[0]); }
                if (q == 42) { d = p3[0] - mw; PIN_V(d); mb[1] = mb[0]; PIN_V(mb[1]); }
                if (q == 43) { a = __builtin_fmaf(b, b, a * a); PIN_V(a); }
                if (q == 44) { SWAP2("v_permlane16_swap_b32", mb[0], mb[1]); ADD1(b, p0[1], p1[1]); }
                if (q == 45) { a = __builtin_fmaf(c, c, a); PIN_V(a); }
                if (q == 50) VMB(1);
                if (q == 55) { a = __builtin_fmaf(d, d, a); PIN_V(a); }
                if (g == 73) ADD1(c, p2[1], p3[1]);
                if (g == 75) ADD1(b, b, c);
                if (g == 89) { a = __builtin_fmaf(160.f, a, b); PIN_V(a); }
                if (g == 91) { a *= 1.0f / D_; PIN_V(a); }
                if (q == 60) asm volatile("v_cvt_pk_bf16_f32 %0, 0, %0" : "+v"(a));
                if (g == 97) { a += 1e-5f; PIN_V(a); }
                if (g == 99) asm volatile("v_cvt_pk_bf16_f32 %0, 0, %0" : "+v"(a));
                if (q == 65) { a = __builtin_amdgcn_sqrtf(a); PIN_V(a); }
                if (g == 105) asm volatile("v_cvt_pk_bf16_f32 %0, 0, %0" : "+v"(a));
                if (q == 66) { rd[0] = __builtin_amdgcn_rcpf(a); PIN_V(rd[0]); }
                if (q == 67) { rd[1] = rd[0]; PIN_V(rd[1]); }
                if (q == 69) SWAP2("v_permlane16_swap_b32", rd[0], rd[1]);
                if constexpr (g >= 72 && g < 104 && q >= 0 && g % 8) {
                    constexpr int e = (g % 8 < 5 ? g % 8 + 2 : 2 * (g % 8) - 10) + (g >= 88), i = g >> 2 & 2;
                    FMA2(acc[e >> 1][e & 1], i, cs[e >> 1], mb[e & 1]);
                }
                if (g == G_B) asm volatile("s_memrealtime %0" : "=s"(now));
                if (g == G_B + 4) asm volatile("s_waitcnt lgkmcnt(0)" : "+s"(now));
                if (g == G_B + 12) { lf = (unsigned)now - X; PIN_S(lf); }
                if (g == G_B + 20) { lf -= E_B; PIN_S(lf); }
                if (g == G_B + 26) asm volatile("s_cmp_lt_i32 %1, 63\n\ts_cselect_b32 %0, %0, -1" : "+s"(lf) : "s"(s) : "scc");
                if (g == G_B + 28) asm volatile("s_cmp_ge_i32 %1, 0\n\ts_cselect_b32 %0, %2, %4\n\ts_cselect_b32 %1, %3, %4" : "=&s"(lb), "+s"(lf) : "s"(sb), "s"(fs), "n"(OOB) : "scc");
                if constexpr (g >= G_B + 29 && g < G_B + 33) look(g - G_B - 29, lb, lf);
                if constexpr (g >= 132 && g < 155 && (g - 132) % 8 < 7) fin((g - 132) / 8, (g - 132) % 8);
                if (g == 155) LDS_BARRIER();
                if (g == 156) { kf[0] = LDS_AT(bf16x8, qb + 17408); qf[0] = LDS_AT(bf16x8, qa); }
                if (g == 157) { kf[1] = LDS_AT(bf16x8, qa + 16384); qf[1] = LDS_AT(bf16x8, qa + 1024); }
                if (g == 158) { kf[2] = LDS_AT(bf16x8, qa + 17408); SEEN(0); }
                if (g == 159) { lf = umax3(st[0][0], st[1][0], st[2][0]); bad = __ballot(lf == ~0u); PIN_S(bad); }
                SCHED0();
            });
            xv = __builtin_amdgcn_readfirstlane(xv);
            asm volatile("s_cmp_eq_u64 %1, 0\n\ts_cselect_b32 %0, %0, %2" : "+s"(lb) : "s"(bad), "n"(OOB) : "scc");
            LDS_AT(unsigned, sKw) = lb;
            X = xv ? xv : X;
            PIN_S(X);
        }
        {
            constexpr float SC = 0.22360679774997896f * 1.4426950408889634f;
            bf16x8 vx[2][2];
            f32x4 sc[4];
            SCHED0();
            fin(3, 0); fin(3, 1);
            SCHED0();
            MF_VVC(sc[0], kf[0], qf[0], wm[2]); SCHED0(); fin(3, 2); SCHED0();
            MF_VVC(sc[1], kf[1], qf[0], wm[1]); SCHED0(); fin(3, 3); SCHED0();
            MF_VVC(sc[2], kf[1], qf[1], wm[0]); SCHED0(); fin(3, 4); SCHED0();
            MF_VVC(sc[3], kf[2], qf[1], wm[1]); SCHED0(); fin(3, 5); fin(3, 6);
            SCHED0();
            float sv[4][2];
            float m0, m1;
#define MAXQ(m, x, y, p, q) asm volatile("v_max_f32 %1, %5, %6\n\tv_max_f32 %2, %7, %8\n\tv_max_f32 %3, %9, %10\n\tv_max_f32 %4, %11, %12\n\tv_max3_f32 %0, %1, %2, %3\n\tv_max_f32 %0, %0, %4" \
    : "=&v"(m), "=&v"(x[0]), "=&v"(x[1]), "=&v"(y[0]), "=&v"(y[1]) : "v"(p[0]), "v"(p[1]), "v"(p[2]), "v"(p[3]), "v"(q[0]), "v"(q[1]), "v"(q[2]), "v"(q[3]))
            MAXQ(m0, sv[0], sv[1], sc[0], sc[1]);
            MAXQ(m1, sv[2], sv[3], sc[2], sc[3]);
            LDS_BARRIER();
            k4 = LDS_AT(u32x4, sK);
            SCHED0();
            auto tr = [](unsigned a) { return __builtin_amdgcn_ds_read_tr16_b64_v4i16((lds_b4*)a); };
#pragma unroll
            for (int dt = 0; dt < 2; ++dt) { vx[0][dt] = cat8(tr(va[4 + dt] + 1024), tr(va[dt])); vx[1][dt] = cat8(tr(va[2 + dt]), tr(va[dt] + 1024)); }
            SCHED0();
            red_max_q2(m0, m1);
            m0 *= -SC; m1 *= -SC;
            USE_V(m0); USE_V(m1);
            SCHED0();
            xs = __builtin_amdgcn_readfirstlane(max(umax3(k4[0], k4[1], k4[2]), k4[3]));
            u32x4 pt[2];
            f32x4 o[2][2], sm[2];
#pragma unroll
            for (int qt = 0; qt < 2; ++qt) {
#pragma unroll
                for (int e = 0; e < 4; ++e) sv[2 * qt + (e >> 1)][e & 1] = __builtin_amdgcn_exp2f(__builtin_fmaf(sv[2 * qt + (e >> 1)][e & 1], SC, qt ? m1 : m0));
                if (!qt) XSET(so, xs + lp(SG_X1), lds0);
#pragma unroll
                for (int e = 0; e < 2; ++e) {
                    const unsigned p = pk2(sv[2 * qt + e][0], sv[2 * qt + e][1]);
                    pt[qt][2 * e] = __builtin_amdgcn_perm(p, p, ps0);
                    pt[qt][2 * e + 1] = __builtin_amdgcn_perm(p, p, ps1);
                }
                USE_V(pt[qt]);
                SCHED0();
                XDMA(so, qt);
                asm volatile("s_nop 0");
                MF_VV0(sm[qt], one, pt[qt]);
                if (!qt) MF_VV0(o[0][0], vx[0][0], pt[0]);
                SCHED0();
            }
            XADV(so);
            MF_VV0(o[1][0], vx[1][0], pt[1]);
            PIN_V(sm[0]);
            const float s0 = __builtin_amdgcn_rcpf(sm[0][0]);
            USE_V(s0);
            XDMA(so, 0);
            MF_VV0(o[0][1], vx[0][1], pt[0]);
            SCHED0();
            PIN_V(o[0][0]);
            MUL2(o[0][0], 0, s0);
            SCHED0();
            MF_VV0(o[1][1], vx[1][1], pt[1]);
            SCHED0();
            PIN_V(sm[1]);
            const float s1 = __builtin_amdgcn_rcpf(sm[1][0]);
            USE_V(s1);
            MUL2(o[0][0], 2, s0);
            XDMA(so, 1);
            PIN_V(o[1][0]);
            MUL2(o[1][0], 0, s1); MUL2(o[1][0], 2, s1);
            {
                const auto a = __builtin_amdgcn_permlane16_swap(pk2(o[0][0][0], o[0][0][1]), pk2(o[1][0][0], o[1][0][1]), false, false);
                const auto b = __builtin_amdgcn_permlane16_swap(pk2(o[0][0][2], o[0][0][3]), pk2(o[1][0][2], o[1][0][3]), false, false);
                sst16(rx, cur, oo0, u32x4{a[0], b[0], a[1], b[1]});
            }
            SCHED0();
            asm volatile("" : "+v"(o[0][1]), "+v"(o[1][1]));
            o[0][1] *= s0; o[1][1] *= s1;
            {
                const auto a = __builtin_amdgcn_permlane16_swap(pk2(o[0][1][0], o[0][1][1]), pk2(o[1][1][0], o[1][1][1]), false, false);
                const auto b = __builtin_amdgcn_permlane16_swap(pk2(o[0][1][2], o[0][1][3]), pk2(o[1][1][2], o[1][1][3]), false, false);
                sst8(rx, cur, oo1, u32x2{a[0], b[0]});
            }
            SCHED0();
            wm[2] = wm[0];
            qa ^= 2048; qb ^= 2048;
            PIN_V(wm[2]); PIN_V(qa); PIN_V(qb);
#pragma unroll
            for (int e = 0; e < 6; ++e) { va[e] ^= 2048; PIN_V(va[e]); }
        }
        pend = cur;
    } while (++s < NSEG_);
    VMB(0);
    sst4(rx, pend, fO, 1u);
    SPIN_END;
}

__device__ __forceinline__ void bo_role(const MegaArgs& A, char* smem, const int L, const int j, const int id) {
    WORK_IDS;
    const unsigned t0 = rtime();
    const int rt = w & 1, hf = w >> 1;
    if (id == 31 && w == 0) probe(A, go, l, id);
    INIT_BEGIN(id >> 5);
    if (L) { INIT_END(56 + j); }
    bf16x8 wf[5][20];
    {
        const void* const base[3] = {A.wo + 160 * j, A.wo + 160 * j, A.wo + 160 * j};
        unsigned vo[3];
        int rp[5];
#pragma unroll
        for (int m = 0; m < 3; ++m) { const int idx = min((4 * m + w) * 64 + l, 639); vo[m] = (idx / 20) * (2 * D_) + (idx % 20) * 16; }
#pragma unroll
        for (int m = 0; m < 5; ++m) { rp[m] = (8 * lq + (l16 >> 2)) * 320 + 32 * (m < 4 ? 4 * hf + m : 8 + hf) + (l & 3) * 8; }
        while (rtime() - t0 < (L ? DLYB1 : DLYB0)) __builtin_amdgcn_s_sleep(8);
        load_coop<3, 0, 5>(smem, w, go, base, vo, 64 * D_, rp, 1280, wf);
    }
    INIT_SEEN;
    const unsigned x0 = lp((5 * j + 2 * hf) * 2048 + rt * 1024 + l16b), x1 = lp((5 * j + 2 * hf + 1) * 2048 + rt * 1024 + l16b), x2 = lp((5 * j + 4) * 2048 + rt * 1024 + l16b + hf * 8);
    const unsigned fO = l < 8 ? lp(SG_FL + FL_O + (8 * j + l) * 128) : OOB, fX = t < 8 ? lp(SG_FL + FL_X + (4 * (t & 3) + j) * 128) + (t & 4) * 2048 : OOB;
    const unsigned sr = lp(SG_ST + ST_2 + rt * 1024 + lq * 256 + l16 * 8), sw = l < 16 ? sr + j * 256 + hf * 128 : OOB;
    auto flag = [&](unsigned b) { sst4(rx, b & ~8192u, fX ^ (b & 8192u), 1u); };
    const rsrc_t ro = mk_rsrc(A.out, S_ * D_ * 2);
    const unsigned oo = ((16 * rt + l16) * D_ + 160 * j + 64 * hf + 4 * lq) * 2, oo2 = oo + (128 - 48 * hf) * 2;
    const char* sX = smem + rt * 1024 + l16b;
    unsigned pend = OOB, sb = seg_base(L, 0), so, xs = OOB;
    unsigned* sK = (unsigned*)(smem + 40960);
    auto first = [&](unsigned b) { XSET(so, b + lp(SG_OB), lds0); XDMA(so, 0); XDMA(so, 1); XADV(so); XDMA(so, 0); XDMA(so, 1); XADV(so); XDMA(so, 0); XDMA(so, 1); };
    int s;
    SZERO(s);
#pragma unroll 1
    do {
        if (xs == OOB) {
            unsigned pv;
            LDA4(pv, fO, sb);
            VMB(1);
            flag(pend);
            pend = OOB;
            WAITV(1, pv);
            if (__ballot(pv == 0) & 255) wave_wait(rx, fO, sb, spins);
            first(sb);
            VMB(4);
        } else VMB(5);
        f32x4 acc[5], xf[5];
        u32x4 r0, r1, k4;
        u32x2 r2;
        unsigned cur;
        {
            bf16x8 xw[3];
            unsigned sx, pv;
            xw[0] = *(const bf16x8*)sX; xw[1] = *(const bf16x8*)(sX + 2048);
            SCHED0();
            sfor<0, 100>([&](auto G) {
                constexpr int g = decltype(G)::value, ks = g / 5, tt = g % 5;
                if constexpr (20 * tt + ks < 64) { if (ks == 0) MF_AV0(acc[tt], wf[tt][0], xw[0]); else MF_AV(acc[tt], wf[tt][ks], xw[ks % 3]); }
                else { if (ks == 0) MF_VV0(acc[tt], wf[tt][0], xw[0]); else MF_VV(acc[tt], wf[tt][ks], xw[ks % 3]); }
                SCHED0();
                if (g == 0 || g == 7) XADV(so);
                if (g == 2 || g == 12) XDMA(so, 0);
                if (g == 5 || g == 15) XDMA(so, 1);
                if (g == 3) { cur = sb; sx = sb + lp(SG_X1); PIN_S(cur); PIN_S(sx); }
                if (g == 10) VMB_LATE(4);
                if (g == 18) LDA(r0, x0, sx, 0);
                if (g == 22) LDA(r1, x1, sx, 0);
                if (g == 25) LDA8(r2, x2, sx, 0);
                if (g == 30) VMB(7);
                if (g == 32) flag(pend);
                if (g == 35) { sb = (s + 1) << 19; PIN_S(sb); }
                if (g == 37) { sx = (sb >> 8) & 8192; PIN_S(sx); }
                if (g == 38) { sb = (sb | sx) + 2 * SG_OFF; PIN_S(sb); }
                if (g == 40) { sb ^= L << 13; PIN_S(sb); }
                if (g == 42) asm volatile("s_cmp_lt_i32 %1, 63\n\ts_cselect_b32 %0, %0, %2" : "+s"(sb) : "s"(s), "n"(OOB) : "scc");
                if (g == 50) VMB(4);
                if constexpr (tt == 1 && ks < 18) xw[(ks + 2) % 3] = *(const bf16x8*)(sX + (ks + 2) * 2048);
                if (g == 60) LDA4(pv, fO, sb);
                if (g == 63) asm volatile("s_waitcnt vmcnt(2)" : "+v"(r0), "+v"(r1), "+v"(r2) :: "memory");
                if constexpr (g >= 65 && g < 90 && (tt == 2 || tt == 3)) {
                    constexpr int e = ks - 13, k = tt - 2;
                    const unsigned u = e < 2 ? r0[2 * (e & 1) + k] : (e < 4 ? r1[2 * (e & 1) + k] : r2[k]);
                    xf[e][2 * k] = lo16(u); xf[e][2 * k + 1] = hi16(u);
                    asm volatile("" : "+v"(xf[e][2 * k]), "+v"(xf[e][2 * k + 1]));
                }
                if (g == 90) WAITV(0, pv);
                if (g == 91) { sx = (unsigned)__ballot(pv == 0); PIN_S(sx); }
                if (g == 92) asm volatile("s_and_b32 %0, %0, 255\n\ts_cselect_b32 %0, %2, %1" : "+s"(sx) : "s"(sb), "n"(OOB) : "scc");
                if (g == 93) sK[w] = sx;
                if (g == 97) LDS_BARRIER();
                if (g == 98) k4 = *(const u32x4*)sK;
                SCHED0();
            });
        }
#pragma unroll
        for (int e = 0; e < 5; ++e) { PIN_V(acc[e]); acc[e] += xf[e]; PIN_V(acc[e]); SCHED0(); }
        xs = __builtin_amdgcn_readfirstlane(max(umax3(k4[0], k4[1], k4[2]), k4[3]));
        if (L) {
            if (xs != OOB) first(xs);
            const unsigned so2 = s * (SEG_ * D_ * 2);
#pragma unroll
            for (int e = 0; e < 5; ++e) __builtin_amdgcn_raw_buffer_store_b64(u32x2{pk2(acc[e][0], acc[e][1]), pk2(acc[e][2], acc[e][3])}, ro, e < 4 ? oo + 32 * e : oo2, so2, OUTAUX);
        } else {
            f32x4 a = ((acc[0] + acc[1]) + (acc[2] + acc[3])) + acc[4];
            const float mw = red_sum_q((a[0] + a[1]) + (a[2] + a[3])) * (1.0f / 80);
#pragma unroll
            for (int e = 0; e < 5; ++e) { xf[e] = acc[e]; acc[e] -= mw; }
            a = acc[0] * acc[0];
#pragma unroll
            for (int e = 1; e < 5; ++e) a = __builtin_elementwise_fma(acc[e], acc[e], a);
            sst8(rx, cur, sw, u32x2{nosent(__float_as_uint(mw)), __float_as_uint(red_sum_q((a[0] + a[1]) + (a[2] + a[3])))});
            SCHED0();
            if (xs != OOB) first(xs);
            r0 = pk8(xf[0], xf[1]); r1 = pk8(xf[2], xf[3]); r2 = u32x2{pk2(xf[4][0], xf[4][1]), pk2(xf[4][2], xf[4][3])};
            const unsigned so2 = cur + lp(SG_XR);
            sst16(rx, so2, x0, r0); sst16(rx, so2, x1, r1); sst8(rx, so2, x2, r2);
            VMW(3);
            u32x2 p, q;
#pragma unroll 1
            for (;;) {
                LDA8(p, sr, cur, 0); LDA8(q, sr, cur, 128);
                LIVE;
                asm volatile("s_waitcnt vmcnt(0)" : "+v"(p), "+v"(q) :: "memory");
                if (!BAD(max(p[0], q[0]))) break;
            }
            const float m0 = __uint_as_float(p[0]), m1 = __uint_as_float(q[0]), mean = red_sum_q(m0 + m1) * 0.125f, e0 = m0 - mean, e1 = m1 - mean;
            const float var = red_sum_q((__uint_as_float(p[1]) + __uint_as_float(q[1])) + 80.f * (e0 * e0 + e1 * e1)) * (1.0f / D_);
            const float rd = __builtin_amdgcn_rcpf(rb(__builtin_amdgcn_sqrtf(rb(rb(var) + 1e-5f)))), c = (mw - rb(mean)) * rd;
#pragma unroll
            for (int e = 0; e < 5; ++e) acc[e] = acc[e] * rd + c;
            sst16(rx, cur, x0, pk8(acc[0], acc[1])); sst16(rx, cur, x1, pk8(acc[2], acc[3])); sst8(rx, cur, x2, u32x2{pk2(acc[4][0], acc[4][1]), pk2(acc[4][2], acc[4][3])});
            pend = cur;
        }
    } while (++s < NSEG_);
    if (id == 159) bst4(go.rh, t < 8 ? t * 1024 + HS_E : OOB, tags);
    VMB(0);
    flag(pend);
    SPIN_END;
}

#define CT0 1
#define CT1 0
constexpr unsigned CODE_SZ = 100 * 1024, CODE_BK = 0;
constexpr int MEGA_LDS = 163840;
__global__ void __launch_bounds__(256, 1) mega_kernel(MegaArgs A) {
    extern __shared__ __attribute__((aligned(16))) char smem[];
    int role = 32 * (blockIdx.x & 7) + (blockIdx.x >> 3);
    PIN_S(role);
    const int pool = role >> 5, k = role & 31;
    {
        const unsigned t0 = rtime();
        if (k >= 8 && k < 13) {
            const char* pc;
            asm volatile("s_getpc_b64 %0" : "=s"(pc));
            auto touch = [&]() { const unsigned x = __builtin_amdgcn_raw_buffer_load_b32(mk_rsrc(pc, CODE_SZ), ((k - 8) * 256 + threadIdx.x) * 128 - CODE_BK, 0, 0); asm volatile("" :: "v"(x)); };
            if (CT0) touch();
            init_area(A, 5 * pool + k - 8, role, t0);
            if (CT1) { while (rtime() - t0 < CT1) __builtin_amdgcn_s_sleep(8); touch(); }
        }
        if (k >= 17 && k < 29 && ((k - 13) & 4)) { first_rows(A, smem, 8 * ((k - 17) / 8 * 4 + ((k - 17) & 3)) + pool, role, t0); while (rtime() - t0 < DLY2) __builtin_amdgcn_s_sleep(8); }
    }
    if (k < 13) {
        if (!(A.roles & 1)) return;
        if (k < 4) s_role(A, smem, 4 * pool + k, role);
        else if (k < 8) vm_role(A, smem, 4 * pool + k - 4, role);
        else c_role(A, smem, pool, k - 8, role);
        return;
    }
    if (k < 21) { if (A.roles & 2) qk_role(A, smem, (k - 13) >> 2, 4 * pool + ((k - 13) & 3), role); }
    else if (k < 29) { if (A.roles & 4) vo_role(A, smem, (k - 21) >> 2, 4 * pool + ((k - 21) & 3), role); }
    else if (k < 31) { if (A.roles & 8) b_role(A, smem, pool >> 2, 2 * (pool & 3) + k - 29, role); }
    else if (A.roles & 16) bo_role(A, smem, pool >> 2, pool & 3, role);
}

#define ROLES 31
extern "C" void kernel_launch(void* const* d_in, const int* in_sizes, int n_in,
                              void* d_out, int out_size, void* d_ws, size_t ws_size,
                              hipStream_t stream) {
    MegaArgs A;
    char* ws = (char*)d_ws;
    A.hs = ws; ws += 262144;
    A.tmo = (unsigned*)ws; ws += 4096;
    A.ar = ws;
    A.x0 = (const bf16_t*)d_in[0]; A.mem0 = (const bf16_t*)d_in[1]; A.Wq = (const bf16_t*)d_in[2]; A.Wk = (const bf16_t*)d_in[3]; A.Wv = (const bf16_t*)d_in[4];
    A.Wo = (const bf16_t*)d_in[5]; A.wqkv = (const bf16_t*)d_in[8]; A.wo = (const bf16_t*)d_in[9];
    A.out = (bf16_t*)d_out; A.pad = 0;
    A.lag = LAGV; A.nit = NSEG_ + LAGV; A.roles = ROLES;

    (void)hipFuncSetAttribute((const void*)mega_kernel, hipFuncAttributeMaxDynamicSharedMemorySize, MEGA_LDS);

    mega_kernel<<<256, 256, MEGA_LDS, stream>>>(A);
}
```

```cpp
#define PER 284
#define E_U 121
#define E_P 137
#define D_1 14
#define A_MAX 10
#include <hip/hip_runtime.h>
#include <hip/hip_bf16.h>
#include <stdint.h>

typedef unsigned short bf16_t;
typedef short bf16x8 __attribute__((ext_vector_type(8)));
typedef short bf16x4 __attribute__((ext_vector_type(4)));
typedef float f32x4 __attribute__((ext_vector_type(4)));
typedef unsigned u32x4 __attribute__((ext_vector_type(4)));
typedef unsigned u32x2 __attribute__((ext_vector_type(2)));
typedef unsigned long long u64;
typedef __bf16 bf16v2 __attribute__((ext_vector_type(2)));
typedef float f32v2 __attribute__((ext_vector_type(2)));

__device__ __forceinline__ float bf2f(bf16_t v) { return __uint_as_float(((unsigned)v) << 16); }
__device__ __forceinline__ unsigned pk2(float a, float b) {
    f32v2 f = {a, b};
    bf16v2 r = __builtin_convertvector(f, bf16v2);
    return __builtin_bit_cast(unsigned, r);
}
__device__ __forceinline__ float rb(float a) { return __uint_as_float((pk2(a, 0.f) & 0xffffu) << 16); }
__device__ __forceinline__ float lo16(unsigned u) { return __uint_as_float(u << 16); }
__device__ __forceinline__ float hi16(unsigned u) { return __uint_as_float(u & 0xffff0000u); }
__device__ __forceinline__ bf16x4 pk4(f32x4 v) {
    u32x2 u = {pk2(v[0], v[1]), pk2(v[2], v[3])};
    return __builtin_bit_cast(bf16x4, u);
}
__device__ __forceinline__ bf16x8 cat8(bf16x4 a, bf16x4 b) { return bf16x8{a[0], a[1], a[2], a[3], b[0], b[1], b[2], b[3]}; }
__device__ __forceinline__ bf16x8 asb8(u32x4 v) { return __builtin_bit_cast(bf16x8, v); }
__device__ __forceinline__ f32x4 asf4(u32x4 v) { return __builtin_bit_cast(f32x4, v); }
__device__ __forceinline__ f32x4 mfma32(bf16x8 a, bf16x8 b, f32x4 c) { return __builtin_amdgcn_mfma_f32_16x16x32_bf16(a, b, c, 0, 0, 0); }
__device__ __forceinline__ f32x4 mfma16(bf16x4 a, bf16x4 b, f32x4 c) { return __builtin_amdgcn_mfma_f32_16x16x16bf16_1k(a, b, c, 0, 0, 0); }

#define MF_(acc, ca, a, cb, b) asm volatile("v_mfma_f32_16x16x32_bf16 %0, %1, %2, %0" : "+v"(acc) : ca(a), cb(b))
#define MF0_(acc, ca, a, cb, b) asm volatile("v_mfma_f32_16x16x32_bf16 %0, %1, %2, 0" : "=&v"(acc) : ca(a), cb(b))
#define MF_AV(acc, a, b) MF_(acc, "a", a, "v", b)
#define MF_VA(acc, a, b) MF_(acc, "v", a, "a", b)
#define MF_VV(acc, a, b) MF_(acc, "v", a, "v", b)
#define MF_AV0(acc, a, b) MF0_(acc, "a", a, "v", b)
#define MF_VA0(acc, a, b) MF0_(acc, "v", a, "a", b)
#define MF_VV0(acc, a, b) MF0_(acc, "v", a, "v", b)
#define MF_VVC(d, a, b, c) asm volatile("v_mfma_f32_16x16x32_bf16 %0, %1, %2, %3" : "=&v"(d) : "v"(a), "v"(b), "v"(c))
#define MH_(acc, ca, a, cb, b) asm volatile("v_mfma_f32_16x16x16_bf16 %0, %1, %2, %0" : "+v"(acc) : ca(a), cb(b))
#define MH0_(acc, ca, a, cb, b) asm volatile("v_mfma_f32_16x16x16_bf16 %0, %1, %2, 0" : "=&v"(acc) : ca(a), cb(b))

constexpr int S_ = 2048, D_ = 640, H_ = 32, DH_ = 192, M_ = 8, SEG_ = 32, NSEG_ = 64;
constexpr int HD_ = H_ * DH_;
#define RLX_AGENT __ATOMIC_RELAXED, __HIP_MEMORY_SCOPE_AGENT
#define AUX_SC1 16
typedef __amdgpu_buffer_rsrc_t rsrc_t;
__device__ __forceinline__ rsrc_t mk_rsrc(const void* p, unsigned bytes) { return __builtin_amdgcn_make_buffer_rsrc((void*)p, 0, (int)bytes, 0x00020000); }
__device__ __forceinline__ u32x4 bld16(rsrc_t r, unsigned off) { return __builtin_amdgcn_raw_buffer_load_b128(r, off, 0, AUX_SC1); }
__device__ __forceinline__ u32x2 bld8(rsrc_t r, unsigned off) { return __builtin_amdgcn_raw_buffer_load_b64(r, off, 0, AUX_SC1); }
__device__ __forceinline__ unsigned bld4(rsrc_t r, unsigned off) { return __builtin_amdgcn_raw_buffer_load_b32(r, off, 0, AUX_SC1); }
__device__ __forceinline__ void bst16(rsrc_t r, unsigned off, u32x4 v) { __builtin_amdgcn_raw_buffer_store_b128(v, r, off, 0, AUX_SC1); }
__device__ __forceinline__ void bst8(rsrc_t r, unsigned off, u32x2 v) { __builtin_amdgcn_raw_buffer_store_b64(v, r, off, 0, AUX_SC1); }
__device__ __forceinline__ void bst4(rsrc_t r, unsigned off, unsigned v) { __builtin_amdgcn_raw_buffer_store_b32(v, r, off, 0, AUX_SC1); }
#define LDS_BARRIER() asm volatile("s_waitcnt lgkmcnt(0)\n\ts_barrier" ::: "memory")
#define DRAIN() asm volatile("s_waitcnt vmcnt(0)" ::: "memory")
#define MEMFENCE() asm volatile("" ::: "memory")
#define SCHED0() __builtin_amdgcn_sched_barrier(0)
__device__ __forceinline__ float red_max_q(float x) {
    auto a = __builtin_amdgcn_permlane16_swap(__float_as_uint(x), __float_as_uint(x), false, false);
    x = fmaxf(__uint_as_float(a[0]), __uint_as_float(a[1]));
    auto c = __builtin_amdgcn_permlane32_swap(__float_as_uint(x), __float_as_uint(x), false, false);
    return fmaxf(__uint_as_float(c[0]), __uint_as_float(c[1]));
}
__device__ __forceinline__ float max3r(float a, float b, float c) { float r; asm("v_max3_f32 %0, %1, %2, %3" : "=v"(r) : "v"(a), "v"(b), "v"(c)); return r; }
__device__ __forceinline__ float max2r(float a, float b) { float r; asm("v_max_f32 %0, %1, %2" : "=v"(r) : "v"(a), "v"(b)); return r; }
__device__ __forceinline__ void red_max_q2(float& x, float& y) {
    float s, t;
    asm("v_mov_b32 %2, %0\n\tv_mov_b32 %3, %1\n\ts_nop 0\n\tv_permlane16_swap_b32 %0, %2\n\tv_permlane16_swap_b32 %1, %3\n\tv_max_f32 %0, %0, %2\n\tv_max_f32 %1, %1, %3\n\t"
        "v_mov_b32 %2, %0\n\tv_mov_b32 %3, %1\n\ts_nop 0\n\tv_permlane32_swap_b32 %0, %2\n\tv_permlane32_swap_b32 %1, %3\n\tv_max_f32 %0, %0, %2\n\tv_max_f32 %1, %1, %3"
        : "+v"(x), "+v"(y), "=&v"(s), "=&v"(t));
}
__device__ __forceinline__ void red_sum_q2(float& x, float& y) {
    float s, t;
    asm("v_mov_b32 %2, %0\n\tv_mov_b32 %3, %1\n\ts_nop 0\n\tv_permlane16_swap_b32 %0, %2\n\tv_permlane16_swap_b32 %1, %3\n\tv_add_f32 %0, %0, %2\n\tv_add_f32 %1, %1, %3\n\t"
        "v_mov_b32 %2, %0\n\tv_mov_b32 %3, %1\n\ts_nop 0\n\tv_permlane32_swap_b32 %0, %2\n\tv_permlane32_swap_b32 %1, %3\n\tv_add_f32 %0, %0, %2\n\tv_add_f32 %1, %1, %3"
        : "+v"(x), "+v"(y), "=&v"(s), "=&v"(t));
}
__device__ __forceinline__ float red_sum_q(float x) {
    auto a = __builtin_amdgcn_permlane16_swap(__float_as_uint(x), __float_as_uint(x), false, false);
    x = __uint_as_float(a[0]) + __uint_as_float(a[1]);
    auto c = __builtin_amdgcn_permlane32_swap(__float_as_uint(x), __float_as_uint(x), false, false);
    return __uint_as_float(c[0]) + __uint_as_float(c[1]);
}
__device__ __forceinline__ u32x4 pk8(f32x4 a, f32x4 b) { return u32x4{pk2(a[0], a[1]), pk2(a[2], a[3]), pk2(b[0], b[1]), pk2(b[2], b[3])}; }
__device__ __forceinline__ u32x4 tag4(f32x4 v, unsigned tg) {
    return u32x4{(__float_as_uint(v[0]) & ~3u) | tg, (__float_as_uint(v[1]) & ~3u) | tg, (__float_as_uint(v[2]) & ~3u) | tg, (__float_as_uint(v[3]) & ~3u) | tg};
}
__device__ __forceinline__ unsigned tagbad(u32x4 v, unsigned tg) { return ((v[0] ^ tg) | (v[1] ^ tg) | (v[2] ^ tg) | (v[3] ^ tg)) & 3u; }

__device__ __host__ __forceinline__ constexpr unsigned xm(unsigned o, unsigned side) { return ((o >> 13) << 14) | (o & 8191u) | ((((o >> 20) ^ side) & 1u) << 13); }
__device__ __host__ __forceinline__ constexpr unsigned lp(unsigned o) { return ((o >> 13) << 14) | (o & 8191u); }
__device__ __host__ __forceinline__ constexpr int hside(int h) { return h >> 4; }
constexpr unsigned XM_OFF = 24576, XM_BUF = 4 * 32768;
constexpr unsigned XP_OFF = XM_OFF + 3 * XM_BUF, XP_BUF = 32768;
constexpr unsigned PT_OFF = XP_OFF + 3 * XP_BUF, PT_BUF = 65536;
constexpr unsigned FI_OFF = PT_OFF + 3 * PT_BUF, FI_N = 80;
constexpr unsigned FX_OFF = FI_OFF + FI_N * 1024;
constexpr unsigned A_END = FX_OFF + FI_N * 256;
constexpr unsigned UM_OFF = 1u << 20, UM_BUF = 40 * 8192;
#define TR 8
constexpr unsigned UT_OFF = 2u << 20;
constexpr unsigned QK_OFF = UT_OFF + TR * (2u << 20);
constexpr unsigned SG_OFF = QK_OFF + TR * (1u << 20);
enum { SG_XL = 0, SG_XR = 40960, SG_X1 = 81920, SG_OB = 122880, SG_ST = 163840, SG_FL = 172032 };
enum { FL_U = 0, FL_X = 2048, FL_O = 4096, ST_2 = 6144 };
constexpr unsigned ARENA = 2 * (SG_OFF + 64 * 262144);
static_assert(A_END <= UM_OFF && UM_OFF + 3 * UM_BUF <= UT_OFF && QK_OFF % (1u << 21) == 0 && SG_OFF % (1u << 21) == 0, "map");
static_assert(ARENA + (1u << 23) < (1u << 28), "workspace");
__device__ __forceinline__ unsigned seg_base(int layer, int seg) { return 2 * SG_OFF + (unsigned)seg * 524288 + ((((seg >> 2) ^ layer) & 1) << 13); }
__device__ __forceinline__ unsigned ut_slot(int seg) { return (unsigned)(seg & (TR - 1)) << 22; }
__device__ __forceinline__ unsigned qk_base(int seg, int side) { return 2 * QK_OFF + ((unsigned)(seg & (TR - 1)) << 21) + (((seg ^ side) & 1) << 13); }
__device__ __forceinline__ unsigned fi_step(int it) { return ((it >> 3) << 14) | ((it & 7) << 10); }
__device__ __forceinline__ unsigned fx_step(int it) { return ((it >> 5) << 14) | ((it & 31) << 8); }
static_assert(FI_OFF % 8192 == 0 && FX_OFF % 8192 == 0, "steps");
__device__ __forceinline__ u32x4 sld16(rsrc_t r, unsigned so, unsigned lo) { return __builtin_amdgcn_raw_buffer_load_b128(r, lo, so, AUX_SC1); }
__device__ __forceinline__ u32x2 sld8(rsrc_t r, unsigned so, unsigned lo) { return __builtin_amdgcn_raw_buffer_load_b64(r, lo, so, AUX_SC1); }
__device__ __forceinline__ unsigned sld4(rsrc_t r, unsigned so, unsigned lo) { return __builtin_amdgcn_raw_buffer_load_b32(r, lo, so, AUX_SC1); }
__device__ __forceinline__ void sst4(rsrc_t r, unsigned so, unsigned lo, unsigned v) { __builtin_amdgcn_raw_buffer_store_b32(v, r, lo, so, AUX_SC1); }
__device__ __forceinline__ void sst16(rsrc_t r, unsigned so, unsigned lo, u32x4 v) { __builtin_amdgcn_raw_buffer_store_b128(v, r, lo, so, AUX_SC1); }
__device__ __forceinline__ void sst8(rsrc_t r, unsigned so, unsigned lo, u32x2 v) { __builtin_amdgcn_raw_buffer_store_b64(v, r, lo, so, AUX_SC1); }
__device__ __forceinline__ unsigned umax3(unsigned a, unsigned b, unsigned c) { return max(max(a, b), c); }
__device__ __forceinline__ unsigned mx4(unsigned m, u32x4 v) { return umax3(m, v[0], v[2]); }
__device__ __forceinline__ unsigned nosent(unsigned x) { return min(x, 0xfffffffeu); }
constexpr unsigned OOB = 0x7f000000u;
constexpr unsigned kSpinCap = 1u << 18;

__device__ __forceinline__ char* arena_base(char* p) { return (char*)(((size_t)p + 0x3fffff) & ~(size_t)0x3fffff); }

typedef __attribute__((address_space(3))) bf16x4 lds_b4;
typedef __attribute__((address_space(3))) volatile int lds_int;
template <int V> struct IC { static constexpr int value = V; };
template <int B, int E, class F> __device__ __forceinline__ void sfor(F&& f) { if constexpr (B < E) { f(IC<B>{}); sfor<B + 1, E>(f); } }
#define WNT ""
#define DMA_NEXT(x) asm volatile("global_load_lds_dwordx4 %0, %1" x "\n\ts_add_u32 m0, m0, %2\n\tv_add_u32 %0, %3, %0" : "+v"(vo) : "s"(base), "s"(step), "s"(adv) : "memory", "m0", "scc")
template <bool NT> __device__ __forceinline__ void dma_next(const void* base, unsigned& vo, unsigned step, unsigned adv) { if (NT) DMA_NEXT(WNT); else DMA_NEXT(""); }
__device__ __forceinline__ void set_m0(unsigned x) { asm volatile("s_mov_b32 m0, %0\n\ts_nop 0" :: "s"(x) : "memory", "m0"); }
__device__ __forceinline__ bf16x8 tr_read(const char* p, int o0, int o1) { return cat8(__builtin_amdgcn_ds_read_tr16_b64_v4i16((lds_b4*)(p + o0)), __builtin_amdgcn_ds_read_tr16_b64_v4i16((lds_b4*)(p + o1))); }
struct Go { rsrc_t rh; unsigned so; u32x2 v; };
#define G_EB 2
#define GO_ASK asm volatile("buffer_load_dwordx2 %0, %1, %2, %3 offen sc1" : "=&v"(go.v) : "v"((threadIdx.x & 63) * 8), "s"(go.rh), "s"(go.so) : "memory")
struct Nop { __device__ void operator()() const {} };
template <int N, int N1, int NI, int NJ, int NS, int SL, bool V, int NE = 0, class ISSUE, class TAKE, class EARLY = Nop>
__device__ __forceinline__ void load_ring(char* smem, const int w, Go& go, ISSUE issue_, TAKE take, EARLY early = Nop{}) {
    constexpr int AH = NS - 1, SH = NE ? (NS - N % NS) % NS : 0, GE = N - 1 - AH - (NE ? G_EB : 0), HE = GE + AH + 1;
    static_assert(HE >= N - NS + 2, "free slots");
    const unsigned sta = (unsigned)(size_t)(__attribute__((address_space(3))) char*)smem + w * 1024;
    auto issue = [&](int st) {
        if ((st + SH) % NS == 0) set_m0(sta); else if (st == 0) set_m0(sta + SH * SL);
        const int n = st < N1 ? NI : NJ;
#pragma unroll
        for (int m = 0; m < n; ++m) issue_(st, m, m + 1 < n ? 4096u : SL - 4096u * (n - 1));
    };
#pragma unroll
    for (int st = 0; st < AH; ++st) issue(st);
    auto run = [&](auto WW) {
        sfor<0, N + 1>([&](auto ST) {
            constexpr int st = decltype(ST)::value, e = st + AH < N ? st + AH : N, a = e < N1 ? e : N1, n1 = a > st + 1 ? a - st - 1 : 0, n = st < N ? n1 * NI + (e - st - 1 - n1) * NJ + (st > GE && st <= GE + AH) + (st > HE ? NE : 0) : 0;
            asm volatile("s_waitcnt vmcnt(%0) lgkmcnt(0)\n\ts_barrier" :: "n"(n) : "memory");
            if constexpr (st > 0) take(IC<st - 1>{}, WW, smem, -1);
            if constexpr (st + AH < N) issue(st + AH);
            if constexpr (st == GE) GO_ASK;
            if constexpr (st == HE || st == N) asm volatile("" : "+v"(go.v));
            constexpr int sl = (st + SH) % NS, hi = sl / (65536 / SL) * (65536 / SL);
            if constexpr (st < N) take(ST, WW, smem + hi * SL, (sl - hi) * SL);
            if constexpr (NE && st == HE) early();
        });
    };
    if (!V || w == 0) run(IC<0>{}); else if (w == 1) run(IC<1>{}); else if (w == 2) run(IC<2>{}); else run(IC<3>{});
}
template <int R, int NTT> __device__ __forceinline__ void take_k(int ks, int ww, const char* p, int o, const int (&rp)[NTT], int hf, bf16x8 (&wf)[NTT][20]) {
#pragma unroll
    for (int tt = 0; tt < NTT; ++tt) {
        const int i = (ks + 20 - R * ww) % 20;
        if (o >= 0) wf[tt][i] = tr_read(p, rp[tt] + o, rp[tt] + hf + o);
        else if (20 * tt + i < 64) asm volatile("" : "+a"(wf[tt][i]));
        else asm volatile("" : "+v"(wf[tt][i]));
    }
}
#define SWP (R != 0)
#define ORD(st) (SWP ? (st & 1 ? 3u : -1u) : 1u)
#define BNT 4
template <int NI, int R, int NTT, int NE = 0, class EARLY = Nop>
__device__ __forceinline__ void load_coop(char* smem, const int w, Go& go, const void* const (&base)[NI], unsigned (&vo)[NI], const unsigned adv, const int (&rp)[NTT], const int hf, bf16x8 (&wf)[NTT][20], EARLY early = Nop{}) {
    load_ring<20, 20, NI, NI, (NI > 4 ? 6 : 8), (NI > 4 ? 24576 : 16384), R != 0, NE>(smem, w, go, [&](int st, int m, unsigned step) { dma_next<(NI > BNT)>(base[m], vo[m], step, ORD(st) * adv); },
        [&](auto ST, auto WW, const char* p, int o) { take_k<R, NTT>(decltype(ST)::value ^ SWP, decltype(WW)::value, p, o, rp, hf, wf); }, early);
}
__device__ __forceinline__ unsigned hd_src(int idx) { const int r = idx / 24, u = idx % 24; return r * (2 * HD_) + ((u * 16) ^ ((r & 6) << 4)); }
__device__ __forceinline__ int hd_pos(int l, int tile) { const int r = l >> 2; return r * 384 + ((32 * tile) ^ ((r & 6) << 4)) + (l & 3) * 8; }
__device__ __forceinline__ unsigned row_src(int idx) { return (idx * 16) ^ ((idx / 80 & 3) << 6); }
__device__ __forceinline__ int row_pos(int r, int piece) { return r * 1280 + ((piece ^ (r & 3)) << 6); }
template <int R, int NE = 0, class TILE, class EARLY = Nop> __device__ __forceinline__ void load_qk(char* smem, int w, int l, Go& go, const bf16_t* Wq, const bf16_t* Wk, TILE tile, bf16x8 (&wf)[6][20], EARLY early = Nop{}) {
    const void* const base[6] = {Wq, Wq, Wq, Wk, Wk, Wk};
    unsigned vo[6];
    int rp[6];
#pragma unroll
    for (int m = 0; m < 6; ++m) { vo[m] = hd_src((4 * (m % 3) + w) * 64 + l) + SWP * 64 * HD_; rp[m] = tile(m) / 12 * 12288 + hd_pos(l, tile(m) % 12); }
    load_coop<6, R, 6, NE>(smem, w, go, base, vo, 64 * HD_, rp, 6144, wf, early);
}
template <int R, class DST> __device__ __forceinline__ void load_vo(char* smem, int w, int l, Go& go, const bf16_t* Wv, const bf16_t* Wo, bf16x8 (&wf)[3][20], DST dst) {
    unsigned vo[3], v2[5];
    int r2[5];
    bf16x4 lo[10];
#pragma unroll
    for (int m = 0; m < 3; ++m) vo[m] = hd_src((4 * m + w) * 64 + l) + SWP * 64 * HD_;
#pragma unroll
    for (int m = 0; m < 5; ++m) v2[m] = row_src((4 * m + w) * 64 + l) + SWP * 20480;
#pragma unroll
    for (int m = 0; m < 5; ++m) r2[m] = row_pos(l >> 2, 5 * w + m) + (l & 3) * 8;
    const int rp[3] = {hd_pos(l, 2 * w), hd_pos(l, 2 * w + 1), hd_pos(l, 8 + w)};
    load_ring<32, 20, 3, 5, 8, 20480, R != 0>(smem, w, go, [&](int st, int m, unsigned step) { if (st < 20) dma_next<true>(Wv, vo[m % 3], step, ORD(st) * (64 * HD_)); else dma_next<true>(Wo, v2[m], step, ORD(st) * 20480u); },
        [&](auto ST, auto WW, const char* p, int o) {
            constexpr int st = decltype(ST)::value;
            if constexpr (st < 20) take_k<R, 3>(st ^ SWP, decltype(WW)::value, p, o, rp, 6144, wf);
            else {
#pragma unroll
                for (int t = 0; t < 10; ++t) {
                    if (o < 0) {
                        if (st & 1) { bf16x8& d = dst(st / 2 - 10, t, decltype(WW)::value); if (t == 0 && st < 28) asm volatile("" : "+a"(d)); else asm volatile("" : "+v"(d)); }
                        continue;
                    }
                    const bf16x4 x = __builtin_amdgcn_ds_read_tr16_b64_v4i16((lds_b4*)(p + r2[t / 2] + o + (t & 1) * 32));
                    if (st & 1) dst(st / 2 - 10, t, decltype(WW)::value) = SWP ? cat8(x, lo[t]) : cat8(lo[t], x); else lo[t] = x;
                }
            }
        });
}

struct MegaArgs {
    const bf16_t* x0; const bf16_t* mem0; const bf16_t* Wq; const bf16_t* Wk; const bf16_t* Wv; const bf16_t* Wo; const bf16_t* wqkv; const bf16_t* wo;
    char* ar; unsigned* tmo; char* hs; bf16_t* out;
    int nit, lag, roles, pad;
};

#define WSLP 2
#define LIVE u64 live_; asm volatile("s_add_u32 %0, %0, 1\n\ts_cmp_gt_u32 %0, %2\n\ts_cselect_b64 %1, 0, -1" : "+s"(spins), "=s"(live_) : "n"(kSpinCap) : "scc")
#define BAD(m) (__ballot(m == ~0u) & live_)
#define SPIN_END if (spins > kSpinCap) __hip_atomic_store(A.tmo, 1u + id, RLX_AGENT)
__device__ __forceinline__ void wave_wait(rsrc_t rx, unsigned off, unsigned so, unsigned& spins) {
#pragma unroll 1
    for (;;) {
        const unsigned v = sld4(rx, so, off);
        LIVE;
        if (!(__ballot(v == 0 && off != OOB) & live_)) break;
        __builtin_amdgcn_s_sleep(WSLP);
        MEMFENCE();
    }
}
__device__ __forceinline__ unsigned rtime() { return (unsigned)__builtin_amdgcn_s_memrealtime(); }
__device__ __forceinline__ void rt_until(unsigned target) {
    asm volatile("1:\n\ts_memrealtime vcc\n\ts_waitcnt lgkmcnt(0)\n\ts_sub_u32 vcc_lo, %0, vcc_lo\n\ts_cmpk_lt_u32 vcc_lo, 600\n\ts_cbranch_scc1 1b" :: "s"(target - 1) : "memory", "scc", "vcc");
}
#define RT_K 26
__device__ __forceinline__ void rt_fine(unsigned target) {
    u64 a, b, c;
    unsigned n;
    asm volatile("1:\n\ts_memrealtime vcc\n\ts_waitcnt lgkmcnt(0)\n\ts_sub_u32 vcc_lo, %4, vcc_lo\n\ts_cmpk_lt_u32 vcc_lo, 600\n\ts_cbranch_scc1 1b\n\t"
                 "s_add_u32 vcc_lo, vcc_lo, 8\n\ts_cbranch_scc0 3f\n\t"
                 "s_memrealtime %0\n\ts_nop 4\n\ts_memrealtime %1\n\ts_nop 4\n\ts_memrealtime %2\n\ts_lshl_b32 %3, %4, 2\n\ts_addk_i32 %3, %5\n\ts_nop 2\n\ts_memrealtime vcc\n\ts_waitcnt lgkmcnt(0)\n\t"
                 "s_sub_u32 %3, %3, vcc_lo\n\ts_mov_b64 vcc, %0\n\ts_sub_u32 %3, %3, vcc_lo\n\ts_mov_b64 vcc, %1\n\ts_sub_u32 %3, %3, vcc_lo\n\ts_mov_b64 vcc, %2\n\ts_sub_u32 %3, %3, vcc_lo\n\t"
                 "s_max_i32 %3, %3, 0\n\ts_min_i32 %3, %3, 64\n\t"
                 "s_branch 2f\n\t"
                 "3:\n\ts_add_u32 %3, %4, 13\n\t"
                 "4:\n\ts_memrealtime vcc\n\ts_waitcnt lgkmcnt(0)\n\ts_sub_u32 vcc_lo, %3, vcc_lo\n\ts_cmpk_lt_u32 vcc_lo, 16\n\ts_cbranch_scc1 4b\n\t"
                 "s_mov_b32 %3, 0\n\t"
                 "2:\n\ts_sub_u32 %3, %3, 5\n\ts_cbranch_scc0 2b"
                 : "=&s"(a), "=&s"(b), "=&s"(c), "=&s"(n) : "s"(target - 14), "n"(56 - RT_K) : "memory", "scc", "vcc");
}
#define LDA(dst, vo, so, imm) asm volatile("buffer_load_dwordx4 %0, %1, %2, %3 offen offset:%4 sc1" : "=v"(dst) : "v"(vo), "s"(rx), "s"(so), "n"(imm) : "memory")
#define LDI(dst, vo, so, imm) asm volatile("buffer_load_dwordx4 %0, %1, %2, %3 offen offset:%4 sc1" : "+v"(dst) : "v"(vo), "s"(rx), "s"(so), "n"(imm) : "memory")
#define LDA8(dst, vo, so, imm) asm volatile("buffer_load_dwordx2 %0, %1, %2, %3 offen offset:%4 sc1" : "=v"(dst) : "v"(vo), "s"(rx), "s"(so), "n"(imm) : "memory")
#define LDA4(dst, vo, so) asm volatile("buffer_load_dword %0, %1, %2, %3 offen sc1" : "=v"(dst) : "v"(vo), "s"(rx), "s"(so) : "memory")
#define WAITV(n, x) asm volatile("s_waitcnt vmcnt(%1)" : "+v"(x) : "n"(n) : "memory")
#define PIN_S(x) asm volatile("" : "+s"(x))
#define SZERO(x) asm volatile("s_mov_b32 %0, 0" : "=s"(x))
#define PIN_V(x) asm volatile("" : "+v"(x))
#define USE_V(x) asm volatile("" :: "v"(x))
#define LDS_AT(T, a) (*(__attribute__((address_space(3))) T*)(a))
__device__ __forceinline__ float dpp_ror8(float x) { return __uint_as_float(__builtin_amdgcn_mov_dpp(__float_as_uint(x), 0x128, 0xf, 0xf, true)); }
__device__ __forceinline__ unsigned lds_addr(const void* p) { return (unsigned)(size_t)(__attribute__((address_space(3))) const char*)p; }
#define DMA1(vo, so, dst, imm) asm volatile("s_mov_b32 m0, %3\n\ts_nop 0\n\tbuffer_load_dwordx4 %0, %1, %2 offen offset:%4 sc1 lds" :: "v"(vo), "s"(rx), "s"(so), "s"(dst), "n"(imm) : "memory", "m0")

enum { HS_A = 432, HS_E = 504, HS_F = 512, N_1 = 60, N_INIT = 108 };
__device__ __forceinline__ unsigned hs_tags(const Go& go, int l) { return (bld4(go.rh, l < 8 ? l * 1024 + HS_E : OOB) + 2) & ~1u; }
__device__ __forceinline__ rsrc_t arena(const MegaArgs& A, unsigned flip) { return mk_rsrc(arena_base(A.ar) + (flip << 21), ARENA); }
__device__ __forceinline__ unsigned hs_wait(Go& go, int l, int n, unsigned off, bool known, unsigned tag, unsigned& spins) {
#pragma unroll 1
    for (;;) {
        if (!known) tag = (__builtin_amdgcn_readlane(go.v[0], 63) + 2) & ~1u;
        LIVE;
        if (!(__ballot(l < n && ((go.v[0] ^ tag) | (go.v[1] ^ tag)) > 1) & live_)) break;
        __builtin_amdgcn_s_sleep(8);
        go.v = sld8(go.rh, go.so, off);
    }
    return __builtin_amdgcn_readfirstlane(go.v[0]) & 1;
}
#define GO_DECL(pool) Go go; go.rh = mk_rsrc(A.hs, 8192); go.so = (pool) * 1024
#define GO_ON(n) const rsrc_t rx = arena(A, hs_wait(go, l, n / 2, l * 8, false, 0, spins))
#define INIT_BEGIN(pool) const unsigned tags = hs_tags(go, l); go.v = sld8(go.rh, go.so, HS_F); INIT_FLIP(pool)
#define INIT_FLIP(pool) const unsigned tag = __builtin_amdgcn_readlane(tags, pool), flip = hs_wait(go, l, 1, HS_F, true, tag, spins); \
    const rsrc_t rx = arena(A, flip)
#define INIT_END(ii) asm volatile("s_waitcnt vmcnt(0)\n\ts_barrier" ::: "memory"); bst4(go.rh, t < 8 ? t * 1024 + (ii) * 4 : OOB, tags | flip)
#define INIT_SEEN hs_wait(go, l, N_INIT / 2, l * 8, true, tag, spins)
__device__ __forceinline__ void probe(const MegaArgs& A, const Go& go, int l, int id) {
    const rsrc_t rf = arena(A, 0);
    const unsigned tags = hs_tags(go, l);
    unsigned tm[2] = {~0u, ~0u};
    bst4(rf, l < 2 ? l * 8192 : OOB, 0u);
    DRAIN();
#pragma unroll
    for (int k = 0; k < 4; ++k) {
        const unsigned long long t0 = __builtin_amdgcn_s_memtime();
        bst4(rf, l ? OOB : (k & 1) * 8192, 0u);
        DRAIN();
        const unsigned d = (unsigned)(__builtin_amdgcn_s_memtime() - t0);
        tm[k & 1] = d < tm[k & 1] ? d : tm[k & 1];
    }
    bst8(go.rh, l < 8 ? l * 1024 + HS_F : OOB, u32x2{tags | (tm[1] < tm[0]), tags});
}
#define OUTAUX 0
#define DLYB0 900u
#define DLYB1 2400u
#define DLY1 0
#define DLY2 900
#define START_IDS const int t = threadIdx.x, l = t & 63, w = __builtin_amdgcn_readfirstlane(t >> 6); const unsigned l16b = l * 16; GO_DECL(id >> 5); unsigned spins; SZERO(spins)
__device__ __forceinline__ void init_area(const MegaArgs& A, const int ii, const int id, const unsigned t0) {
    START_IDS;
    INIT_BEGIN(id >> 5);
    const int i = 4 * ii + w;
    const bf16_t* mp = A.mem0 + (l & 7) * D_ + 32 * (i % 20) + 4 * (l >> 4);
    const u32x2 x = *(const u32x2*)mp, y = *(const u32x2*)(mp + 16);
    while (rtime() - t0 < DLY1) __builtin_amdgcn_s_sleep(4);
    sst16(rx, xm(XM_OFF + (i / 5 & 15) * 8192 + i % 5 * 1024, i / 80), l16b, u32x4{nosent(x[0]), x[1], nosent(y[0]), y[1]});
    auto fill = [&](unsigned o, unsigned n, unsigned f) {
#pragma unroll 2
        for (unsigned u = i * 1024; u < 2 * n; u += 163840) sst16(rx, 2 * o + u, l16b, u32x4{f, f, f, f});
    };
    fill(XM_OFF + XM_BUF, FI_OFF - PT_BUF - XM_OFF - XM_BUF, ~0u);
    fill(FI_OFF - PT_BUF, PT_BUF, 0u);
    fill(UM_OFF, 3 * UM_BUF, ~0u);
    for (unsigned u = i; u < 208; u += 160) {
        const unsigned f = u < 160 && (u / 16 * 8 + (u & 7) - (l & 1) * A.lag) >= NSEG_;
        sst16(rx, 2 * FI_OFF + u * 1024, l16b, u32x4{f, f, f, f});
    }
    INIT_END(16 + ii);
}
static_assert(FI_N == 80 && FX_OFF + 24576 <= UM_OFF && SG_FL == SG_ST + 8192 && SG_ST % 8192 == 0, "init");
__device__ __forceinline__ void first_rows(const MegaArgs& A, char* smem, const int sg, const int id, const unsigned t0) {
    START_IDS;
    set_m0(lds_addr(smem) + w * 1024);
    {
        unsigned vo[5];
#pragma unroll
        for (int m = 0; m < 5; ++m) vo[m] = row_src((4 * m + w) * 64 + l);
#pragma unroll
        for (int m = 0; m < 10; ++m) dma_next<true>(A.x0 + sg * (SEG_ * D_), vo[m % 5], 4096u, 20480u);
    }
    asm volatile("s_waitcnt vmcnt(0)\n\ts_barrier" ::: "memory");
    const unsigned tags = hs_tags(go, l);
    u32x4 xr[2][5], xl[2][5];
#pragma unroll 1
    for (int rt = 0; rt < 2; ++rt) {
        f32x4 v[10], a;
#pragma unroll
        for (int e = 0; e < 5; ++e) { xr[0][e] = xr[1][e]; xl[0][e] = xl[1][e]; }
#pragma unroll
        for (int e = 0; e < 10; ++e) {
            const u32x2 x = *(const u32x2*)(smem + rt * 20480 + row_pos(l & 15, 5 * w + e / 2) + (e & 1) * 32 + (l >> 4) * 8);
            xr[1][e / 2][2 * (e & 1)] = x[0]; xr[1][e / 2][2 * (e & 1) + 1] = x[1];
            v[e] = f32x4{lo16(x[0]), hi16(x[0]), lo16(x[1]), hi16(x[1])};
            a = e ? a + v[e] : v[0];
        }
        const float mw = red_sum_q((a[0] + a[1]) + (a[2] + a[3])) * (1.0f / 160);
#pragma unroll
        for (int e = 0; e < 10; ++e) { const f32x4 d = v[e] - mw; a = e ? __builtin_elementwise_fma(d, d, a) : d * d; }
        f32v2* sp = (f32v2*)(smem + 49152) + rt * 64 + (l & 15);
        sp[16 * w] = f32v2{mw, red_sum_q((a[0] + a[1]) + (a[2] + a[3]))};
        LDS_BARRIER();
        go.v = sld8(go.rh, go.so, HS_F);
        const f32v2 p = sp[l & 48];
        const float mean = red_sum_q(p[0]) * 0.25f, var = red_sum_q(p[1] + 160.f * (p[0] - mean) * (p[0] - mean)) * (1.0f / D_);
        const float mb = rb(mean), rd = 1.0f / rb(sqrtf(rb(rb(var) + 1e-5f)));
#pragma unroll
        for (int e = 0; e < 10; ++e) {
            const unsigned x = pk2(v[e][0] - mb, v[e][1] - mb), y = pk2(v[e][2] - mb, v[e][3] - mb);
            v[e] = f32x4{lo16(x), hi16(x), lo16(y), hi16(y)} * rd;
            if (e & 1) xl[1][e / 2] = pk8(v[e - 1], v[e]);
        }
    }
    INIT_FLIP(id >> 5);
    while (rtime() - t0 < (sg < 8 ? 0 : (sg < 16 ? DLY1 : DLY2))) __builtin_amdgcn_s_sleep(4);
#pragma unroll
    for (int e = 0; e < 20; ++e) {
        sst16(rx, seg_base(0, sg) + lp((e < 10 ? SG_XL : SG_XR) + (5 * w + e % 10 / 2) * 2048), l16b + (e & 1) * 1024, (e < 10 ? xl : xr)[e & 1][e % 10 / 2]);
        if (e == 9 && sg == 0) { asm volatile("s_waitcnt vmcnt(0)\n\ts_barrier" ::: "memory"); bst8(go.rh, t < 8 ? t * 1024 + HS_A : OOB, u32x2{tags | flip, tags}); }
    }
    const unsigned f = (w >> 1) - 1;
#pragma unroll
    for (int e = 0; e < 8; ++e) sst16(rx, 2 * (SG_OFF + SG_ST) + sg * 524288 + w * 8192 + e / 4 * 4096, l16b + (e & 3) * 1024, u32x4{f, f, f, f});
    INIT_END(sg + (sg < 16 ? 0 : 44));
}
#define LAGV 7
static_assert(LAGV < TR, "rings");
#define NTOUCH 0
#define VSLP 1
#define E_W (L ? 110 : 135)
#define K_W 5
#define K_0 2
#define E_X 40
#define E_B 95
#define E_S 22
#define E_T 10
#define E_G 54
#define IT_1 61
#define D_2 8
#define E_L 160
#define E_Z 60
#define E_C 0
#define E_V 150
#define PVU_E 1
#define XV_L 1
#define E_N 0
#define E_H 0
#define FARF 1
#define E_M 4
#define FASTR 1
#define A_0 0
#define A_Q 6
#define A_UP 3
#define A_K 8
__device__ __forceinline__ bool adapt(int& mg, unsigned xv, unsigned xp, int mx) {
    const int e = xv - xp;
    const bool late = (unsigned)(e + 16) >= 36u;
    mg = max(mg + (xv && !late ? e : 0), 0);
    PIN_S(mg);
    mg = min(mg, mx);
    PIN_S(mg);
    return late;
}
#define TIGHT(x, it, d) asm volatile("s_cmp_ge_i32 %1, %2\n\ts_cselect_b32 %0, %3, 0" : "=s"(x) : "s"(it), "s"(IT_1), "s"(d) : "scc")
#define CHAIN_WAIT(pv, off, so, code) if (__builtin_expect(__ballot(pv == 0) != 0, 0)) { if (so != OOB) { wave_wait(rx, off, so, spins); } }
#define GAPN 11
#define GAP() asm volatile("s_nop %0" :: "n"(GAPN))
#define LD5(v, so) do { LDA(v[0], l16b, so, 0); GAP(); LDA(v[1], l16b, so, 1024); GAP(); LDA(v[2], l16b, so, 2048); GAP(); LDA(v[3], l16b, so, 3072); GAP(); LDA(v[4], l16k, so, 0); } while (0)
#define CHAIN_IDS const int t = threadIdx.x, l = t & 63, l16 = l & 15, lq = l >> 4; const int w = __builtin_amdgcn_readfirstlane(t >> 6); \
    GO_DECL(id >> 5); const int lag = A.lag, itend = A.nit; const unsigned l16b = l * 16, l16k = l * 16 + 4096; unsigned spins = 0
#define CHAIN_STAGE const char* sg1 = smem + ((w + 1) & 3) * 5120 + l * 16; const char* sg2 = smem + ((w + 2) & 3) * 5120 + l * 16; const char* sg3 = smem + ((w + 3) & 3) * 5120 + l * 16; \
    char* sg0 = smem + w * 5120 + l * 16; auto mrd = [&](int i) { return *(const bf16x8*)((i < 10 ? sg1 : (i < 15 ? sg2 : sg3)) + (i % 5) * 1024); }; \
    const unsigned mo0 = xm(XM_OFF + (pool & 3) * 32768 + w * 8192, pool >> 2), fX = xm(FX_OFF + (pool & 3) * 64, pool >> 2); unsigned fXv = fX; PIN_V(fXv)
#define GATHER(take) do { _Pragma("unroll 1") for (;;) { LD5(v, mo); LIVE; if (!take(live_)) break; } } while (0)
#define CVT2(o0, o1, x) do { o0 = pk2(x[0], x[1]); o1 = pk2(x[2], x[3]); asm volatile("" : "+v"(o0), "+v"(o1)); } while (0)
#define RR_(op, d, x, y, n) op "_dpp " d ", " x ", " y " row_ror:" n " row_mask:0xf bank_mask:0xf\n\t"
#define ROWRED(op, x, y, f0, f1) f0 RR_(op, y, x, x, "1") RR_(op, y, x, y, "2") RR_(op, y, x, y, "3") f1 RR_(op, x, y, y, "4") RR_(op, x, y, x, "8") RR_(op, x, y, x, "12")
#define BIAS(x, a, b, c, d) asm volatile("v_mov_b32 %0, 0xff800000\n\tv_cndmask_b32 %1, %0, 0, %5\n\tv_cndmask_b32 %2, %0, 0, %6\n\tv_cndmask_b32 %3, %0, 0, %7\n\tv_cndmask_b32 %0, %0, 0, %4" \
    : "=&v"(x[0]), "=&v"(x[1]), "=&v"(x[2]), "=&v"(x[3]) : "s"(a), "s"(b), "s"(c), "s"(d))
#define AND2(o0, o1, a, b, m) do { o0 = a & m; o1 = b & m; asm volatile("" : "+v"(o0), "+v"(o1)); } while (0)

constexpr int SL_S = 20480, SS_W = 3136, SL_KM = SL_S + 4 * SS_W, SL_KT = SL_KM + 6144, SL_W = SL_KT + 49152, SL_END = SL_W + 4 * 18432;
static_assert(SL_END <= 163840, "LDS");
#define G_LK 100
#define G_K 32
#define G_KS 6
__device__ __forceinline__ void s_role(const MegaArgs& A, char* smem, const int h, const int id) {
    CHAIN_IDS;
    const int pool = h >> 2, hs = hside(h);
    bf16x8 wf[6][20];
    const char* sW = smem + SL_W + w * 18432 + l * 16 - 2048;
    load_qk<5>(smem, w, l, go, A.Wq + h * DH_, A.Wk + h * DH_, [&](int m) { return m / 3 * 12 + (m % 3 < 2 ? 2 * w + m % 3 : 8 + w); }, wf);
#pragma unroll
    for (int i = 2; i < 20; ++i) *(bf16x8*)(sW + i * 1024) = wf[5][i];
    GO_ON(N_1);
    *(f32x4*)(smem + SL_S + w * SS_W + 3120) = f32x4{-INFINITY, -INFINITY, -INFINITY, -INFINITY};
#pragma unroll
    for (int i = 0; i < 12; ++i) *(u32x4*)(smem + SL_KT + w * 12288 + i * 1024 + l * 16) = u32x4{0u, 0u, 0u, 0u};
    asm volatile("s_waitcnt lgkmcnt(0)" ::: "memory");
    CHAIN_STAGE;
    char* sSw = smem + SL_S + w * SS_W + lq * 256 + ((l16 + 4 * lq) & 15) * 16;
    const int kq = l16, sL = w >> 1, rt = w & 1, row8 = (4 * w + lq) & 7;
    const bool kval = kq < 8 || (kq >> 1) == 4 + sL;
    const char* sSr = smem + SL_S + (kval ? (kq >> 2) * 1040 + (kq & 3) * 256 + ((4 * w + lq + 4 * kq) & 15) * 16 : 3120);
    const unsigned ppos = !kval ? OOB : xm(XP_OFF + (h >> 3) * 8192 +
        (kq < 8 ? ((h & 7) >> 1) * 1024 + ((kq & 3) * 16 + (h & 1) * 8 + row8) * 16 + (kq >> 2) * 8 : 4096 + ((h & 3) * 16 + ((h >> 2) & 1) * 8 + row8) * 16 + (kq & 1) * 8), sL);
    const unsigned pt1 = xm(PT_OFF + (h >> 3) * 16384 + (rt ? (h & 7) * 1024 : 8192 + ((h & 7) >> 1) * 1024 + (h & 1) * 8) + l * 16, sL);
    const unsigned pt2 = lq == 2 * sL ? xm(PT_OFF + (h >> 3) * 16384 + 12288 + (((h >> 2) & 1) * 2 + rt) * 1024 + ((h & 3) * 16 + l16) * 16, sL) : OOB;
    const u64 cm0 = __ballot(4 * lq <= l16), cm1 = __ballot(4 * lq + 1 <= l16), cm2 = __ballot(4 * lq + 2 <= l16), cm3 = __ballot(4 * lq + 3 <= l16), mval = __ballot((lq >> 1) == sL), oval = __ballot(rt);
    const unsigned ma = l16 < 8 ? ~0u : 0u, mb2 = ~ma;
    const unsigned fK = xm(FI_OFF + (h & 15) * 64 + (4 * sL + (l & 3)) * 4, hs);
    const unsigned qo = ((sL * 16 + (h & 15)) * 4 + rt) * 16384, ktl = lds_addr(smem + SL_KT) + w * 12288;
    const char* sKT = smem + SL_KT + l * 16;
    char* sKM = smem + SL_KM + l * 16;
    char* sKX = smem + SL_KT + l * 16 + 4096 + (w >> 1) * 1024 + (w & 1) * 8;
    unsigned X = rtime() - PER;
    int mg = A_0;
    auto qk_so = [&](int it) { const int sg = it - sL * lag; return (unsigned)sg < NSEG_ ? qk_base(sg, hs) + qo : OOB; };
    auto dm0 = [&](int e) {
        if (e == 0) asm volatile("s_mov_b32 m0, %0" :: "s"(ktl) : "m0");
        if (e == 4 || e == 6 || e == 10) asm volatile("s_add_u32 m0, m0, %0" :: "n"(e == 6 ? 2048 : 4096) : "m0", "scc");
    };
    auto dma = [&](int e, unsigned sq, unsigned sk, bool set = true) {
        if (set && (e == 0 || e == 4 || e == 6 || e == 10)) { dm0(e); asm volatile("s_nop 0"); }
        asm volatile("buffer_load_dwordx4 %0, %1, %2 offen offset:%3 sc1 lds" :: "v"(e % 6 < 4 ? l16b : l16k), "s"(rx), "s"(e < 6 ? sk : sq), "n"((e % 6 & 3) * 1024) : "memory");
    };
    {
        const unsigned pv0 = bld4(rx, fK), z = 0, so = qk_so(0);
        CHAIN_WAIT(pv0, fK, z, 1);
#pragma unroll
        for (int e = 0; e < 12; ++e) dma(e, so, so + 32768);
        asm volatile("s_waitcnt vmcnt(0)\n\ts_barrier" ::: "memory");
    }

    unsigned c0 = 0, c1 = 65536, c2 = 131072, mo = mo0, fs = -256, fs1 = 1024, sg = -sL * lag, q0 = 2 * QK_OFF + qo + hs * 8192;
    static_assert(2 * XP_BUF == 65536 && PT_BUF == 65536 && XM_BUF == 131072, "strides");
    PIN_S(c0); PIN_S(c1); PIN_S(c2); PIN_S(fs); PIN_S(fs1); PIN_S(sg); PIN_S(q0);
    for (int it = 0; it < itend; ++it) {
        unsigned ps, pr, ts, tr, qn, kn, tz, fk, per, xp;
        bf16x8 wl[3];
        f32x4 sc[3];
        u32x4 v[5], kp[2][2], kx[2];
        unsigned pvK;
        {
            bf16x8 ka[3], kb[3], q[3];
            auto krd = [&](int f, int e) {
                if (e == 0) kb[f % 3] = *(const bf16x8*)(sKT + (w & 2) * 12288 + f * 1024);
                if (e == 1) q[f % 3] = *(const bf16x8*)(sKT + w * 12288 + 6144 + f * 1024);
                if (e == 2) ka[f % 3] = *(const bf16x8*)(sKT + w * 12288 + f * 1024);
            };
            BIAS(sc[0], oval, oval, oval, oval); BIAS(sc[1], cm0, cm1, cm2, cm3);
            asm volatile("s_waitcnt vmcnt(%0)" :: "n"(5) : "memory");
#pragma unroll
            for (int e = 0; e < 9; ++e) { krd(e / 3, e % 3); SCHED0(); }
            PIN_V(ka[2]);
            rt_fine(X - E_G);
            sfor<0, 13>([&](auto G) {
                constexpr int g = decltype(G)::value, f = g / 2;
                if constexpr (g % 3 == 0) LDA(v[g / 3], g < 12 ? l16b : l16k, mo, (g / 3 & 3) * 1024);
                if constexpr (g < 12) MF_VV(sc[g & 1], (g & 1 ? ka : kb)[f % 3], q[f % 3]);
                SCHED0();
                if (g < 6) { if (g & 1) { krd(f + 3, 1); krd(f + 3, 2); } else krd(f + 3, 0); }
                SCHED0();
            });
#pragma unroll
            for (int e = 0; e < 2; ++e) {
                const u32x2 ka = *(const u32x2*)(sKX + e * 12288), kb = *(const u32x2*)(sKX + (e + 2) * 12288);
                kx[e] = u32x4{ka[0], ka[1], kb[0], kb[1]};
            }
            sg += 1;
            qn = (sg & (TR - 1)) << 21;
            qn += ((qn >> 8) & 8192) ^ q0;
            qn = sg < NSEG_ ? qn : OOB;
            kn = qn + 32768;
            PIN_S(sg); PIN_S(qn); PIN_S(kn);
            if (!G_LK) LDA4(pvK, fK, fs1);
            PIN_V(kx[0]); PIN_V(kx[1]);
        }
        f32x4 acc[6];
        auto mf = [&](int i, int tt, const bf16x8 xb) {
            if (i == 0) { if (tt < 4) MF_AV0(acc[tt], wf[tt][0], xb); else MF_VV0(acc[tt], wf[tt][0], xb); }
            else if (20 * tt + i < 64) MF_AV(acc[tt], wf[tt][i], xb);
            else if (tt < 5 || i < 2) MF_VV(acc[tt], wf[tt][i], xb);
            else MF_VV(acc[5], wl[i % 3], xb);
        };
        auto take = [&](u64 live_, auto N) {
            unsigned m = 0;
            u64 bad;
            sfor<0, 22>([&](auto G) {
                constexpr int g = decltype(G)::value;
                if constexpr (g < 20 && g % 4 == 0) { WAITV(4 + (decltype(N)::value && !G_LK) - g / 4, v[g / 4]); *(u32x4*)(sg0 + g / 4 * 1024) = v[g / 4]; SCHED0(); }
                mf(g / 6, g % 6, asb8(v[g / 6]));
                SCHED0();
                if constexpr (g < 20 && g % 4 == 0) { m = mx4(m, v[g / 4]); if (g < 16) PIN_V(m); else { bad = __ballot(m == ~0u); PIN_S(bad); } }
                if constexpr (g == 1 || g == 2 || g == 6 || g == 13) { constexpr int e = g < 3 ? g - 1 : 2 + g / 8; kp[e >> 1][e & 1] = *(const u32x4*)(sKT + e * 12288 + w * 1024); }
                if constexpr (g == 5 || g == 10 || g == 18) wl[(g / 5 + 1) % 3] = *(const bf16x8*)(sW + (g / 5 + 1) * 1024);
                if (g == 18 && !decltype(N)::value) { bad &= live_; PIN_S(bad); }
                if (g == 20 && decltype(N)::value && !G_LK) WAITV(0, pvK);
                SCHED0();
            });
            return bad;
        };
        if (__builtin_expect(take(~0ull, IC<1>{}) != 0, 0)) {
            auto again = [&](u64 live_) { return take(live_, IC<0>{}); };
            GATHER(again);
            asm volatile("s_nop 7" : "+v"(acc[0]), "+v"(acc[1]), "+v"(acc[2]), "+v"(acc[3]), "+v"(acc[4]), "+v"(acc[5]));
        }
        unsigned xv, kx0;
        u64 nk;
        u32x4 ko, kz;
        {
            bf16x8 mm[3];
            u32x4 qp, qa, qb, qz;
            unsigned z0, z1;
            f32x4 s[2], sm;
            sfor<22, 128>([&](auto G) {
                constexpr int g = decltype(G)::value, kt[13] = {5, 5, 5, 3, 4, -1, -1, 3, -1, 4, -1, 3, 4}, ki[13] = {17, 18, 19, 17, 17, 0, 0, 18, 0, 18, 0, 19, 19};
                constexpr int i = g < 102 ? g / 6 : (g < 111 ? 17 + (g - 102) / 3 : (g < 124 ? ki[g < 111 || g > 123 ? 0 : g - 111] : 0));
                constexpr int tt = g < 102 ? g % 6 : (g < 111 ? (g - 102) % 3 : (g < 124 ? kt[g < 111 || g > 123 ? 0 : g - 111] : -1));
                if constexpr (tt >= 0) mf(i, tt, i < 5 ? asb8(v[i < 5 ? i : 0]) : mm[(i < 5 ? 0 : i - 5) % 3]);
                if constexpr (g == 116 || g == 117) MF_VV0(s[g & 1], kp[0][g & 1], qa);
                if constexpr (g == 119 || g == 124) MF_VV(s[~g & 1], kp[1][~g & 1], qb);
                if constexpr (g == 121 || g == 125) MF_VV(s[g == 125], kx[g == 125], qz);
                if constexpr (g == 126) MF_VV0(sm, kz, qz);
                if constexpr (g == 127) MF_VV(sm, ko, qp);
                SCHED0();
                if (g == 22) { LDS_BARRIER(); }
                if (g == 23) mm[0] = mrd(5);
                if (g == 24) wl[2] = *(const bf16x8*)(sW + 5120);
                if constexpr (g > 24 && g < 103 && g % 6 == (g < 102)) mm[(g / 6 - 3) % 3] = mrd(g / 6 + 2);
                if constexpr (g > 24 && g < 102 && g % 6 == 3) wl[(g / 6 + 2) % 3] = *(const bf16x8*)(sW + (g / 6 + 2) * 1024);
                if (g == 104) wl[1] = *(const bf16x8*)(sW + 19 * 1024);
                if (g == 110) PIN_V(wl[1]);
                if (g == G_LK) LDA4(pvK, fK, fk);
                if (g == 26 && !G_LK) { nk = __ballot(pvK == 0); PIN_S(nk); dm0(0); }
                if (g == 30 && !G_LK) asm volatile("s_cmp_eq_u64 %1, 0\n\ts_cselect_b32 %0, %2, %3" : "=s"(kx0) : "s"(nk), "s"(kn), "n"(OOB) : "scc");
                if constexpr (!G_LK && g >= G_K && g <= G_K + 5 * G_KS && (g - G_K) % G_KS == 0) dma((g - G_K) / G_KS, qn, kx0, false);
                if (g == G_K + 4 * G_KS - 2 && !G_LK) dm0(4);
                if (g == 36) { ps = c0; pr = c2; PIN_S(ps); PIN_S(pr); }
                if (g == 42) { c2 = c0; c0 = c1; PIN_S(c2); PIN_S(c0); }
                if (g == 48) { c1 = pr; ts = 2 * ps; PIN_S(c1); PIN_S(ts); }
                if (g == 60) { mo = mo0 + 4 * c0; PIN_S(mo); }
                if (g == 66) { fs += 256; tz = fs & 8192; PIN_S(fs); PIN_S(tz); }
                if (g == 72) { fs += tz; tr = 2 * c0; PIN_S(fs); PIN_S(tr); }
                if (g == 84) { fk = fs1; fs1 += 1024; PIN_S(fk); PIN_S(fs1); }
                if (g == 90) { fs1 += fs1 & 8192; PIN_S(fs1); }
                if (g == 54) TIGHT(per, it, D_1);
                if (g == 78) { per = PER - per; PIN_S(per); }
                if (g == 96) { xp = X; X += per + mg; PIN_S(xp); PIN_S(X); }
                if (g == 111) CVT2(qp[0], qp[1], acc[0]);
                if (g == 112) CVT2(qp[2], qp[3], acc[1]);
                if (g == 113) AND2(qa[0], qa[1], qp[0], qp[1], ma);
                if (g == 114) AND2(qa[2], qa[3], qp[2], qp[3], ma);
                if (g == 115) CVT2(z0, z1, acc[2]);
                if (g == 118) AND2(qz[0], qz[1], z0, z1, ma);
                if (g == 119) AND2(qz[2], qz[3], z0, z1, mb2);
                if (g == 116) AND2(qb[0], qb[1], qp[0], qp[1], mb2);
                if (g == 117) AND2(qb[2], qb[3], qp[2], qp[3], mb2);
                if (g == 120) CVT2(kz[0], kz[1], acc[5]);
                if (g == 121) asm volatile("v_cvt_pk_bf16_f32 %0, %2, %3\n\tv_cvt_pk_bf16_f32 %1, %4, %5" : "=&v"(kz[2]), "=&v"(kz[3]) : "v"(acc[5][0]), "v"(acc[5][1]), "v"(acc[5][2]), "v"(acc[5][3]));
                if (g == 123) *(f32x4*)sSw = s[0];
                if (g == 124) CVT2(ko[0], ko[1], acc[3]);
                if (g == 125) CVT2(ko[2], ko[3], acc[4]);
                if (g == 127) *(f32x4*)(sSw + 1040) = s[1];
                SCHED0();
            });
            asm volatile("s_nop 6" : "+v"(sm) :: "memory");
            *(f32x4*)(sSw + 2080) = sm;
        }
        LDS_BARRIER();
        constexpr float SC = 0.07216878364870322f * 1.4426950408889634f;
        {
            const f32x4 s0 = *(const f32x4*)sSr, s1 = *(const f32x4*)(sSr + SS_W), s2 = *(const f32x4*)(sSr + 2 * SS_W), s3 = *(const f32x4*)(sSr + 3 * SS_W);
            SCHED0();
            f32x4 sv = ((s0 + s1) + s2) + s3;
            u32x2 p;
            p[0] = __float_as_uint(fmaxf(fmaxf(fmaxf(sv[0], sv[1]), sv[2]), sv[3]));
            asm volatile(ROWRED("v_max_f32", "%0", "%1", "v_mul_f32 %2, %6, %2\n\tv_mul_f32 %3, %6, %3\n\t", "v_mul_f32 %4, %6, %4\n\tv_mul_f32 %5, %6, %5\n\t")
                         "v_fma_f32 %2, -%0, %6, %2\n\tv_fma_f32 %3, -%0, %6, %3\n\tv_fma_f32 %4, -%0, %6, %4\n\tv_fma_f32 %5, -%0, %6, %5\n\t"
                         "v_exp_f32 %2, %2\n\tv_exp_f32 %3, %3\n\tv_exp_f32 %4, %4\n\tv_exp_f32 %5, %5\n\t"
                         "v_add_f32 %0, %2, %3\n\tv_add_f32 %0, %0, %4\n\tv_add_f32 %0, %0, %5\n\t"
                         ROWRED("v_add_f32", "%0", "%1", "s_nop 1\n\t", "s_nop 1\n\t")
                         "v_rcp_f32 %0, %0\n\ts_nop 0\n\tv_mul_f32 %2, %0, %2\n\tv_mul_f32 %3, %0, %3\n\tv_mul_f32 %4, %0, %4\n\tv_mul_f32 %5, %0, %5\n\t"
                         "v_cvt_pk_bf16_f32 %0, %2, |%3|\n\tv_cvt_pk_bf16_f32 %1, %4, %5"
                         : "+v"(p[0]), "=&v"(p[1]), "+v"(sv[0]), "+v"(sv[1]), "+v"(sv[2]), "+v"(sv[3]) : "s"(SC));
            sst8(rx, ps, ppos, p);
        }
        SCHED0();
        {
            if (G_LK) { WAITV(1, pvK); nk = __ballot(pvK == 0); }
            if (__builtin_expect(nk != 0, 0)) {
                wave_wait(rx, fK, fk, spins);
#pragma unroll
                for (int e = 0; e < 6; ++e) if (!G_LK) dma(e, qn, kn);
            }
            bf16x8 km[6], q[6];
            *(u32x4*)(sKM + w * 1024) = ko;
            *(u32x2*)(sKX + (SL_KM - SL_KT)) = u32x2{kz[0], kz[1]};
            SCHED0();
#pragma unroll
            for (int f = 0; f < 6; ++f) { q[f] = *(const bf16x8*)(sKT + w * 12288 + 6144 + f * 1024); SCHED0(); if (G_LK) dma(f, qn, kn); SCHED0(); }
            LDA4(xv, fXv, fs);
            dm0(6);
            asm volatile("s_waitcnt lgkmcnt(5)\n\ts_barrier" ::: "memory");
#pragma unroll
            for (int f = 0; f < 6; ++f) { km[f] = *(const bf16x8*)(sKM + f * 1024); SCHED0(); }
            dma(6, qn, kn, false);
            BIAS(sc[2], mval, mval, mval, mval);
            PIN_V(q[5]);
            dma(7, qn, kn, false);
            float mx = max3r(max3r(sc[0][0], sc[0][1], sc[0][2]), max3r(sc[0][3], sc[1][0], sc[1][1]), max2r(sc[1][2], sc[1][3])), tm;
            PIN_V(mx);
#pragma unroll
            for (int f = 0; f < 6; ++f) { MF_VV(sc[2], km[f], q[f]); if (f & 1) dma(8 + f / 2, qn, kn, false); if (f == 4) dm0(10); SCHED0(); }
            asm volatile("s_nop 5" : "+v"(sc[2]));
            mx = max3r(max3r(mx, sc[2][0], sc[2][1]), sc[2][2], sc[2][3]);
            asm volatile("v_mov_b32 %1, %0\n\ts_nop 1\n\tv_permlane16_swap_b32 %0, %1\n\tv_max_f32 %0, %0, %1\n\tv_mov_b32 %1, %0\n\ts_nop 1\n\tv_permlane32_swap_b32 %0, %1\n\tv_max_f32 %0, %0, %1" : "+v"(mx), "=&v"(tm));
            dma(11, qn, kn);
            mx *= -SC;
#pragma unroll
            for (int e = 0; e < 3; ++e) {
#pragma unroll
                for (int r = 0; r < 4; ++r) sc[e][r] = __builtin_fmaf(sc[e][r], SC, mx);
                PIN_V(sc[e]);
            }
#pragma unroll
            for (int e = 0; e < 3; ++e) {
#pragma unroll
                for (int r = 0; r < 4; ++r) sc[e][r] = __builtin_amdgcn_exp2f(sc[e][r]);
            }
            const f32x4 t4 = (sc[0] + sc[1]) + sc[2];
            float sum = (t4[0] + t4[1]) + (t4[2] + t4[3]);
            PIN_V(sum);
            float inv = __builtin_amdgcn_rcpf(red_sum_q(sum));
            PIN_V(inv);
            unsigned u[6];
#pragma unroll
            for (int e = 0; e < 6; ++e) { u[e] = pk2(sc[e >> 1][2 * (e & 1)] * inv, sc[e >> 1][2 * (e & 1) + 1] * inv); if (!(e & 1)) u[e] = nosent(u[e]); }
            const auto a4 = __builtin_amdgcn_permlane16_swap(u[4], u[4], false, false), a5 = __builtin_amdgcn_permlane16_swap(u[5], u[5], false, false);
            sst16(rx, ts, pt2, u32x4{u[4], u[5], a4[1], a5[1]});
            u32x2 o0, o1;
            asm volatile("v_mov_b64 %0, -1\n\tv_mov_b64 %1, -1" : "=v"(o0), "=v"(o1));
            const u32x4 ones = {o0[0], o0[1], o1[0], o1[1]};
            if (rt) { sst16(rx, ts, pt1, u32x4{u[0], u[1], u[2], u[3]}); sst16(rx, tr, pt1, ones); } else { sst8(rx, ts, pt1, u32x2{u[2], u[3]}); sst8(rx, tr, pt1, u32x2{ones[0], ones[1]}); }
            sst8(rx, pr, ppos, u32x2{ones[0], ones[1]});
            sst16(rx, tr, pt2, ones);
            asm volatile("s_waitcnt vmcnt(11)\n\ts_barrier" : "+v"(xv) :: "memory");
            xv = __builtin_amdgcn_readfirstlane(xv);
            adapt(mg, xv, xp, A_MAX);
            X = xv ? xv + per + mg : X;
        }
    }
    SPIN_END;
}

constexpr int CL_R = 0, CL_C = CL_R + 16384, CL_END = CL_C + 4096;
__device__ __forceinline__ void c_role(const MegaArgs& A, char* smem, const int pool, const int ku, const int id) {
    CHAIN_IDS;
    const int cL = pool >> 2, j = 5 * (pool & 3) + ku;
    go.v = sld8(go.rh, go.so, l * 8);
    GO_ON(N_1);
    unsigned fXv = xm(FX_OFF + (pool & 3) * 64, cL);
    PIN_V(fXv);
    const int hi = (l >> 3) & 1, tk = l & 7;
    const unsigned pos = xm(XM_OFF + w * 32768 + (j / 5) * 8192 + (j % 5) * 1024 + (lq * 16 + cL * 8 + tk) * 16 + hi * 8, 0);
    const unsigned umo = xm(UM_OFF + (16 * j + 2 * w) * 1024, cL), po0 = xm(XP_OFF + w * 8192, cL), pto = xm(PT_OFF + w * 16384, cL), uto = xm(UT_OFF + (64 * j + 8 * w) * 1024, cL);
    const unsigned fU = lp(SG_FL + FL_U + (8 * w + tk) * 64 + (pool & 3) * 4);
    const unsigned xo = lp((2 * j + (w >> 1)) * 1024 + (lq * 16 + 8 * (w & 1) + tk) * 16 + hi * 8), sto = l < 8 ? lp(SG_ST + (4 * j + w) * 64 + l * 8) : OOB;
    u32x2 alt;
    { const u32x2 m = *(const u32x2*)(A.mem0 + tk * D_ + 32 * j + 16 * hi + 4 * lq); alt = u32x2{nosent(m[0]), m[1]}; }
    f32x4* sRw0 = (f32x4*)(smem + CL_R) + w * 256 + l;
    f32x4* sRw1 = (f32x4*)(smem + CL_R) + w * 256 + 128 + (l ^ 8);
    const f32x4* sRr = (const f32x4*)(smem + CL_R) + (hi * 2 + (w >> 1)) * 64 + lq * 16 + ((8 * (w & 1) + tk) ^ (8 * hi));
    f32x4* sC = (f32x4*)(smem + CL_C);
    int mb = 0;
    u32x4 ua[16], ub[16], um[4];
#pragma unroll
    for (int e = 0; e < 16; ++e) ua[e] = u32x4{0u, 0u, 0u, 0u};
#pragma unroll
    for (int e = 0; e < 4; ++e) um[e] = u32x4{0u, 0u, 0u, 0u};
    for (int s = 0; s < NTOUCH; ++s) sst8(rx, seg_base(cL, s) + lp(SG_X1), xo, u32x2{0u, 0u});
    unsigned X = rtime() - 2 * PER;
    auto seg_so = [&](int sg) { return (unsigned)sg < NSEG_ ? seg_base(cL, sg) : OOB; };
    int mg = A_0;
    const unsigned lead = ((w >> 1) == cL ? E_N : 0) + (w >> 1) * E_H;
    unsigned dx = 0, sb0 = OOB, sb1 = seg_so(-cL * lag), pvU = sld4(rx, sb1, fU), ts = pto + 4 * PT_BUF, ts2 = ts + 16384;
    auto step = [&](const int it, auto LAST, u32x4 (&up)[16], u32x4 (&ut)[16]) {
        constexpr bool last = decltype(LAST)::value == 1, fin = decltype(LAST)::value == 2;
        const int nb = (mb == 2) ? 0 : mb + 1, pb = mb ? mb - 1 : 2, sg = it - cL * lag;
        if (!last) CHAIN_WAIT(pvU, fU, sb1, 0);
        u32x4 pt[16];
        u32x2 xr;
        auto pt_load = [&](int e) { LDA(pt[e], e < 4 || e >= 12 ? l16k : l16b, e < 8 ? ts2 : ts, (e & 3) * 1024); };
        if (!last && E_C) rt_until(X + E_C - dx);
        pt_load(0);
        SCHED0();
        unsigned uo = sb1 != OOB ? uto + ut_slot(sg) : OOB, uo2 = uo + 65536;
        PIN_S(uo); PIN_S(uo2);
        auto ut_load = [&](int e) {
            if (!last) LDA(ut[e], (e & 4) ? l16k : l16b, e < 8 ? uo : uo2, (e & 3) * 1024);
        };
        pt_load(1);
        SCHED0();
        unsigned sbr = (sb0 ^ (cL << 13)) + lp(SG_XR), sbx = sb0 + lp(SG_X1), sb2 = seg_so(sg + 1);
        PIN_S(sbr); PIN_S(sbx); PIN_S(sb2);
        pt_load(2);
        SCHED0();
        unsigned fs = fx_step(it), cu = umo + mb * (2 * UM_BUF), cu2 = cu + 16384, cp = po0 + mb * (2 * XP_BUF), cr = pb * (2 * XM_BUF), cr2 = cr + 8192;
        PIN_S(fs); PIN_S(cu); PIN_S(cu2); PIN_S(cp); PIN_S(cr); PIN_S(cr2);
        pt_load(3);
        SCHED0();
        unsigned cs = sg < 0 ? OOB : nb * (2 * XM_BUF) + FARF * ((cL ^ 1) << 13), cs2 = cs ^ 8192;
        X += PER - dx + mg;
        PIN_S(cs); PIN_S(cs2); PIN_S(X);
#pragma unroll
        for (int e = 4; e < 13; ++e) { pt_load(e); if (e < 12) GAP(); }
        u32x2 st;
        {
            f32x4 d[2][2];
            auto check = [&](auto E0, auto N, auto W) {
                constexpr int e0 = decltype(E0)::value, n = decltype(N)::value;
                auto look = [&](auto WW) {
                    asm volatile("s_waitcnt vmcnt(%2)" : "+v"(pt[e0]), "+v"(pt[e0 + 1]) : "n"(decltype(WW)::value) : "memory");
                    if (n == 4) asm volatile("" : "+v"(pt[e0 + n - 2]), "+v"(pt[e0 + n - 1]));
                    unsigned m = 0;
#pragma unroll
                    for (int e = e0; e < e0 + n; ++e) m = mx4(m, pt[e]);
                    return m; };
                if (__builtin_expect(__ballot(look(W) == ~0u) != 0, 0)) {
#pragma unroll 1
                    for (;;) {
#pragma unroll
                        for (int e = e0; e < e0 + n; ++e) pt_load(e);
                        LIVE;
                        if (!BAD(look(IC<0>{}))) break;
                    }
                }
            };
            sfor<0, 40>([&](auto G) {
                constexpr int g = decltype(G)::value, n = g & 1;
                if constexpr (g == 0) check(IC<0>{}, IC<4>{}, IC<9>{});
                if constexpr (g == 8) check(IC<4>{}, IC<2>{}, IC<10>{});
                if constexpr (g == 16) check(IC<6>{}, IC<2>{}, IC<(last ? 9 : 11)>{});
                if constexpr (g == 24) check(IC<8>{}, IC<4>{}, IC<(last ? 5 : 10)>{});
                if constexpr (g == 32) check(IC<12>{}, IC<2>{}, IC<(last ? 3 : 11)>{});
                if constexpr (g == 36) { check(IC<14>{}, IC<2>{}, IC<(last ? 0 : 10)>{}); PIN_V(xr); }
                if constexpr (g < 4) MF_VV0(d[n][g >> 1], um[2 * n], pt[g >> 1]);
                else if constexpr (g < 8) MF_VV(d[n][(g >> 1) & 1], um[2 * n + 1], pt[g >> 1]);
                else if constexpr (g < 24) {
                    constexpr int hh = (g - 8) >> 1;
                    const u32x4 x = up[8 * n + hh], z = pt[4 + (hh >> 1)];
                    const u32x2 xl = {x[0], x[1]}, zl = {z[2 * (hh & 1)], z[2 * (hh & 1) + 1]};
                    MH_(d[n][0], "v", xl, "v", zl);
                } else MF_VV(d[n][1], up[8 * n + ((g - 24) >> 1)], pt[8 + ((g - 24) >> 1)]);
                constexpr int rq = g < 32 ? ((g & 7) == 2 ? 0 : ((g & 7) == 5 ? 1 : ((g & 7) == 7 ? 2 : -99))) + 3 * (g >> 3) : (g == 33 ? 12 : (g == 35 ? 13 : -99));
                if constexpr (rq >= 0 && rq < 3) pt_load(13 + rq); else if constexpr (rq == 3) LDA8(xr, xo, sbr, 0); else if constexpr (rq > 3) ut_load(rq - 4);
                if (g == 24) sRw0[0] = d[0][0];
                if (g == 27) sRw1[0] = d[1][0];
                SCHED0();
            });
            const f32x4 xf = {lo16(xr[0]), hi16(xr[0]), lo16(xr[1]), hi16(xr[1])};
            asm volatile("" : "+v"(d[0][1]) : "v"(xf) : "memory");
            sRw0[64] = d[0][1];
            ut_load(10);
            asm volatile("s_nop 2" : "+v"(d[1][1]) :: "memory");
            sRw1[64] = d[1][1];
            ut_load(11);
            LDS_BARRIER();
            const f32x4 s0 = sRr[0], s1 = sRr[256], s2 = sRr[512], s3 = sRr[768];
            ut_load(12);
            SCHED0();
            f32x4 s = (((xf + s0) + s1) + s2) + s3;
            sst8(rx, sbx, xo, u32x2{pk2(s[0], s[1]), pk2(s[2], s[3])});
            float sm = (s[0] + s[1]) + (s[2] + s[3]);
            sm += dpp_ror8(sm);
            PIN_V(sm);
            ut_load(13);
            sm = red_sum_q(sm) * (1.0f / 32);
            PIN_V(sm);
            ut_load(14);
            s -= sm;
            float sq = __builtin_fmaf(s[3], s[3], __builtin_fmaf(s[2], s[2], __builtin_fmaf(s[1], s[1], s[0] * s[0])));
            sq += dpp_ror8(sq);
            PIN_V(sq);
            ut_load(15);
            sq = red_sum_q(sq);
            st = u32x2{nosent(__float_as_uint(sm)), __float_as_uint(sq)};
            PIN_V(st);
        }
        SCHED0();
        if (last) { DRAIN(); sst8(rx, sb0, sto, st); return; }
        unsigned xv;
        rt_until(X + E_X);
        LDA4(xv, fXv, fs);
        ts = pto + mb * (2 * PT_BUF); ts2 = ts + 16384;
        PIN_S(ts); PIN_S(ts2);
        asm volatile("s_waitcnt vmcnt(4)" ::: "memory");
        sst8(rx, sb0, sto, st);
        sst8(rx, cr, pos, u32x2{~0u, ~0u});
        sst8(rx, cr2, pos, u32x2{~0u, ~0u});
        WAITV(3, xv);
        const unsigned xp = X;
        X = __builtin_amdgcn_readfirstlane(xv);
        PIN_S(X);
        if (__builtin_expect(X == 0, 0)) {
#pragma unroll 1
            for (;;) {
                LDA4(xv, fXv, fs);
                LIVE;
                WAITV(0, xv);
                X = __builtin_amdgcn_readfirstlane(xv);
                if (X || !live_) break;
            }
        }
        unsigned d2;
        TIGHT(dx, it, D_1); TIGHT(d2, it, D_2);
        adapt(mg, X, xp, A_MAX);
        d2 -= mg >> 1;
        if (fin) {
#pragma unroll 1
            for (;;) {
                rt_until(X + E_U - d2);
#pragma unroll
                for (int i = 0; i < 4; ++i) LDI(um[i], l16b, (i & 2) ? cu2 : cu, (i & 1) * 1024);
                LIVE;
                asm volatile("s_waitcnt vmcnt(0)" : "+v"(um[0]), "+v"(um[1]), "+v"(um[2]), "+v"(um[3]) :: "memory");
                if (!BAD(mx4(mx4(mx4(mx4(0u, um[0]), um[1]), um[2]), um[3]))) break;
            }
            rt_until(X + E_L);
            mb = nb; sb0 = sb1; sb1 = sb2;
            return;
        }
        u32x4 p[5];
        f32x4 a[2], b[2];
        auto take2 = [&](u64 live_) {
            unsigned m = 0;
            u64 bad;
#pragma unroll
            for (int i = 0; i < 4; ++i) { WAITV(9 - PVU_E - i, um[i]); m = mx4(m, um[i]); PIN_V(m); }
#pragma unroll
            for (int i = 0; i < 5; ++i) {
                WAITV(4 - i, p[i]);
#pragma unroll
                for (int e = 0; e < 4; ++e) {
                    f32x4& d = (e & 1) ? b[e >> 1] : a[e >> 1];
                    if (i == 0) MF_VV0(d, ut[8 * (e >> 1) + (e & 1)], p[0]); else if (i < 4) MF_VV(d, ut[8 * (e >> 1) + 2 * (i & 3) + (e & 1)], p[i]); else MF_VV(d, um[e], p[4]);
                    SCHED0();
                    if (e == 0) { m = mx4(m, p[i]); if (i < 4) PIN_V(m); else { bad = __ballot(m == ~0u); PIN_S(bad); } }
                    if (e == 1 && i == 4) { bad &= live_; PIN_S(bad); }
                    SCHED0();
                }
            }
            return bad;
        };
        {
            unsigned tP = X + E_P - d2 - lead, tU = X + E_U - d2 - E_M * (4 - ku) - lead, ask = 1;
            PIN_S(ask);
            if (PVU_E) LDA4(pvU, fU, sb2);
            rt_until(tU);
#pragma unroll 1
            for (;;) {
#pragma unroll
                for (int i = 0; i < 4; ++i) LDI(um[i], l16b, (i & 2) ? cu2 : cu, (i & 1) * 1024);
                if (!PVU_E) LDA4(pvU, fU, sb2);
                LIVE;
                if (__builtin_expect(ask | !FASTR, 1)) rt_fine(tP);
                LD5(p, cp);
                ask = 0; PIN_S(ask);
                SCHED0();
                if (!take2(live_)) break;
            }
        }
        f32x4 x;
        asm volatile("v_add_f32_dpp %0, %8, %4 row_ror:8 row_mask:0xf bank_mask:0x3\n\tv_add_f32_dpp %1, %9, %5 row_ror:8 row_mask:0xf bank_mask:0x3\n\t"
                     "v_add_f32_dpp %2, %10, %6 row_ror:8 row_mask:0xf bank_mask:0x3\n\tv_add_f32_dpp %3, %11, %7 row_ror:8 row_mask:0xf bank_mask:0x3"
                     : "=&v"(x[0]), "=&v"(x[1]), "=&v"(x[2]), "=&v"(x[3]) : "v"(a[0][0]), "v"(a[0][1]), "v"(a[0][2]), "v"(a[0][3]), "v"(b[0][0]), "v"(b[0][1]), "v"(b[0][2]), "v"(b[0][3]));
        asm volatile("s_nop 0\n\tv_add_f32_dpp %0, %8, %4 row_ror:8 row_mask:0xf bank_mask:0xc\n\tv_add_f32_dpp %1, %9, %5 row_ror:8 row_mask:0xf bank_mask:0xc\n\t"
                     "v_add_f32_dpp %2, %10, %6 row_ror:8 row_mask:0xf bank_mask:0xc\n\tv_add_f32_dpp %3, %11, %7 row_ror:8 row_mask:0xf bank_mask:0xc"
                     : "+v"(x[0]), "+v"(x[1]), "+v"(x[2]), "+v"(x[3]) : "v"(b[1][0]), "v"(b[1][1]), "v"(b[1][2]), "v"(b[1][3]), "v"(a[1][0]), "v"(a[1][1]), "v"(a[1][2]), "v"(a[1][3]));
        sC[w * 64 + l] = x;
        LDS_BARRIER();
        const f32x4 s = ((sC[l] + sC[64 + l]) + sC[128 + l]) + sC[192 + l];
        const u32x2 r = {nosent(pk2(s[0], s[1])), pk2(s[2], s[3])};
        sst8(rx, cs, pos, r);
        sst8(rx, cs2, pos, r);
        SCHED0();
        if (__builtin_expect(cs == OOB, 0)) { sst8(rx, nb * (2 * XM_BUF), pos, alt); sst8(rx, nb * (2 * XM_BUF) + 8192, pos, alt); }
        PIN_V(pvU);
        mb = nb; sb0 = sb1; sb1 = sb2;
    };
    int it = 0;
    PIN_S(spins); PIN_V(pvU); PIN_V(alt);
#pragma unroll 1
    do { step(it, IC<0>{}, ua, ub); step(it + 1, IC<0>{}, ub, ua); it += 2; } while (it + 2 < itend);
    if (LAGV & 1) { step(it, IC<2>{}, ua, ub); step(it + 1, IC<1>{}, ub, ua); } else { step(it, IC<0>{}, ua, ub); step(it + 1, IC<2>{}, ub, ua); step(it + 2, IC<1>{}, ua, ub); }
    SPIN_END;
}

constexpr int VL_V = 20480, VL_W = VL_V + 6144, VL_END = VL_W + 81920;
__device__ __forceinline__ void vm_role(const MegaArgs& A, char* smem, const int h, const int id) {
    CHAIN_IDS;
    const int pool = h >> 2;
    bf16x8 wf[3][20], wo[10][4];
    char* sWo = smem + VL_W + w * 20480 + l * 16;
    {
        bf16x8 tmp[20];
        load_vo<5>(smem, w, l, go, A.Wv + h * DH_, A.Wo + (size_t)(h * DH_) * D_, wf, [&](int k, int ct, int ww) -> bf16x8& { return k < 4 ? wo[ct][(k - ww) & 3] : tmp[4 * (ct >> 1) + 2 * (k & 1) + (ct & 1)]; });
#pragma unroll
        for (int i = 0; i < 20; ++i) *(bf16x8*)(sWo + i * 1024) = tmp[i];
    }
    GO_ON(N_1);
    CHAIN_STAGE;
    unsigned upos[5];
#pragma unroll
    for (int m = 0; m < 5; ++m) upos[m] = xm(UM_OFF + ((10 * w + 2 * m + (lq & 1)) * 8 + (h >> 2)) * 1024 + ((h & 3) * 16 + l16) * 16, lq >> 1);
    char* sV = smem + VL_V + l * 16;
    const char* sV1 = sV + ((w + 1) & 3) * 1024; const char* sV2 = sV + ((w + 2) & 3) * 1024; const char* sV3 = sV + ((w + 3) & 3) * 1024;
    const unsigned xw = (h == 1 && t < 8) ? xm(FX_OFF + (t & 3) * 64, t >> 2) : OOB;
    int mb = 0;
    unsigned X = rtime() - 2 * PER, dx = 0;
    int mg = A_0, cnt = 0;
    for (int it = 0; it < itend; ++it) {
        const int nb = (mb == 2) ? 0 : mb + 1, pb = mb ? mb - 1 : 2;
        unsigned mo = mo0 + mb * (2 * XM_BUF), fs = fx_step(it), us = mb * (2 * UM_BUF), ur = pb * (2 * UM_BUF);
        X += PER - dx + mg;
        const unsigned xp = X;
        PIN_S(mo); PIN_S(fs); PIN_S(us); PIN_S(ur); PIN_S(X);
        rt_fine(X - E_G);
        f32x4 acc[3];
        u32x4 v[5];
        auto take = [&](u64 live_) {
            unsigned m = 0;
            u64 bad;
            sfor<0, 11>([&](auto G) {
                constexpr int g = decltype(G)::value, e = g < 9 && !(g & 1) ? g / 2 : -1;
                if constexpr (e >= 0) { WAITV(4 - e, v[e]); *(u32x4*)(sg0 + e * 1024) = v[e]; SCHED0(); }
                if (g < 3) MF_AV0(acc[g % 3], wf[g % 3][0], v[0]); else MF_AV(acc[g % 3], wf[g % 3][g / 3], v[g / 3]);
                SCHED0();
                if constexpr (e >= 0) { m = mx4(m, v[e]); if (e < 4) PIN_V(m); else { bad = __ballot(m == ~0u); PIN_S(bad); } }
                if (g == 9) { bad &= live_; PIN_S(bad); }
                SCHED0();
            });
            return bad;
        };
        GATHER(take);
        bf16x8 wl[4];
        u32x4 vf[6], o;
        f32x4 d[10];
        unsigned xv;
        {
            constexpr int MW = 5;
            bf16x8 mm[MW];
            u32x2 vz;
            u64 tb64;
            unsigned t0, t1;
            asm volatile("s_waitcnt lgkmcnt(0)\n\ts_barrier\n\ts_memrealtime %0" : "=s"(tb64) :: "memory");
            mm[0] = mrd(5);
            SCHED0();
            sfor<11, 60>([&](auto G) {
                constexpr int g = decltype(G)::value, i = g < 45 ? g / 3 : (g < 50 ? g - 30 : 15 + (g - 50) / 2), tt = g < 45 ? g % 3 : (g < 50 ? 2 : g & 1);
                const bf16x8 xb = i < 5 ? asb8(v[i < 5 ? i : 0]) : mm[(i < 5 ? 0 : i - 5) % MW];
                MF_AV(acc[tt], wf[tt][i], xb);
                SCHED0();
                if constexpr (g < 15) mm[g - 10] = mrd(g - 5);
                if constexpr (g >= 18 && g < 46 && g % 3 == 0) mm[(g / 3 - 6) % MW] = mrd(g / 3 + 4);
                if constexpr (g >= 54 && g < 58) wl[g - 54] = *(const bf16x8*)(sWo + (g - 54) * 1024);
                if (g == 17) asm volatile("s_waitcnt lgkmcnt(0)" : "+s"(tb64) :: "memory");
                if (g == 19) asm volatile("s_sub_u32 %0, %2, %3\n\ts_add_u32 %1, %2, %4" : "=&s"(t0), "=&s"(t1) : "s"((unsigned)tb64), "s"(X), "s"(E_S) : "scc");
                if (g == 22) asm volatile("s_cmp_gt_i32 %1, %3\n\ts_cselect_b32 %0, %2, %0" : "+s"(X) : "s"(t0), "s"(t1), "s"(E_T) : "scc");
                if (g == 23) asm volatile("v_or_b32 %0, 1, %1" : "=v"(xv) : "s"(X));
                if (g == 25) sst4(rx, fs, xw, xv);
                if (g == 52) CVT2(vz[0], vz[1], acc[2]);
                if (g == 53) *(u32x2*)(sV + 4096 + (w >> 1) * 1024 + (w & 1) * 8) = vz;
                SCHED0();
            });
        }
        asm volatile("s_nop 2");
        SCHED0();
        CVT2(vf[0][0], vf[0][1], acc[0]);
        SCHED0();
        asm volatile("s_nop 1");
        SCHED0();
        CVT2(vf[0][2], vf[0][3], acc[1]);
        *(u32x4*)(sV + w * 1024) = vf[0];
        SCHED0();
        asm volatile("s_nop 0");
        sfor<0, 67>([&](auto G) {
            constexpr int g = decltype(G)::value, m = g < 10 ? 0 : (g - 10) / 10, r = g < 10 ? 0 : (g - 10) % 10, kk = g < 10 ? 0 : 1 + r / 2, ct = g < 10 ? g : 2 * m + (r & 1);
            if constexpr (g < 60) {
                if constexpr (kk >= 4) MF_VV(d[ct], vf[kk], wl[2 * (kk & 1) + (r & 1)]);
                else if constexpr (ct == 0) { if (kk == 0) MF_VA0(d[0], vf[0], wo[0][0]); else MF_VA(d[0], vf[kk], wo[0][kk]); }
                else { if (kk == 0) MF_VV0(d[ct], vf[0], wo[ct][0]); else MF_VV(d[ct], vf[kk], wo[ct][kk]); }
            }
            SCHED0();
            if (g == 3) { LDS_BARRIER(); }
            if (g == 4) vf[1] = *(const u32x4*)sV1;
            if (g == 5) vf[2] = *(const u32x4*)sV2;
            if (g == 6) vf[3] = *(const u32x4*)sV3;
            if (g == 7) vf[4] = *(const u32x4*)(sV + 4096);
            if (g == 8) vf[5] = *(const u32x4*)(sV + 5120);
            if constexpr (g >= 17 && g < 51 && (g - 17) % 10 < 4) wl[(g - 17) % 10] = *(const bf16x8*)(sWo + ((g - 17) / 10 * 4 + 4 + (g - 17) % 10) * 1024);
            if (g == (XV_L ? 66 : 12)) { if (XV_L) sst16(rx, us, upos[4], o); LDA4(xv, fXv, fs); }
            if constexpr (m > 0) {
                if (g == 60) asm volatile("s_nop 3");
                if (r == 1) CVT2(o[0], o[1], d[2 * m - 2]);
                SCHED0();
                if (g == 61) asm volatile("s_nop 1");
                if (r == 2) CVT2(o[2], o[3], d[2 * m - 1]);
                if (r == 3) { o[0] = nosent(o[0]); o[2] = nosent(o[2]); asm volatile("" : "+v"(o[0]), "+v"(o[2])); }
                if (r == 4) asm volatile("v_permlane16_swap_b32 %0, %1" : "+v"(o[1]), "+v"(o[3]));
                if (r == 5) asm volatile("v_permlane16_swap_b32 %0, %1" : "+v"(o[0]), "+v"(o[2]));
                if (r == 6 && (m < 5 || !XV_L)) sst16(rx, us, upos[m - 1], o);
            }
            SCHED0();
        });
        if (E_V) rt_until(X + E_V);
#pragma unroll
        for (int m = 0; m < 5; ++m) { __builtin_amdgcn_s_sleep(VSLP); sst16(rx, ur, upos[m], u32x4{~0u, ~0u, ~0u, ~0u}); SCHED0(); }
        WAITV(XV_L ? 5 : 10, xv);
        xv = __builtin_amdgcn_readfirstlane(xv);
        X = xv ? xv : X;
        cnt = adapt(mg, X | 1, xp, A_MAX) && it > 3 ? max(cnt, 0) + A_UP : cnt - 1;
        PIN_S(cnt);
        if (cnt >= A_K) { mg = min(mg + 2, A_MAX); cnt -= A_UP; }
        PIN_S(mg); PIN_S(cnt);
        if (cnt <= -A_Q) { mg = max(mg - 2, 0); cnt = 0; }
        PIN_S(mg); PIN_S(cnt);
        TIGHT(dx, it, D_1);
        mb = nb;
    }
    SPIN_END;
}
#define DBG_LD 0
#define WORK_IDS const int t = threadIdx.x, l = t & 63, l16 = l & 15, lq = l >> 4; const int w = __builtin_amdgcn_readfirstlane(t >> 6); \
    GO_DECL(id >> 5); const unsigned l16b = l * 16, dvo = w * 2048 + l * 16 + DBG_LD, lds0 = lds_addr(smem) + w * 2048; unsigned spins; SZERO(spins)
#define XSET(so, base, ld) asm volatile("s_mov_b32 m0, %2\n\ts_mov_b32 %0, %1" : "=s"(so) : "s"(base), "s"(ld) : "m0")
#define XADV(so) asm volatile("s_add_u32 m0, m0, 0x2000\n\ts_add_u32 %0, %0, 0x4000" : "+s"(so) :: "m0", "scc")
#define XDMA(so, rt) asm volatile("buffer_load_dwordx4 %0, %1, %2 offen offset:%3 sc1 lds" :: "v"(dvo), "s"(rx), "s"(so), "n"((rt) * 1024) : "memory")
#define XALL(so, base, ld) _Pragma("unroll") for (int e_ = 0; e_ < 10; ++e_) { if (e_ % 5 == 0) XSET(so, base, ld); else XADV(so); XDMA(so, e_ / 5); }
#define XALL2(so, base, ld) _Pragma("unroll") for (int e_ = 0; e_ < 5; ++e_) { if (e_ == 0) XSET(so, base, ld); else XADV(so); XDMA(so, 0); XDMA(so, 1); }
#define VMB(n) asm volatile("s_waitcnt vmcnt(%0)\n\ts_barrier" :: "n"(n) : "memory")
#define VMB_LATE(k) asm volatile("s_cmp_lg_u32 %0, %1\n\ts_cbranch_scc1 1f\n\ts_waitcnt vmcnt(%2)\n1:\n\ts_barrier" :: "s"(xs), "n"(OOB), "n"(k) : "memory", "scc")
#define VMW(n) asm volatile("s_waitcnt vmcnt(%0)" :: "n"(n) : "memory")
#define NEXT_DECL(asked) const unsigned fXo = l < 4 ? lp(SG_FL + FL_X + ((h & 12) + l) * 128) : OOB, fx1 = (((TR >> 2) & ~L & 1) ^ hside(h)) << 13; const int dl = __builtin_amdgcn_readfirstlane((L ^ 1) * TR), ld0 = __builtin_amdgcn_readfirstlane(L ? -K_W : K_0); \
    unsigned xb = seg_base(0, 0), xs, fb = seg_base(hside(h), 0), msk, so, pv, pend = OOB, fXv = xm(FX_OFF + ((h >> 2) & 3) * 64, hside(h)), xv, fs; PIN_V(fXv); if (L) wave_wait(rx, fXo, fb, spins); if (!(asked)) { XALL(so, xb, lds0); } VMB(5); int s; SZERO(s)
#define G_L1 150
#define G_E1 182
#define G_L2 210
#define NEXT_ADDR(a, st) \
    if (g == a) { s = __builtin_amdgcn_readfirstlane(s); PIN_S(s); xb = (s + 1) << 19; PIN_S(xb); } \
    if (g == a + st) { xs = (xb >> 8) & 8192; PIN_S(xs); } \
    if (g == a + 2 * st) { xb = (xb | xs) + 2 * SG_OFF; PIN_S(xb); } \
    if (g == a + 3 * st) { fb = (xb ^ fx1) - (dl << 19); PIN_S(fb); } \
    if (g == a + 5 * st) asm volatile("s_cmp_lt_i32 %1, %2\n\ts_cselect_b32 %0, 0, 15" : "=s"(msk) : "s"(s), "s"(dl - 1) : "scc"); \
    if (g == a + 6 * st) asm volatile("s_cmp_lt_i32 %1, 63\n\ts_cselect_b32 %0, %0, %2" : "+s"(fb) : "s"(s), "n"(OOB) : "scc"); \
    if (g == a + 7 * st) { PIN_S(s); fs = (s - ld0) << 8; PIN_S(fs); } \
    if (g == a + 8 * st) { fs += fs & ~8191u; PIN_S(fs); } \
    if (g == a + 9 * st) asm volatile("s_cmp_lt_i32 %0, 0\n\ts_cselect_b32 %0, %1, %0" : "+s"(fs) : "n"(OOB) : "scc")
#define NEXT_LOOK(n, ld) \
    if (g == G_L1 || g == G_L2) LDA4(pv, fXo, fb); \
    if (g == G_E1) { WAITV(n, pv); xs = (unsigned)__ballot(pv == 0); PIN_S(xs); } \
    if (g == G_E1 + 2) asm volatile("s_and_b32 %0, %0, %2\n\ts_cselect_b32 %0, %3, %1" : "+s"(xs) : "s"(xb), "s"(msk), "n"(OOB) : "scc")
#define T_D 20
#define NEXT_GO(n, ld, fl, nt) \
    WAITV(n, pv); \
    if (xs != OOB) { asm volatile("s_barrier" ::: "memory"); prime(); LDA4(xv, fXv, fs); SCHED0(); tail(); WAITV(nt, xv); } \
    else { \
        tail(); \
        if ((unsigned)__ballot(pv == 0) & msk) { DRAIN(); sst4(rx, pend, fl, 1u); if (s == NSEG_ - 1) { SPIN_END; __builtin_amdgcn_endpgm(); } pend = OOB; wave_wait(rx, fXo, fb, spins); } \
        LDA4(xv, fXv, fs); \
        XALL(so, xb, ld); \
        WAITV(10, xv); sst4(rx, pend, fl, 1u); pend = OOB; \
        VMB(6); prime(); \
    } \
    so = __builtin_amdgcn_readfirstlane(xv); \
    if (pend != OOB && (so == 0 || (int)(so + E_W - rtime()) >= T_D)) { DRAIN(); sst4(rx, pend, fl, 1u); pend = OOB; } \
    PIN_S(pend); \
    if (__builtin_expect(so == 0, 0) && fs != OOB) { \
        _Pragma("unroll 1") for (int k_ = 0; k_ < 8 && so == 0; ++k_) { __builtin_amdgcn_s_sleep(4); so = __builtin_amdgcn_readfirstlane(sld4(rx, fs, fXv)); MEMFENCE(); } \
    } \
    so += E_W; PIN_S(so); rt_until(so)

constexpr int QL_W = 81920;
__device__ __forceinline__ void qk_role(const MegaArgs& A, char* smem, const int L, const int h, const int id) {
    WORK_IDS;
    bf16x8 wf[6][20];
    char* sW = smem + QL_W + w * 20480 + l * 16;
    unsigned x0;
    load_qk<0, 11>(smem, w, l, go, A.Wq + h * DH_, A.Wk + h * DH_, [&](int m) { return 6 * w + m; }, wf, [&]() {
        const unsigned tag = (__builtin_amdgcn_readlane(go.v[0], 63) + 2) & ~1u, a0 = __builtin_amdgcn_readlane(go.v[0], HS_A / 8), a1 = __builtin_amdgcn_readlane(go.v[1], HS_A / 8);
        const rsrc_t rx = arena(A, a0 & 1);
        x0 = ((a0 ^ tag) | (a1 ^ tag) | 2 * L) > 1 ? OOB : seg_base(0, 0);
        PIN_S(x0);
        unsigned so;
        XALL(so, x0, lds0);
        GO_ASK;
    });
#pragma unroll
    for (int i = 0; i < 20; ++i) *(bf16x8*)(sW + i * 1024) = wf[5][i];
    GO_ON(N_1);
    const unsigned fI = l == 0 ? xm(FI_OFF + (h & 15) * 64 + (4 * L + w) * 4, hside(h)) : OOB;
    const unsigned po0 = 2 * QK_OFF + ((L * 16 + (h & 15)) * 4 + (w & 2)) * 16384 + (w & 1) * 3072 + hside(h) * 8192;
    const int it0 = L * A.lag;
    const unsigned l16s = l16b;
    NEXT_DECL(x0 != OOB);
    unsigned vx = l16b, ld = lds0, po;
    bf16x8 xw[3], wl[2];
    f32x4 acc[2][6];
    u32x4 o[2];
    auto prime = [&]() { xw[0] = *(const bf16x8*)(smem + vx); xw[1] = *(const bf16x8*)(smem + vx + 2048); };
    auto tail = [&]() {
#pragma unroll
        for (int f = 0; f < 3; ++f) { CVT2(o[f & 1][0], o[f & 1][1], acc[1][2 * f]); CVT2(o[f & 1][2], o[f & 1][3], acc[1][2 * f + 1]); sst16(rx, po, l16s + f * 1024, o[f & 1]); SCHED0(); }
    };
    prime();
    wl[0] = *(const bf16x8*)sW;
#pragma unroll 1
    for (;; ++s) {
        SCHED0();
        sfor<0, 240>([&](auto G) {
            constexpr int g = decltype(G)::value, i = g / 6, tt = g % 6, ks = i % 20;
            f32x4& d = acc[i / 20][tt];
            if constexpr (tt == 5) { if (ks == 0) MF_VV0(d, wl[i & 1], xw[i % 3]); else MF_VV(d, wl[i & 1], xw[i % 3]); }
            else if constexpr (20 * tt + ks < 64) { if (ks == 0) MF_AV0(d, wf[tt][ks], xw[i % 3]); else MF_AV(d, wf[tt][ks], xw[i % 3]); }
            else { if (ks == 0) MF_VV0(d, wf[tt][ks], xw[i % 3]); else MF_VV(d, wf[tt][ks], xw[i % 3]); }
            SCHED0();
            if constexpr (tt == 1 && i < 38) xw[(i + 2) % 3] = *(const bf16x8*)(smem + vx + (i + 2) % 20 * 2048 + (i + 2) / 20 * 1024);
            if constexpr (tt == 3) wl[~i & 1] = *(const bf16x8*)(sW + (i + 1) % 20 * 1024);
            NEXT_ADDR(2, 6);
            if (g == 4) { PIN_S(s); po = (s & (TR - 1)) << 21; PIN_S(po); }
            if (g == 10) { so = (po >> 8) & 8192; PIN_S(so); }
            if (g == 16) { po = (po0 ^ so) + po; PIN_S(po); }
            if (g == 58) { VMW(0); }
            if (g == 59) sst4(rx, pend, fI, 1u);
            if (g == 60) asm volatile("s_barrier" ::: "memory");
            if (g == 62) { PIN_S(s); pend = s + it0; so = pend & 7; PIN_S(pend); PIN_S(so); }
            if (g == 64) { pend = (pend >> 3) << 4; PIN_S(pend); }
            if (g == 66) { pend = (pend | so) << 10; PIN_S(pend); }
            if (g == 68) { ld = 2 * lds0 + 40960 - ld; PIN_S(ld); }
            if constexpr (g >= 122 && g < 140) {
                constexpr int f = (g - 122) / 6;
                if (tt == 2) CVT2(o[0][0], o[0][1], acc[0][2 * f]);
                if (tt == 4) CVT2(o[0][2], o[0][3], acc[0][2 * f + 1]);
                if (tt == 0) sst16(rx, po, l16s + f * 1024, o[0]);
            }
            if (g == 140) { po += 16384; PIN_S(po); }
            NEXT_LOOK(0, ld);
            {
                constexpr int a[10] = {186, 190, 194, 198, 202, 206, 214, 218, 222, 226};
#pragma unroll
                for (int e = 0; e < 10; ++e) {
                    if (g == a[e]) { if (e % 5 == 0) XSET(so, xs, ld); else XADV(so); }
                    if (g == a[e] + (e == 5 ? 6 : 2)) XDMA(so, e / 5);
                }
            }
            if (g == 236) { vx ^= 40960; PIN_V(vx); }
            SCHED0();
        });
        NEXT_GO(5, ld, fI, 3);
    }
}

constexpr int OL_V = 40960, OL_W = OL_V + 12288;
__device__ __forceinline__ void vo_role(const MegaArgs& A, char* smem, const int L, const int h, const int id) {
    WORK_IDS;
    bf16x8 wf[3][20], wo[10][6];
    char* sW = smem + OL_W + w * 24576 + l * 16;
    {
        load_vo<0>(smem, w, l, go, A.Wv + h * DH_, A.Wo + (size_t)(h * DH_) * D_, wf, [&](int k, int ct, int) -> bf16x8& { return wo[ct][k]; });
#pragma unroll
        for (int i = 0; i < 24; ++i) *(bf16x8*)(sW + i * 1024) = wo[4 + i / 8 * 2 + (i & 1)][2 + (i >> 1 & 3)];
    }
    GO_ON(N_1);
    const unsigned fU = l == 0 ? lp(SG_FL + FL_U + h * 64 + w * 4) : OOB;
    const unsigned l16s = l16b;
    const unsigned ua = xm(UT_OFF + (320 * w + h) * 1024, L) + l16s, ub = ua ^ (w == 3 ? 8192 : 0);
    char* sV = smem + OL_V + l * 16;
    char* sVw = sV + w * 1024;
    char* sVh = sV + 4096 + (w >> 1) * 1024 + (w & 1) * 8;
    NEXT_DECL(false);
    unsigned uo;
    constexpr int XW = 4;
    bf16x8 xw[XW], wl[4];
    f32x4 d[4][2];
    u32x4 o[2];
    auto prime = [&]() {
#pragma unroll
        for (int i = 0; i < XW - 1; ++i) xw[i] = *(const bf16x8*)(smem + l16b + i * 2048);
    };
    auto tail = [&]() {
#pragma unroll
        for (int f = 0; f < 2; ++f) { CVT2(o[f][0], o[f][1], d[f][0]); CVT2(o[f][2], o[f][3], d[f][1]); sst16(rx, uo + f * 65536, ub, o[f]); SCHED0(); }
    };
    prime();
#pragma unroll 1
    for (;; ++s) {
        f32x4 acc[2][3];
        u32x4 vf[2][6];
        unsigned cur = xb;
        SCHED0();
        sfor<0, 240>([&](auto G) {
            constexpr int g = decltype(G)::value;
            constexpr int rt = g < 120 ? g / 60 : (g < 144 ? 0 : (g < 168 ? 1 : g & 1));
            constexpr int ct = g < 144 ? g & 3 : (g < 168 ? (g - 144) / 12 * 2 + (g & 1) : (g - 120) / 24 * 2 + (g >> 1 & 1));
            constexpr int kk = g < 144 ? (g + 24) % 24 / 4 : (g < 168 ? g % 12 / 2 : g % 24 / 4), wi = (g - 168) / 24 * 8 + (g % 24 - 8) / 2;
            if constexpr (g < 120) { if (g % 60 < 3) MF_AV0(acc[rt][g % 3], wf[g % 3][0], xw[g / 3 % XW]); else MF_AV(acc[rt][g % 3], wf[g % 3][g % 60 / 3], xw[g / 3 % XW]); }
            else if constexpr (ct >= 4 && kk >= 2) MF_VV(d[ct & 3][rt], vf[rt][kk], wl[wi & 3]);
            else if constexpr (ct == 0 && kk < 4) { if (kk == 0) MF_VA0(d[0][rt], vf[rt][0], wo[0][0]); else MF_VA(d[0][rt], vf[rt][kk], wo[0][kk]); }
            else { if (kk == 0) MF_VV0(d[ct & 3][rt], vf[rt][0], wo[ct][0]); else MF_VV(d[ct & 3][rt], vf[rt][kk], wo[ct][kk]); }
            SCHED0();
            if constexpr (g % 3 == 1 && g / 3 + XW - 1 < 40) { constexpr int i = g / 3 + XW - 1; xw[i % XW] = *(const bf16x8*)(smem + l16b + i % 20 * 2048 + i / 20 * 1024); }
            NEXT_ADDR(2, 3);
            if (g == 24) { PIN_S(s); uo = (s & (TR - 1)) << 22; PIN_S(uo); }
            if (g == 48) { VMW(0); }
            if (g == 49) sst4(rx, pend, fU, 1u);
            if (g == 50) asm volatile("s_barrier" ::: "memory");
            if (g == 51) { pend = cur ^ (L << 13); PIN_S(pend); }
            if constexpr (g >= 60 && g < 140 && g % 60 < 40) {
                constexpr int r = g / 60 - 1, q = r ? g - 120 : (g % 3 == 1 ? -1 : (g - 60) / 3 * 2 + g % 3 / 2);
                if (q == 1) CVT2(o[0][0], o[0][1], acc[r][0]);
                if (q == 2) CVT2(o[0][2], o[0][3], acc[r][1]);
                if (q == 3) CVT2(o[1][0], o[1][1], acc[r][2]);
                if (q == 4) *(u32x4*)(sVw + r * 6144) = o[0];
                if (q == 5) *(u32x2*)(sVh + r * 6144) = u32x2{o[1][0], o[1][1]};
                if (q == 11) asm volatile("s_waitcnt lgkmcnt(%0)\n\ts_barrier" :: "n"(r ? 0 : 3) : "memory");
                if constexpr (q >= 12 && q < 18) vf[r][q - 12] = *(const u32x4*)(sV + r * 6144 + (q - 12) * 1024);
            }
            if constexpr (g >= 168 && g % 24 < 18 && g % 24 != 8 && !(g & 1)) {
                constexpr int j = (g - 168) / 24 * 8 + g % 24 / 2 - (g % 24 > 8);
                wl[j & 3] = *(const bf16x8*)(sW + j * 1024);
            }
            if constexpr (g >= 156) {
                constexpr int p = g < 168 ? 0 : (g - 144) / 24, q = g < 168 ? g - 156 : g % 24, c0 = 2 * p & 3;
                if (q == 1) CVT2(o[0][0], o[0][1], d[c0][0]);
                if (q == 3) CVT2(o[0][2], o[0][3], d[c0][1]);
                if (q == 5) sst16(rx, uo, p ? ub : ua, o[0]);
                if (q == 7) CVT2(o[1][0], o[1][1], d[c0 + 1][0]);
                if (q == 8) { uo += 65536; PIN_S(uo); }
                if (q == 9) CVT2(o[1][2], o[1][3], d[c0 + 1][1]);
                if (q == 11) sst16(rx, uo, p ? ub : ua, o[1]);
            }
            if constexpr (g == 168 || g == 181 || g == 205 || g == 229) { uo += 65536; PIN_S(uo); }
            NEXT_LOOK(4, lds0);
            {
                constexpr int a[10] = {186, 189, 192, 200, 205, 209, 213, 216, 224, 231}, b[10] = {188, 191, 196, 204, 207, 212, 215, 220, 228, 233};
#pragma unroll
                for (int e = 0; e < 10; ++e) {
                    if (g == a[e]) { if (e % 5 == 0) XSET(so, xs, lds0); else XADV(so); }
                    if (g == b[e]) XDMA(so, e / 5);
                }
            }
            SCHED0();
        });
        NEXT_GO(7, lds0, fU, 2);
    }
}

#define G_B 83
#define BSLP 6
constexpr int BL_Q = 40960, BL_P = BL_Q + 49152;
__device__ __forceinline__ void b_role(const MegaArgs& A, char* smem, const int L, const int hb, const int id) {
    WORK_IDS;
    const unsigned t0 = rtime();
    bf16x8 wf[4][20];
    unsigned dst[4];
    const int sw = (l16 & 6) << 3;
    {
        const void* const base[4] = {A.wqkv + 80 * hb, A.wqkv + 80 * hb, A.wqkv + 80 * hb, A.wqkv + 80 * hb};
        unsigned vo[4];
        int rp[4];
#pragma unroll
        for (int m = 0; m < 4; ++m) {
            const int idx = min((4 * m + w) * 64 + l, 959), c = idx % 30, T = min(4 * m + w, 14), e = 16 * (T % 5) + 4 * lq;
            vo[m] = (idx / 30) * 3840 + (c / 10) * (2 * D_) + (c % 10) * 16;
            rp[m] = (4 * lq + (l16 >> 2)) * 480 + 32 * T + (l & 3) * 8;
            dst[m] = lds_addr(smem) + BL_Q + (T / 5 * 4 + e / 20) * 4096 + l16 * 64 + ((e % 20 * 2) ^ sw);
        }
        while (rtime() - t0 < (L ? DLYB1 : DLYB0)) __builtin_amdgcn_s_sleep(8);
        load_coop<4, 0, 4>(smem, w, go, base, vo, 32 * 3840, rp, 7680, wf);
    }
    GO_ON(N_1);
    f32x4 cs[4];
    u32x4 one = {0x3f803f80u, 0x3f803f80u, 0x3f803f80u, 0x3f803f80u};
    {
        asm volatile("s_nop 1" : "+v"(one));
        sfor<0, 80>([&](auto G) { constexpr int g = decltype(G)::value, tt = g & 3, ks = g >> 2;
            if (ks == 0) MF_AV0(cs[tt], wf[tt][0], one); else if (tt < 3 || ks < 4) MF_AV(cs[tt], wf[tt][ks], one); else MF_VV(cs[tt], wf[tt][ks], one); });
        asm volatile("s_nop 7" : "+v"(cs[0]), "+v"(cs[1]), "+v"(cs[2]), "+v"(cs[3]));
    }
#pragma unroll
    for (int i = 0; i < 12; ++i) *(u32x4*)(smem + BL_Q + i * 4096 + t * 16) = u32x4{0u, 0u, 0u, 0u};
    const bool odd = l16 & 1;
    f32x4 wm[3];
#pragma unroll
    for (int e = 0; e < 8; ++e) { const int d = l16 + 16 - 16 * (e >> 2) - 4 * lq - (e & 3); wm[e >> 2][e & 3] = d >= 0 && d <= 14 && !(d & 1) ? 0.f : -INFINITY; }
    wm[2] = f32x4{-INFINITY, -INFINITY, -INFINITY, -INFINITY};
    const unsigned ps0 = odd ? 0x01000c0cu : 0x0c0c0100u, ps1 = odd ? 0x03020c0cu : 0x0c0c0302u;
    unsigned qa = lds_addr(smem) + BL_Q + w * 4096 + l16 * 64 + ((lq * 16) ^ sw), qb = qa ^ 2048, va[6];
    {
        const int row = 4 * lq + (l16 >> 2), vs = (row & 6) << 3;
        va[0] = va[2] = lds_addr(smem) + BL_Q + (8 + w) * 4096 + row * 64 + ((8 * (w & 1) + 8 * (l & 3)) ^ vs);
        va[1] = va[3] = lds_addr(smem) + BL_Q + (8 + w) * 4096 + row * 64 + ((32 * (~w & 1) + 8 * (l & 3)) ^ vs);
        va[4] = va[0] ^ 2048; va[5] = va[1] ^ 2048;
#pragma unroll
        for (int e = 0; e < 6; ++e) PIN_V(va[e]);
    }
    const int g0 = (20 * hb + 5 * w + (w & 1) + (lq & 2)) >> 1, g1 = (20 * hb + 5 * w + 4 * (~w & 1)) >> 1;
    const unsigned oo0 = lp(SG_OB + (g0 >> 2) * 2048 + (lq & 1) * 1024 + (g0 & 3) * 256 + l16 * 16);
    const unsigned oo1 = lq < 2 ? lp(SG_OB + (g1 >> 2) * 2048 + lq * 1024 + (g1 & 3) * 256 + l16 * 16 + (w & 1) * 8) : OOB;
    const unsigned fO = t < 4 ? lp(SG_FL + FL_O + (8 * t + hb) * 128) : OOB;
    const unsigned st0 = lp(SG_ST + (4 * (l >> 5) + w) * 256 + (l & 31) * 8), st2 = l < 32 ? st0 + 4096 : OOB;
    unsigned fXv = xm(FX_OFF + (hb >> 1) * 64, L), xv, X = rtime() - 2 * PER, xs = OOB;
    PIN_V(fXv);
    const float z2 = l < 32 ? 1.f : 0.f;
    f32v2* sP = (f32v2*)(smem + BL_P) + (l & 31);
    unsigned sK = lds_addr(smem) + BL_P + 1024, sKw = sK + 4 * w;
    PIN_V(sK); PIN_V(sKw);
    unsigned pend = OOB, sb = seg_base(L, 0), so, fs = fx_step(1 + L * A.lag);
    const int i0 = 1 + L * A.lag;
    for (int s = 0; s < NTOUCH; ++s) { sst16(rx, seg_base(L, s), oo0, u32x4{0u, 0u, 0u, 0u}); sst8(rx, seg_base(L, s), oo1, u32x2{0u, 0u}); }
    u32x2 st[3];
    auto look = [&](int e, unsigned b, unsigned f) { if (e == 0) LDA8(st[0], st0, b, 0); if (e == 1) LDA8(st[1], st0, b, 2048); if (e == 2) LDA8(st[2], st2, b, 0); if (e == 3) LDA4(xv, fXv, f); };
#define SEEN(n) asm volatile("s_waitcnt vmcnt(%4)" : "+v"(st[0]), "+v"(st[1]), "+v"(st[2]), "+v"(xv) : "n"(n) : "memory")
#define SWAP2(op, a, b) asm volatile(op " %0, %1" : "+v"(a), "+v"(b))
    int s;
    SZERO(s);
#pragma unroll 1
    do {
        if (xs == OOB) {
            bool e = (int)(rtime() - X - E_B) >= 0;
#pragma unroll
            for (int i = 0; i < 4; ++i) look(i, e ? sb : OOB, e ? fs : OOB);
            VMB(4);
            sst4(rx, pend, fO, 1u);
            pend = OOB;
            SEEN(1);
#pragma unroll 1
            for (;;) {
                xv = __builtin_amdgcn_readfirstlane(xv);
                X = xv ? xv : X;
                LIVE;
                if (e) { if (!BAD(umax3(st[0][0], st[1][0], st[2][0]))) break; __builtin_amdgcn_s_sleep(BSLP); }
                e = true;
                rt_until(X + E_B);
#pragma unroll
                for (int i = 0; i < 4; ++i) look(i, sb, fs);
                SEEN(0);
            }
            XSET(so, sb + lp(SG_X1), lds0); XDMA(so, 0); XDMA(so, 1); XADV(so); XDMA(so, 0); XDMA(so, 1);
        }
        VMB(2);
        f32x4 acc[4][2];
        unsigned cur;
        float rd[2];
        u32x2 y[2];
        bf16x8 kf[3], qf[2];
        u32x4 k4;
#define MUL2(x, i, r) do { asm volatile("v_mul_f32 %0, %1, %0" : "+v"(x[i]) : "v"(r)); x[i + 1] *= r; USE_V(x[i + 1]); } while (0)
#define FMA2(x, i, c, m) do { asm volatile("v_fma_f32 %0, -%2, %1, %0" : "+v"(x[i]) : "v"(c[i]), "v"(m)); x[i + 1] = __builtin_fmaf(-m, c[i + 1], x[i + 1]); USE_V(x[i + 1]); } while (0)
#define ADD1(d, x, z) asm volatile("v_add_f32 %0, %1, %2" : "=v"(d) : "v"(x), "v"(z))
        auto fin = [&](int e, int u) {
            if (u < 6 && u % 3 < 2) MUL2(acc[e][u / 3], 2 * (u % 3), rd[u / 3]);
            if (u % 3 == 2) CVT2(y[u / 3][0], y[u / 3][1], acc[e][u / 3]);
            if (u == 6) { LDS_AT(u32x2, dst[e]) = y[0]; LDS_AT(u32x2, dst[e] + 1024) = y[1]; dst[e] ^= 2048; USE_V(dst[e]); }
        };
        {
            bf16x8 xw[6][2];
#pragma unroll
            for (int e = 0; e < 4; ++e) xw[e >> 1][e & 1] = *(const bf16x8*)(smem + l16b + e * 1024);
            SCHED0();
            u64 now, bad;
            float a, b, c, d, mw, mb[2];
            unsigned lb, lf;
            f32v2 p0, p1, p2, p3;
            sfor<0, 160>([&](auto G) {
                constexpr int g = decltype(G)::value, ks = g < 112 ? g / 8 : (g < 124 ? 14 + (g - 112) / 6 : (g < 148 ? 16 + (g - 124) % 8 / 2 : 14 + (g - 148) / 2));
                constexpr int tt = g < 112 ? g % 8 / 2 : (g < 124 ? (g - 112) % 6 / 2 : (g < 148 ? (g - 124) / 8 : 3)), rt = g & 1;
                constexpr int q = g >= 112 || g % 8 == 1 || g % 8 == 3 || g % 8 == 7 ? -1 : g / 8 * 5 + (g % 8 < 4 ? g % 8 / 2 : g % 8 - 2);
                if (ks == 0) MF_AV0(acc[tt][rt], wf[tt][0], xw[0][rt]);
                else if (tt < 3 || ks < 4) MF_AV(acc[tt][rt], wf[tt][ks], xw[ks % 6][rt]); else MF_VV(acc[tt][rt], wf[tt][ks], xw[ks % 6][rt]);
                SCHED0();
                constexpr int rq = g < 112 ? (g % 8 == 1 || g % 8 == 3 ? 2 * ks + 4 + g % 8 / 2 : -1) : (g < 124 && g % 3 != 0 ? 32 + (g - 112) / 3 * 2 + (g - 112) % 3 : -1);
                if constexpr (rq >= 0) xw[rq / 2 % 6][rq & 1] = *(const bf16x8*)(smem + l16b + rq * 1024);
                if (q == 0 || q == 5 || q == 11) XADV(so);
                if (q == 1 || q == 6 || q == 13) XDMA(so, 0);
                if (q == 3 || q == 8 || q == 15) XDMA(so, 1);
                if (q == 10) VMB_LATE(4);
                if (q == 2) { ADD1(c, st[0][1], st[1][1]); a = __uint_as_float(st[0][0]) + __uint_as_float(st[1][0]); USE_V(a); }
                if (q == 4) { ADD1(b, a, st[2][0]); a += __uint_as_float(st[2][0]); USE_V(a); }
                if (q == 7) { SWAP2("v_permlane32_swap_b32", a, b); SCHED0(); c += __uint_as_float(st[2][1]); USE_V(c); }
                if (q == 9) { mw = (a + b) * 0.2f; USE_V(mw); }
                if (q == 12) { a = __uint_as_float(st[0][0]) - mw; b = __uint_as_float(st[1][0]) - mw; USE_V(a); USE_V(b); }
                if (q == 14) { d = (__uint_as_float(st[2][0]) - mw) * z2; USE_V(d); }
                if (q == 16) { a = __builtin_fmaf(b, b, a * a); USE_V(a); }
                if (q == 17) { c = __builtin_fmaf(32.f, __builtin_fmaf(d, d, a), c); USE_V(c); }
                if (q == 18) { b = c; PIN_V(b); }
                if (q == 19) SWAP2("v_permlane32_swap_b32", c, b);
                if (q == 21) { c += b; sP[w * 32] = f32v2{mw, c}; }
                if (q == 22) { cur = sb; sb = (s + 1) << 19; PIN_S(cur); PIN_S(sb); }
                if (q == 23) { lb = (sb >> 8) & 8192; PIN_S(lb); }
                if (q == 24) { sb = (sb | lb) + 2 * SG_OFF; PIN_S(sb); }
                if (q == 26) { sb ^= L << 13; X += PER; PIN_S(sb); PIN_S(X); }
                if (q == 27) { fs += 256; lb = fs & 8192; PIN_S(fs); PIN_S(lb); }
                if (q == 28) { fs += lb; PIN_S(fs); }
                if (q == 34) { TIGHT(lb, s + i0, D_1); X -= lb; PIN_S(X); }
                if (q == 35) { asm volatile("s_cmp_eq_u32 %1, %2\n\ts_cselect_b32 %0, %3, 0" : "=s"(lb) : "s"(s + i0), "n"(NSEG_ + LAGV - 1), "s"(E_Z) : "scc"); X -= lb; PIN_S(X); }
                if (q == 30) asm volatile("s_waitcnt vmcnt(4) lgkmcnt(2)\n\ts_barrier" ::: "memory");
                if (q == 31) sst4(rx, pend, fO, 1u);
                if (q == 32) { p0 = sP[0]; p1 = sP[32]; }
                if (q == 33) { p2 = sP[64]; p3 = sP[96]; }
                if (q == 36) ADD1(a, p0[0], p1[0]);
                if (q == 37) { b = p2[0] + p3[0]; USE_V(b); }
                if (q == 38) { mw = (a + b) * 0.25f; USE_V(mw); }
                if (q == 39) { a = p0[0] - mw; PIN_V(a); b = p1[0] - mw; PIN_V(b); }
                if (q == 40) { c = p2[0] - mw; PIN_V(c); }
                if (q == 41) { mb[0] = rb(mw); PIN_V(mb[0]); }
                if (q == 42) { d = p3[0] - mw; PIN_V(d); mb[1] = mb[0]; PIN_V(mb[1]); }
                if (q == 43) { a = __builtin_fmaf(b, b, a * a); PIN_V(a); }
                if (q == 44) { SWAP2("v_permlane16_swap_b32", mb[0], mb[1]); ADD1(b, p0[1], p1[1]); }
                if (q == 45) { a = __builtin_fmaf(c, c, a); PIN_V(a); }
                if (q == 50) VMB(1);
                if (q == 55) { a = __builtin_fmaf(d, d, a); PIN_V(a); }
                if (g == 73) ADD1(c, p2[1], p3[1]);
                if (g == 75) ADD1(b, b, c);
                if (g == 89) { a = __builtin_fmaf(160.f, a, b); PIN_V(a); }
                if (g == 91) { a *= 1.0f / D_; PIN_V(a); }
                if (q == 60) asm volatile("v_cvt_pk_bf16_f32 %0, 0, %0" : "+v"(a));
                if (g == 97) { a += 1e-5f; PIN_V(a); }
                if (g == 99) asm volatile("v_cvt_pk_bf16_f32 %0, 0, %0" : "+v"(a));
                if (q == 65) { a = __builtin_amdgcn_sqrtf(a); PIN_V(a); }
                if (g == 105) asm volatile("v_cvt_pk_bf16_f32 %0, 0, %0" : "+v"(a));
                if (q == 66) { rd[0] = __builtin_amdgcn_rcpf(a); PIN_V(rd[0]); }
                if (q == 67) { rd[1] = rd[0]; PIN_V(rd[1]); }
                if (q == 69) SWAP2("v_permlane16_swap_b32", rd[0], rd[1]);
                if constexpr (g >= 72 && g < 104 && q >= 0 && g % 8) {
                    constexpr int e = (g % 8 < 5 ? g % 8 + 2 : 2 * (g % 8) - 10) + (g >= 88), i = g >> 2 & 2;
                    FMA2(acc[e >> 1][e & 1], i, cs[e >> 1], mb[e & 1]);
                }
                if (g == G_B) asm volatile("s_memrealtime %0" : "=s"(now));
                if (g == G_B + 4) asm volatile("s_waitcnt lgkmcnt(0)" : "+s"(now));
                if (g == G_B + 12) { lf = (unsigned)now - X; PIN_S(lf); }
                if (g == G_B + 20) { lf -= E_B; PIN_S(lf); }
                if (g == G_B + 26) asm volatile("s_cmp_lt_i32 %1, 63\n\ts_cselect_b32 %0, %0, -1" : "+s"(lf) : "s"(s) : "scc");
                if (g == G_B + 28) asm volatile("s_cmp_ge_i32 %1, 0\n\ts_cselect_b32 %0, %2, %4\n\ts_cselect_b32 %1, %3, %4" : "=&s"(lb), "+s"(lf) : "s"(sb), "s"(fs), "n"(OOB) : "scc");
                if constexpr (g >= G_B + 29 && g < G_B + 33) look(g - G_B - 29, lb, lf);
                if constexpr (g >= 132 && g < 155 && (g - 132) % 8 < 7) fin((g - 132) / 8, (g - 132) % 8);
                if (g == 155) LDS_BARRIER();
                if (g == 156) { kf[0] = LDS_AT(bf16x8, qb + 17408); qf[0] = LDS_AT(bf16x8, qa); }
                if (g == 157) { kf[1] = LDS_AT(bf16x8, qa + 16384); qf[1] = LDS_AT(bf16x8, qa + 1024); }
                if (g == 158) { kf[2] = LDS_AT(bf16x8, qa + 17408); SEEN(0); }
                if (g == 159) { lf = umax3(st[0][0], st[1][0], st[2][0]); bad = __ballot(lf == ~0u); PIN_S(bad); }
                SCHED0();
            });
            xv = __builtin_amdgcn_readfirstlane(xv);
            asm volatile("s_cmp_eq_u64 %1, 0\n\ts_cselect_b32 %0, %0, %2" : "+s"(lb) : "s"(bad), "n"(OOB) : "scc");
            LDS_AT(unsigned, sKw) = lb;
            X = xv ? xv : X;
            PIN_S(X);
        }
        {
            constexpr float SC = 0.22360679774997896f * 1.4426950408889634f;
            bf16x8 vx[2][2];
            f32x4 sc[4];
            SCHED0();
            fin(3, 0); fin(3, 1);
            SCHED0();
            MF_VVC(sc[0], kf[0], qf[0], wm[2]); SCHED0(); fin(3, 2); SCHED0();
            MF_VVC(sc[1], kf[1], qf[0], wm[1]); SCHED0(); fin(3, 3); SCHED0();
            MF_VVC(sc[2], kf[1], qf[1], wm[0]); SCHED0(); fin(3, 4); SCHED0();
            MF_VVC(sc[3], kf[2], qf[1], wm[1]); SCHED0(); fin(3, 5); fin(3, 6);
            SCHED0();
            float sv[4][2];
            float m0, m1;
#define MAXQ(m, x, y, p, q) asm volatile("v_max_f32 %1, %5, %6\n\tv_max_f32 %2, %7, %8\n\tv_max_f32 %3, %9, %10\n\tv_max_f32 %4, %11, %12\n\tv_max3_f32 %0, %1, %2, %3\n\tv_max_f32 %0, %0, %4" \
    : "=&v"(m), "=&v"(x[0]), "=&v"(x[1]), "=&v"(y[0]), "=&v"(y[1]) : "v"(p[0]), "v"(p[1]), "v"(p[2]), "v"(p[3]), "v"(q[0]), "v"(q[1]), "v"(q[2]), "v"(q[3]))
            MAXQ(m0, sv[0], sv[1], sc[0], sc[1]);
            MAXQ(m1, sv[2], sv[3], sc[2], sc[3]);
            LDS_BARRIER();
            k4 = LDS_AT(u32x4, sK);
            SCHED0();
            auto tr = [](unsigned a) { return __builtin_amdgcn_ds_read_tr16_b64_v4i16((lds_b4*)a); };
#pragma unroll
            for (int dt = 0; dt < 2; ++dt) { vx[0][dt] = cat8(tr(va[4 + dt] + 1024), tr(va[dt])); vx[1][dt] = cat8(tr(va[2 + dt]), tr(va[dt] + 1024)); }
            SCHED0();
            red_max_q2(m0, m1);
            m0 *= -SC; m1 *= -SC;
            USE_V(m0); USE_V(m1);
            SCHED0();
            xs = __builtin_amdgcn_readfirstlane(max(umax3(k4[0], k4[1], k4[2]), k4[3]));
            u32x4 pt[2];
            f32x4 o[2][2], sm[2];
#pragma unroll
            for (int qt = 0; qt < 2; ++qt) {
#pragma unroll
                for (int e = 0; e < 4; ++e) sv[2 * qt + (e >> 1)][e & 1] = __builtin_amdgcn_exp2f(__builtin_fmaf(sv[2 * qt + (e >> 1)][e & 1], SC, qt ? m1 : m0));
                if (!qt) XSET(so, xs + lp(SG_X1), lds0);
#pragma unroll
                for (int e = 0; e < 2; ++e) {
                    const unsigned p = pk2(sv[2 * qt + e][0], sv[2 * qt + e][1]);
                    pt[qt][2 * e] = __builtin_amdgcn_perm(p, p, ps0);
                    pt[qt][2 * e + 1] = __builtin_amdgcn_perm(p, p, ps1);
                }
                USE_V(pt[qt]);
                SCHED0();
                XDMA(so, qt);
                asm volatile("s_nop 0");
                MF_VV0(sm[qt], one, pt[qt]);
                if (!qt) MF_VV0(o[0][0], vx[0][0], pt[0]);
                SCHED0();
            }
            XADV(so);
            MF_VV0(o[1][0], vx[1][0], pt[1]);
            PIN_V(sm[0]);
            const float s0 = __builtin_amdgcn_rcpf(sm[0][0]);
            USE_V(s0);
            XDMA(so, 0);
            MF_VV0(o[0][1], vx[0][1], pt[0]);
            SCHED0();
            PIN_V(o[0][0]);
            MUL2(o[0][0], 0, s0);
            SCHED0();
            MF_VV0(o[1][1], vx[1][1], pt[1]);
            SCHED0();
            PIN_V(sm[1]);
            const float s1 = __builtin_amdgcn_rcpf(sm[1][0]);
            USE_V(s1);
            MUL2(o[0][0], 2, s0);
            XDMA(so, 1);
            PIN_V(o[1][0]);
            MUL2(o[1][0], 0, s1); MUL2(o[1][0], 2, s1);
            {
                const auto a = __builtin_amdgcn_permlane16_swap(pk2(o[0][0][0], o[0][0][1]), pk2(o[1][0][0], o[1][0][1]), false, false);
                const auto b = __builtin_amdgcn_permlane16_swap(pk2(o[0][0][2], o[0][0][3]), pk2(o[1][0][2], o[1][0][3]), false, false);
                sst16(rx, cur, oo0, u32x4{a[0], b[0], a[1], b[1]});
            }
            SCHED0();
            asm volatile("" : "+v"(o[0][1]), "+v"(o[1][1]));
            o[0][1] *= s0; o[1][1] *= s1;
            {
                const auto a = __builtin_amdgcn_permlane16_swap(pk2(o[0][1][0], o[0][1][1]), pk2(o[1][1][0], o[1][1][1]), false, false);
                const auto b = __builtin_amdgcn_permlane16_swap(pk2(o[0][1][2], o[0][1][3]), pk2(o[1][1][2], o[1][1][3]), false, false);
                sst8(rx, cur, oo1, u32x2{a[0], b[0]});
            }
            SCHED0();
            wm[2] = wm[0];
            qa ^= 2048; qb ^= 2048;
            PIN_V(wm[2]); PIN_V(qa); PIN_V(qb);
#pragma unroll
            for (int e = 0; e < 6; ++e) { va[e] ^= 2048; PIN_V(va[e]); }
        }
        pend = cur;
    } while (++s < NSEG_);
    VMB(0);
    sst4(rx, pend, fO, 1u);
    SPIN_END;
}

__device__ __forceinline__ void bo_role(const MegaArgs& A, char* smem, const int L, const int j, const int id) {
    WORK_IDS;
    const unsigned t0 = rtime();
    const int rt = w & 1, hf = w >> 1;
    if (id == 31 && w == 0) probe(A, go, l, id);
    INIT_BEGIN(id >> 5);
    if (L) { INIT_END(56 + j); }
    bf16x8 wf[5][20];
    {
        const void* const base[3] = {A.wo + 160 * j, A.wo + 160 * j, A.wo + 160 * j};
        unsigned vo[3];
        int rp[5];
#pragma unroll
        for (int m = 0; m < 3; ++m) { const int idx = min((4 * m + w) * 64 + l, 639); vo[m] = (idx / 20) * (2 * D_) + (idx % 20) * 16; }
#pragma unroll
        for (int m = 0; m < 5; ++m) { rp[m] = (8 * lq + (l16 >> 2)) * 320 + 32 * (m < 4 ? 4 * hf + m : 8 + hf) + (l & 3) * 8; }
        while (rtime() - t0 < (L ? DLYB1 : DLYB0)) __builtin_amdgcn_s_sleep(8);
        load_coop<3, 0, 5>(smem, w, go, base, vo, 64 * D_, rp, 1280, wf);
    }
    INIT_SEEN;
    const unsigned x0 = lp((5 * j + 2 * hf) * 2048 + rt * 1024 + l16b), x1 = lp((5 * j + 2 * hf + 1) * 2048 + rt * 1024 + l16b), x2 = lp((5 * j + 4) * 2048 + rt * 1024 + l16b + hf * 8);
    const unsigned fO = l < 8 ? lp(SG_FL + FL_O + (8 * j + l) * 128) : OOB, fX = t < 8 ? lp(SG_FL + FL_X + (4 * (t & 3) + j) * 128) + (t & 4) * 2048 : OOB;
    const unsigned sr = lp(SG_ST + ST_2 + rt * 1024 + lq * 256 + l16 * 8), sw = l < 16 ? sr + j * 256 + hf * 128 : OOB;
    auto flag = [&](unsigned b) { sst4(rx, b & ~8192u, fX ^ (b & 8192u), 1u); };
    const rsrc_t ro = mk_rsrc(A.out, S_ * D_ * 2);
    const unsigned oo = ((16 * rt + l16) * D_ + 160 * j + 64 * hf + 4 * lq) * 2, oo2 = oo + (128 - 48 * hf) * 2;
    const char* sX = smem + rt * 1024 + l16b;
    unsigned pend = OOB, sb = seg_base(L, 0), so, xs = OOB;
    unsigned* sK = (unsigned*)(smem + 40960);
    auto first = [&](unsigned b) { XSET(so, b + lp(SG_OB), lds0); XDMA(so, 0); XDMA(so, 1); XADV(so); XDMA(so, 0); XDMA(so, 1); XADV(so); XDMA(so, 0); XDMA(so, 1); };
    int s;
    SZERO(s);
#pragma unroll 1
    do {
        if (xs == OOB) {
            unsigned pv;
            LDA4(pv, fO, sb);
            VMB(1);
            flag(pend);
            pend = OOB;
            WAITV(1, pv);
            if (__ballot(pv == 0) & 255) wave_wait(rx, fO, sb, spins);
            first(sb);
            VMB(4);
        } else VMB(5);
        f32x4 acc[5], xf[5];
        u32x4 r0, r1, k4;
        u32x2 r2;
        unsigned cur;
        {
            bf16x8 xw[3];
            unsigned sx, pv;
            xw[0] = *(const bf16x8*)sX; xw[1] = *(const bf16x8*)(sX + 2048);
            SCHED0();
            sfor<0, 100>([&](auto G) {
                constexpr int g = decltype(G)::value, ks = g / 5, tt = g % 5;
                if constexpr (20 * tt + ks < 64) { if (ks == 0) MF_AV0(acc[tt], wf[tt][0], xw[0]); else MF_AV(acc[tt], wf[tt][ks], xw[ks % 3]); }
                else { if (ks == 0) MF_VV0(acc[tt], wf[tt][0], xw[0]); else MF_VV(acc[tt], wf[tt][ks], xw[ks % 3]); }
                SCHED0();
                if (g == 0 || g == 7) XADV(so);
                if (g == 2 || g == 12) XDMA(so, 0);
                if (g == 5 || g == 15) XDMA(so, 1);
                if (g == 3) { cur = sb; sx = sb + lp(SG_X1); PIN_S(cur); PIN_S(sx); }
                if (g == 10) VMB_LATE(4);
                if (g == 18) LDA(r0, x0, sx, 0);
                if (g == 22) LDA(r1, x1, sx, 0);
                if (g == 25) LDA8(r2, x2, sx, 0);
                if (g == 30) VMB(7);
                if (g == 32) flag(pend);
                if (g == 35) { sb = (s + 1) << 19; PIN_S(sb); }
                if (g == 37) { sx = (sb >> 8) & 8192; PIN_S(sx); }
                if (g == 38) { sb = (sb | sx) + 2 * SG_OFF; PIN_S(sb); }
                if (g == 40) { sb ^= L << 13; PIN_S(sb); }
                if (g == 42) asm volatile("s_cmp_lt_i32 %1, 63\n\ts_cselect_b32 %0, %0, %2" : "+s"(sb) : "s"(s), "n"(OOB) : "scc");
                if (g == 50) VMB(4);
                if constexpr (tt == 1 && ks < 18) xw[(ks + 2) % 3] = *(const bf16x8*)(sX + (ks + 2) * 2048);
                if (g == 60) LDA4(pv, fO, sb);
                if (g == 63) asm volatile("s_waitcnt vmcnt(2)" : "+v"(r0), "+v"(r1), "+v"(r2) :: "memory");
                if constexpr (g >= 65 && g < 90 && (tt == 2 || tt == 3)) {
                    constexpr int e = ks - 13, k = tt - 2;
                    const unsigned u = e < 2 ? r0[2 * (e & 1) + k] : (e < 4 ? r1[2 * (e & 1) + k] : r2[k]);
                    xf[e][2 * k] = lo16(u); xf[e][2 * k + 1] = hi16(u);
                    asm volatile("" : "+v"(xf[e][2 * k]), "+v"(xf[e][2 * k + 1]));
                }
                if (g == 90) WAITV(0, pv);
                if (g == 91) { sx = (unsigned)__ballot(pv == 0); PIN_S(sx); }
                if (g == 92) asm volatile("s_and_b32 %0, %0, 255\n\ts_cselect_b32 %0, %2, %1" : "+s"(sx) : "s"(sb), "n"(OOB) : "scc");
                if (g == 93) sK[w] = sx;
                if (g == 97) LDS_BARRIER();
                if (g == 98) k4 = *(const u32x4*)sK;
                SCHED0();
            });
        }
#pragma unroll
        for (int e = 0; e < 5; ++e) { PIN_V(acc[e]); acc[e] += xf[e]; PIN_V(acc[e]); SCHED0(); }
        xs = __builtin_amdgcn_readfirstlane(max(umax3(k4[0], k4[1], k4[2]), k4[3]));
        if (L) {
            if (xs != OOB) first(xs);
            const unsigned so2 = s * (SEG_ * D_ * 2);
#pragma unroll
            for (int e = 0; e < 5; ++e) __builtin_amdgcn_raw_buffer_store_b64(u32x2{pk2(acc[e][0], acc[e][1]), pk2(acc[e][2], acc[e][3])}, ro, e < 4 ? oo + 32 * e : oo2, so2, OUTAUX);
        } else {
            f32x4 a = ((acc[0] + acc[1]) + (acc[2] + acc[3])) + acc[4];
            const float mw = red_sum_q((a[0] + a[1]) + (a[2] + a[3])) * (1.0f / 80);
#pragma unroll
            for (int e = 0; e < 5; ++e) { xf[e] = acc[e]; acc[e] -= mw; }
            a = acc[0] * acc[0];
#pragma unroll
            for (int e = 1; e < 5; ++e) a = __builtin_elementwise_fma(acc[e], acc[e], a);
            sst8(rx, cur, sw, u32x2{nosent(__float_as_uint(mw)), __float_as_uint(red_sum_q((a[0] + a[1]) + (a[2] + a[3])))});
            SCHED0();
            if (xs != OOB) first(xs);
            r0 = pk8(xf[0], xf[1]); r1 = pk8(xf[2], xf[3]); r2 = u32x2{pk2(xf[4][0], xf[4][1]), pk2(xf[4][2], xf[4][3])};
            const unsigned so2 = cur + lp(SG_XR);
            sst16(rx, so2, x0, r0); sst16(rx, so2, x1, r1); sst8(rx, so2, x2, r2);
            VMW(3);
            u32x2 p, q;
#pragma unroll 1
            for (;;) {
                LDA8(p, sr, cur, 0); LDA8(q, sr, cur, 128);
                LIVE;
                asm volatile("s_waitcnt vmcnt(0)" : "+v"(p), "+v"(q) :: "memory");
                if (!BAD(max(p[0], q[0]))) break;
            }
            const float m0 = __uint_as_float(p[0]), m1 = __uint_as_float(q[0]), mean = red_sum_q(m0 + m1) * 0.125f, e0 = m0 - mean, e1 = m1 - mean;
            const float var = red_sum_q((__uint_as_float(p[1]) + __uint_as_float(q[1])) + 80.f * (e0 * e0 + e1 * e1)) * (1.0f / D_);
            const float rd = __builtin_amdgcn_rcpf(rb(__builtin_amdgcn_sqrtf(rb(rb(var) + 1e-5f)))), c = (mw - rb(mean)) * rd;
#pragma unroll
            for (int e = 0; e < 5; ++e) acc[e] = acc[e] * rd + c;
            sst16(rx, cur, x0, pk8(acc[0], acc[1])); sst16(rx, cur, x1, pk8(acc[2], acc[3])); sst8(rx, cur, x2, u32x2{pk2(acc[4][0], acc[4][1]), pk2(acc[4][2], acc[4][3])});
            pend = cur;
        }
    } while (++s < NSEG_);
    if (id == 159) bst4(go.rh, t < 8 ? t * 1024 + HS_E : OOB, tags);
    VMB(0);
    flag(pend);
    SPIN_END;
}

#define CT0 1
#define CT1 0
constexpr unsigned CODE_SZ = 100 * 1024, CODE_BK = 0;
constexpr int MEGA_LDS = 163840;
__global__ void __launch_bounds__(256, 1) mega_kernel(MegaArgs A) {
    extern __shared__ __attribute__((aligned(16))) char smem[];
    int role = 32 * (blockIdx.x & 7) + (blockIdx.x >> 3);
    PIN_S(role);
    const int pool = role >> 5, k = role & 31;
    {
        const unsigned t0 = rtime();
        if (k >= 8 && k < 13) {
            const char* pc;
            asm volatile("s_getpc_b64 %0" : "=s"(pc));
            auto touch = [&]() { const unsigned x = __builtin_amdgcn_raw_buffer_load_b32(mk_rsrc(pc, CODE_SZ), ((k - 8) * 256 + threadIdx.x) * 128 - CODE_BK, 0, 0); asm volatile("" :: "v"(x)); };
            if (CT0) touch();
            init_area(A, 5 * pool + k - 8, role, t0);
            if (CT1) { while (rtime() - t0 < CT1) __builtin_amdgcn_s_sleep(8); touch(); }
        }
        if (k >= 17 && k < 29 && ((k - 13) & 4)) { first_rows(A, smem, 8 * ((k - 17) / 8 * 4 + ((k - 17) & 3)) + pool, role, t0); while (rtime() - t0 < DLY2) __builtin_amdgcn_s_sleep(8); }
    }
    if (k < 13) {
        if (!(A.roles & 1)) return;
        if (k < 4) s_role(A, smem, 4 * pool + k, role);
        else if (k < 8) vm_role(A, smem, 4 * pool + k - 4, role);
        else c_role(A, smem, pool, k - 8, role);
        return;
    }
    if (k < 21) { if (A.roles & 2) qk_role(A, smem, (k - 13) >> 2, 4 * pool + ((k - 13) & 3), role); }
    else if (k < 29) { if (A.roles & 4) vo_role(A, smem, (k - 21) >> 2, 4 * pool + ((k - 21) & 3), role); }
    else if (k < 31) { if (A.roles & 8) b_role(A, smem, pool >> 2, 2 * (pool & 3) + k - 29, role); }
    else if (A.roles & 16) bo_role(A, smem, pool >> 2, pool & 3, role);
}

#define ROLES 31
extern "C" void kernel_launch(void* const* d_in, const int* in_sizes, int n_in,
                              void* d_out, int out_size, void* d_ws, size_t ws_size,
                              hipStream_t stream) {
    MegaArgs A;
    char* ws = (char*)d_ws;
    A.hs = ws; ws += 262144;
    A.tmo = (unsigned*)ws; ws += 4096;
    A.ar = ws;
    A.x0 = (const bf16_t*)d_in[0]; A.mem0 = (const bf16_t*)d_in[1]; A.Wq = (const bf16_t*)d_in[2]; A.Wk = (const bf16_t*)d_in[3]; A.Wv = (const bf16_t*)d_in[4];
    A.Wo = (const bf16_t*)d_in[5]; A.wqkv = (const bf16_t*)d_in[8]; A.wo = (const bf16_t*)d_in[9];
    A.out = (bf16_t*)d_out; A.pad = 0;
    A.lag = LAGV; A.nit = NSEG_ + LAGV; A.roles = ROLES;

    (void)hipFuncSetAttribute((const void*)mega_kernel, hipFuncAttributeMaxDynamicSharedMemorySize, MEGA_LDS);

    mega_kernel<<<256, 256, MEGA_LDS, stream>>>(A);
}
```

```cpp
#include <hip/hip_runtime.h>
#include <hip/hip_bf16.h>
#include <stdint.h>

typedef unsigned short bf16_t;
typedef short bf16x8 __attribute__((ext_vector_type(8)));
typedef short bf16x4 __attribute__((ext_vector_type(4)));
typedef float f32x4 __attribute__((ext_vector_type(4)));
typedef unsigned u32x4 __attribute__((ext_vector_type(4)));
typedef unsigned u32x2 __attribute__((ext_vector_type(2)));
typedef unsigned long long u64;
typedef __bf16 bf16v2 __attribute__((ext_vector_type(2)));
typedef float f32v2 __attribute__((ext_vector_type(2)));

__device__ __forceinline__ float bf2f(bf16_t v) { return __uint_as_float(((unsigned)v) << 16); }
__device__ __forceinline__ unsigned pk2(float a, float b) {
    f32v2 f = {a, b};
    bf16v2 r = __builtin_convertvector(f, bf16v2);
    return __builtin_bit_cast(unsigned, r);
}
__device__ __forceinline__ float rb(float a) { return __uint_as_float((pk2(a, 0.f) & 0xffffu) << 16); }
__device__ __forceinline__ float lo16(unsigned u) { return __uint_as_float(u << 16); }
__device__ __forceinline__ float hi16(unsigned u) { return __uint_as_float(u & 0xffff0000u); }
__device__ __forceinline__ bf16x4 pk4(f32x4 v) {
    u32x2 u = {pk2(v[0], v[1]), pk2(v[2], v[3])};
    return __builtin_bit_cast(bf16x4, u);
}
__device__ __forceinline__ bf16x8 cat8(bf16x4 a, bf16x4 b) { return bf16x8{a[0], a[1], a[2], a[3], b[0], b[1], b[2], b[3]}; }
__device__ __forceinline__ bf16x8 asb8(u32x4 v) { return __builtin_bit_cast(bf16x8, v); }
__device__ __forceinline__ f32x4 asf4(u32x4 v) { return __builtin_bit_cast(f32x4, v); }
__device__ __forceinline__ f32x4 mfma32(bf16x8 a, bf16x8 b, f32x4 c) { return __builtin_amdgcn_mfma_f32_16x16x32_bf16(a, b, c, 0, 0, 0); }
__device__ __forceinline__ f32x4 mfma16(bf16x4 a, bf16x4 b, f32x4 c) { return __builtin_amdgcn_mfma_f32_16x16x16bf16_1k(a, b, c, 0, 0, 0); }

#define MF_(acc, ca, a, cb, b) asm volatile("v_mfma_f32_16x16x32_bf16 %0, %1, %2, %0" : "+v"(acc) : ca(a), cb(b))
#define MF0_(acc, ca, a, cb, b) asm volatile("v_mfma_f32_16x16x32_bf16 %0, %1, %2, 0" : "=&v"(acc) : ca(a), cb(b))
#define MF_AV(acc, a, b) MF_(acc, "a", a, "v", b)
#define MF_VA(acc, a, b) MF_(acc, "v", a, "a", b)
#define MF_VV(acc, a, b) MF_(acc, "v", a, "v", b)
#define MF_AV0(acc, a, b) MF0_(acc, "a", a, "v", b)
#define MF_VA0(acc, a, b) MF0_(acc, "v", a, "a", b)
#define MF_VV0(acc, a, b) MF0_(acc, "v", a, "v", b)
#define MF_VVC(d, a, b, c) asm volatile("v_mfma_f32_16x16x32_bf16 %0, %1, %2, %3" : "=&v"(d) : "v"(a), "v"(b), "v"(c))
#define MH_(acc, ca, a, cb, b) asm volatile("v_mfma_f32_16x16x16_bf16 %0, %1, %2, %0" : "+v"(acc) : ca(a), cb(b))
#define MH0_(acc, ca, a, cb, b) asm volatile("v_mfma_f32_16x16x16_bf16 %0, %1, %2, 0" : "=&v"(acc) : ca(a), cb(b))

constexpr int S_ = 2048, D_ = 640, H_ = 32, DH_ = 192, M_ = 8, SEG_ = 32, NSEG_ = 64;
constexpr int HD_ = H_ * DH_;
#define RLX_AGENT __ATOMIC_RELAXED, __HIP_MEMORY_SCOPE_AGENT
#define AUX_SC1 16
typedef __amdgpu_buffer_rsrc_t rsrc_t;
__device__ __forceinline__ rsrc_t mk_rsrc(const void* p, unsigned bytes) { return __builtin_amdgcn_make_buffer_rsrc((void*)p, 0, (int)bytes, 0x00020000); }
__device__ __forceinline__ u32x4 bld16(rsrc_t r, unsigned off) { return __builtin_amdgcn_raw_buffer_load_b128(r, off, 0, AUX_SC1); }
__device__ __forceinline__ u32x2 bld8(rsrc_t r, unsigned off) { return __builtin_amdgcn_raw_buffer_load_b64(r, off, 0, AUX_SC1); }
__device__ __forceinline__ unsigned bld4(rsrc_t r, unsigned off) { return __builtin_amdgcn_raw_buffer_load_b32(r, off, 0, AUX_SC1); }
__device__ __forceinline__ void bst16(rsrc_t r, unsigned off, u32x4 v) { __builtin_amdgcn_raw_buffer_store_b128(v, r, off, 0, AUX_SC1); }
__device__ __forceinline__ void bst8(rsrc_t r, unsigned off, u32x2 v) { __builtin_amdgcn_raw_buffer_store_b64(v, r, off, 0, AUX_SC1); }
__device__ __forceinline__ void bst4(rsrc_t r, unsigned off, unsigned v) { __builtin_amdgcn_raw_buffer_store_b32(v, r, off, 0, AUX_SC1); }
#define LDS_BARRIER() asm volatile("s_waitcnt lgkmcnt(0)\n\ts_barrier" ::: "memory")
#define DRAIN() asm volatile("s_waitcnt vmcnt(0)" ::: "memory")
#define MEMFENCE() asm volatile("" ::: "memory")
#define SCHED0() __builtin_amdgcn_sched_barrier(0)
__device__ __forceinline__ float red_max_q(float x) {
    auto a = __builtin_amdgcn_permlane16_swap(__float_as_uint(x), __float_as_uint(x), false, false);
    x = fmaxf(__uint_as_float(a[0]), __uint_as_float(a[1]));
    auto c = __builtin_amdgcn_permlane32_swap(__float_as_uint(x), __float_as_uint(x), false, false);
    return fmaxf(__uint_as_float(c[0]), __uint_as_float(c[1]));
}
__device__ __forceinline__ float max3r(float a, float b, float c) { float r; asm("v_max3_f32 %0, %1, %2, %3" : "=v"(r) : "v"(a), "v"(b), "v"(c)); return r; }
__device__ __forceinline__ float max2r(float a, float b) { float r; asm("v_max_f32 %0, %1, %2" : "=v"(r) : "v"(a), "v"(b)); return r; }
__device__ __forceinline__ void red_max_q2(float& x, float& y) {
    float s, t;
    asm("v_mov_b32 %2, %0\n\tv_mov_b32 %3, %1\n\ts_nop 0\n\tv_permlane16_swap_b32 %0, %2\n\tv_permlane16_swap_b32 %1, %3\n\tv_max_f32 %0, %0, %2\n\tv_max_f32 %1, %1, %3\n\t"
        "v_mov_b32 %2, %0\n\tv_mov_b32 %3, %1\n\ts_nop 0\n\tv_permlane32_swap_b32 %0, %2\n\tv_permlane32_swap_b32 %1, %3\n\tv_max_f32 %0, %0, %2\n\tv_max_f32 %1, %1, %3"
        : "+v"(x), "+v"(y), "=&v"(s), "=&v"(t));
}
__device__ __forceinline__ void red_sum_q2(float& x, float& y) {
    float s, t;
    asm("v_mov_b32 %2, %0\n\tv_mov_b32 %3, %1\n\ts_nop 0\n\tv_permlane16_swap_b32 %0, %2\n\tv_permlane16_swap_b32 %1, %3\n\tv_add_f32 %0, %0, %2\n\tv_add_f32 %1, %1, %3\n\t"
        "v_mov_b32 %2, %0\n\tv_mov_b32 %3, %1\n\ts_nop 0\n\tv_permlane32_swap_b32 %0, %2\n\tv_permlane32_swap_b32 %1, %3\n\tv_add_f32 %0, %0, %2\n\tv_add_f32 %1, %1, %3"
        : "+v"(x), "+v"(y), "=&v"(s), "=&v"(t));
}
__device__ __forceinline__ float red_sum_q(float x) {
    auto a = __builtin_amdgcn_permlane16_swap(__float_as_uint(x), __float_as_uint(x), false, false);
    x = __uint_as_float(a[0]) + __uint_as_float(a[1]);
    auto c = __builtin_amdgcn_permlane32_swap(__float_as_uint(x), __float_as_uint(x), false, false);
    return __uint_as_float(c[0]) + __uint_as_float(c[1]);
}
__device__ __forceinline__ u32x4 pk8(f32x4 a, f32x4 b) { return u32x4{pk2(a[0], a[1]), pk2(a[2], a[3]), pk2(b[0], b[1]), pk2(b[2], b[3])}; }
__device__ __forceinline__ u32x4 tag4(f32x4 v, unsigned tg) {
    return u32x4{(__float_as_uint(v[0]) & ~3u) | tg, (__float_as_uint(v[1]) & ~3u) | tg, (__float_as_uint(v[2]) & ~3u) | tg, (__float_as_uint(v[3]) & ~3u) | tg};
}
__device__ __forceinline__ unsigned tagbad(u32x4 v, unsigned tg) { return ((v[0] ^ tg) | (v[1] ^ tg) | (v[2] ^ tg) | (v[3] ^ tg)) & 3u; }

__device__ __host__ __forceinline__ constexpr unsigned xm(unsigned o, unsigned side) { return ((o >> 13) << 14) | (o & 8191u) | ((((o >> 20) ^ side) & 1u) << 13); }
__device__ __host__ __forceinline__ constexpr unsigned lp(unsigned o) { return ((o >> 13) << 14) | (o & 8191u); }
__device__ __host__ __forceinline__ constexpr int hside(int h) { return h >> 4; }
constexpr unsigned XM_OFF = 24576, XM_BUF = 4 * 32768;
constexpr unsigned XP_OFF = XM_OFF + 3 * XM_BUF, XP_BUF = 32768;
constexpr unsigned PT_OFF = XP_OFF + 3 * XP_BUF, PT_BUF = 65536;
constexpr unsigned FI_OFF = PT_OFF + 3 * PT_BUF, FI_N = 80;
constexpr unsigned FX_OFF = FI_OFF + FI_N * 1024;
constexpr unsigned A_END = FX_OFF + FI_N * 256;
constexpr unsigned UM_OFF = 1u << 20, UM_BUF = 40 * 8192;
#define TR 8
constexpr unsigned UT_OFF = 2u << 20;
constexpr unsigned QK_OFF = UT_OFF + TR * (2u << 20);
constexpr unsigned SG_OFF = QK_OFF + TR * (1u << 20);
enum { SG_XL = 0, SG_XR = 40960, SG_X1 = 81920, SG_OB = 122880, SG_ST = 163840, SG_FL = 172032 };
enum { FL_U = 0, FL_X = 2048, FL_O = 4096, ST_2 = 6144 };
constexpr unsigned ARENA = 2 * (SG_OFF + 64 * 262144);
static_assert(A_END <= UM_OFF && UM_OFF + 3 * UM_BUF <= UT_OFF && QK_OFF % (1u << 21) == 0 && SG_OFF % (1u << 21) == 0, "map");
static_assert(ARENA + (1u << 23) < (1u << 28), "workspace");
__device__ __forceinline__ unsigned seg_base(int layer, int seg) { return 2 * SG_OFF + (unsigned)seg * 524288 + ((((seg >> 2) ^ layer) & 1) << 13); }
__device__ __forceinline__ unsigned ut_slot(int seg) { return (unsigned)(seg & (TR - 1)) << 22; }
__device__ __forceinline__ unsigned qk_base(int seg, int side) { return 2 * QK_OFF + ((unsigned)(seg & (TR - 1)) << 21) + (((seg ^ side) & 1) << 13); }
__device__ __forceinline__ unsigned fi_step(int it) { return ((it >> 3) << 14) | ((it & 7) << 10); }
__device__ __forceinline__ unsigned fx_step(int it) { return ((it >> 5) << 14) | ((it & 31) << 8); }
static_assert(FI_OFF % 8192 == 0 && FX_OFF % 8192 == 0, "steps");
__device__ __forceinline__ u32x4 sld16(rsrc_t r, unsigned so, unsigned lo) { return __builtin_amdgcn_raw_buffer_load_b128(r, lo, so, AUX_SC1); }
__device__ __forceinline__ u32x2 sld8(rsrc_t r, unsigned so, unsigned lo) { return __builtin_amdgcn_raw_buffer_load_b64(r, lo, so, AUX_SC1); }
__device__ __forceinline__ unsigned sld4(rsrc_t r, unsigned so, unsigned lo) { return __builtin_amdgcn_raw_buffer_load_b32(r, lo, so, AUX_SC1); }
__device__ __forceinline__ void sst4(rsrc_t r, unsigned so, unsigned lo, unsigned v) { __builtin_amdgcn_raw_buffer_store_b32(v, r, lo, so, AUX_SC1); }
__device__ __forceinline__ void sst16(rsrc_t r, unsigned so, unsigned lo, u32x4 v) { __builtin_amdgcn_raw_buffer_store_b128(v, r, lo, so, AUX_SC1); }
__device__ __forceinline__ void sst8(rsrc_t r, unsigned so, unsigned lo, u32x2 v) { __builtin_amdgcn_raw_buffer_store_b64(v, r, lo, so, AUX_SC1); }
__device__ __forceinline__ unsigned umax3(unsigned a, unsigned b, unsigned c) { return max(max(a, b), c); }
__device__ __forceinline__ unsigned mx4(unsigned m, u32x4 v) { return umax3(m, v[0], v[2]); }
__device__ __forceinline__ unsigned nosent(unsigned x) { return min(x, 0xfffffffeu); }
constexpr unsigned OOB = 0x7f000000u;
constexpr unsigned kSpinCap = 1u << 18;

__device__ __forceinline__ char* arena_base(char* p) { return (char*)(((size_t)p + 0x3fffff) & ~(size_t)0x3fffff); }

typedef __attribute__((address_space(3))) bf16x4 lds_b4;
typedef __attribute__((address_space(3))) volatile int lds_int;
template <int V> struct IC { static constexpr int value = V; };
template <int B, int E, class F> __device__ __forceinline__ void sfor(F&& f) { if constexpr (B < E) { f(IC<B>{}); sfor<B + 1, E>(f); } }
#define WNT ""
#define DMA_NEXT(x) asm volatile("global_load_lds_dwordx4 %0, %1" x "\n\ts_add_u32 m0, m0, %2\n\tv_add_u32 %0, %3, %0" : "+v"(vo) : "s"(base), "s"(step), "s"(adv) : "memory", "m0", "scc")
template <bool NT> __device__ __forceinline__ void dma_next(const void* base, unsigned& vo, unsigned step, unsigned adv) { if (NT) DMA_NEXT(WNT); else DMA_NEXT(""); }
__device__ __forceinline__ void set_m0(unsigned x) { asm volatile("s_mov_b32 m0, %0\n\ts_nop 0" :: "s"(x) : "memory", "m0"); }
__device__ __forceinline__ bf16x8 tr_read(const char* p, int o0, int o1) { return cat8(__builtin_amdgcn_ds_read_tr16_b64_v4i16((lds_b4*)(p + o0)), __builtin_amdgcn_ds_read_tr16_b64_v4i16((lds_b4*)(p + o1))); }
struct Go { rsrc_t rh; unsigned so; u32x2 v; };
#define G_EB 2
#define GO_ASK asm volatile("buffer_load_dwordx2 %0, %1, %2, %3 offen sc1" : "=&v"(go.v) : "v"((threadIdx.x & 63) * 8), "s"(go.rh), "s"(go.so) : "memory")
struct Nop { __device__ void operator()() const {} };
template <int N, int N1, int NI, int NJ, int NS, int SL, bool V, int NE = 0, class ISSUE, class TAKE, class EARLY = Nop>
__device__ __forceinline__ void load_ring(char* smem, const int w, Go& go, ISSUE issue_, TAKE take, EARLY early = Nop{}) {
    constexpr int AH = NS - 1, SH = NE ? (NS - N % NS) % NS : 0, GE = N - 1 - AH - (NE ? G_EB : 0), HE = GE + AH + 1;
    static_assert(HE >= N - NS + 2, "free slots");
    const unsigned sta = (unsigned)(size_t)(__attribute__((address_space(3))) char*)smem + w * 1024;
    auto issue = [&](int st) {
        if ((st + SH) % NS == 0) set_m0(sta); else if (st == 0) set_m0(sta + SH * SL);
        const int n = st < N1 ? NI : NJ;
#pragma unroll
        for (int m = 0; m < n; ++m) issue_(st, m, m + 1 < n ? 4096u : SL - 4096u * (n - 1));
    };
#pragma unroll
    for (int st = 0; st < AH; ++st) issue(st);
    auto run = [&](auto WW) {
        sfor<0, N + 1>([&](auto ST) {
            constexpr int st = decltype(ST)::value, e = st + AH < N ? st + AH : N, a = e < N1 ? e : N1, n1 = a > st + 1 ? a - st - 1 : 0, n = st < N ? n1 * NI + (e - st - 1 - n1) * NJ + (st > GE && st <= GE + AH) + (st > HE ? NE : 0) : 0;
            asm volatile("s_waitcnt vmcnt(%0) lgkmcnt(0)\n\ts_barrier" :: "n"(n) : "memory");
            if constexpr (st > 0) take(IC<st - 1>{}, WW, smem, -1);
            if constexpr (st + AH < N) issue(st + AH);
            if constexpr (st == GE) GO_ASK;
            if constexpr (st == HE || st == N) asm volatile("" : "+v"(go.v));
            constexpr int sl = (st + SH) % NS, hi = sl / (65536 / SL) * (65536 / SL);
            if constexpr (st < N) take(ST, WW, smem + hi * SL, (sl - hi) * SL);
            if constexpr (NE && st == HE) early();
        });
    };
    if (!V || w == 0) run(IC<0>{}); else if (w == 1) run(IC<1>{}); else if (w == 2) run(IC<2>{}); else run(IC<3>{});
}
template <int R, int NTT> __device__ __forceinline__ void take_k(int ks, int ww, const char* p, int o, const int (&rp)[NTT], int hf, bf16x8 (&wf)[NTT][20]) {
#pragma unroll
    for (int tt = 0; tt < NTT; ++tt) {
        const int i = (ks + 20 - R * ww) % 20;
        if (o >= 0) wf[tt][i] = tr_read(p, rp[tt] + o, rp[tt] + hf + o);
        else if (20 * tt + i < 64) asm volatile("" : "+a"(wf[tt][i]));
        else asm volatile("" : "+v"(wf[tt][i]));
    }
}
#define SWP (R != 0)
#define ORD(st) (SWP ? (st & 1 ? 3u : -1u) : 1u)
#define BNT 4
template <int NI, int R, int NTT, int NE = 0, class EARLY = Nop>
__device__ __forceinline__ void load_coop(char* smem, const int w, Go& go, const void* const (&base)[NI], unsigned (&vo)[NI], const unsigned adv, const int (&rp)[NTT], const int hf, bf16x8 (&wf)[NTT][20], EARLY early = Nop{}) {
    load_ring<20, 20, NI, NI, (NI > 4 ? 6 : 8), (NI > 4 ? 24576 : 16384), R != 0, NE>(smem, w, go, [&](int st, int m, unsigned step) { dma_next<(NI > BNT)>(base[m], vo[m], step, ORD(st) * adv); },
        [&](auto ST, auto WW, const char* p, int o) { take_k<R, NTT>(decltype(ST)::value ^ SWP, decltype(WW)::value, p, o, rp, hf, wf); }, early);
}
__device__ __forceinline__ unsigned hd_src(int idx) { const int r = idx / 24, u = idx % 24; return r * (2 * HD_) + ((u * 16) ^ ((r & 6) << 4)); }
__device__ __forceinline__ int hd_pos(int l, int tile) { const int r = l >> 2; return r * 384 + ((32 * tile) ^ ((r & 6) << 4)) + (l & 3) * 8; }
__device__ __forceinline__ unsigned row_src(int idx) { return (idx * 16) ^ ((idx / 80 & 3) << 6); }
__device__ __forceinline__ int row_pos(int r, int piece) { return r * 1280 + ((piece ^ (r & 3)) << 6); }
template <int R, int NE = 0, class TILE, class EARLY = Nop> __device__ __forceinline__ void load_qk(char* smem, int w, int l, Go& go, const bf16_t* Wq, const bf16_t* Wk, TILE tile, bf16x8 (&wf)[6][20], EARLY early = Nop{}) {
    const void* const base[6] = {Wq, Wq, Wq, Wk, Wk, Wk};
    unsigned vo[6];
    int rp[6];
#pragma unroll
    for (int m = 0; m < 6; ++m) { vo[m] = hd_src((4 * (m % 3) + w) * 64 + l) + SWP * 64 * HD_; rp[m] = tile(m) / 12 * 12288 + hd_pos(l, tile(m) % 12); }
    load_coop<6, R, 6, NE>(smem, w, go, base, vo, 64 * HD_, rp, 6144, wf, early);
}
template <int R, class DST> __device__ __forceinline__ void load_vo(char* smem, int w, int l, Go& go, const bf16_t* Wv, const bf16_t* Wo, bf16x8 (&wf)[3][20], DST dst) {
    unsigned vo[3], v2[5];
    int r2[5];
    bf16x4 lo[10];
#pragma unroll
    for (int m = 0; m < 3; ++m) vo[m] = hd_src((4 * m + w) * 64 + l) + SWP * 64 * HD_;
#pragma unroll
    for (int m = 0; m < 5; ++m) v2[m] = row_src((4 * m + w) * 64 + l) + SWP * 20480;
#pragma unroll
    for (int m = 0; m < 5; ++m) r2[m] = row_pos(l >> 2, 5 * w + m) + (l & 3) * 8;
    const int rp[3] = {hd_pos(l, 2 * w), hd_pos(l, 2 * w + 1), hd_pos(l, 8 + w)};
    load_ring<32, 20, 3, 5, 8, 20480, R != 0>(smem, w, go, [&](int st, int m, unsigned step) { if (st < 20) dma_next<true>(Wv, vo[m % 3], step, ORD(st) * (64 * HD_)); else dma_next<true>(Wo, v2[m], step, ORD(st) * 20480u); },
        [&](auto ST, auto WW, const char* p, int o) {
            constexpr int st = decltype(ST)::value;
            if constexpr (st < 20) take_k<R, 3>(st ^ SWP, decltype(WW)::value, p, o, rp, 6144, wf);
            else {
#pragma unroll
                for (int t = 0; t < 10; ++t) {
                    if (o < 0) {
                        if (st & 1) { bf16x8& d = dst(st / 2 - 10, t, decltype(WW)::value); if (t == 0 && st < 28) asm volatile("" : "+a"(d)); else asm volatile("" : "+v"(d)); }
                        continue;
                    }
                    const bf16x4 x = __builtin_amdgcn_ds_read_tr16_b64_v4i16((lds_b4*)(p + r2[t / 2] + o + (t & 1) * 32));
                    if (st & 1) dst(st / 2 - 10, t, decltype(WW)::value) = SWP ? cat8(x, lo[t]) : cat8(lo[t], x); else lo[t] = x;
                }
            }
        });
}

struct MegaArgs {
    const bf16_t* x0; const bf16_t* mem0; const bf16_t* Wq; const bf16_t* Wk; const bf16_t* Wv; const bf16_t* Wo; const bf16_t* wqkv; const bf16_t* wo;
    char* ar; unsigned* tmo; char* hs; bf16_t* out;
    int nit, lag, roles, pad;
};

#define WSLP 2
#define LIVE u64 live_; asm volatile("s_add_u32 %0, %0, 1\n\ts_cmp_gt_u32 %0, %2\n\ts_cselect_b64 %1, 0, -1" : "+s"(spins), "=s"(live_) : "n"(kSpinCap) : "scc")
#define BAD(m) (__ballot(m == ~0u) & live_)
#define SPIN_END if (spins > kSpinCap) __hip_atomic_store(A.tmo, 1u + id, RLX_AGENT)
__device__ __forceinline__ void wave_wait(rsrc_t rx, unsigned off, unsigned so, unsigned& spins) {
#pragma unroll 1
    for (;;) {
        const unsigned v = sld4(rx, so, off);
        LIVE;
        if (!(__ballot(v == 0 && off != OOB) & live_)) break;
        __builtin_amdgcn_s_sleep(WSLP);
        MEMFENCE();
    }
}
__device__ __forceinline__ unsigned rtime() { return (unsigned)__builtin_amdgcn_s_memrealtime(); }
__device__ __forceinline__ void rt_until(unsigned target) {
    asm volatile("1:\n\ts_memrealtime vcc\n\ts_waitcnt lgkmcnt(0)\n\ts_sub_u32 vcc_lo, %0, vcc_lo\n\ts_cmpk_lt_u32 vcc_lo, 600\n\ts_cbranch_scc1 1b" :: "s"(target - 1) : "memory", "scc", "vcc");
}
#define RT_K 26
__device__ __forceinline__ void rt_fine(unsigned target) {
    u64 a, b, c;
    unsigned n;
    asm volatile("1:\n\ts_memrealtime vcc\n\ts_waitcnt lgkmcnt(0)\n\ts_sub_u32 vcc_lo, %4, vcc_lo\n\ts_cmpk_lt_u32 vcc_lo, 600\n\ts_cbranch_scc1 1b\n\t"
                 "s_add_u32 vcc_lo, vcc_lo, 8\n\ts_cbranch_scc0 3f\n\t"
                 "s_memrealtime %0\n\ts_nop 4\n\ts_memrealtime %1\n\ts_nop 4\n\ts_memrealtime %2\n\ts_lshl_b32 %3, %4, 2\n\ts_addk_i32 %3, %5\n\ts_nop 2\n\ts_memrealtime vcc\n\ts_waitcnt lgkmcnt(0)\n\t"
                 "s_sub_u32 %3, %3, vcc_lo\n\ts_mov_b64 vcc, %0\n\ts_sub_u32 %3, %3, vcc_lo\n\ts_mov_b64 vcc, %1\n\ts_sub_u32 %3, %3, vcc_lo\n\ts_mov_b64 vcc, %2\n\ts_sub_u32 %3, %3, vcc_lo\n\t"
                 "s_max_i32 %3, %3, 0\n\ts_min_i32 %3, %3, 64\n\t"
                 "s_branch 2f\n\t"
                 "3:\n\ts_add_u32 %3, %4, 13\n\t"
                 "4:\n\ts_memrealtime vcc\n\ts_waitcnt lgkmcnt(0)\n\ts_sub_u32 vcc_lo, %3, vcc_lo\n\ts_cmpk_lt_u32 vcc_lo, 16\n\ts_cbranch_scc1 4b\n\t"
                 "s_mov_b32 %3, 0\n\t"
                 "2:\n\ts_sub_u32 %3, %3, 5\n\ts_cbranch_scc0 2b"
                 : "=&s"(a), "=&s"(b), "=&s"(c), "=&s"(n) : "s"(target - 14), "n"(56 - RT_K) : "memory", "scc", "vcc");
}
#define LDA(dst, vo, so, imm) asm volatile("buffer_load_dwordx4 %0, %1, %2, %3 offen offset:%4 sc1" : "=v"(dst) : "v"(vo), "s"(rx), "s"(so), "n"(imm) : "memory")
#define LDI(dst, vo, so, imm) asm volatile("buffer_load_dwordx4 %0, %1, %2, %3 offen offset:%4 sc1" : "+v"(dst) : "v"(vo), "s"(rx), "s"(so), "n"(imm) : "memory")
#define LDA8(dst, vo, so, imm) asm volatile("buffer_load_dwordx2 %0, %1, %2, %3 offen offset:%4 sc1" : "=v"(dst) : "v"(vo), "s"(rx), "s"(so), "n"(imm) : "memory")
#define LDA4(dst, vo, so) asm volatile("buffer_load_dword %0, %1, %2, %3 offen sc1" : "=v"(dst) : "v"(vo), "s"(rx), "s"(so) : "memory")
#define WAITV(n, x) asm volatile("s_waitcnt vmcnt(%1)" : "+v"(x) : "n"(n) : "memory")
#define PIN_S(x) asm volatile("" : "+s"(x))
#define SZERO(x) asm volatile("s_mov_b32 %0, 0" : "=s"(x))
#define PIN_V(x) asm volatile("" : "+v"(x))
#define USE_V(x) asm volatile("" :: "v"(x))
#define LDS_AT(T, a) (*(__attribute__((address_space(3))) T*)(a))
__device__ __forceinline__ float dpp_ror8(float x) { return __uint_as_float(__builtin_amdgcn_mov_dpp(__float_as_uint(x), 0x128, 0xf, 0xf, true)); }
__device__ __forceinline__ unsigned lds_addr(const void* p) { return (unsigned)(size_t)(__attribute__((address_space(3))) const char*)p; }
#define DMA1(vo, so, dst, imm) asm volatile("s_mov_b32 m0, %3\n\ts_nop 0\n\tbuffer_load_dwordx4 %0, %1, %2 offen offset:%4 sc1 lds" :: "v"(vo), "s"(rx), "s"(so), "s"(dst), "n"(imm) : "memory", "m0")

enum { HS_A = 432, HS_E = 504, HS_F = 512, N_1 = 60, N_INIT = 108 };
__device__ __forceinline__ unsigned hs_tags(const Go& go, int l) { return (bld4(go.rh, l < 8 ? l * 1024 + HS_E : OOB) + 2) & ~1u; }
__device__ __forceinline__ rsrc_t arena(const MegaArgs& A, unsigned flip) { return mk_rsrc(arena_base(A.ar) + (flip << 21), ARENA); }
__device__ __forceinline__ unsigned hs_wait(Go& go, int l, int n, unsigned off, bool known, unsigned tag, unsigned& spins) {
#pragma unroll 1
    for (;;) {
        if (!known) tag = (__builtin_amdgcn_readlane(go.v[0], 63) + 2) & ~1u;
        LIVE;
        if (!(__ballot(l < n && ((go.v[0] ^ tag) | (go.v[1] ^ tag)) > 1) & live_)) break;
        __builtin_amdgcn_s_sleep(8);
        go.v = sld8(go.rh, go.so, off);
    }
    return __builtin_amdgcn_readfirstlane(go.v[0]) & 1;
}
#define GO_DECL(pool) Go go; go.rh = mk_rsrc(A.hs, 8192); go.so = (pool) * 1024
#define GO_ON(n) const rsrc_t rx = arena(A, hs_wait(go, l, n / 2, l * 8, false, 0, spins))
#define INIT_BEGIN(pool) const unsigned tags = hs_tags(go, l); go.v = sld8(go.rh, go.so, HS_F); INIT_FLIP(pool)
#define INIT_FLIP(pool) const unsigned tag = __builtin_amdgcn_readlane(tags, pool), flip = hs_wait(go, l, 1, HS_F, true, tag, spins); \
    const rsrc_t rx = arena(A, flip)
#define INIT_END(ii) asm volatile("s_waitcnt vmcnt(0)\n\ts_barrier" ::: "memory"); bst4(go.rh, t < 8 ? t * 1024 + (ii) * 4 : OOB, tags | flip)
#define INIT_SEEN hs_wait(go, l, N_INIT / 2, l * 8, true, tag, spins)
__device__ __forceinline__ void probe(const MegaArgs& A, const Go& go, int l, int id) {
    const rsrc_t rf = arena(A, 0);
    const unsigned tags = hs_tags(go, l);
    unsigned tm[2] = {~0u, ~0u};
    bst4(rf, l < 2 ? l * 8192 : OOB, 0u);
    DRAIN();
#pragma unroll
    for (int k = 0; k < 4; ++k) {
        const unsigned long long t0 = __builtin_amdgcn_s_memtime();
        bst4(rf, l ? OOB : (k & 1) * 8192, 0u);
        DRAIN();
        const unsigned d = (unsigned)(__builtin_amdgcn_s_memtime() - t0);
        tm[k & 1] = d < tm[k & 1] ? d : tm[k & 1];
    }
    bst8(go.rh, l < 8 ? l * 1024 + HS_F : OOB, u32x2{tags | (tm[1] < tm[0]), tags});
}
#define OUTAUX 0
#define DLYB0 900u
#define DLYB1 2400u
#define DLY1 0
#define DLY2 900
#define START_IDS const int t = threadIdx.x, l = t & 63, w = __builtin_amdgcn_readfirstlane(t >> 6); const unsigned l16b = l * 16; GO_DECL(id >> 5); unsigned spins; SZERO(spins)
__device__ __forceinline__ void init_area(const MegaArgs& A, const int ii, const int id, const unsigned t0) {
    START_IDS;
    INIT_BEGIN(id >> 5);
    const int i = 4 * ii + w;
    const bf16_t* mp = A.mem0 + (l & 7) * D_ + 32 * (i % 20) + 4 * (l >> 4);
    const u32x2 x = *(const u32x2*)mp, y = *(const u32x2*)(mp + 16);
    while (rtime() - t0 < DLY1) __builtin_amdgcn_s_sleep(4);
    sst16(rx, xm(XM_OFF + (i / 5 & 15) * 8192 + i % 5 * 1024, i / 80), l16b, u32x4{nosent(x[0]), x[1], nosent(y[0]), y[1]});
    auto fill = [&](unsigned o, unsigned n, unsigned f) {
#pragma unroll 2
        for (unsigned u = i * 1024; u < 2 * n; u += 163840) sst16(rx, 2 * o + u, l16b, u32x4{f, f, f, f});
    };
    fill(XM_OFF + XM_BUF, FI_OFF - PT_BUF - XM_OFF - XM_BUF, ~0u);
    fill(FI_OFF - PT_BUF, PT_BUF, 0u);
    fill(UM_OFF, 3 * UM_BUF, ~0u);
    for (unsigned u = i; u < 208; u += 160) {
        const unsigned f = u < 160 && (u / 16 * 8 + (u & 7) - (l & 1) * A.lag) >= NSEG_;
        sst16(rx, 2 * FI_OFF + u * 1024, l16b, u32x4{f, f, f, f});
    }
    INIT_END(16 + ii);
}
static_assert(FI_N == 80 && FX_OFF + 24576 <= UM_OFF && SG_FL == SG_ST + 8192 && SG_ST % 8192 == 0, "init");
__device__ __forceinline__ void first_rows(const MegaArgs& A, char* smem, const int sg, const int id, const unsigned t0) {
    START_IDS;
    set_m0(lds_addr(smem) + w * 1024);
    {
        unsigned vo[5];
#pragma unroll
        for (int m = 0; m < 5; ++m) vo[m] = row_src((4 * m + w) * 64 + l);
#pragma unroll
        for (int m = 0; m < 10; ++m) dma_next<true>(A.x0 + sg * (SEG_ * D_), vo[m % 5], 4096u, 20480u);
    }
    asm volatile("s_waitcnt vmcnt(0)\n\ts_barrier" ::: "memory");
    const unsigned tags = hs_tags(go, l);
    u32x4 xr[2][5], xl[2][5];
#pragma unroll 1
    for (int rt = 0; rt < 2; ++rt) {
        f32x4 v[10], a;
#pragma unroll
        for (int e = 0; e < 5; ++e) { xr[0][e] = xr[1][e]; xl[0][e] = xl[1][e]; }
#pragma unroll
        for (int e = 0; e < 10; ++e) {
            const u32x2 x = *(const u32x2*)(smem + rt * 20480 + row_pos(l & 15, 5 * w + e / 2) + (e & 1) * 32 + (l >> 4) * 8);
            xr[1][e / 2][2 * (e & 1)] = x[0]; xr[1][e / 2][2 * (e & 1) + 1] = x[1];
            v[e] = f32x4{lo16(x[0]), hi16(x[0]), lo16(x[1]), hi16(x[1])};
            a = e ? a + v[e] : v[0];
        }
        const float mw = red_sum_q((a[0] + a[1]) + (a[2] + a[3])) * (1.0f / 160);
#pragma unroll
        for (int e = 0; e < 10; ++e) { const f32x4 d = v[e] - mw; a = e ? __builtin_elementwise_fma(d, d, a) : d * d; }
        f32v2* sp = (f32v2*)(smem + 49152) + rt * 64 + (l & 15);
        sp[16 * w] = f32v2{mw, red_sum_q((a[0] + a[1]) + (a[2] + a[3]))};
        LDS_BARRIER();
        go.v = sld8(go.rh, go.so, HS_F);
        const f32v2 p = sp[l & 48];
        const float mean = red_sum_q(p[0]) * 0.25f, var = red_sum_q(p[1] + 160.f * (p[0] - mean) * (p[0] - mean)) * (1.0f / D_);
        const float mb = rb(mean), rd = 1.0f / rb(sqrtf(rb(rb(var) + 1e-5f)));
#pragma unroll
        for (int e = 0; e < 10; ++e) {
            const unsigned x = pk2(v[e][0] - mb, v[e][1] - mb), y = pk2(v[e][2] - mb, v[e][3] - mb);
            v[e] = f32x4{lo16(x), hi16(x), lo16(y), hi16(y)} * rd;
            if (e & 1) xl[1][e / 2] = pk8(v[e - 1], v[e]);
        }
    }
    INIT_FLIP(id >> 5);
    while (rtime() - t0 < (sg < 8 ? 0 : (sg < 16 ? DLY1 : DLY2))) __builtin_amdgcn_s_sleep(4);
#pragma unroll
    for (int e = 0; e < 20; ++e) {
        sst16(rx, seg_base(0, sg) + lp((e < 10 ? SG_XL : SG_XR) + (5 * w + e % 10 / 2) * 2048), l16b + (e & 1) * 1024, (e < 10 ? xl : xr)[e & 1][e % 10 / 2]);
        if (e == 9 && sg == 0) { asm volatile("s_waitcnt vmcnt(0)\n\ts_barrier" ::: "memory"); bst8(go.rh, t < 8 ? t * 1024 + HS_A : OOB, u32x2{tags | flip, tags}); }
    }
    const unsigned f = (w >> 1) - 1;
#pragma unroll
    for (int e = 0; e < 8; ++e) sst16(rx, 2 * (SG_OFF + SG_ST) + sg * 524288 + w * 8192 + e / 4 * 4096, l16b + (e & 3) * 1024, u32x4{f, f, f, f});
    INIT_END(sg + (sg < 16 ? 0 : 44));
}
#define LAGV 7
static_assert(LAGV < TR, "rings");
#define NTOUCH 0
#define VSLP 1
#define PER 284
#define E_U 121
#define E_P 137
#define E_W (L ? 110 : 135)
#define K_W 5
#define K_0 2
#define E_X 40
#define E_B 95
#define E_S 22
#define E_T 10
#define E_G 54
#define IT_1 62
#define D_1 20
#define D_2 10
#define E_L 152
#define E_Z 66
#define E_C 0
#define E_V 150
#define PVU_E 1
#define XV_L 1
#define E_N 0
#define E_H 0
#define FARF 1
#define E_M 4
#define FASTR 1
#define A_0 0
#define A_Q 6
#define A_UP 3
#define A_K 8
#define A_MAX 10
__device__ __forceinline__ bool adapt(int& mg, unsigned xv, unsigned xp, int mx) {
    const int e = xv - xp;
    const bool late = (unsigned)(e + 16) >= 36u;
    mg = max(mg + (xv && !late ? e : 0), 0);
    PIN_S(mg);
    mg = min(mg, mx);
    PIN_S(mg);
    return late;
}
#define TIGHT(x, it, d) asm volatile("s_cmp_ge_i32 %1, %2\n\ts_cselect_b32 %0, %3, 0" : "=s"(x) : "s"(it), "s"(IT_1), "s"(d) : "scc")
#define CHAIN_WAIT(pv, off, so, code) if (__builtin_expect(__ballot(pv == 0) != 0, 0)) { if (so != OOB) { wave_wait(rx, off, so, spins); } }
#define GAPN 11
#define GAP() asm volatile("s_nop %0" :: "n"(GAPN))
#define LD5(v, so) do { LDA(v[0], l16b, so, 0); GAP(); LDA(v[1], l16b, so, 1024); GAP(); LDA(v[2], l16b, so, 2048); GAP(); LDA(v[3], l16b, so, 3072); GAP(); LDA(v[4], l16k, so, 0); } while (0)
#define CHAIN_IDS const int t = threadIdx.x, l = t & 63, l16 = l & 15, lq = l >> 4; const int w = __builtin_amdgcn_readfirstlane(t >> 6); \
    GO_DECL(id >> 5); const int lag = A.lag, itend = A.nit; const unsigned l16b = l * 16, l16k = l * 16 + 4096; unsigned spins = 0
#define CHAIN_STAGE const char* sg1 = smem + ((w + 1) & 3) * 5120 + l * 16; const char* sg2 = smem + ((w + 2) & 3) * 5120 + l * 16; const char* sg3 = smem + ((w + 3) & 3) * 5120 + l * 16; \
    char* sg0 = smem + w * 5120 + l * 16; auto mrd = [&](int i) { return *(const bf16x8*)((i < 10 ? sg1 : (i < 15 ? sg2 : sg3)) + (i % 5) * 1024); }; \
    const unsigned mo0 = xm(XM_OFF + (pool & 3) * 32768 + w * 8192, pool >> 2), fX = xm(FX_OFF + (pool & 3) * 64, pool >> 2); unsigned fXv = fX; PIN_V(fXv)
#define GATHER(take) do { _Pragma("unroll 1") for (;;) { LD5(v, mo); LIVE; if (!take(live_)) break; } } while (0)
#define CVT2(o0, o1, x) do { o0 = pk2(x[0], x[1]); o1 = pk2(x[2], x[3]); asm volatile("" : "+v"(o0), "+v"(o1)); } while (0)
#define RR_(op, d, x, y, n) op "_dpp " d ", " x ", " y " row_ror:" n " row_mask:0xf bank_mask:0xf\n\t"
#define ROWRED(op, x, y, f0, f1) f0 RR_(op, y, x, x, "1") RR_(op, y, x, y, "2") RR_(op, y, x, y, "3") f1 RR_(op, x, y, y, "4") RR_(op, x, y, x, "8") RR_(op, x, y, x, "12")
#define BIAS(x, a, b, c, d) asm volatile("v_mov_b32 %0, 0xff800000\n\tv_cndmask_b32 %1, %0, 0, %5\n\tv_cndmask_b32 %2, %0, 0, %6\n\tv_cndmask_b32 %3, %0, 0, %7\n\tv_cndmask_b32 %0, %0, 0, %4" \
    : "=&v"(x[0]), "=&v"(x[1]), "=&v"(x[2]), "=&v"(x[3]) : "s"(a), "s"(b), "s"(c), "s"(d))
#define AND2(o0, o1, a, b, m) do { o0 = a & m; o1 = b & m; asm volatile("" : "+v"(o0), "+v"(o1)); } while (0)

constexpr int SL_S = 20480, SS_W = 3136, SL_KM = SL_S + 4 * SS_W, SL_KT = SL_KM + 6144, SL_W = SL_KT + 49152, SL_END = SL_W + 4 * 18432;
static_assert(SL_END <= 163840, "LDS");
#define G_LK 100
#define G_K 32
#define G_KS 6
__device__ __forceinline__ void s_role(const MegaArgs& A, char* smem, const int h, const int id) {
    CHAIN_IDS;
    const int pool = h >> 2, hs = hside(h);
    bf16x8 wf[6][20];
    const char* sW = smem + SL_W + w * 18432 + l * 16 - 2048;
    load_qk<5>(smem, w, l, go, A.Wq + h * DH_, A.Wk + h * DH_, [&](int m) { return m / 3 * 12 + (m % 3 < 2 ? 2 * w + m % 3 : 8 + w); }, wf);
#pragma unroll
    for (int i = 2; i < 20; ++i) *(bf16x8*)(sW + i * 1024) = wf[5][i];
    GO_ON(N_1);
    *(f32x4*)(smem + SL_S + w * SS_W + 3120) = f32x4{-INFINITY, -INFINITY, -INFINITY, -INFINITY};
#pragma unroll
    for (int i = 0; i < 12; ++i) *(u32x4*)(smem + SL_KT + w * 12288 + i * 1024 + l * 16) = u32x4{0u, 0u, 0u, 0u};
    asm volatile("s_waitcnt lgkmcnt(0)" ::: "memory");
    CHAIN_STAGE;
    char* sSw = smem + SL_S + w * SS_W + lq * 256 + ((l16 + 4 * lq) & 15) * 16;
    const int kq = l16, sL = w >> 1, rt = w & 1, row8 = (4 * w + lq) & 7;
    const bool kval = kq < 8 || (kq >> 1) == 4 + sL;
    const char* sSr = smem + SL_S + (kval ? (kq >> 2) * 1040 + (kq & 3) * 256 + ((4 * w + lq + 4 * kq) & 15) * 16 : 3120);
    const unsigned ppos = !kval ? OOB : xm(XP_OFF + (h >> 3) * 8192 +
        (kq < 8 ? ((h & 7) >> 1) * 1024 + ((kq & 3) * 16 + (h & 1) * 8 + row8) * 16 + (kq >> 2) * 8 : 4096 + ((h & 3) * 16 + ((h >> 2) & 1) * 8 + row8) * 16 + (kq & 1) * 8), sL);
    const unsigned pt1 = xm(PT_OFF + (h >> 3) * 16384 + (rt ? (h & 7) * 1024 : 8192 + ((h & 7) >> 1) * 1024 + (h & 1) * 8) + l * 16, sL);
    const unsigned pt2 = lq == 2 * sL ? xm(PT_OFF + (h >> 3) * 16384 + 12288 + (((h >> 2) & 1) * 2 + rt) * 1024 + ((h & 3) * 16 + l16) * 16, sL) : OOB;
    const u64 cm0 = __ballot(4 * lq <= l16), cm1 = __ballot(4 * lq + 1 <= l16), cm2 = __ballot(4 * lq + 2 <= l16), cm3 = __ballot(4 * lq + 3 <= l16), mval = __ballot((lq >> 1) == sL), oval = __ballot(rt);
    const unsigned ma = l16 < 8 ? ~0u : 0u, mb2 = ~ma;
    const unsigned fK = xm(FI_OFF + (h & 15) * 64 + (4 * sL + (l & 3)) * 4, hs);
    const unsigned qo = ((sL * 16 + (h & 15)) * 4 + rt) * 16384, ktl = lds_addr(smem + SL_KT) + w * 12288;
    const char* sKT = smem + SL_KT + l * 16;
    char* sKM = smem + SL_KM + l * 16;
    char* sKX = smem + SL_KT + l * 16 + 4096 + (w >> 1) * 1024 + (w & 1) * 8;
    unsigned X = rtime() - PER;
    int mg = A_0;
    auto qk_so = [&](int it) { const int sg = it - sL * lag; return (unsigned)sg < NSEG_ ? qk_base(sg, hs) + qo : OOB; };
    auto dm0 = [&](int e) {
        if (e == 0) asm volatile("s_mov_b32 m0, %0" :: "s"(ktl) : "m0");
        if (e == 4 || e == 6 || e == 10) asm volatile("s_add_u32 m0, m0, %0" :: "n"(e == 6 ? 2048 : 4096) : "m0", "scc");
    };
    auto dma = [&](int e, unsigned sq, unsigned sk, bool set = true) {
        if (set && (e == 0 || e == 4 || e == 6 || e == 10)) { dm0(e); asm volatile("s_nop 0"); }
        asm volatile("buffer_load_dwordx4 %0, %1, %2 offen offset:%3 sc1 lds" :: "v"(e % 6 < 4 ? l16b : l16k), "s"(rx), "s"(e < 6 ? sk : sq), "n"((e % 6 & 3) * 1024) : "memory");
    };
    {
        const unsigned pv0 = bld4(rx, fK), z = 0, so = qk_so(0);
        CHAIN_WAIT(pv0, fK, z, 1);
#pragma unroll
        for (int e = 0; e < 12; ++e) dma(e, so, so + 32768);
        asm volatile("s_waitcnt vmcnt(0)\n\ts_barrier" ::: "memory");
    }

    unsigned c0 = 0, c1 = 65536, c2 = 131072, mo = mo0, fs = -256, fs1 = 1024, sg = -sL * lag, q0 = 2 * QK_OFF + qo + hs * 8192;
    static_assert(2 * XP_BUF == 65536 && PT_BUF == 65536 && XM_BUF == 131072, "strides");
    PIN_S(c0); PIN_S(c1); PIN_S(c2); PIN_S(fs); PIN_S(fs1); PIN_S(sg); PIN_S(q0);
    for (int it = 0; it < itend; ++it) {
        unsigned ps, pr, ts, tr, qn, kn, tz, fk, per, xp;
        bf16x8 wl[3];
        f32x4 sc[3];
        u32x4 v[5], kp[2][2], kx[2];
        unsigned pvK;
        {
            bf16x8 ka[3], kb[3], q[3];
            auto krd = [&](int f, int e) {
                if (e == 0) kb[f % 3] = *(const bf16x8*)(sKT + (w & 2) * 12288 + f * 1024);
                if (e == 1) q[f % 3] = *(const bf16x8*)(sKT + w * 12288 + 6144 + f * 1024);
                if (e == 2) ka[f % 3] = *(const bf16x8*)(sKT + w * 12288 + f * 1024);
            };
            BIAS(sc[0], oval, oval, oval, oval); BIAS(sc[1], cm0, cm1, cm2, cm3);
            asm volatile("s_waitcnt vmcnt(%0)" :: "n"(5) : "memory");
#pragma unroll
            for (int e = 0; e < 9; ++e) { krd(e / 3, e % 3); SCHED0(); }
            PIN_V(ka[2]);
            rt_fine(X - E_G);
            sfor<0, 13>([&](auto G) {
                constexpr int g = decltype(G)::value, f = g / 2;
                if constexpr (g % 3 == 0) LDA(v[g / 3], g < 12 ? l16b : l16k, mo, (g / 3 & 3) * 1024);
                if constexpr (g < 12) MF_VV(sc[g & 1], (g & 1 ? ka : kb)[f % 3], q[f % 3]);
                SCHED0();
                if (g < 6) { if (g & 1) { krd(f + 3, 1); krd(f + 3, 2); } else krd(f + 3, 0); }
                SCHED0();
            });
#pragma unroll
            for (int e = 0; e < 2; ++e) {
                const u32x2 ka = *(const u32x2*)(sKX + e * 12288), kb = *(const u32x2*)(sKX + (e + 2) * 12288);
                kx[e] = u32x4{ka[0], ka[1], kb[0], kb[1]};
            }
            sg += 1;
            qn = (sg & (TR - 1)) << 21;
            qn += ((qn >> 8) & 8192) ^ q0;
            qn = sg < NSEG_ ? qn : OOB;
            kn = qn + 32768;
            PIN_S(sg); PIN_S(qn); PIN_S(kn);
            if (!G_LK) LDA4(pvK, fK, fs1);
            PIN_V(kx[0]); PIN_V(kx[1]);
        }
        f32x4 acc[6];
        auto mf = [&](int i, int tt, const bf16x8 xb) {
            if (i == 0) { if (tt < 4) MF_AV0(acc[tt], wf[tt][0], xb); else MF_VV0(acc[tt], wf[tt][0], xb); }
            else if (20 * tt + i < 64) MF_AV(acc[tt], wf[tt][i], xb);
            else if (tt < 5 || i < 2) MF_VV(acc[tt], wf[tt][i], xb);
            else MF_VV(acc[5], wl[i % 3], xb);
        };
        auto take = [&](u64 live_, auto N) {
            unsigned m = 0;
            u64 bad;
            sfor<0, 22>([&](auto G) {
                constexpr int g = decltype(G)::value;
                if constexpr (g < 20 && g % 4 == 0) { WAITV(4 + (decltype(N)::value && !G_LK) - g / 4, v[g / 4]); *(u32x4*)(sg0 + g / 4 * 1024) = v[g / 4]; SCHED0(); }
                mf(g / 6, g % 6, asb8(v[g / 6]));
                SCHED0();
                if constexpr (g < 20 && g % 4 == 0) { m = mx4(m, v[g / 4]); if (g < 16) PIN_V(m); else { bad = __ballot(m == ~0u); PIN_S(bad); } }
                if constexpr (g == 1 || g == 2 || g == 6 || g == 13) { constexpr int e = g < 3 ? g - 1 : 2 + g / 8; kp[e >> 1][e & 1] = *(const u32x4*)(sKT + e * 12288 + w * 1024); }
                if constexpr (g == 5 || g == 10 || g == 18) wl[(g / 5 + 1) % 3] = *(const bf16x8*)(sW + (g / 5 + 1) * 1024);
                if (g == 18 && !decltype(N)::value) { bad &= live_; PIN_S(bad); }
                if (g == 20 && decltype(N)::value && !G_LK) WAITV(0, pvK);
                SCHED0();
            });
            return bad;
        };
        if (__builtin_expect(take(~0ull, IC<1>{}) != 0, 0)) {
            auto again = [&](u64 live_) { return take(live_, IC<0>{}); };
            GATHER(again);
            asm volatile("s_nop 7" : "+v"(acc[0]), "+v"(acc[1]), "+v"(acc[2]), "+v"(acc[3]), "+v"(acc[4]), "+v"(acc[5]));
        }
        unsigned xv, kx0;
        u64 nk;
        u32x4 ko, kz;
        {
            bf16x8 mm[3];
            u32x4 qp, qa, qb, qz;
            unsigned z0, z1;
            f32x4 s[2], sm;
            sfor<22, 128>([&](auto G) {
                constexpr int g = decltype(G)::value, kt[13] = {5, 5, 5, 3, 4, -1, -1, 3, -1, 4, -1, 3, 4}, ki[13] = {17, 18, 19, 17, 17, 0, 0, 18, 0, 18, 0, 19, 19};
                constexpr int i = g < 102 ? g / 6 : (g < 111 ? 17 + (g - 102) / 3 : (g < 124 ? ki[g < 111 || g > 123 ? 0 : g - 111] : 0));
                constexpr int tt = g < 102 ? g % 6 : (g < 111 ? (g - 102) % 3 : (g < 124 ? kt[g < 111 || g > 123 ? 0 : g - 111] : -1));
                if constexpr (tt >= 0) mf(i, tt, i < 5 ? asb8(v[i < 5 ? i : 0]) : mm[(i < 5 ? 0 : i - 5) % 3]);
                if constexpr (g == 116 || g == 117) MF_VV0(s[g & 1], kp[0][g & 1], qa);
                if constexpr (g == 119 || g == 124) MF_VV(s[~g & 1], kp[1][~g & 1], qb);
                if constexpr (g == 121 || g == 125) MF_VV(s[g == 125], kx[g == 125], qz);
                if constexpr (g == 126) MF_VV0(sm, kz, qz);
                if constexpr (g == 127) MF_VV(sm, ko, qp);
                SCHED0();
                if (g == 22) { LDS_BARRIER(); }
                if (g == 23) mm[0] = mrd(5);
                if (g == 24) wl[2] = *(const bf16x8*)(sW + 5120);
                if constexpr (g > 24 && g < 103 && g % 6 == (g < 102)) mm[(g / 6 - 3) % 3] = mrd(g / 6 + 2);
                if constexpr (g > 24 && g < 102 && g % 6 == 3) wl[(g / 6 + 2) % 3] = *(const bf16x8*)(sW + (g / 6 + 2) * 1024);
                if (g == 104) wl[1] = *(const bf16x8*)(sW + 19 * 1024);
                if (g == 110) PIN_V(wl[1]);
                if (g == G_LK) LDA4(pvK, fK, fk);
                if (g == 26 && !G_LK) { nk = __ballot(pvK == 0); PIN_S(nk); dm0(0); }
                if (g == 30 && !G_LK) asm volatile("s_cmp_eq_u64 %1, 0\n\ts_cselect_b32 %0, %2, %3" : "=s"(kx0) : "s"(nk), "s"(kn), "n"(OOB) : "scc");
                if constexpr (!G_LK && g >= G_K && g <= G_K + 5 * G_KS && (g - G_K) % G_KS == 0) dma((g - G_K) / G_KS, qn, kx0, false);
                if (g == G_K + 4 * G_KS - 2 && !G_LK) dm0(4);
                if (g == 36) { ps = c0; pr = c2; PIN_S(ps); PIN_S(pr); }
                if (g == 42) { c2 = c0; c0 = c1; PIN_S(c2); PIN_S(c0); }
                if (g == 48) { c1 = pr; ts = 2 * ps; PIN_S(c1); PIN_S(ts); }
                if (g == 60) { mo = mo0 + 4 * c0; PIN_S(mo); }
                if (g == 66) { fs += 256; tz = fs & 8192; PIN_S(fs); PIN_S(tz); }
                if (g == 72) { fs += tz; tr = 2 * c0; PIN_S(fs); PIN_S(tr); }
                if (g == 84) { fk = fs1; fs1 += 1024; PIN_S(fk); PIN_S(fs1); }
                if (g == 90) { fs1 += fs1 & 8192; PIN_S(fs1); }
                if (g == 54) TIGHT(per, it, D_1);
                if (g == 78) { per = PER - per; PIN_S(per); }
                if (g == 96) { xp = X; X += per + mg; PIN_S(xp); PIN_S(X); }
                if (g == 111) CVT2(qp[0], qp[1], acc[0]);
                if (g == 112) CVT2(qp[2], qp[3], acc[1]);
                if (g == 113) AND2(qa[0], qa[1], qp[0], qp[1], ma);
                if (g == 114) AND2(qa[2], qa[3], qp[2], qp[3], ma);
                if (g == 115) CVT2(z0, z1, acc[2]);
                if (g == 118) AND2(qz[0], qz[1], z0, z1, ma);
                if (g == 119) AND2(qz[2], qz[3], z0, z1, mb2);
                if (g == 116) AND2(qb[0], qb[1], qp[0], qp[1], mb2);
                if (g == 117) AND2(qb[2], qb[3], qp[2], qp[3], mb2);
                if (g == 120) CVT2(kz[0], kz[1], acc[5]);
                if (g == 121) asm volatile("v_cvt_pk_bf16_f32 %0, %2, %3\n\tv_cvt_pk_bf16_f32 %1, %4, %5" : "=&v"(kz[2]), "=&v"(kz[3]) : "v"(acc[5][0]), "v"(acc[5][1]), "v"(acc[5][2]), "v"(acc[5][3]));
                if (g == 123) *(f32x4*)sSw = s[0];
                if (g == 124) CVT2(ko[0], ko[1], acc[3]);
                if (g == 125) CVT2(ko[2], ko[3], acc[4]);
                if (g == 127) *(f32x4*)(sSw + 1040) = s[1];
                SCHED0();
            });
            asm volatile("s_nop 6" : "+v"(sm) :: "memory");
            *(f32x4*)(sSw + 2080) = sm;
        }
        LDS_BARRIER();
        constexpr float SC = 0.07216878364870322f * 1.4426950408889634f;
        {
            const f32x4 s0 = *(const f32x4*)sSr, s1 = *(const f32x4*)(sSr + SS_W), s2 = *(const f32x4*)(sSr + 2 * SS_W), s3 = *(const f32x4*)(sSr + 3 * SS_W);
            SCHED0();
            f32x4 sv = ((s0 + s1) + s2) + s3;
            u32x2 p;
            p[0] = __float_as_uint(fmaxf(fmaxf(fmaxf(sv[0], sv[1]), sv[2]), sv[3]));
            asm volatile(ROWRED("v_max_f32", "%0", "%1", "v_mul_f32 %2, %6, %2\n\tv_mul_f32 %3, %6, %3\n\t", "v_mul_f32 %4, %6, %4\n\tv_mul_f32 %5, %6, %5\n\t")
                         "v_fma_f32 %2, -%0, %6, %2\n\tv_fma_f32 %3, -%0, %6, %3\n\tv_fma_f32 %4, -%0, %6, %4\n\tv_fma_f32 %5, -%0, %6, %5\n\t"
                         "v_exp_f32 %2, %2\n\tv_exp_f32 %3, %3\n\tv_exp_f32 %4, %4\n\tv_exp_f32 %5, %5\n\t"
                         "v_add_f32 %0, %2, %3\n\tv_add_f32 %0, %0, %4\n\tv_add_f32 %0, %0, %5\n\t"
                         ROWRED("v_add_f32", "%0", "%1", "s_nop 1\n\t", "s_nop 1\n\t")
                         "v_rcp_f32 %0, %0\n\ts_nop 0\n\tv_mul_f32 %2, %0, %2\n\tv_mul_f32 %3, %0, %3\n\tv_mul_f32 %4, %0, %4\n\tv_mul_f32 %5, %0, %5\n\t"
                         "v_cvt_pk_bf16_f32 %0, %2, |%3|\n\tv_cvt_pk_bf16_f32 %1, %4, %5"
                         : "+v"(p[0]), "=&v"(p[1]), "+v"(sv[0]), "+v"(sv[1]), "+v"(sv[2]), "+v"(sv[3]) : "s"(SC));
            sst8(rx, ps, ppos, p);
        }
        SCHED0();
        {
            if (G_LK) { WAITV(1, pvK); nk = __ballot(pvK == 0); }
            if (__builtin_expect(nk != 0, 0)) {
                wave_wait(rx, fK, fk, spins);
#pragma unroll
                for (int e = 0; e < 6; ++e) if (!G_LK) dma(e, qn, kn);
            }
            bf16x8 km[6], q[6];
            *(u32x4*)(sKM + w * 1024) = ko;
            *(u32x2*)(sKX + (SL_KM - SL_KT)) = u32x2{kz[0], kz[1]};
            SCHED0();
#pragma unroll
            for (int f = 0; f < 6; ++f) { q[f] = *(const bf16x8*)(sKT + w * 12288 + 6144 + f * 1024); SCHED0(); if (G_LK) dma(f, qn, kn); SCHED0(); }
            LDA4(xv, fXv, fs);
            dm0(6);
            asm volatile("s_waitcnt lgkmcnt(5)\n\ts_barrier" ::: "memory");
#pragma unroll
            for (int f = 0; f < 6; ++f) { km[f] = *(const bf16x8*)(sKM + f * 1024); SCHED0(); }
            dma(6, qn, kn, false);
            BIAS(sc[2], mval, mval, mval, mval);
            PIN_V(q[5]);
            dma(7, qn, kn, false);
            float mx = max3r(max3r(sc[0][0], sc[0][1], sc[0][2]), max3r(sc[0][3], sc[1][0], sc[1][1]), max2r(sc[1][2], sc[1][3])), tm;
            PIN_V(mx);
#pragma unroll
            for (int f = 0; f < 6; ++f) { MF_VV(sc[2], km[f], q[f]); if (f & 1) dma(8 + f / 2, qn, kn, false); if (f == 4) dm0(10); SCHED0(); }
            asm volatile("s_nop 5" : "+v"(sc[2]));
            mx = max3r(max3r(mx, sc[2][0], sc[2][1]), sc[2][2], sc[2][3]);
            asm volatile("v_mov_b32 %1, %0\n\ts_nop 1\n\tv_permlane16_swap_b32 %0, %1\n\tv_max_f32 %0, %0, %1\n\tv_mov_b32 %1, %0\n\ts_nop 1\n\tv_permlane32_swap_b32 %0, %1\n\tv_max_f32 %0, %0, %1" : "+v"(mx), "=&v"(tm));
            dma(11, qn, kn);
            mx *= -SC;
#pragma unroll
            for (int e = 0; e < 3; ++e) {
#pragma unroll
                for (int r = 0; r < 4; ++r) sc[e][r] = __builtin_fmaf(sc[e][r], SC, mx);
                PIN_V(sc[e]);
            }
#pragma unroll
            for (int e = 0; e < 3; ++e) {
#pragma unroll
                for (int r = 0; r < 4; ++r) sc[e][r] = __builtin_amdgcn_exp2f(sc[e][r]);
            }
            const f32x4 t4 = (sc[0] + sc[1]) + sc[2];
            float sum = (t4[0] + t4[1]) + (t4[2] + t4[3]);
            PIN_V(sum);
            float inv = __builtin_amdgcn_rcpf(red_sum_q(sum));
            PIN_V(inv);
            unsigned u[6];
#pragma unroll
            for (int e = 0; e < 6; ++e) { u[e] = pk2(sc[e >> 1][2 * (e & 1)] * inv, sc[e >> 1][2 * (e & 1) + 1] * inv); if (!(e & 1)) u[e] = nosent(u[e]); }
            const auto a4 = __builtin_amdgcn_permlane16_swap(u[4], u[4], false, false), a5 = __builtin_amdgcn_permlane16_swap(u[5], u[5], false, false);
            sst16(rx, ts, pt2, u32x4{u[4], u[5], a4[1], a5[1]});
            u32x2 o0, o1;
            asm volatile("v_mov_b64 %0, -1\n\tv_mov_b64 %1, -1" : "=v"(o0), "=v"(o1));
            const u32x4 ones = {o0[0], o0[1], o1[0], o1[1]};
            if (rt) { sst16(rx, ts, pt1, u32x4{u[0], u[1], u[2], u[3]}); sst16(rx, tr, pt1, ones); } else { sst8(rx, ts, pt1, u32x2{u[2], u[3]}); sst8(rx, tr, pt1, u32x2{ones[0], ones[1]}); }
            sst8(rx, pr, ppos, u32x2{ones[0], ones[1]});
            sst16(rx, tr, pt2, ones);
            asm volatile("s_waitcnt vmcnt(11)\n\ts_barrier" : "+v"(xv) :: "memory");
            xv = __builtin_amdgcn_readfirstlane(xv);
            adapt(mg, xv, xp, A_MAX);
            X = xv ? xv + per + mg : X;
        }
    }
    SPIN_END;
}

constexpr int CL_R = 0, CL_C = CL_R + 16384, CL_END = CL_C + 4096;
__device__ __forceinline__ void c_role(const MegaArgs& A, char* smem, const int pool, const int ku, const int id) {
    CHAIN_IDS;
    const int cL = pool >> 2, j = 5 * (pool & 3) + ku;
    go.v = sld8(go.rh, go.so, l * 8);
    GO_ON(N_1);
    unsigned fXv = xm(FX_OFF + (pool & 3) * 64, cL);
    PIN_V(fXv);
    const int hi = (l >> 3) & 1, tk = l & 7;
    const unsigned pos = xm(XM_OFF + w * 32768 + (j / 5) * 8192 + (j % 5) * 1024 + (lq * 16 + cL * 8 + tk) * 16 + hi * 8, 0);
    const unsigned umo = xm(UM_OFF + (16 * j + 2 * w) * 1024, cL), po0 = xm(XP_OFF + w * 8192, cL), pto = xm(PT_OFF + w * 16384, cL), uto = xm(UT_OFF + (64 * j + 8 * w) * 1024, cL);
    const unsigned fU = lp(SG_FL + FL_U + (8 * w + tk) * 64 + (pool & 3) * 4);
    const unsigned xo = lp((2 * j + (w >> 1)) * 1024 + (lq * 16 + 8 * (w & 1) + tk) * 16 + hi * 8), sto = l < 8 ? lp(SG_ST + (4 * j + w) * 64 + l * 8) : OOB;
    u32x2 alt;
    { const u32x2 m = *(const u32x2*)(A.mem0 + tk * D_ + 32 * j + 16 * hi + 4 * lq); alt = u32x2{nosent(m[0]), m[1]}; }
    f32x4* sRw0 = (f32x4*)(smem + CL_R) + w * 256 + l;
    f32x4* sRw1 = (f32x4*)(smem + CL_R) + w * 256 + 128 + (l ^ 8);
    const f32x4* sRr = (const f32x4*)(smem + CL_R) + (hi * 2 + (w >> 1)) * 64 + lq * 16 + ((8 * (w & 1) + tk) ^ (8 * hi));
    f32x4* sC = (f32x4*)(smem + CL_C);
    int mb = 0;
    u32x4 ua[16], ub[16], um[4];
#pragma unroll
    for (int e = 0; e < 16; ++e) ua[e] = u32x4{0u, 0u, 0u, 0u};
#pragma unroll
    for (int e = 0; e < 4; ++e) um[e] = u32x4{0u, 0u, 0u, 0u};
    for (int s = 0; s < NTOUCH; ++s) sst8(rx, seg_base(cL, s) + lp(SG_X1), xo, u32x2{0u, 0u});
    unsigned X = rtime() - 2 * PER;
    auto seg_so = [&](int sg) { return (unsigned)sg < NSEG_ ? seg_base(cL, sg) : OOB; };
    int mg = A_0;
    const unsigned lead = ((w >> 1) == cL ? E_N : 0) + (w >> 1) * E_H;
    unsigned dx = 0, sb0 = OOB, sb1 = seg_so(-cL * lag), pvU = sld4(rx, sb1, fU), ts = pto + 4 * PT_BUF, ts2 = ts + 16384;
    auto step = [&](const int it, auto LAST, u32x4 (&up)[16], u32x4 (&ut)[16]) {
        constexpr bool last = decltype(LAST)::value == 1, fin = decltype(LAST)::value == 2;
        const int nb = (mb == 2) ? 0 : mb + 1, pb = mb ? mb - 1 : 2, sg = it - cL * lag;
        if (!last) CHAIN_WAIT(pvU, fU, sb1, 0);
        u32x4 pt[16];
        u32x2 xr;
        auto pt_load = [&](int e) { LDA(pt[e], e < 4 || e >= 12 ? l16k : l16b, e < 8 ? ts2 : ts, (e & 3) * 1024); };
        if (!last && E_C) rt_until(X + E_C - dx);
        pt_load(0);
        SCHED0();
        unsigned uo = sb1 != OOB ? uto + ut_slot(sg) : OOB, uo2 = uo + 65536;
        PIN_S(uo); PIN_S(uo2);
        auto ut_load = [&](int e) {
            if (!last) LDA(ut[e], (e & 4) ? l16k : l16b, e < 8 ? uo : uo2, (e & 3) * 1024);
        };
        pt_load(1);
        SCHED0();
        unsigned sbr = (sb0 ^ (cL << 13)) + lp(SG_XR), sbx = sb0 + lp(SG_X1), sb2 = seg_so(sg + 1);
        PIN_S(sbr); PIN_S(sbx); PIN_S(sb2);
        pt_load(2);
        SCHED0();
        unsigned fs = fx_step(it), cu = umo + mb * (2 * UM_BUF), cu2 = cu + 16384, cp = po0 + mb * (2 * XP_BUF), cr = pb * (2 * XM_BUF), cr2 = cr + 8192;
        PIN_S(fs); PIN_S(cu); PIN_S(cu2); PIN_S(cp); PIN_S(cr); PIN_S(cr2);
        pt_load(3);
        SCHED0();
        unsigned cs = sg < 0 ? OOB : nb * (2 * XM_BUF) + FARF * ((cL ^ 1) << 13), cs2 = cs ^ 8192;
        X += PER - dx + mg;
        PIN_S(cs); PIN_S(cs2); PIN_S(X);
#pragma unroll
        for (int e = 4; e < 13; ++e) { pt_load(e); if (e < 12) GAP(); }
        u32x2 st;
        {
            f32x4 d[2][2];
            auto check = [&](auto E0, auto N, auto W) {
                constexpr int e0 = decltype(E0)::value, n = decltype(N)::value;
                auto look = [&](auto WW) {
                    asm volatile("s_waitcnt vmcnt(%2)" : "+v"(pt[e0]), "+v"(pt[e0 + 1]) : "n"(decltype(WW)::value) : "memory");
                    if (n == 4) asm volatile("" : "+v"(pt[e0 + n - 2]), "+v"(pt[e0 + n - 1]));
                    unsigned m = 0;
#pragma unroll
                    for (int e = e0; e < e0 + n; ++e) m = mx4(m, pt[e]);
                    return m; };
                if (__builtin_expect(__ballot(look(W) == ~0u) != 0, 0)) {
#pragma unroll 1
                    for (;;) {
#pragma unroll
                        for (int e = e0; e < e0 + n; ++e) pt_load(e);
                        LIVE;
                        if (!BAD(look(IC<0>{}))) break;
                    }
                }
            };
            sfor<0, 40>([&](auto G) {
                constexpr int g = decltype(G)::value, n = g & 1;
                if constexpr (g == 0) check(IC<0>{}, IC<4>{}, IC<9>{});
                if constexpr (g == 8) check(IC<4>{}, IC<2>{}, IC<10>{});
                if constexpr (g == 16) check(IC<6>{}, IC<2>{}, IC<(last ? 9 : 11)>{});
                if constexpr (g == 24) check(IC<8>{}, IC<4>{}, IC<(last ? 5 : 10)>{});
                if constexpr (g == 32) check(IC<12>{}, IC<2>{}, IC<(last ? 3 : 11)>{});
                if constexpr (g == 36) { check(IC<14>{}, IC<2>{}, IC<(last ? 0 : 10)>{}); PIN_V(xr); }
                if constexpr (g < 4) MF_VV0(d[n][g >> 1], um[2 * n], pt[g >> 1]);
                else if constexpr (g < 8) MF_VV(d[n][(g >> 1) & 1], um[2 * n + 1], pt[g >> 1]);
                else if constexpr (g < 24) {
                    constexpr int hh = (g - 8) >> 1;
                    const u32x4 x = up[8 * n + hh], z = pt[4 + (hh >> 1)];
                    const u32x2 xl = {x[0], x[1]}, zl = {z[2 * (hh & 1)], z[2 * (hh & 1) + 1]};
                    MH_(d[n][0], "v", xl, "v", zl);
                } else MF_VV(d[n][1], up[8 * n + ((g - 24) >> 1)], pt[8 + ((g - 24) >> 1)]);
                constexpr int rq = g < 32 ? ((g & 7) == 2 ? 0 : ((g & 7) == 5 ? 1 : ((g & 7) == 7 ? 2 : -99))) + 3 * (g >> 3) : (g == 33 ? 12 : (g == 35 ? 13 : -99));
                if constexpr (rq >= 0 && rq < 3) pt_load(13 + rq); else if constexpr (rq == 3) LDA8(xr, xo, sbr, 0); else if constexpr (rq > 3) ut_load(rq - 4);
                if (g == 24) sRw0[0] = d[0][0];
                if (g == 27) sRw1[0] = d[1][0];
                SCHED0();
            });
            const f32x4 xf = {lo16(xr[0]), hi16(xr[0]), lo16(xr[1]), hi16(xr[1])};
            asm volatile("" : "+v"(d[0][1]) : "v"(xf) : "memory");
            sRw0[64] = d[0][1];
            ut_load(10);
            asm volatile("s_nop 2" : "+v"(d[1][1]) :: "memory");
            sRw1[64] = d[1][1];
            ut_load(11);
            LDS_BARRIER();
            const f32x4 s0 = sRr[0], s1 = sRr[256], s2 = sRr[512], s3 = sRr[768];
            ut_load(12);
            SCHED0();
            f32x4 s = (((xf + s0) + s1) + s2) + s3;
            sst8(rx, sbx, xo, u32x2{pk2(s[0], s[1]), pk2(s[2], s[3])});
            float sm = (s[0] + s[1]) + (s[2] + s[3]);
            sm += dpp_ror8(sm);
            PIN_V(sm);
            ut_load(13);
            sm = red_sum_q(sm) * (1.0f / 32);
            PIN_V(sm);
            ut_load(14);
            s -= sm;
            float sq = __builtin_fmaf(s[3], s[3], __builtin_fmaf(s[2], s[2], __builtin_fmaf(s[1], s[1], s[0] * s[0])));
            sq += dpp_ror8(sq);
            PIN_V(sq);
            ut_load(15);
            sq = red_sum_q(sq);
            st = u32x2{nosent(__float_as_uint(sm)), __float_as_uint(sq)};
            PIN_V(st);
        }
        SCHED0();
        if (last) { DRAIN(); sst8(rx, sb0, sto, st); return; }
        unsigned xv;
        rt_until(X + E_X);
        LDA4(xv, fXv, fs);
        ts = pto + mb * (2 * PT_BUF); ts2 = ts + 16384;
        PIN_S(ts); PIN_S(ts2);
        asm volatile("s_waitcnt vmcnt(4)" ::: "memory");
        sst8(rx, sb0, sto, st);
        sst8(rx, cr, pos, u32x2{~0u, ~0u});
        sst8(rx, cr2, pos, u32x2{~0u, ~0u});
        WAITV(3, xv);
        const unsigned xp = X;
        X = __builtin_amdgcn_readfirstlane(xv);
        PIN_S(X);
        if (__builtin_expect(X == 0, 0)) {
#pragma unroll 1
            for (;;) {
                LDA4(xv, fXv, fs);
                LIVE;
                WAITV(0, xv);
                X = __builtin_amdgcn_readfirstlane(xv);
                if (X || !live_) break;
            }
        }
        unsigned d2;
        TIGHT(dx, it, D_1); TIGHT(d2, it, D_2);
        adapt(mg, X, xp, A_MAX);
        d2 -= mg >> 1;
        if (fin) {
#pragma unroll 1
            for (;;) {
                rt_until(X + E_U - d2);
#pragma unroll
                for (int i = 0; i < 4; ++i) LDI(um[i], l16b, (i & 2) ? cu2 : cu, (i & 1) * 1024);
                LIVE;
                asm volatile("s_waitcnt vmcnt(0)" : "+v"(um[0]), "+v"(um[1]), "+v"(um[2]), "+v"(um[3]) :: "memory");
                if (!BAD(mx4(mx4(mx4(mx4(0u, um[0]), um[1]), um[2]), um[3]))) break;
            }
            rt_until(X + E_L);
            mb = nb; sb0 = sb1; sb1 = sb2;
            return;
        }
        u32x4 p[5];
        f32x4 a[2], b[2];
        auto take2 = [&](u64 live_) {
            unsigned m = 0;
            u64 bad;
#pragma unroll
            for (int i = 0; i < 4; ++i) { WAITV(9 - PVU_E - i, um[i]); m = mx4(m, um[i]); PIN_V(m); }
#pragma unroll
            for (int i = 0; i < 5; ++i) {
                WAITV(4 - i, p[i]);
#pragma unroll
                for (int e = 0; e < 4; ++e) {
                    f32x4& d = (e & 1) ? b[e >> 1] : a[e >> 1];
                    if (i == 0) MF_VV0(d, ut[8 * (e >> 1) + (e & 1)], p[0]); else if (i < 4) MF_VV(d, ut[8 * (e >> 1) + 2 * (i & 3) + (e & 1)], p[i]); else MF_VV(d, um[e], p[4]);
                    SCHED0();
                    if (e == 0) { m = mx4(m, p[i]); if (i < 4) PIN_V(m); else { bad = __ballot(m == ~0u); PIN_S(bad); } }
                    if (e == 1 && i == 4) { bad &= live_; PIN_S(bad); }
                    SCHED0();
                }
            }
            return bad;
        };
        {
            unsigned tP = X + E_P - d2 - lead, tU = X + E_U - d2 - E_M * (4 - ku) - lead, ask = 1;
            PIN_S(ask);
            if (PVU_E) LDA4(pvU, fU, sb2);
            rt_until(tU);
#pragma unroll 1
            for (;;) {
#pragma unroll
                for (int i = 0; i < 4; ++i) LDI(um[i], l16b, (i & 2) ? cu2 : cu, (i & 1) * 1024);
                if (!PVU_E) LDA4(pvU, fU, sb2);
                LIVE;
                if (__builtin_expect(ask | !FASTR, 1)) rt_fine(tP);
                LD5(p, cp);
                ask = 0; PIN_S(ask);
                SCHED0();
                if (!take2(live_)) break;
            }
        }
        f32x4 x;
        asm volatile("v_add_f32_dpp %0, %8, %4 row_ror:8 row_mask:0xf bank_mask:0x3\n\tv_add_f32_dpp %1, %9, %5 row_ror:8 row_mask:0xf bank_mask:0x3\n\t"
                     "v_add_f32_dpp %2, %10, %6 row_ror:8 row_mask:0xf bank_mask:0x3\n\tv_add_f32_dpp %3, %11, %7 row_ror:8 row_mask:0xf bank_mask:0x3"
                     : "=&v"(x[0]), "=&v"(x[1]), "=&v"(x[2]), "=&v"(x[3]) : "v"(a[0][0]), "v"(a[0][1]), "v"(a[0][2]), "v"(a[0][3]), "v"(b[0][0]), "v"(b[0][1]), "v"(b[0][2]), "v"(b[0][3]));
        asm volatile("s_nop 0\n\tv_add_f32_dpp %0, %8, %4 row_ror:8 row_mask:0xf bank_mask:0xc\n\tv_add_f32_dpp %1, %9, %5 row_ror:8 row_mask:0xf bank_mask:0xc\n\t"
                     "v_add_f32_dpp %2, %10, %6 row_ror:8 row_mask:0xf bank_mask:0xc\n\tv_add_f32_dpp %3, %11, %7 row_ror:8 row_mask:0xf bank_mask:0xc"
                     : "+v"(x[0]), "+v"(x[1]), "+v"(x[2]), "+v"(x[3]) : "v"(b[1][0]), "v"(b[1][1]), "v"(b[1][2]), "v"(b[1][3]), "v"(a[1][0]), "v"(a[1][1]), "v"(a[1][2]), "v"(a[1][3]));
        sC[w * 64 + l] = x;
        LDS_BARRIER();
        const f32x4 s = ((sC[l] + sC[64 + l]) + sC[128 + l]) + sC[192 + l];
        const u32x2 r = {nosent(pk2(s[0], s[1])), pk2(s[2], s[3])};
        sst8(rx, cs, pos, r);
        sst8(rx, cs2, pos, r);
        SCHED0();
        if (__builtin_expect(cs == OOB, 0)) { sst8(rx, nb * (2 * XM_BUF), pos, alt); sst8(rx, nb * (2 * XM_BUF) + 8192, pos, alt); }
        PIN_V(pvU);
        mb = nb; sb0 = sb1; sb1 = sb2;
    };
    int it = 0;
    PIN_S(spins); PIN_V(pvU); PIN_V(alt);
#pragma unroll 1
    do { step(it, IC<0>{}, ua, ub); step(it + 1, IC<0>{}, ub, ua); it += 2; } while (it + 2 < itend);
    if (LAGV & 1) { step(it, IC<2>{}, ua, ub); step(it + 1, IC<1>{}, ub, ua); } else { step(it, IC<0>{}, ua, ub); step(it + 1, IC<2>{}, ub, ua); step(it + 2, IC<1>{}, ua, ub); }
    SPIN_END;
}

constexpr int VL_V = 20480, VL_W = VL_V + 6144, VL_END = VL_W + 81920;
__device__ __forceinline__ void vm_role(const MegaArgs& A, char* smem, const int h, const int id) {
    CHAIN_IDS;
    const int pool = h >> 2;
    bf16x8 wf[3][20], wo[10][4];
    char* sWo = smem + VL_W + w * 20480 + l * 16;
    {
        bf16x8 tmp[20];
        load_vo<5>(smem, w, l, go, A.Wv + h * DH_, A.Wo + (size_t)(h * DH_) * D_, wf, [&](int k, int ct, int ww) -> bf16x8& { return k < 4 ? wo[ct][(k - ww) & 3] : tmp[4 * (ct >> 1) + 2 * (k & 1) + (ct & 1)]; });
#pragma unroll
        for (int i = 0; i < 20; ++i) *(bf16x8*)(sWo + i * 1024) = tmp[i];
    }
    GO_ON(N_1);
    CHAIN_STAGE;
    unsigned upos[5];
#pragma unroll
    for (int m = 0; m < 5; ++m) upos[m] = xm(UM_OFF + ((10 * w + 2 * m + (lq & 1)) * 8 + (h >> 2)) * 1024 + ((h & 3) * 16 + l16) * 16, lq >> 1);
    char* sV = smem + VL_V + l * 16;
    const char* sV1 = sV + ((w + 1) & 3) * 1024; const char* sV2 = sV + ((w + 2) & 3) * 1024; const char* sV3 = sV + ((w + 3) & 3) * 1024;
    const unsigned xw = (h == 1 && t < 8) ? xm(FX_OFF + (t & 3) * 64, t >> 2) : OOB;
    int mb = 0;
    unsigned X = rtime() - 2 * PER, dx = 0;
    int mg = A_0, cnt = 0;
    for (int it = 0; it < itend; ++it) {
        const int nb = (mb == 2) ? 0 : mb + 1, pb = mb ? mb - 1 : 2;
        unsigned mo = mo0 + mb * (2 * XM_BUF), fs = fx_step(it), us = mb * (2 * UM_BUF), ur = pb * (2 * UM_BUF);
        X += PER - dx + mg;
        const unsigned xp = X;
        PIN_S(mo); PIN_S(fs); PIN_S(us); PIN_S(ur); PIN_S(X);
        rt_fine(X - E_G);
        f32x4 acc[3];
        u32x4 v[5];
        auto take = [&](u64 live_) {
            unsigned m = 0;
            u64 bad;
            sfor<0, 11>([&](auto G) {
                constexpr int g = decltype(G)::value, e = g < 9 && !(g & 1) ? g / 2 : -1;
                if constexpr (e >= 0) { WAITV(4 - e, v[e]); *(u32x4*)(sg0 + e * 1024) = v[e]; SCHED0(); }
                if (g < 3) MF_AV0(acc[g % 3], wf[g % 3][0], v[0]); else MF_AV(acc[g % 3], wf[g % 3][g / 3], v[g / 3]);
                SCHED0();
                if constexpr (e >= 0) { m = mx4(m, v[e]); if (e < 4) PIN_V(m); else { bad = __ballot(m == ~0u); PIN_S(bad); } }
                if (g == 9) { bad &= live_; PIN_S(bad); }
                SCHED0();
            });
            return bad;
        };
        GATHER(take);
        bf16x8 wl[4];
        u32x4 vf[6], o;
        f32x4 d[10];
        unsigned xv;
        {
            constexpr int MW = 5;
            bf16x8 mm[MW];
            u32x2 vz;
            u64 tb64;
            unsigned t0, t1;
            asm volatile("s_waitcnt lgkmcnt(0)\n\ts_barrier\n\ts_memrealtime %0" : "=s"(tb64) :: "memory");
            mm[0] = mrd(5);
            SCHED0();
            sfor<11, 60>([&](auto G) {
                constexpr int g = decltype(G)::value, i = g < 45 ? g / 3 : (g < 50 ? g - 30 : 15 + (g - 50) / 2), tt = g < 45 ? g % 3 : (g < 50 ? 2 : g & 1);
                const bf16x8 xb = i < 5 ? asb8(v[i < 5 ? i : 0]) : mm[(i < 5 ? 0 : i - 5) % MW];
                MF_AV(acc[tt], wf[tt][i], xb);
                SCHED0();
                if constexpr (g < 15) mm[g - 10] = mrd(g - 5);
                if constexpr (g >= 18 && g < 46 && g % 3 == 0) mm[(g / 3 - 6) % MW] = mrd(g / 3 + 4);
                if constexpr (g >= 54 && g < 58) wl[g - 54] = *(const bf16x8*)(sWo + (g - 54) * 1024);
                if (g == 17) asm volatile("s_waitcnt lgkmcnt(0)" : "+s"(tb64) :: "memory");
                if (g == 19) asm volatile("s_sub_u32 %0, %2, %3\n\ts_add_u32 %1, %2, %4" : "=&s"(t0), "=&s"(t1) : "s"((unsigned)tb64), "s"(X), "s"(E_S) : "scc");
                if (g == 22) asm volatile("s_cmp_gt_i32 %1, %3\n\ts_cselect_b32 %0, %2, %0" : "+s"(X) : "s"(t0), "s"(t1), "s"(E_T) : "scc");
                if (g == 23) asm volatile("v_or_b32 %0, 1, %1" : "=v"(xv) : "s"(X));
                if (g == 25) sst4(rx, fs, xw, xv);
                if (g == 52) CVT2(vz[0], vz[1], acc[2]);
                if (g == 53) *(u32x2*)(sV + 4096 + (w >> 1) * 1024 + (w & 1) * 8) = vz;
                SCHED0();
            });
        }
        asm volatile("s_nop 2");
        SCHED0();
        CVT2(vf[0][0], vf[0][1], acc[0]);
        SCHED0();
        asm volatile("s_nop 1");
        SCHED0();
        CVT2(vf[0][2], vf[0][3], acc[1]);
        *(u32x4*)(sV + w * 1024) = vf[0];
        SCHED0();
        asm volatile("s_nop 0");
        sfor<0, 67>([&](auto G) {
            constexpr int g = decltype(G)::value, m = g < 10 ? 0 : (g - 10) / 10, r = g < 10 ? 0 : (g - 10) % 10, kk = g < 10 ? 0 : 1 + r / 2, ct = g < 10 ? g : 2 * m + (r & 1);
            if constexpr (g < 60) {
                if constexpr (kk >= 4) MF_VV(d[ct], vf[kk], wl[2 * (kk & 1) + (r & 1)]);
                else if constexpr (ct == 0) { if (kk == 0) MF_VA0(d[0], vf[0], wo[0][0]); else MF_VA(d[0], vf[kk], wo[0][kk]); }
                else { if (kk == 0) MF_VV0(d[ct], vf[0], wo[ct][0]); else MF_VV(d[ct], vf[kk], wo[ct][kk]); }
            }
            SCHED0();
            if (g == 3) { LDS_BARRIER(); }
            if (g == 4) vf[1] = *(const u32x4*)sV1;
            if (g == 5) vf[2] = *(const u32x4*)sV2;
            if (g == 6) vf[3] = *(const u32x4*)sV3;
            if (g == 7) vf[4] = *(const u32x4*)(sV + 4096);
            if (g == 8) vf[5] = *(const u32x4*)(sV + 5120);
            if constexpr (g >= 17 && g < 51 && (g - 17) % 10 < 4) wl[(g - 17) % 10] = *(const bf16x8*)(sWo + ((g - 17) / 10 * 4 + 4 + (g - 17) % 10) * 1024);
            if (g == (XV_L ? 66 : 12)) { if (XV_L) sst16(rx, us, upos[4], o); LDA4(xv, fXv, fs); }
            if constexpr (m > 0) {
                if (g == 60) asm volatile("s_nop 3");
                if (r == 1) CVT2(o[0], o[1], d[2 * m - 2]);
                SCHED0();
                if (g == 61) asm volatile("s_nop 1");
                if (r == 2) CVT2(o[2], o[3], d[2 * m - 1]);
                if (r == 3) { o[0] = nosent(o[0]); o[2] = nosent(o[2]); asm volatile("" : "+v"(o[0]), "+v"(o[2])); }
                if (r == 4) asm volatile("v_permlane16_swap_b32 %0, %1" : "+v"(o[1]), "+v"(o[3]));
                if (r == 5) asm volatile("v_permlane16_swap_b32 %0, %1" : "+v"(o[0]), "+v"(o[2]));
                if (r == 6 && (m < 5 || !XV_L)) sst16(rx, us, upos[m - 1], o);
            }
            SCHED0();
        });
        if (E_V) rt_until(X + E_V);
#pragma unroll
        for (int m = 0; m < 5; ++m) { __builtin_amdgcn_s_sleep(VSLP); sst16(rx, ur, upos[m], u32x4{~0u, ~0u, ~0u, ~0u}); SCHED0(); }
        WAITV(XV_L ? 5 : 10, xv);
        xv = __builtin_amdgcn_readfirstlane(xv);
        X = xv ? xv : X;
        cnt = adapt(mg, X | 1, xp, A_MAX) && it > 3 ? max(cnt, 0) + A_UP : cnt - 1;
        PIN_S(cnt);
        if (cnt >= A_K) { mg = min(mg + 2, A_MAX); cnt -= A_UP; }
        PIN_S(mg); PIN_S(cnt);
        if (cnt <= -A_Q) { mg = max(mg - 2, 0); cnt = 0; }
        PIN_S(mg); PIN_S(cnt);
        TIGHT(dx, it, D_1);
        mb = nb;
    }
    SPIN_END;
}
#define DBG_LD 0
#define WORK_IDS const int t = threadIdx.x, l = t & 63, l16 = l & 15, lq = l >> 4; const int w = __builtin_amdgcn_readfirstlane(t >> 6); \
    GO_DECL(id >> 5); const unsigned l16b = l * 16, dvo = w * 2048 + l * 16 + DBG_LD, lds0 = lds_addr(smem) + w * 2048; unsigned spins; SZERO(spins)
#define XSET(so, base, ld) asm volatile("s_mov_b32 m0, %2\n\ts_mov_b32 %0, %1" : "=s"(so) : "s"(base), "s"(ld) : "m0")
#define XADV(so) asm volatile("s_add_u32 m0, m0, 0x2000\n\ts_add_u32 %0, %0, 0x4000" : "+s"(so) :: "m0", "scc")
#define XDMA(so, rt) asm volatile("buffer_load_dwordx4 %0, %1, %2 offen offset:%3 sc1 lds" :: "v"(dvo), "s"(rx), "s"(so), "n"((rt) * 1024) : "memory")
#define XALL(so, base, ld) _Pragma("unroll") for (int e_ = 0; e_ < 10; ++e_) { if (e_ % 5 == 0) XSET(so, base, ld); else XADV(so); XDMA(so, e_ / 5); }
#define XALL2(so, base, ld) _Pragma("unroll") for (int e_ = 0; e_ < 5; ++e_) { if (e_ == 0) XSET(so, base, ld); else XADV(so); XDMA(so, 0); XDMA(so, 1); }
#define VMB(n) asm volatile("s_waitcnt vmcnt(%0)\n\ts_barrier" :: "n"(n) : "memory")
#define VMB_LATE(k) asm volatile("s_cmp_lg_u32 %0, %1\n\ts_cbranch_scc1 1f\n\ts_waitcnt vmcnt(%2)\n1:\n\ts_barrier" :: "s"(xs), "n"(OOB), "n"(k) : "memory", "scc")
#define VMW(n) asm volatile("s_waitcnt vmcnt(%0)" :: "n"(n) : "memory")
#define NEXT_DECL(asked) const unsigned fXo = l < 4 ? lp(SG_FL + FL_X + ((h & 12) + l) * 128) : OOB, fx1 = (((TR >> 2) & ~L & 1) ^ hside(h)) << 13; const int dl = __builtin_amdgcn_readfirstlane((L ^ 1) * TR), ld0 = __builtin_amdgcn_readfirstlane(L ? -K_W : K_0); \
    unsigned xb = seg_base(0, 0), xs, fb = seg_base(hside(h), 0), msk, so, pv, pend = OOB, fXv = xm(FX_OFF + ((h >> 2) & 3) * 64, hside(h)), xv, fs; PIN_V(fXv); if (L) wave_wait(rx, fXo, fb, spins); if (!(asked)) { XALL(so, xb, lds0); } VMB(5); int s; SZERO(s)
#define G_L1 150
#define G_E1 182
#define G_L2 210
#define NEXT_ADDR(a, st) \
    if (g == a) { s = __builtin_amdgcn_readfirstlane(s); PIN_S(s); xb = (s + 1) << 19; PIN_S(xb); } \
    if (g == a + st) { xs = (xb >> 8) & 8192; PIN_S(xs); } \
    if (g == a + 2 * st) { xb = (xb | xs) + 2 * SG_OFF; PIN_S(xb); } \
    if (g == a + 3 * st) { fb = (xb ^ fx1) - (dl << 19); PIN_S(fb); } \
    if (g == a + 5 * st) asm volatile("s_cmp_lt_i32 %1, %2\n\ts_cselect_b32 %0, 0, 15" : "=s"(msk) : "s"(s), "s"(dl - 1) : "scc"); \
    if (g == a + 6 * st) asm volatile("s_cmp_lt_i32 %1, 63\n\ts_cselect_b32 %0, %0, %2" : "+s"(fb) : "s"(s), "n"(OOB) : "scc"); \
    if (g == a + 7 * st) { PIN_S(s); fs = (s - ld0) << 8; PIN_S(fs); } \
    if (g == a + 8 * st) { fs += fs & ~8191u; PIN_S(fs); } \
    if (g == a + 9 * st) asm volatile("s_cmp_lt_i32 %0, 0\n\ts_cselect_b32 %0, %1, %0" : "+s"(fs) : "n"(OOB) : "scc")
#define NEXT_LOOK(n, ld) \
    if (g == G_L1 || g == G_L2) LDA4(pv, fXo, fb); \
    if (g == G_E1) { WAITV(n, pv); xs = (unsigned)__ballot(pv == 0); PIN_S(xs); } \
    if (g == G_E1 + 2) asm volatile("s_and_b32 %0, %0, %2\n\ts_cselect_b32 %0, %3, %1" : "+s"(xs) : "s"(xb), "s"(msk), "n"(OOB) : "scc")
#define T_D 20
#define NEXT_GO(n, ld, fl, nt) \
    WAITV(n, pv); \
    if (xs != OOB) { asm volatile("s_barrier" ::: "memory"); prime(); LDA4(xv, fXv, fs); SCHED0(); tail(); WAITV(nt, xv); } \
    else { \
        tail(); \
        if ((unsigned)__ballot(pv == 0) & msk) { DRAIN(); sst4(rx, pend, fl, 1u); if (s == NSEG_ - 1) { SPIN_END; __builtin_amdgcn_endpgm(); } pend = OOB; wave_wait(rx, fXo, fb, spins); } \
        LDA4(xv, fXv, fs); \
        XALL(so, xb, ld); \
        WAITV(10, xv); sst4(rx, pend, fl, 1u); pend = OOB; \
        VMB(6); prime(); \
    } \
    so = __builtin_amdgcn_readfirstlane(xv); \
    if (pend != OOB && (so == 0 || (int)(so + E_W - rtime()) >= T_D)) { DRAIN(); sst4(rx, pend, fl, 1u); pend = OOB; } \
    PIN_S(pend); \
    if (__builtin_expect(so == 0, 0) && fs != OOB) { \
        _Pragma("unroll 1") for (int k_ = 0; k_ < 8 && so == 0; ++k_) { __builtin_amdgcn_s_sleep(4); so = __builtin_amdgcn_readfirstlane(sld4(rx, fs, fXv)); MEMFENCE(); } \
    } \
    so += E_W; PIN_S(so); rt_until(so)

constexpr int QL_W = 81920;
__device__ __forceinline__ void qk_role(const MegaArgs& A, char* smem, const int L, const int h, const int id) {
    WORK_IDS;
    bf16x8 wf[6][20];
    char* sW = smem + QL_W + w * 20480 + l * 16;
    unsigned x0;
    load_qk<0, 11>(smem, w, l, go, A.Wq + h * DH_, A.Wk + h * DH_, [&](int m) { return 6 * w + m; }, wf, [&]() {
        const unsigned tag = (__builtin_amdgcn_readlane(go.v[0], 63) + 2) & ~1u, a0 = __builtin_amdgcn_readlane(go.v[0], HS_A / 8), a1 = __builtin_amdgcn_readlane(go.v[1], HS_A / 8);
        const rsrc_t rx = arena(A, a0 & 1);
        x0 = ((a0 ^ tag) | (a1 ^ tag) | 2 * L) > 1 ? OOB : seg_base(0, 0);
        PIN_S(x0);
        unsigned so;
        XALL(so, x0, lds0);
        GO_ASK;
    });
#pragma unroll
    for (int i = 0; i < 20; ++i) *(bf16x8*)(sW + i * 1024) = wf[5][i];
    GO_ON(N_1);
    const unsigned fI = l == 0 ? xm(FI_OFF + (h & 15) * 64 + (4 * L + w) * 4, hside(h)) : OOB;
    const unsigned po0 = 2 * QK_OFF + ((L * 16 + (h & 15)) * 4 + (w & 2)) * 16384 + (w & 1) * 3072 + hside(h) * 8192;
    const int it0 = L * A.lag;
    const unsigned l16s = l16b;
    NEXT_DECL(x0 != OOB);
    unsigned vx = l16b, ld = lds0, po;
    bf16x8 xw[3], wl[2];
    f32x4 acc[2][6];
    u32x4 o[2];
    auto prime = [&]() { xw[0] = *(const bf16x8*)(smem + vx); xw[1] = *(const bf16x8*)(smem + vx + 2048); };
    auto tail = [&]() {
#pragma unroll
        for (int f = 0; f < 3; ++f) { CVT2(o[f & 1][0], o[f & 1][1], acc[1][2 * f]); CVT2(o[f & 1][2], o[f & 1][3], acc[1][2 * f + 1]); sst16(rx, po, l16s + f * 1024, o[f & 1]); SCHED0(); }
    };
    prime();
    wl[0] = *(const bf16x8*)sW;
#pragma unroll 1
    for (;; ++s) {
        SCHED0();
        sfor<0, 240>([&](auto G) {
            constexpr int g = decltype(G)::value, i = g / 6, tt = g % 6, ks = i % 20;
            f32x4& d = acc[i / 20][tt];
            if constexpr (tt == 5) { if (ks == 0) MF_VV0(d, wl[i & 1], xw[i % 3]); else MF_VV(d, wl[i & 1], xw[i % 3]); }
            else if constexpr (20 * tt + ks < 64) { if (ks == 0) MF_AV0(d, wf[tt][ks], xw[i % 3]); else MF_AV(d, wf[tt][ks], xw[i % 3]); }
            else { if (ks == 0) MF_VV0(d, wf[tt][ks], xw[i % 3]); else MF_VV(d, wf[tt][ks], xw[i % 3]); }
            SCHED0();
            if constexpr (tt == 1 && i < 38) xw[(i + 2) % 3] = *(const bf16x8*)(smem + vx + (i + 2) % 20 * 2048 + (i + 2) / 20 * 1024);
            if constexpr (tt == 3) wl[~i & 1] = *(const bf16x8*)(sW + (i + 1) % 20 * 1024);
            NEXT_ADDR(2, 6);
            if (g == 4) { PIN_S(s); po = (s & (TR - 1)) << 21; PIN_S(po); }
            if (g == 10) { so = (po >> 8) & 8192; PIN_S(so); }
            if (g == 16) { po = (po0 ^ so) + po; PIN_S(po); }
            if (g == 58) { VMW(0); }
            if (g == 59) sst4(rx, pend, fI, 1u);
            if (g == 60) asm volatile("s_barrier" ::: "memory");
            if (g == 62) { PIN_S(s); pend = s + it0; so = pend & 7; PIN_S(pend); PIN_S(so); }
            if (g == 64) { pend = (pend >> 3) << 4; PIN_S(pend); }
            if (g == 66) { pend = (pend | so) << 10; PIN_S(pend); }
            if (g == 68) { ld = 2 * lds0 + 40960 - ld; PIN_S(ld); }
            if constexpr (g >= 122 && g < 140) {
                constexpr int f = (g - 122) / 6;
                if (tt == 2) CVT2(o[0][0], o[0][1], acc[0][2 * f]);
                if (tt == 4) CVT2(o[0][2], o[0][3], acc[0][2 * f + 1]);
                if (tt == 0) sst16(rx, po, l16s + f * 1024, o[0]);
            }
            if (g == 140) { po += 16384; PIN_S(po); }
            NEXT_LOOK(0, ld);
            {
                constexpr int a[10] = {186, 190, 194, 198, 202, 206, 214, 218, 222, 226};
#pragma unroll
                for (int e = 0; e < 10; ++e) {
                    if (g == a[e]) { if (e % 5 == 0) XSET(so, xs, ld); else XADV(so); }
                    if (g == a[e] + (e == 5 ? 6 : 2)) XDMA(so, e / 5);
                }
            }
            if (g == 236) { vx ^= 40960; PIN_V(vx); }
            SCHED0();
        });
        NEXT_GO(5, ld, fI, 3);
    }
}

constexpr int OL_V = 40960, OL_W = OL_V + 12288;
__device__ __forceinline__ void vo_role(const MegaArgs& A, char* smem, const int L, const int h, const int id) {
    WORK_IDS;
    bf16x8 wf[3][20], wo[10][6];
    char* sW = smem + OL_W + w * 24576 + l * 16;
    {
        load_vo<0>(smem, w, l, go, A.Wv + h * DH_, A.Wo + (size_t)(h * DH_) * D_, wf, [&](int k, int ct, int) -> bf16x8& { return wo[ct][k]; });
#pragma unroll
        for (int i = 0; i < 24; ++i) *(bf16x8*)(sW + i * 1024) = wo[4 + i / 8 * 2 + (i & 1)][2 + (i >> 1 & 3)];
    }
    GO_ON(N_1);
    const unsigned fU = l == 0 ? lp(SG_FL + FL_U + h * 64 + w * 4) : OOB;
    const unsigned l16s = l16b;
    const unsigned ua = xm(UT_OFF + (320 * w + h) * 1024, L) + l16s, ub = ua ^ (w == 3 ? 8192 : 0);
    char* sV = smem + OL_V + l * 16;
    char* sVw = sV + w * 1024;
    char* sVh = sV + 4096 + (w >> 1) * 1024 + (w & 1) * 8;
    NEXT_DECL(false);
    unsigned uo;
    constexpr int XW = 4;
    bf16x8 xw[XW], wl[4];
    f32x4 d[4][2];
    u32x4 o[2];
    auto prime = [&]() {
#pragma unroll
        for (int i = 0; i < XW - 1; ++i) xw[i] = *(const bf16x8*)(smem + l16b + i * 2048);
    };
    auto tail = [&]() {
#pragma unroll
        for (int f = 0; f < 2; ++f) { CVT2(o[f][0], o[f][1], d[f][0]); CVT2(o[f][2], o[f][3], d[f][1]); sst16(rx, uo + f * 65536, ub, o[f]); SCHED0(); }
    };
    prime();
#pragma unroll 1
    for (;; ++s) {
        f32x4 acc[2][3];
        u32x4 vf[2][6];
        unsigned cur = xb;
        SCHED0();
        sfor<0, 240>([&](auto G) {
            constexpr int g = decltype(G)::value;
            constexpr int rt = g < 120 ? g / 60 : (g < 144 ? 0 : (g < 168 ? 1 : g & 1));
            constexpr int ct = g < 144 ? g & 3 : (g < 168 ? (g - 144) / 12 * 2 + (g & 1) : (g - 120) / 24 * 2 + (g >> 1 & 1));
            constexpr int kk = g < 144 ? (g + 24) % 24 / 4 : (g < 168 ? g % 12 / 2 : g % 24 / 4), wi = (g - 168) / 24 * 8 + (g % 24 - 8) / 2;
            if constexpr (g < 120) { if (g % 60 < 3) MF_AV0(acc[rt][g % 3], wf[g % 3][0], xw[g / 3 % XW]); else MF_AV(acc[rt][g % 3], wf[g % 3][g % 60 / 3], xw[g / 3 % XW]); }
            else if constexpr (ct >= 4 && kk >= 2) MF_VV(d[ct & 3][rt], vf[rt][kk], wl[wi & 3]);
            else if constexpr (ct == 0 && kk < 4) { if (kk == 0) MF_VA0(d[0][rt], vf[rt][0], wo[0][0]); else MF_VA(d[0][rt], vf[rt][kk], wo[0][kk]); }
            else { if (kk == 0) MF_VV0(d[ct & 3][rt], vf[rt][0], wo[ct][0]); else MF_VV(d[ct & 3][rt], vf[rt][kk], wo[ct][kk]); }
            SCHED0();
            if constexpr (g % 3 == 1 && g / 3 + XW - 1 < 40) { constexpr int i = g / 3 + XW - 1; xw[i % XW] = *(const bf16x8*)(smem + l16b + i % 20 * 2048 + i / 20 * 1024); }
            NEXT_ADDR(2, 3);
            if (g == 24) { PIN_S(s); uo = (s & (TR - 1)) << 22; PIN_S(uo); }
            if (g == 48) { VMW(0); }
            if (g == 49) sst4(rx, pend, fU, 1u);
            if (g == 50) asm volatile("s_barrier" ::: "memory");
            if (g == 51) { pend = cur ^ (L << 13); PIN_S(pend); }
            if constexpr (g >= 60 && g < 140 && g % 60 < 40) {
                constexpr int r = g / 60 - 1, q = r ? g - 120 : (g % 3 == 1 ? -1 : (g - 60) / 3 * 2 + g % 3 / 2);
                if (q == 1) CVT2(o[0][0], o[0][1], acc[r][0]);
                if (q == 2) CVT2(o[0][2], o[0][3], acc[r][1]);
                if (q == 3) CVT2(o[1][0], o[1][1], acc[r][2]);
                if (q == 4) *(u32x4*)(sVw + r * 6144) = o[0];
                if (q == 5) *(u32x2*)(sVh + r * 6144) = u32x2{o[1][0], o[1][1]};
                if (q == 11) asm volatile("s_waitcnt lgkmcnt(%0)\n\ts_barrier" :: "n"(r ? 0 : 3) : "memory");
                if constexpr (q >= 12 && q < 18) vf[r][q - 12] = *(const u32x4*)(sV + r * 6144 + (q - 12) * 1024);
            }
            if constexpr (g >= 168 && g % 24 < 18 && g % 24 != 8 && !(g & 1)) {
                constexpr int j = (g - 168) / 24 * 8 + g % 24 / 2 - (g % 24 > 8);
                wl[j & 3] = *(const bf16x8*)(sW + j * 1024);
            }
            if constexpr (g >= 156) {
                constexpr int p = g < 168 ? 0 : (g - 144) / 24, q = g < 168 ? g - 156 : g % 24, c0 = 2 * p & 3;
                if (q == 1) CVT2(o[0][0], o[0][1], d[c0][0]);
                if (q == 3) CVT2(o[0][2], o[0][3], d[c0][1]);
                if (q == 5) sst16(rx, uo, p ? ub : ua, o[0]);
                if (q == 7) CVT2(o[1][0], o[1][1], d[c0 + 1][0]);
                if (q == 8) { uo += 65536; PIN_S(uo); }
                if (q == 9) CVT2(o[1][2], o[1][3], d[c0 + 1][1]);
                if (q == 11) sst16(rx, uo, p ? ub : ua, o[1]);
            }
            if constexpr (g == 168 || g == 181 || g == 205 || g == 229) { uo += 65536; PIN_S(uo); }
            NEXT_LOOK(4, lds0);
            {
                constexpr int a[10] = {186, 189, 192, 200, 205, 209, 213, 216, 224, 231}, b[10] = {188, 191, 196, 204, 207, 212, 215, 220, 228, 233};
#pragma unroll
                for (int e = 0; e < 10; ++e) {
                    if (g == a[e]) { if (e % 5 == 0) XSET(so, xs, lds0); else XADV(so); }
                    if (g == b[e]) XDMA(so, e / 5);
                }
            }
            SCHED0();
        });
        NEXT_GO(7, lds0, fU, 2);
    }
}

#define G_B 83
#define BSLP 6
constexpr int BL_Q = 40960, BL_P = BL_Q + 49152;
__device__ __forceinline__ void b_role(const MegaArgs& A, char* smem, const int L, const int hb, const int id) {
    WORK_IDS;
    const unsigned t0 = rtime();
    bf16x8 wf[4][20];
    unsigned dst[4];
    const int sw = (l16 & 6) << 3;
    {
        const void* const base[4] = {A.wqkv + 80 * hb, A.wqkv + 80 * hb, A.wqkv + 80 * hb, A.wqkv + 80 * hb};
        unsigned vo[4];
        int rp[4];
#pragma unroll
        for (int m = 0; m < 4; ++m) {
            const int idx = min((4 * m + w) * 64 + l, 959), c = idx % 30, T = min(4 * m + w, 14), e = 16 * (T % 5) + 4 * lq;
            vo[m] = (idx / 30) * 3840 + (c / 10) * (2 * D_) + (c % 10) * 16;
            rp[m] = (4 * lq + (l16 >> 2)) * 480 + 32 * T + (l & 3) * 8;
            dst[m] = lds_addr(smem) + BL_Q + (T / 5 * 4 + e / 20) * 4096 + l16 * 64 + ((e % 20 * 2) ^ sw);
        }
        while (rtime() - t0 < (L ? DLYB1 : DLYB0)) __builtin_amdgcn_s_sleep(8);
        load_coop<4, 0, 4>(smem, w, go, base, vo, 32 * 3840, rp, 7680, wf);
    }
    GO_ON(N_1);
    f32x4 cs[4];
    u32x4 one = {0x3f803f80u, 0x3f803f80u, 0x3f803f80u, 0x3f803f80u};
    {
        asm volatile("s_nop 1" : "+v"(one));
        sfor<0, 80>([&](auto G) { constexpr int g = decltype(G)::value, tt = g & 3, ks = g >> 2;
            if (ks == 0) MF_AV0(cs[tt], wf[tt][0], one); else if (tt < 3 || ks < 4) MF_AV(cs[tt], wf[tt][ks], one); else MF_VV(cs[tt], wf[tt][ks], one); });
        asm volatile("s_nop 7" : "+v"(cs[0]), "+v"(cs[1]), "+v"(cs[2]), "+v"(cs[3]));
    }
#pragma unroll
    for (int i = 0; i < 12; ++i) *(u32x4*)(smem + BL_Q + i * 4096 + t * 16) = u32x4{0u, 0u, 0u, 0u};
    const bool odd = l16 & 1;
    f32x4 wm[3];
#pragma unroll
    for (int e = 0; e < 8; ++e) { const int d = l16 + 16 - 16 * (e >> 2) - 4 * lq - (e & 3); wm[e >> 2][e & 3] = d >= 0 && d <= 14 && !(d & 1) ? 0.f : -INFINITY; }
    wm[2] = f32x4{-INFINITY, -INFINITY, -INFINITY, -INFINITY};
    const unsigned ps0 = odd ? 0x01000c0cu : 0x0c0c0100u, ps1 = odd ? 0x03020c0cu : 0x0c0c0302u;
    unsigned qa = lds_addr(smem) + BL_Q + w * 4096 + l16 * 64 + ((lq * 16) ^ sw), qb = qa ^ 2048, va[6];
    {
        const int row = 4 * lq + (l16 >> 2), vs = (row & 6) << 3;
        va[0] = va[2] = lds_addr(smem) + BL_Q + (8 + w) * 4096 + row * 64 + ((8 * (w & 1) + 8 * (l & 3)) ^ vs);
        va[1] = va[3] = lds_addr(smem) + BL_Q + (8 + w) * 4096 + row * 64 + ((32 * (~w & 1) + 8 * (l & 3)) ^ vs);
        va[4] = va[0] ^ 2048; va[5] = va[1] ^ 2048;
#pragma unroll
        for (int e = 0; e < 6; ++e) PIN_V(va[e]);
    }
    const int g0 = (20 * hb + 5 * w + (w & 1) + (lq & 2)) >> 1, g1 = (20 * hb + 5 * w + 4 * (~w & 1)) >> 1;
    const unsigned oo0 = lp(SG_OB + (g0 >> 2) * 2048 + (lq & 1) * 1024 + (g0 & 3) * 256 + l16 * 16);
    const unsigned oo1 = lq < 2 ? lp(SG_OB + (g1 >> 2) * 2048 + lq * 1024 + (g1 & 3) * 256 + l16 * 16 + (w & 1) * 8) : OOB;
    const unsigned fO = t < 4 ? lp(SG_FL + FL_O + (8 * t + hb) * 128) : OOB;
    const unsigned st0 = lp(SG_ST + (4 * (l >> 5) + w) * 256 + (l & 31) * 8), st2 = l < 32 ? st0 + 4096 : OOB;
    unsigned fXv = xm(FX_OFF + (hb >> 1) * 64, L), xv, X = rtime() - 2 * PER, xs = OOB;
    PIN_V(fXv);
    const float z2 = l < 32 ? 1.f : 0.f;
    f32v2* sP = (f32v2*)(smem + BL_P) + (l & 31);
    unsigned sK = lds_addr(smem) + BL_P + 1024, sKw = sK + 4 * w;
    PIN_V(sK); PIN_V(sKw);
    unsigned pend = OOB, sb = seg_base(L, 0), so, fs = fx_step(1 + L * A.lag);
    const int i0 = 1 + L * A.lag;
    for (int s = 0; s < NTOUCH; ++s) { sst16(rx, seg_base(L, s), oo0, u32x4{0u, 0u, 0u, 0u}); sst8(rx, seg_base(L, s), oo1, u32x2{0u, 0u}); }
    u32x2 st[3];
    auto look = [&](int e, unsigned b, unsigned f) { if (e == 0) LDA8(st[0], st0, b, 0); if (e == 1) LDA8(st[1], st0, b, 2048); if (e == 2) LDA8(st[2], st2, b, 0); if (e == 3) LDA4(xv, fXv, f); };
#define SEEN(n) asm volatile("s_waitcnt vmcnt(%4)" : "+v"(st[0]), "+v"(st[1]), "+v"(st[2]), "+v"(xv) : "n"(n) : "memory")
#define SWAP2(op, a, b) asm volatile(op " %0, %1" : "+v"(a), "+v"(b))
    int s;
    SZERO(s);
#pragma unroll 1
    do {
        if (xs == OOB) {
            bool e = (int)(rtime() - X - E_B) >= 0;
#pragma unroll
            for (int i = 0; i < 4; ++i) look(i, e ? sb : OOB, e ? fs : OOB);
            VMB(4);
            sst4(rx, pend, fO, 1u);
            pend = OOB;
            SEEN(1);
#pragma unroll 1
            for (;;) {
                xv = __builtin_amdgcn_readfirstlane(xv);
                X = xv ? xv : X;
                LIVE;
                if (e) { if (!BAD(umax3(st[0][0], st[1][0], st[2][0]))) break; __builtin_amdgcn_s_sleep(BSLP); }
                e = true;
                rt_until(X + E_B);
#pragma unroll
                for (int i = 0; i < 4; ++i) look(i, sb, fs);
                SEEN(0);
            }
            XSET(so, sb + lp(SG_X1), lds0); XDMA(so, 0); XDMA(so, 1); XADV(so); XDMA(so, 0); XDMA(so, 1);
        }
        VMB(2);
        f32x4 acc[4][2];
        unsigned cur;
        float rd[2];
        u32x2 y[2];
        bf16x8 kf[3], qf[2];
        u32x4 k4;
#define MUL2(x, i, r) do { asm volatile("v_mul_f32 %0, %1, %0" : "+v"(x[i]) : "v"(r)); x[i + 1] *= r; USE_V(x[i + 1]); } while (0)
#define FMA2(x, i, c, m) do { asm volatile("v_fma_f32 %0, -%2, %1, %0" : "+v"(x[i]) : "v"(c[i]), "v"(m)); x[i + 1] = __builtin_fmaf(-m, c[i + 1], x[i + 1]); USE_V(x[i + 1]); } while (0)
#define ADD1(d, x, z) asm volatile("v_add_f32 %0, %1, %2" : "=v"(d) : "v"(x), "v"(z))
        auto fin = [&](int e, int u) {
            if (u < 6 && u % 3 < 2) MUL2(acc[e][u / 3], 2 * (u % 3), rd[u / 3]);
            if (u % 3 == 2) CVT2(y[u / 3][0], y[u / 3][1], acc[e][u / 3]);
            if (u == 6) { LDS_AT(u32x2, dst[e]) = y[0]; LDS_AT(u32x2, dst[e] + 1024) = y[1]; dst[e] ^= 2048; USE_V(dst[e]); }
        };
        {
            bf16x8 xw[6][2];
#pragma unroll
            for (int e = 0; e < 4; ++e) xw[e >> 1][e & 1] = *(const bf16x8*)(smem + l16b + e * 1024);
            SCHED0();
            u64 now, bad;
            float a, b, c, d, mw, mb[2];
            unsigned lb, lf;
            f32v2 p0, p1, p2, p3;
            sfor<0, 160>([&](auto G) {
                constexpr int g = decltype(G)::value, ks = g < 112 ? g / 8 : (g < 124 ? 14 + (g - 112) / 6 : (g < 148 ? 16 + (g - 124) % 8 / 2 : 14 + (g - 148) / 2));
                constexpr int tt = g < 112 ? g % 8 / 2 : (g < 124 ? (g - 112) % 6 / 2 : (g < 148 ? (g - 124) / 8 : 3)), rt = g & 1;
                constexpr int q = g >= 112 || g % 8 == 1 || g % 8 == 3 || g % 8 == 7 ? -1 : g / 8 * 5 + (g % 8 < 4 ? g % 8 / 2 : g % 8 - 2);
                if (ks == 0) MF_AV0(acc[tt][rt], wf[tt][0], xw[0][rt]);
                else if (tt < 3 || ks < 4) MF_AV(acc[tt][rt], wf[tt][ks], xw[ks % 6][rt]); else MF_VV(acc[tt][rt], wf[tt][ks], xw[ks % 6][rt]);
                SCHED0();
                constexpr int rq = g < 112 ? (g % 8 == 1 || g % 8 == 3 ? 2 * ks + 4 + g % 8 / 2 : -1) : (g < 124 && g % 3 != 0 ? 32 + (g - 112) / 3 * 2 + (g - 112) % 3 : -1);
                if constexpr (rq >= 0) xw[rq / 2 % 6][rq & 1] = *(const bf16x8*)(smem + l16b + rq * 1024);
                if (q == 0 || q == 5 || q == 11) XADV(so);
                if (q == 1 || q == 6 || q == 13) XDMA(so, 0);
                if (q == 3 || q == 8 || q == 15) XDMA(so, 1);
                if (q == 10) VMB_LATE(4);
                if (q == 2) { ADD1(c, st[0][1], st[1][1]); a = __uint_as_float(st[0][0]) + __uint_as_float(st[1][0]); USE_V(a); }
                if (q == 4) { ADD1(b, a, st[2][0]); a += __uint_as_float(st[2][0]); USE_V(a); }
                if (q == 7) { SWAP2("v_permlane32_swap_b32", a, b); SCHED0(); c += __uint_as_float(st[2][1]); USE_V(c); }
                if (q == 9) { mw = (a + b) * 0.2f; USE_V(mw); }
                if (q == 12) { a = __uint_as_float(st[0][0]) - mw; b = __uint_as_float(st[1][0]) - mw; USE_V(a); USE_V(b); }
                if (q == 14) { d = (__uint_as_float(st[2][0]) - mw) * z2; USE_V(d); }
                if (q == 16) { a = __builtin_fmaf(b, b, a * a); USE_V(a); }
                if (q == 17) { c = __builtin_fmaf(32.f, __builtin_fmaf(d, d, a), c); USE_V(c); }
                if (q == 18) { b = c; PIN_V(b); }
                if (q == 19) SWAP2("v_permlane32_swap_b32", c, b);
                if (q == 21) { c += b; sP[w * 32] = f32v2{mw, c}; }
                if (q == 22) { cur = sb; sb = (s + 1) << 19; PIN_S(cur); PIN_S(sb); }
                if (q == 23) { lb = (sb >> 8) & 8192; PIN_S(lb); }
                if (q == 24) { sb = (sb | lb) + 2 * SG_OFF; PIN_S(sb); }
                if (q == 26) { sb ^= L << 13; X += PER; PIN_S(sb); PIN_S(X); }
                if (q == 27) { fs += 256; lb = fs & 8192; PIN_S(fs); PIN_S(lb); }
                if (q == 28) { fs += lb; PIN_S(fs); }
                if (q == 34) { TIGHT(lb, s + i0, D_1); X -= lb; PIN_S(X); }
                if (q == 35) { asm volatile("s_cmp_eq_u32 %1, %2\n\ts_cselect_b32 %0, %3, 0" : "=s"(lb) : "s"(s + i0), "n"(NSEG_ + LAGV - 1), "s"(E_Z) : "scc"); X -= lb; PIN_S(X); }
                if (q == 30) asm volatile("s_waitcnt vmcnt(4) lgkmcnt(2)\n\ts_barrier" ::: "memory");
                if (q == 31) sst4(rx, pend, fO, 1u);
                if (q == 32) { p0 = sP[0]; p1 = sP[32]; }
                if (q == 33) { p2 = sP[64]; p3 = sP[96]; }
                if (q == 36) ADD1(a, p0[0], p1[0]);
                if (q == 37) { b = p2[0] + p3[0]; USE_V(b); }
                if (q == 38) { mw = (a + b) * 0.25f; USE_V(mw); }
                if (q == 39) { a = p0[0] - mw; PIN_V(a); b = p1[0] - mw; PIN_V(b); }
                if (q == 40) { c = p2[0] - mw; PIN_V(c); }
                if (q == 41) { mb[0] = rb(mw); PIN_V(mb[0]); }
                if (q == 42) { d = p3[0] - mw; PIN_V(d); mb[1] = mb[0]; PIN_V(mb[1]); }
                if (q == 43) { a = __builtin_fmaf(b, b, a * a); PIN_V(a); }
                if (q == 44) { SWAP2("v_permlane16_swap_b32", mb[0], mb[1]); ADD1(b, p0[1], p1[1]); }
                if (q == 45) { a = __builtin_fmaf(c, c, a); PIN_V(a); }
                if (q == 50) VMB(1);
                if (q == 55) { a = __builtin_fmaf(d, d, a); PIN_V(a); }
                if (g == 73) ADD1(c, p2[1], p3[1]);
                if (g == 75) ADD1(b, b, c);
                if (g == 89) { a = __builtin_fmaf(160.f, a, b); PIN_V(a); }
                if (g == 91) { a *= 1.0f / D_; PIN_V(a); }
                if (q == 60) asm volatile("v_cvt_pk_bf16_f32 %0, 0, %0" : "+v"(a));
                if (g == 97) { a += 1e-5f; PIN_V(a); }
                if (g == 99) asm volatile("v_cvt_pk_bf16_f32 %0, 0, %0" : "+v"(a));
                if (q == 65) { a = __builtin_amdgcn_sqrtf(a); PIN_V(a); }
                if (g == 105) asm volatile("v_cvt_pk_bf16_f32 %0, 0, %0" : "+v"(a));
                if (q == 66) { rd[0] = __builtin_amdgcn_rcpf(a); PIN_V(rd[0]); }
                if (q == 67) { rd[1] = rd[0]; PIN_V(rd[1]); }
                if (q == 69) SWAP2("v_permlane16_swap_b32", rd[0], rd[1]);
                if constexpr (g >= 72 && g < 104 && q >= 0 && g % 8) {
                    constexpr int e = (g % 8 < 5 ? g % 8 + 2 : 2 * (g % 8) - 10) + (g >= 88), i = g >> 2 & 2;
                    FMA2(acc[e >> 1][e & 1], i, cs[e >> 1], mb[e & 1]);
                }
                if (g == G_B) asm volatile("s_memrealtime %0" : "=s"(now));
                if (g == G_B + 4) asm volatile("s_waitcnt lgkmcnt(0)" : "+s"(now));
                if (g == G_B + 12) { lf = (unsigned)now - X; PIN_S(lf); }
                if (g == G_B + 20) { lf -= E_B; PIN_S(lf); }
                if (g == G_B + 26) asm volatile("s_cmp_lt_i32 %1, 63\n\ts_cselect_b32 %0, %0, -1" : "+s"(lf) : "s"(s) : "scc");
                if (g == G_B + 28) asm volatile("s_cmp_ge_i32 %1, 0\n\ts_cselect_b32 %0, %2, %4\n\ts_cselect_b32 %1, %3, %4" : "=&s"(lb), "+s"(lf) : "s"(sb), "s"(fs), "n"(OOB) : "scc");
                if constexpr (g >= G_B + 29 && g < G_B + 33) look(g - G_B - 29, lb, lf);
                if constexpr (g >= 132 && g < 155 && (g - 132) % 8 < 7) fin((g - 132) / 8, (g - 132) % 8);
                if (g == 155) LDS_BARRIER();
                if (g == 156) { kf[0] = LDS_AT(bf16x8, qb + 17408); qf[0] = LDS_AT(bf16x8, qa); }
                if (g == 157) { kf[1] = LDS_AT(bf16x8, qa + 16384); qf[1] = LDS_AT(bf16x8, qa + 1024); }
                if (g == 158) { kf[2] = LDS_AT(bf16x8, qa + 17408); SEEN(0); }
                if (g == 159) { lf = umax3(st[0][0], st[1][0], st[2][0]); bad = __ballot(lf == ~0u); PIN_S(bad); }
                SCHED0();
            });
            xv = __builtin_amdgcn_readfirstlane(xv);
            asm volatile("s_cmp_eq_u64 %1, 0\n\ts_cselect_b32 %0, %0, %2" : "+s"(lb) : "s"(bad), "n"(OOB) : "scc");
            LDS_AT(unsigned, sKw) = lb;
            X = xv ? xv : X;
            PIN_S(X);
        }
        {
            constexpr float SC = 0.22360679774997896f * 1.4426950408889634f;
            bf16x8 vx[2][2];
            f32x4 sc[4];
            SCHED0();
            fin(3, 0); fin(3, 1);
            SCHED0();
            MF_VVC(sc[0], kf[0], qf[0], wm[2]); SCHED0(); fin(3, 2); SCHED0();
            MF_VVC(sc[1], kf[1], qf[0], wm[1]); SCHED0(); fin(3, 3); SCHED0();
            MF_VVC(sc[2], kf[1], qf[1], wm[0]); SCHED0(); fin(3, 4); SCHED0();
            MF_VVC(sc[3], kf[2], qf[1], wm[1]); SCHED0(); fin(3, 5); fin(3, 6);
            SCHED0();
            float sv[4][2];
            float m0, m1;
#define MAXQ(m, x, y, p, q) asm volatile("v_max_f32 %1, %5, %6\n\tv_max_f32 %2, %7, %8\n\tv_max_f32 %3, %9, %10\n\tv_max_f32 %4, %11, %12\n\tv_max3_f32 %0, %1, %2, %3\n\tv_max_f32 %0, %0, %4" \
    : "=&v"(m), "=&v"(x[0]), "=&v"(x[1]), "=&v"(y[0]), "=&v"(y[1]) : "v"(p[0]), "v"(p[1]), "v"(p[2]), "v"(p[3]), "v"(q[0]), "v"(q[1]), "v"(q[2]), "v"(q[3]))
            MAXQ(m0, sv[0], sv[1], sc[0], sc[1]);
            MAXQ(m1, sv[2], sv[3], sc[2], sc[3]);
            LDS_BARRIER();
            k4 = LDS_AT(u32x4, sK);
            SCHED0();
            auto tr = [](unsigned a) { return __builtin_amdgcn_ds_read_tr16_b64_v4i16((lds_b4*)a); };
#pragma unroll
            for (int dt = 0; dt < 2; ++dt) { vx[0][dt] = cat8(tr(va[4 + dt] + 1024), tr(va[dt])); vx[1][dt] = cat8(tr(va[2 + dt]), tr(va[dt] + 1024)); }
            SCHED0();
            red_max_q2(m0, m1);
            m0 *= -SC; m1 *= -SC;
            USE_V(m0); USE_V(m1);
            SCHED0();
            xs = __builtin_amdgcn_readfirstlane(max(umax3(k4[0], k4[1], k4[2]), k4[3]));
            u32x4 pt[2];
            f32x4 o[2][2], sm[2];
#pragma unroll
            for (int qt = 0; qt < 2; ++qt) {
#pragma unroll
                for (int e = 0; e < 4; ++e) sv[2 * qt + (e >> 1)][e & 1] = __builtin_amdgcn_exp2f(__builtin_fmaf(sv[2 * qt + (e >> 1)][e & 1], SC, qt ? m1 : m0));
                if (!qt) XSET(so, xs + lp(SG_X1), lds0);
#pragma unroll
                for (int e = 0; e < 2; ++e) {
                    const unsigned p = pk2(sv[2 * qt + e][0], sv[2 * qt + e][1]);
                    pt[qt][2 * e] = __builtin_amdgcn_perm(p, p, ps0);
                    pt[qt][2 * e + 1] = __builtin_amdgcn_perm(p, p, ps1);
                }
                USE_V(pt[qt]);
                SCHED0();
                XDMA(so, qt);
                asm volatile("s_nop 0");
                MF_VV0(sm[qt], one, pt[qt]);
                if (!qt) MF_VV0(o[0][0], vx[0][0], pt[0]);
                SCHED0();
            }
            XADV(so);
            MF_VV0(o[1][0], vx[1][0], pt[1]);
            PIN_V(sm[0]);
            const float s0 = __builtin_amdgcn_rcpf(sm[0][0]);
            USE_V(s0);
            XDMA(so, 0);
            MF_VV0(o[0][1], vx[0][1], pt[0]);
            SCHED0();
            PIN_V(o[0][0]);
            MUL2(o[0][0], 0, s0);
            SCHED0();
            MF_VV0(o[1][1], vx[1][1], pt[1]);
            SCHED0();
            PIN_V(sm[1]);
            const float s1 = __builtin_amdgcn_rcpf(sm[1][0]);
            USE_V(s1);
            MUL2(o[0][0], 2, s0);
            XDMA(so, 1);
            PIN_V(o[1][0]);
            MUL2(o[1][0], 0, s1); MUL2(o[1][0], 2, s1);
            {
                const auto a = __builtin_amdgcn_permlane16_swap(pk2(o[0][0][0], o[0][0][1]), pk2(o[1][0][0], o[1][0][1]), false, false);
                const auto b = __builtin_amdgcn_permlane16_swap(pk2(o[0][0][2], o[0][0][3]), pk2(o[1][0][2], o[1][0][3]), false, false);
                sst16(rx, cur, oo0, u32x4{a[0], b[0], a[1], b[1]});
            }
            SCHED0();
            asm volatile("" : "+v"(o[0][1]), "+v"(o[1][1]));
            o[0][1] *= s0; o[1][1] *= s1;
            {
                const auto a = __builtin_amdgcn_permlane16_swap(pk2(o[0][1][0], o[0][1][1]), pk2(o[1][1][0], o[1][1][1]), false, false);
                const auto b = __builtin_amdgcn_permlane16_swap(pk2(o[0][1][2], o[0][1][3]), pk2(o[1][1][2], o[1][1][3]), false, false);
                sst8(rx, cur, oo1, u32x2{a[0], b[0]});
            }
            SCHED0();
            wm[2] = wm[0];
            qa ^= 2048; qb ^= 2048;
            PIN_V(wm[2]); PIN_V(qa); PIN_V(qb);
#pragma unroll
            for (int e = 0; e < 6; ++e) { va[e] ^= 2048; PIN_V(va[e]); }
        }
        pend = cur;
    } while (++s < NSEG_);
    VMB(0);
    sst4(rx, pend, fO, 1u);
    SPIN_END;
}

__device__ __forceinline__ void bo_role(const MegaArgs& A, char* smem, const int L, const int j, const int id) {
    WORK_IDS;
    const unsigned t0 = rtime();
    const int rt = w & 1, hf = w >> 1;
    if (id == 31 && w == 0) probe(A, go, l, id);
    INIT_BEGIN(id >> 5);
    if (L) { INIT_END(56 + j); }
    bf16x8 wf[5][20];
    {
        const void* const base[3] = {A.wo + 160 * j, A.wo + 160 * j, A.wo + 160 * j};
        unsigned vo[3];
        int rp[5];
#pragma unroll
        for (int m = 0; m < 3; ++m) { const int idx = min((4 * m + w) * 64 + l, 639); vo[m] = (idx / 20) * (2 * D_) + (idx % 20) * 16; }
#pragma unroll
        for (int m = 0; m < 5; ++m) { rp[m] = (8 * lq + (l16 >> 2)) * 320 + 32 * (m < 4 ? 4 * hf + m : 8 + hf) + (l & 3) * 8; }
        while (rtime() - t0 < (L ? DLYB1 : DLYB0)) __builtin_amdgcn_s_sleep(8);
        load_coop<3, 0, 5>(smem, w, go, base, vo, 64 * D_, rp, 1280, wf);
    }
    INIT_SEEN;
    const unsigned x0 = lp((5 * j + 2 * hf) * 2048 + rt * 1024 + l16b), x1 = lp((5 * j + 2 * hf + 1) * 2048 + rt * 1024 + l16b), x2 = lp((5 * j + 4) * 2048 + rt * 1024 + l16b + hf * 8);
    const unsigned fO = l < 8 ? lp(SG_FL + FL_O + (8 * j + l) * 128) : OOB, fX = t < 8 ? lp(SG_FL + FL_X + (4 * (t & 3) + j) * 128) + (t & 4) * 2048 : OOB;
    const unsigned sr = lp(SG_ST + ST_2 + rt * 1024 + lq * 256 + l16 * 8), sw = l < 16 ? sr + j * 256 + hf * 128 : OOB;
    auto flag = [&](unsigned b) { sst4(rx, b & ~8192u, fX ^ (b & 8192u), 1u); };
    const rsrc_t ro = mk_rsrc(A.out, S_ * D_ * 2);
    const unsigned oo = ((16 * rt + l16) * D_ + 160 * j + 64 * hf + 4 * lq) * 2, oo2 = oo + (128 - 48 * hf) * 2;
    const char* sX = smem + rt * 1024 + l16b;
    unsigned pend = OOB, sb = seg_base(L, 0), so, xs = OOB;
    unsigned* sK = (unsigned*)(smem + 40960);
    auto first = [&](unsigned b) { XSET(so, b + lp(SG_OB), lds0); XDMA(so, 0); XDMA(so, 1); XADV(so); XDMA(so, 0); XDMA(so, 1); XADV(so); XDMA(so, 0); XDMA(so, 1); };
    int s;
    SZERO(s);
#pragma unroll 1
    do {
        if (xs == OOB) {
            unsigned pv;
            LDA4(pv, fO, sb);
            VMB(1);
            flag(pend);
            pend = OOB;
            WAITV(1, pv);
            if (__ballot(pv == 0) & 255) wave_wait(rx, fO, sb, spins);
            first(sb);
            VMB(4);
        } else VMB(5);
        f32x4 acc[5], xf[5];
        u32x4 r0, r1, k4;
        u32x2 r2;
        unsigned cur;
        {
            bf16x8 xw[3];
            unsigned sx, pv;
            xw[0] = *(const bf16x8*)sX; xw[1] = *(const bf16x8*)(sX + 2048);
            SCHED0();
            sfor<0, 100>([&](auto G) {
                constexpr int g = decltype(G)::value, ks = g / 5, tt = g % 5;
                if constexpr (20 * tt + ks < 64) { if (ks == 0) MF_AV0(acc[tt], wf[tt][0], xw[0]); else MF_AV(acc[tt], wf[tt][ks], xw[ks % 3]); }
                else { if (ks == 0) MF_VV0(acc[tt], wf[tt][0], xw[0]); else MF_VV(acc[tt], wf[tt][ks], xw[ks % 3]); }
                SCHED0();
                if (g == 0 || g == 7) XADV(so);
                if (g == 2 || g == 12) XDMA(so, 0);
                if (g == 5 || g == 15) XDMA(so, 1);
                if (g == 3) { cur = sb; sx = sb + lp(SG_X1); PIN_S(cur); PIN_S(sx); }
                if (g == 10) VMB_LATE(4);
                if (g == 18) LDA(r0, x0, sx, 0);
                if (g == 22) LDA(r1, x1, sx, 0);
                if (g == 25) LDA8(r2, x2, sx, 0);
                if (g == 30) VMB(7);
                if (g == 32) flag(pend);
                if (g == 35) { sb = (s + 1) << 19; PIN_S(sb); }
                if (g == 37) { sx = (sb >> 8) & 8192; PIN_S(sx); }
                if (g == 38) { sb = (sb | sx) + 2 * SG_OFF; PIN_S(sb); }
                if (g == 40) { sb ^= L << 13; PIN_S(sb); }
                if (g == 42) asm volatile("s_cmp_lt_i32 %1, 63\n\ts_cselect_b32 %0, %0, %2" : "+s"(sb) : "s"(s), "n"(OOB) : "scc");
                if (g == 50) VMB(4);
                if constexpr (tt == 1 && ks < 18) xw[(ks + 2) % 3] = *(const bf16x8*)(sX + (ks + 2) * 2048);
                if (g == 60) LDA4(pv, fO, sb);
                if (g == 63) asm volatile("s_waitcnt vmcnt(2)" : "+v"(r0), "+v"(r1), "+v"(r2) :: "memory");
                if constexpr (g >= 65 && g < 90 && (tt == 2 || tt == 3)) {
                    constexpr int e = ks - 13, k = tt - 2;
                    const unsigned u = e < 2 ? r0[2 * (e & 1) + k] : (e < 4 ? r1[2 * (e & 1) + k] : r2[k]);
                    xf[e][2 * k] = lo16(u); xf[e][2 * k + 1] = hi16(u);
                    asm volatile("" : "+v"(xf[e][2 * k]), "+v"(xf[e][2 * k + 1]));
                }
                if (g == 90) WAITV(0, pv);
                if (g == 91) { sx = (unsigned)__ballot(pv == 0); PIN_S(sx); }
                if (g == 92) asm volatile("s_and_b32 %0, %0, 255\n\ts_cselect_b32 %0, %2, %1" : "+s"(sx) : "s"(sb), "n"(OOB) : "scc");
                if (g == 93) sK[w] = sx;
                if (g == 97) LDS_BARRIER();
                if (g == 98) k4 = *(const u32x4*)sK;
                SCHED0();
            });
        }
#pragma unroll
        for (int e = 0; e < 5; ++e) { PIN_V(acc[e]); acc[e] += xf[e]; PIN_V(acc[e]); SCHED0(); }
        xs = __builtin_amdgcn_readfirstlane(max(umax3(k4[0], k4[1], k4[2]), k4[3]));
        if (L) {
            if (xs != OOB) first(xs);
            const unsigned so2 = s * (SEG_ * D_ * 2);
#pragma unroll
            for (int e = 0; e < 5; ++e) __builtin_amdgcn_raw_buffer_store_b64(u32x2{pk2(acc[e][0], acc[e][1]), pk2(acc[e][2], acc[e][3])}, ro, e < 4 ? oo + 32 * e : oo2, so2, OUTAUX);
        } else {
            f32x4 a = ((acc[0] + acc[1]) + (acc[2] + acc[3])) + acc[4];
            const float mw = red_sum_q((a[0] + a[1]) + (a[2] + a[3])) * (1.0f / 80);
#pragma unroll
            for (int e = 0; e < 5; ++e) { xf[e] = acc[e]; acc[e] -= mw; }
            a = acc[0] * acc[0];
#pragma unroll
            for (int e = 1; e < 5; ++e) a = __builtin_elementwise_fma(acc[e], acc[e], a);
            sst8(rx, cur, sw, u32x2{nosent(__float_as_uint(mw)), __float_as_uint(red_sum_q((a[0] + a[1]) + (a[2] + a[3])))});
            SCHED0();
            if (xs != OOB) first(xs);
            r0 = pk8(xf[0], xf[1]); r1 = pk8(xf[2], xf[3]); r2 = u32x2{pk2(xf[4][0], xf[4][1]), pk2(xf[4][2], xf[4][3])};
            const unsigned so2 = cur + lp(SG_XR);
            sst16(rx, so2, x0, r0); sst16(rx, so2, x1, r1); sst8(rx, so2, x2, r2);
            VMW(3);
            u32x2 p, q;
#pragma unroll 1
            for (;;) {
                LDA8(p, sr, cur, 0); LDA8(q, sr, cur, 128);
                LIVE;
                asm volatile("s_waitcnt vmcnt(0)" : "+v"(p), "+v"(q) :: "memory");
                if (!BAD(max(p[0], q[0]))) break;
            }
            const float m0 = __uint_as_float(p[0]), m1 = __uint_as_float(q[0]), mean = red_sum_q(m0 + m1) * 0.125f, e0 = m0 - mean, e1 = m1 - mean;
            const float var = red_sum_q((__uint_as_float(p[1]) + __uint_as_float(q[1])) + 80.f * (e0 * e0 + e1 * e1)) * (1.0f / D_);
            const float rd = __builtin_amdgcn_rcpf(rb(__builtin_amdgcn_sqrtf(rb(rb(var) + 1e-5f)))), c = (mw - rb(mean)) * rd;
#pragma unroll
            for (int e = 0; e < 5; ++e) acc[e] = acc[e] * rd + c;
            sst16(rx, cur, x0, pk8(acc[0], acc[1])); sst16(rx, cur, x1, pk8(acc[2], acc[3])); sst8(rx, cur, x2, u32x2{pk2(acc[4][0], acc[4][1]), pk2(acc[4][2], acc[4][3])});
            pend = cur;
        }
    } while (++s < NSEG_);
    if (id == 159) bst4(go.rh, t < 8 ? t * 1024 + HS_E : OOB, tags);
    VMB(0);
    flag(pend);
    SPIN_END;
}

#define CT0 1
#define CT1 0
constexpr unsigned CODE_SZ = 100 * 1024, CODE_BK = 0;
constexpr int MEGA_LDS = 163840;
__global__ void __launch_bounds__(256, 1) mega_kernel(MegaArgs A) {
    extern __shared__ __attribute__((aligned(16))) char smem[];
    int role = 32 * (blockIdx.x & 7) + (blockIdx.x >> 3);
    PIN_S(role);
    const int pool = role >> 5, k = role & 31;
    {
        const unsigned t0 = rtime();
        if (k >= 8 && k < 13) {
            const char* pc;
            asm volatile("s_getpc_b64 %0" : "=s"(pc));
            auto touch = [&]() { const unsigned x = __builtin_amdgcn_raw_buffer_load_b32(mk_rsrc(pc, CODE_SZ), ((k - 8) * 256 + threadIdx.x) * 128 - CODE_BK, 0, 0); asm volatile("" :: "v"(x)); };
            if (CT0) touch();
            init_area(A, 5 * pool + k - 8, role, t0);
            if (CT1) { while (rtime() - t0 < CT1) __builtin_amdgcn_s_sleep(8); touch(); }
        }
        if (k >= 17 && k < 29 && ((k - 13) & 4)) { first_rows(A, smem, 8 * ((k - 17) / 8 * 4 + ((k - 17) & 3)) + pool, role, t0); while (rtime() - t0 < DLY2) __builtin_amdgcn_s_sleep(8); }
    }
    if (k < 13) {
        if (!(A.roles & 1)) return;
        if (k < 4) s_role(A, smem, 4 * pool + k, role);
        else if (k < 8) vm_role(A, smem, 4 * pool + k - 4, role);
        else c_role(A, smem, pool, k - 8, role);
        return;
    }
    if (k < 21) { if (A.roles & 2) qk_role(A, smem, (k - 13) >> 2, 4 * pool + ((k - 13) & 3), role); }
    else if (k < 29) { if (A.roles & 4) vo_role(A, smem, (k - 21) >> 2, 4 * pool + ((k - 21) & 3), role); }
    else if (k < 31) { if (A.roles & 8) b_role(A, smem, pool >> 2, 2 * (pool & 3) + k - 29, role); }
    else if (A.roles & 16) bo_role(A, smem, pool >> 2, pool & 3, role);
}

#define ROLES 31
extern "C" void kernel_launch(void* const* d_in, const int* in_sizes, int n_in,
                              void* d_out, int out_size, void* d_ws, size_t ws_size,
                              hipStream_t stream) {
    MegaArgs A;
    char* ws = (char*)d_ws;
    A.hs = ws; ws += 262144;
    A.tmo = (unsigned*)ws; ws += 4096;
    A.ar = ws;
    A.x0 = (const bf16_t*)d_in[0]; A.mem0 = (const bf16_t*)d_in[1]; A.Wq = (const bf16_t*)d_in[2]; A.Wk = (const bf16_t*)d_in[3]; A.Wv = (const bf16_t*)d_in[4];
    A.Wo = (const bf16_t*)d_in[5]; A.wqkv = (const bf16_t*)d_in[8]; A.wo = (const bf16_t*)d_in[9];
    A.out = (bf16_t*)d_out; A.pad = 0;
    A.lag = LAGV; A.nit = NSEG_ + LAGV; A.roles = ROLES;

    (void)hipFuncSetAttribute((const void*)mega_kernel, hipFuncAttributeMaxDynamicSharedMemorySize, MEGA_LDS);

    mega_kernel<<<256, 256, MEGA_LDS, stream>>>(A);
}
```
